```python
import torch

def _softmax(x, axis=-1):
    return torch.softmax(x, dim=axis)
def _gelu(x):
    return torch.nn.functional.gelu(x, approximate="tanh")
def _silu(x):
    return torch.nn.functional.silu(x)
def _sigmoid(x):
    return torch.sigmoid(x)
def _layernorm(x, axis=-1, eps=1e-5):
    m = x.mean(dim=axis, keepdim=True)
    v = x.var(dim=axis, keepdim=True, unbiased=False)
    return (x - m) / torch.sqrt(v + eps)
def _scatter_add(tgt, idx, src, axis=0):
    return tgt.clone().index_add_(axis, idx, src)
def _segment_sum(src, seg, n):
    out = torch.zeros((n,) + src.shape[1:], dtype=src.dtype)
    return out.index_add_(0, seg, src)
def _pad(x, pw):
    flat = tuple(v for pair in reversed(pw) for v in pair)
    return torch.nn.functional.pad(x, flat)

kernel_name = 'parallel_sum__ssm_conv_s4d__dense_activation_squared_relu__prompt'

def setup_inputs():

    _w = (lambda a: (lambda t: t.to(torch.bfloat16) if t.is_floating_point() else t)(torch.as_tensor(a)))
    def _A_setup():
        Bsz, L, D, N = 1, 2048, 512, 128
        return {
            "u": _w((lambda *s: torch.randn(*s, dtype=torch.bfloat16) * 0.05)(Bsz, L, D)),
            "log_A_real": _w((lambda *s: torch.randn(*s, dtype=torch.bfloat16) * 3.2)(D, N) * 0.5),
            "A_imag": _w((lambda *s: torch.randn(*s, dtype=torch.bfloat16) * 0.2)(D, N) * 3.14),
            "C_real": _w((lambda *s: torch.randn(*s, dtype=torch.bfloat16) * 3.2)(D, N) / N ** 0.5),
            "C_imag": _w((lambda *s: torch.randn(*s, dtype=torch.bfloat16) * 3.2)(D, N) / N ** 0.5),
            "log_dt": _w((lambda *s: torch.randn(*s, dtype=torch.bfloat16) * 0.2)(D) * 0.5 - 4.0),
            "D_skip": _w((lambda *s: torch.randn(*s, dtype=torch.bfloat16) * 0.4)(D)),
        }
    _A_in = _A_setup()

    def _B_setup():
        B, S, D, F = 1, 2048, 512, 1376
        return {
            "x": _w((lambda *s: torch.randn(*s, dtype=torch.bfloat16) * 0.05)(B, S, D)),
            "w1": _w((lambda *s: torch.randn(*s, dtype=torch.bfloat16) * 0.05)(D, F)),
            "b1": _w((lambda *s: torch.randn(*s, dtype=torch.bfloat16) * 0.2)(F)),
            "w2": _w((lambda *s: torch.randn(*s, dtype=torch.bfloat16) * 0.05)(F, D)),
            "b2": _w((lambda *s: torch.randn(*s, dtype=torch.bfloat16) * 0.8)(D)),
        }
    _B_in = _B_setup()

    return {
        "A_u": _A_in["u"],
        "A_log_A_real": _A_in["log_A_real"],
        "A_A_imag": _A_in["A_imag"],
        "A_C_real": _A_in["C_real"],
        "A_C_imag": _A_in["C_imag"],
        "A_log_dt": _A_in["log_dt"],
        "A_D_skip": _A_in["D_skip"],
        "B_x": _B_in["x"],
        "B_w1": _B_in["w1"],
        "B_b1": _B_in["b1"],
        "B_w2": _B_in["w2"],
        "B_b2": _B_in["b2"],
    }

def reference(A_u, A_log_A_real, A_A_imag, A_C_real, A_C_imag, A_log_dt, A_D_skip, B_x, B_w1, B_b1, B_w2, B_b2):
    def _A_ref(u, log_A_real, A_imag, C_real, C_imag, log_dt, D_skip):
        L = u.shape[1]
        dt = torch.exp(log_dt)
        A = -torch.exp(log_A_real) + 1j * A_imag
        C = (C_real + 1j * C_imag) * dt[:, None]
        pos = (lambda x, dt: x.to(dt))(torch.arange(L), log_dt.dtype)
        P = torch.exp((A * dt[:, None])[:, :, None] * pos[None, None, :])
        K = 2.0 * torch.einsum("dn,dnl->dl", C, P).real
        L2 = 2 * L
        u_t = (lambda x, *d: x.permute(*d))(u, 0, 2, 1)
        Kf = (lambda x, *a, **k: torch.fft.rfft(x.float(), *a, **k))(K, L2)
        Uf = (lambda x, *a, **k: torch.fft.rfft(x.float(), *a, **k))(u_t, L2)
        y = (lambda x, *a, **k: torch.fft.irfft(x, *a, **k).to(torch.bfloat16))(Uf * Kf[None, :, :], L2)[:, :, :L]
        y = (lambda x, *d: x.permute(*d))(y, 0, 2, 1)
        return y + u * D_skip[None, None, :]

    def _B_ref(x, w1, b1, w2, b2):
        h = torch.einsum("bsd,df->bsf", x, w1) + b1
        zero = torch.zeros_like(h)
        r = torch.where(h > zero, h, zero)
        h = r * r
        out = torch.einsum("bsf,fd->bsd", h, w2) + b2
        return out

    x = A_u
    xn = _layernorm(x, axis=-1)
    return x + _A_ref(xn, A_log_A_real, A_A_imag, A_C_real, A_C_imag, A_log_dt, A_D_skip) + _B_ref(xn, B_w1, B_b1, B_w2, B_b2)
```

```cpp
#include <hip/hip_runtime.h>
#include <hip/hip_bf16.h>
#include <stdint.h>

typedef unsigned short u16;
static constexpr int L = 2048, D = 512, N = 128, F = 1376;

__device__ __forceinline__ float bf2f(u16 v) { return __uint_as_float(((unsigned)v) << 16); }
__device__ __forceinline__ u16 f2bf(float f) {
    __hip_bfloat16 h = __float2bfloat16(f);
    return *reinterpret_cast<u16*>(&h);
}
__device__ __forceinline__ float rbf(float f) { return bf2f(f2bf(f)); }

__global__ void k_ln(const u16* x, u16* xn) {
    int row = blockIdx.x; int t = threadIdx.x;
    float v[8]; float s = 0;
    for (int i = 0; i < 8; i++) { v[i] = bf2f(x[row * D + t * 8 + i]); s += v[i]; }
    for (int m = 32; m >= 1; m >>= 1) s += __shfl_xor(s, m);
    float mean = s / D; float q = 0;
    for (int i = 0; i < 8; i++) { float d = v[i] - mean; q += d * d; }
    for (int m = 32; m >= 1; m >>= 1) q += __shfl_xor(q, m);
    float var = rbf(q / D); float mb = rbf(mean);
    float sd = rbf(sqrtf(rbf(var + 1e-5f)));
    for (int i = 0; i < 8; i++) xn[row * D + t * 8 + i] = f2bf(rbf(v[i] - mb) / sd);
}

__global__ void k_kgen(const u16* logA, const u16* Aim, const u16* Cr, const u16* Ci, const u16* logdt, float* K) {
    int d = blockIdx.x;
    __shared__ float zr[N], zi[N], cr[N], ci[N];
    float dt = rbf(expf(bf2f(logdt[d])));
    if (threadIdx.x < N) {
        int n = threadIdx.x;
        zr[n] = -rbf(expf(bf2f(logA[d * N + n]))) * dt;
        zi[n] = bf2f(Aim[d * N + n]) * dt;
        cr[n] = bf2f(Cr[d * N + n]) * dt;
        ci[n] = bf2f(Ci[d * N + n]) * dt;
    }
    __syncthreads();
    for (int l = threadIdx.x; l < L; l += blockDim.x) {
        float p = rbf((float)l);
        float acc = 0;
        for (int n = 0; n < N; n++) {
            float e = __expf(zr[n] * p); float s, c; __sincosf(zi[n] * p, &s, &c);
            acc += e * (cr[n] * c - ci[n] * s);
        }
        K[d * L + l] = 2.f * acc;
    }
}

__global__ void k_conv(const u16* x, const u16* xn, const float* K, const u16* Dskip, u16* yA) {
    int d = blockIdx.x;
    __shared__ float k[L], u[L];
    for (int l = threadIdx.x; l < L; l += blockDim.x) { k[l] = K[d * L + l]; u[l] = bf2f(xn[l * D + d]); }
    __syncthreads();
    float ds = bf2f(Dskip[d]);
    for (int l = threadIdx.x; l < L; l += blockDim.x) {
        float acc = 0;
        for (int j = 0; j <= l; j++) acc += k[j] * u[l - j];
        float a = rbf(rbf(acc) + rbf(u[l] * ds));
        yA[l * D + d] = f2bf(bf2f(x[l * D + d]) + a);
    }
}

template <int MODE>
__global__ void k_gemm(const u16* A, const u16* B, const u16* bias, const u16* extra, u16* C, int M, int Nn, int K) {
    __shared__ float As[16][65], Bs[16][65];
    int bm = blockIdx.y * 64, bn = blockIdx.x * 64;
    int tx = threadIdx.x & 15, ty = threadIdx.x >> 4;
    float acc[4][4] = {};
    for (int k0 = 0; k0 < K; k0 += 16) {
        for (int i = threadIdx.x; i < 1024; i += 256) {
            int r = i >> 4, c = i & 15; As[c][r] = bf2f(A[(bm + r) * K + k0 + c]);
            int kk = i >> 6, n = i & 63; Bs[kk][n] = (bn + n < Nn) ? bf2f(B[(k0 + kk) * Nn + bn + n]) : 0.f;
        }
        __syncthreads();
        for (int k = 0; k < 16; k++)
            for (int i = 0; i < 4; i++) for (int j = 0; j < 4; j++) acc[i][j] += As[k][ty * 4 + i] * Bs[k][tx * 4 + j];
        __syncthreads();
    }
    for (int i = 0; i < 4; i++) for (int j = 0; j < 4; j++) {
        int r = bm + ty * 4 + i, c = bn + tx * 4 + j; if (c >= Nn) continue;
        float h = rbf(rbf(acc[i][j]) + bf2f(bias[c]));
        if (MODE == 0) { float rr = h > 0.f ? h : 0.f; C[r * Nn + c] = f2bf(rr * rr); }
        else C[r * Nn + c] = f2bf(bf2f(extra[r * Nn + c]) + h);
    }
}

extern "C" void kernel_launch(void* const* d_in, const int* in_sizes, int n_in,
                              void* d_out, int out_size, void* d_ws, size_t ws_size,
                              hipStream_t stream) {
    const u16* x = (const u16*)d_in[0];
    const u16* logA = (const u16*)d_in[1]; const u16* Aim = (const u16*)d_in[2];
    const u16* Cr = (const u16*)d_in[3]; const u16* Ci = (const u16*)d_in[4];
    const u16* logdt = (const u16*)d_in[5]; const u16* Dskip = (const u16*)d_in[6];
    const u16* w1 = (const u16*)d_in[8]; const u16* b1 = (const u16*)d_in[9];
    const u16* w2 = (const u16*)d_in[10]; const u16* b2 = (const u16*)d_in[11];
    char* ws = (char*)d_ws;
    u16* xn = (u16*)ws; u16* yA = (u16*)(ws + (2 << 20)); float* K = (float*)(ws + (4 << 20)); u16* H = (u16*)(ws + (8 << 20));
    k_ln<<<L, 64, 0, stream>>>(x, xn);
    k_kgen<<<D, 256, 0, stream>>>(logA, Aim, Cr, Ci, logdt, K);
    k_conv<<<D, 256, 0, stream>>>(x, xn, K, Dskip, yA);
    k_gemm<0><<<dim3((F + 63) / 64, L / 64), 256, 0, stream>>>(xn, w1, b1, nullptr, H, L, F, D);
    k_gemm<1><<<dim3(D / 64, L / 64), 256, 0, stream>>>(H, w2, b2, yA, (u16*)d_out, L, D, F);
}
```

```cpp
#include <hip/hip_runtime.h>
#include <stdint.h>

typedef unsigned short u16;
typedef unsigned int u32;
typedef unsigned long long u64;
typedef u32 v4u __attribute__((ext_vector_type(4)));
typedef u32 v2u __attribute__((ext_vector_type(2)));
typedef float f32x2 __attribute__((ext_vector_type(2)));
typedef float f32x4 __attribute__((ext_vector_type(4)));
typedef float f32x16 __attribute__((ext_vector_type(16)));
typedef __bf16 bf16x8 __attribute__((ext_vector_type(8)));
typedef __bf16 bf16x2 __attribute__((ext_vector_type(2)));
typedef _Float16 f16x8 __attribute__((ext_vector_type(8)));
typedef _Float16 f16x2 __attribute__((ext_vector_type(2)));
typedef short s16x4 __attribute__((ext_vector_type(4)));
#define GAS __attribute__((address_space(1)))
#define LAS __attribute__((address_space(3)))
#define DI __device__ __forceinline__
#define RLX_AGENT __ATOMIC_RELAXED, __HIP_MEMORY_SCOPE_AGENT
#define RLX_WG __ATOMIC_RELAXED, __HIP_MEMORY_SCOPE_WORKGROUP
#define SB0() __builtin_amdgcn_sched_barrier(0)

constexpr int OFF_F1 = 4096, OFF_F2 = 16384, OFF_F3 = 32768, OFF_ST = 65536;
constexpr int OFF_U = 1 << 20, OFF_A = 4 << 20, OFF_H = 8 << 20;
constexpr int U_PITCH = 4224, U_BYTES = 512 * U_PITCH;
constexpr int H_BYTES = 64 * 43 * 2048;
constexpr int L_C = 131072;
constexpr int L_FLG = L_C, L_CNT = L_C + 128, L_LNS = L_C + 192, L_SF = L_C + 224, L_E = L_C + 240, L_STAT = L_C + 256, L_B1 = L_C + 768, L_CSP = L_C + 1024;
constexpr int L_KREV = L_C + 3072, L_UB = L_C + 12288;
constexpr int SPIN_CAP = 1 << 16;
enum { C_A = 0, C_C = 1, C_D = 2, C_E = 3, C_F = 4, C_X = 5 };

#define STAMP(i)

template <typename T> DI T lds_ld(u32 a) { return *(LAS T*)(size_t)a; }
template <typename T> DI void lds_st(u32 a, T v) { *(LAS T*)(size_t)a = v; }
DI v2u lds_tr(u32 a) { return __builtin_bit_cast(v2u, __builtin_amdgcn_ds_read_tr16_b64_v4i16((LAS s16x4*)(size_t)a)); }
DI v4u mkrsrc(const void* p, u32 bytes) { u64 a = (u64)p; v4u r; r.x = (u32)a; r.y = (u32)(a >> 32) & 0xffffu; r.z = bytes; r.w = 0x00020000u; return r; }
DI void glds(v4u rsrc, u32 voff, u32 soff, u32 lds) {
    asm volatile("s_mov_b32 m0, %3\n\ts_nop 0\n\tbuffer_load_dwordx4 %0, %1, %2 offen lds" :: "v"(voff), "s"(rsrc), "s"(soff), "s"(lds) : "memory");
}
DI void glds_sc1(v4u rsrc, u32 voff, u32 soff, u32 lds) {
    asm volatile("s_mov_b32 m0, %3\n\ts_nop 0\n\tbuffer_load_dwordx4 %0, %1, %2 offen sc1 lds" :: "v"(voff), "s"(rsrc), "s"(soff), "s"(lds) : "memory");
}
DI void flag_dma(v4u rsrc, u32 lds) {
    asm volatile("s_mov_b32 m0, %2\n\ts_mov_b64 exec, 1\n\tbuffer_load_dword off, %0, %1 lds\n\ts_mov_b64 exec, -1" :: "s"(rsrc), "s"(rsrc.z), "s"(lds) : "memory");
}
DI void zero_dma(v4u rsrc, u32 lds, u64 mask) {
    asm volatile("s_mov_b32 m0, %2\n\ts_mov_b64 exec, %3\n\tbuffer_load_dwordx4 off, %0, %1 lds\n\ts_mov_b64 exec, -1" :: "s"(rsrc), "s"(rsrc.z), "s"(lds), "s"(mask) : "memory");
}
DI void st_sc1(v4u v, v4u rsrc, u32 voff, u32 soff) {
    asm volatile("buffer_store_dwordx4 %0, %1, %2, %3 offen sc1\n\ts_nop 1" :: "v"(v), "v"(voff), "s"(rsrc), "s"(soff) : "memory");
}
DI u32 opq(u32 a) { asm volatile("" : "+v"(a)); return a; }
template <typename T> DI T lds_ldv(u32 a) { return *(volatile LAS T*)(size_t)a; }
DI void spin_zero4(u32 a) {
    int n = 0;
    while (true) { v4u f = lds_ldv<v4u>(a); if (__builtin_amdgcn_readfirstlane(f.x | f.y | f.z | f.w) == 0 || ++n > (SPIN_CAP << 4)) break; }
    asm volatile("" ::: "memory");
}
template <int N> DI void wait_vm() { asm volatile("s_waitcnt vmcnt(%0)" :: "i"(N) : "memory"); }
DI void wait_lgkm0() { asm volatile("s_waitcnt lgkmcnt(0)" ::: "memory"); }
DI u64 mix64(u64 e) { e += 0x9E3779B97F4A7C15ull; e ^= e >> 31; e *= 0xBF58476D1CE4E5B9ull; e ^= e >> 29; return e; }
DI u64 get_tag() { const v2u e = lds_ld<v2u>(L_E); return mix64(((u64)(u32)__builtin_amdgcn_readfirstlane(e.y) << 32) | (u32)__builtin_amdgcn_readfirstlane(e.x)); }
DI void sig(int c) { asm volatile("s_mov_b64 exec, 1\n\tds_add_u32 %0, %1\n\ts_mov_b64 exec, -1" :: "v"(L_CNT + 4 * c), "v"(1u) : "memory"); }
DI void spin(int c, u32 target) {
    int n = 0;
    while ((u32)__builtin_amdgcn_readfirstlane(__hip_atomic_load((LAS u32*)(size_t)(L_CNT + 4 * c), RLX_WG)) < target && ++n < (SPIN_CAP << 4)) {}
    asm volatile("" ::: "memory");
}
DI void poll_flags(const GAS u64* p, u64 tag) {
    int n = 0;
    while (true) {
        u64 f = __hip_atomic_load(p, RLX_AGENT);
        if (__all(f == tag) || ++n > SPIN_CAP) break;
        __builtin_amdgcn_s_sleep(2);
    }
    asm volatile("" ::: "memory");
}
DI float bf_lo(u32 v) { return __uint_as_float(v << 16); }
DI float bf_hi(u32 v) { return __uint_as_float(v & 0xffff0000u); }
DI u32 pk_bf(float a, float b) { f32x2 v = {a, b}; return __builtin_bit_cast(u32, __builtin_convertvector(v, bf16x2)); }
DI float rbf(float a) { return bf_lo(pk_bf(a, a)); }
DI u32 pk_h(f32x2 v) { return __builtin_bit_cast(u32, __builtin_convertvector(v, f16x2)); }
DI f32x16 mfma32(v4u a, v4u b, f32x16 c) { return __builtin_amdgcn_mfma_f32_32x32x16_bf16(__builtin_bit_cast(bf16x8, a), __builtin_bit_cast(bf16x8, b), c, 0, 0, 0); }
DI f32x4 mfma16(v4u a, v4u b, f32x4 c) { return __builtin_amdgcn_mfma_f32_16x16x32_bf16(__builtin_bit_cast(bf16x8, a), __builtin_bit_cast(bf16x8, b), c, 0, 0, 0); }
DI f32x4 mfma16h(v4u a, v4u b, f32x4 c) { return __builtin_amdgcn_mfma_f32_16x16x32_f16(__builtin_bit_cast(f16x8, a), __builtin_bit_cast(f16x8, b), c, 0, 0, 0); }
template <int CTRL> DI float dpp_add(float v) { return v + __builtin_bit_cast(float, __builtin_amdgcn_update_dpp(0, __builtin_bit_cast(int, v), CTRL, 0xf, 0xf, true)); }
template <int CTRL> DI float dpp_max(float v) { return fmaxf(v, __builtin_bit_cast(float, __builtin_amdgcn_update_dpp(0, __builtin_bit_cast(int, v), CTRL, 0xf, 0xf, true))); }
DI float rl(float v, int l) { return __builtin_bit_cast(float, __builtin_amdgcn_readlane(__builtin_bit_cast(int, v), l)); }
DI float wave_sum(float v) {
    v = dpp_add<0xB1>(v); v = dpp_add<0x4E>(v); v = dpp_add<0x141>(v); v = dpp_add<0x140>(v);
    return (rl(v, 0) + rl(v, 16)) + (rl(v, 32) + rl(v, 48));
}
DI f32x2 wave_sum2(f32x2 v) {
    float a = v.x, c = v.y;
    a = dpp_add<0xB1>(a); c = dpp_add<0xB1>(c); a = dpp_add<0x4E>(a); c = dpp_add<0x4E>(c);
    a = dpp_add<0x141>(a); c = dpp_add<0x141>(c); a = dpp_add<0x140>(a); c = dpp_add<0x140>(c);
    return ((f32x2){rl(a, 0), rl(c, 0)} + (f32x2){rl(a, 16), rl(c, 16)}) + ((f32x2){rl(a, 32), rl(c, 32)} + (f32x2){rl(a, 48), rl(c, 48)});
}
DI float wave_max(float v) {
    v = dpp_max<0xB1>(v); v = dpp_max<0x4E>(v); v = dpp_max<0x141>(v); v = dpp_max<0x140>(v);
    return fmaxf(fmaxf(rl(v, 0), rl(v, 16)), fmaxf(rl(v, 32), rl(v, 48)));
}
DI f32x2 cmul(f32x2 a, f32x2 b) { f32x2 t = (f32x2){-a.y, a.y} * (f32x2){b.y, b.x}; return __builtin_elementwise_fma((f32x2){a.x, a.x}, b, t); }
DI f32x16 mfma32h(v4u a, v4u b, f32x16 c) { return __builtin_amdgcn_mfma_f32_32x32x16_f16(__builtin_bit_cast(f16x8, a), __builtin_bit_cast(f16x8, b), c, 0, 0, 0); }
DI u32 pk_h2(float a, float b) { return pk_h((f32x2){a, b}); }
DI f32x2 cexp_pow(float lr, float th, float tl, float e) {
    float a = __builtin_amdgcn_fractf(th * e) + tl * e;
    float m = __builtin_amdgcn_exp2f(lr * e);
    f32x2 r; r.x = m * __builtin_amdgcn_cosf(a); r.y = m * __builtin_amdgcn_sinf(a); return r;
}
DI f32x4 sel4(bool c, f32x4 a, f32x4 b) { return c ? a : b; }
constexpr int w2loc(int n) { return n >= 32 ? w2loc(n - 32) : n < 9 ? 3 + n : n < 13 ? 24 + (n - 9) : n < 16 ? 12 + (n - 13) : n < 20 ? 28 + (n - 16) : n < 29 ? 15 + (n - 20) : n - 29; }

__global__ void __launch_bounds__(512, 2)
fused_kernel(const u16* __restrict__ x, const u16* __restrict__ logA, const u16* __restrict__ Aim, const u16* __restrict__ Cre,
             const u16* __restrict__ Cim, const u16* __restrict__ logdt, const u16* __restrict__ Dskip, const u16* __restrict__ w1,
             const u16* __restrict__ b1, const u16* __restrict__ w2, const u16* __restrict__ b2, u16* __restrict__ out, char* ws) {
    __shared__ __attribute__((aligned(16))) char smem[163840];
    asm volatile("" :: "v"((u32)(size_t)(LAS char*)smem));
    const int tid = threadIdx.x, lane = tid & 63, wave = __builtin_amdgcn_readfirstlane(tid >> 6), w = wave & 3;
    const int b = blockIdx.x, xcd = b & 7, slot = b >> 3;
    const int P = 4 * (xcd >> 1) + (slot >> 3), j = 8 * (xcd & 1) + (slot & 7);
    const int tr = 4 * xcd + (slot >> 3), tc = slot & 7;
    const int u0 = (j * 43) >> 4;
    STAMP(0);
    const int h = lane >> 5, l31 = lane & 31, l15 = lane & 15, g = lane >> 4;

    if (wave < 4) {
        const int rot = (2 * (slot >> 3) + (slot & 7)) & 7;
        const v4u rx = mkrsrc(x + (128 * P + 32 * w) * 512, 32 * 1024);
        const v4u rw1 = mkrsrc(w1, 512 * 1376 * 2);
        const v4u rw2 = mkrsrc(w2, 512 * 1376 * 2);
        const u32 vx0 = (lane >> 3) * 1024 + ((lane & 7) ^ g) * 16, vx1 = vx0 ^ 64;
        const u32 vw = (lane >> 2) * 2752 + (lane & 3) * 16;
        const u32 vv = (lane >> 3) * 1024 + ((lane & 7) ^ ((g & 1) << 2)) * 16;
        const u32 lw = w * 32768;
        const u32 sw1 = 16 * w * 2752 + u0 * 64, sw2 = 352 * w * 1024 + tc * 128;
        auto ldX = [&](int c, int i) { glds(rx, (i & 1) ? vx1 : vx0, ((c + rot) & 7) * 128 + i * 8192, lw + (24 + (c & 1) * 4 + i) * 1024); };
        auto ldW = [&](int c, int u) { glds(rw1, vw, ((c + rot) & 7) * (64 * 2752) + sw1 + u * 64, lw + (3 * c + u) * 1024); };
        auto ldV = [&](int n) { glds(rw2, vv, sw2 + n * 8192, lw + w2loc(n) * 1024); };
        auto flg = [&](int c) { flag_dma(rx, L_FLG + c * 16 + w * 4); };
        if (lane < 8) lds_st<u32>(L_FLG + lane * 16 + w * 4, 1u);
        if (lane < 24) lds_st<v4u>(L_CSP + w * 384 + lane * 16, (v4u){~0u, ~0u, ~0u, ~0u});
#pragma unroll
        for (int u = 0; u < 3; u++) ldW(0, u);
#pragma unroll
        for (int i = 0; i < 4; i++) ldX(0, i);
        wait_lgkm0();
        flg(0);
#pragma unroll
        for (int u = 0; u < 3; u++) ldW(1, u);
#pragma unroll
        for (int i = 0; i < 4; i++) ldX(1, i);
        flg(1);
#pragma unroll
        for (int u = 0; u < 6; u++) ldW(2 + u / 3, u % 3);
        __builtin_amdgcn_s_setprio(1);
        u32 aW[4], aX[4];
#pragma unroll
        for (int s = 0; s < 4; s++) {
            const int ks = (s + w) & 3;
            aW[s] = opq(ks * 32768 + (8 * h + (l15 >> 2)) * 64 + (g & 1) * 32 + (lane & 3) * 8);
            aX[s] = opq(lw + 24576 + l31 * 128 + (((2 * ks + h) ^ ((l31 >> 1) & 7)) * 16));
        }
        const u32 aF = opq(L_FLG);
        f32x16 acc[3]; f32x4 cs = {0.f, 0.f, 0.f, 0.f};
        v4u sel[3];
#pragma unroll
        for (int u = 0; u < 3; u++) { for (int i = 0; i < 16; i++) acc[u][i] = 0.f; const u32 sv = (l15 == 2 * u + (g & 1)) ? 0x3F803F80u : 0u; sel[u] = (v4u){sv, sv, sv, sv}; }
        __builtin_amdgcn_s_barrier();
        asm volatile("" ::: "memory");
        v4u fa[3][3], fb[2][4];
        auto rdW = [&](int n, int u) { const u32 a = aW[n & 3] + (3 * (n >> 2) + u) * 1024; v2u lo = lds_tr(a), hi = lds_tr(a + 256); fa[n % 3][u] = (v4u){lo.x, lo.y, hi.x, hi.y}; };
        auto rdX = [&](int c, int s) { fb[c & 1][s] = lds_ld<v4u>(aX[s] + (c & 1) * 4096); };
        spin_zero4(aF);
        STAMP(1);
#pragma unroll
        for (int s = 0; s < 4; s++) rdX(0, s);
#pragma unroll
        for (int u = 0; u < 6; u++) rdW(u / 3, u % 3);
        asm volatile("" : "+v"(fb[0][0]), "+v"(fb[0][1]), "+v"(fb[0][2]), "+v"(fb[0][3]));
#pragma unroll
        for (int i = 0; i < 4; i++) ldX(2, i);
        flg(2);
        v4u fl;
#pragma unroll
        for (int n = 0; n < 32; n++) {
            const int c = n >> 2, s = n & 3;
            auto req = [&](int k) {
                if (s == 3) {
                    if (c < 5) { if (k < 2) { ldX(c + 3, 2 * k); ldX(c + 3, 2 * k + 1); } else flg(c + 3); }
                    else if (c < 7 && k < 2) { ldV(7 * c - 26 + 2 * k); ldV(7 * c - 25 + 2 * k); }
                } else if (k == 1) {
                    if (c < 4) ldW(c + 4, s); else ldV(c < 7 ? 3 * (c - 4) + s : 13 + s);
                }
            };
            SB0();
            if (s == 2 && c < 7) {
                if (__builtin_expect(__builtin_amdgcn_readfirstlane(fl.x | fl.y | fl.z | fl.w) != 0, 0)) spin_zero4(aF + (c + 1) * 16);
                asm volatile("" ::: "memory");
            }
            if (s == 3 && c < 7) asm volatile("" : "+v"(fb[(c + 1) & 1][0]), "+v"(fb[(c + 1) & 1][1]), "+v"(fb[(c + 1) & 1][2]), "+v"(fb[(c + 1) & 1][3]));
#pragma unroll
            for (int u = 0; u < 3; u++) {
                acc[u] = mfma32(fa[n % 3][u], fb[c & 1][s], acc[u]);
                if (s == 0) cs = mfma16(fa[n % 3][u], sel[u], cs);
                SB0();
                if (s == 2 && c < 7 && u < 2) { rdX(c + 1, 2 * u); rdX(c + 1, 2 * u + 1); }
                if (n + 2 < 32) rdW(n + 2, u);
                if (s == 1 && u == 0 && c < 7) fl = lds_ldv<v4u>(aF + (c + 1) * 16);
                if (n == 29 && u == 2 && l15 < 6) lds_st<f32x4>(L_CSP + w * 384 + l15 * 64 + g * 16, cs);
                req(u);
                SB0();
            }
        }
        STAMP(2);
        const u32 aV0 = opq(lw + (4 * h + (l15 >> 2)) * 128 + ((l15 >> 3) & 1) * 64 + (g & 1) * 32 + (lane & 3) * 8), aV1 = opq(aV0 ^ 64);
        auto rdV = [&](int s, int dt) { v2u lo = lds_tr((dt ? aV1 : aV0) + w2loc(2 * s) * 1024), hi = lds_tr((dt ? aV1 : aV0) + w2loc(2 * s + 1) * 1024); return (v4u){lo.x, lo.y, hi.x, hi.y}; };
        v4u park[6][2];
        {
            float cc[3];
            {
                int n = 0;
                const u32 ca = opq(L_CSP + l31 * 4);
                while (true) {
                    u32 all = lds_ldv<u32>(L_SF) == 0 ? 0u : ~0u, bad = 0;
#pragma unroll
                    for (int u = 0; u < 3; u++) {
                        const u32 p0 = lds_ldv<u32>(ca + u * 128), p1 = lds_ldv<u32>(ca + u * 128 + 384), p2 = lds_ldv<u32>(ca + u * 128 + 768), p3 = lds_ldv<u32>(ca + u * 128 + 1152);
                        bad |= (p0 == ~0u) | (p1 == ~0u) | (p2 == ~0u) | (p3 == ~0u);
                        cc[u] = -((__uint_as_float(p0) + __uint_as_float(p1)) + (__uint_as_float(p2) + __uint_as_float(p3)));
                    }
                    if (!__any(bad | all) || ++n > (SPIN_CAP << 2)) break;
                }
                asm volatile("" ::: "memory");
            }
            STAMP(3);
            const u64 tag = get_tag();
            const u32 stw = lds_ld<u32>(L_STAT + (32 * w + l31) * 4);
            const u32 mm2 = (stw & 0xffffu) * 0x10001u;
            const v4u fbs = {h ? 0u : mm2, h ? 0u : stw, 0u, 0u};
#pragma unroll
            for (int u = 0; u < 3; u++) {
                const float c = cc[u], c0 = rbf(c), c1 = rbf(c - c0), c2 = (c - c0) - c1;
                const u32 bb = lds_ld<u16>(L_B1 + (u * 32 + l31) * 2);
                const v4u fas = {pk_bf(c0, c1), (pk_bf(c2, c2) & 0xffffu) | (bb << 16), 0u, 0u};
                acc[u] = mfma32(fas, fbs, acc[u]);
            }
            const float rs = __builtin_amdgcn_rcpf(bf_hi(stw));
            const v4u rhs = mkrsrc(ws + OFF_H, H_BYTES);
            const __amdgpu_buffer_rsrc_t rh = __builtin_amdgcn_make_buffer_rsrc(ws + OFF_H, 0, H_BYTES, 0x00020000);
            const int nown = (((j + 1) * 43) >> 4) - u0;
            const u32 sh = ((4 * P + w) * 43 + u0) * 2048;
#pragma unroll
            for (int u = 0; u < 3; u++) {
#pragma unroll
                for (int s = 0; s < 2; s++) {
                    v4u o;
#pragma unroll
                    for (int e = 0; e < 4; e++) {
                        f32x2 r = (f32x2){acc[u][8 * s + 2 * e], acc[u][8 * s + 2 * e + 1]} * rs;
                        r.x = fmaxf(r.x, 0.f); r.y = fmaxf(r.y, 0.f); r = r * r;
                        o[e] = pk_bf(r.x, r.y);
                    }
                    st_sc1(o, rhs, lane * 16, (u < 2 || nown == 3) ? sh + u * 2048 + s * 1024 : (u32)H_BYTES);
                    if (2 * u + s < 5) ldV(20 + 2 * u + s);
                    SB0();
                }
            }
#pragma unroll
            for (int n = 25; n < 29; n++) ldV(n);
            wait_vm<22>();
#pragma unroll
            for (int s = 0; s < 6; s++) { park[s][0] = rdV(s, 0); park[s][1] = rdV(s, 1); }
#pragma unroll
            for (int s = 0; s < 6; s++) asm volatile("" : "+v"(park[s][0]), "+v"(park[s][1]));
#pragma unroll
            for (int n = 32; n < 38; n++) ldV(n);
            wait_vm<10>();
            STAMP(4);
            if (lane == 0) __hip_atomic_store((GAS u64*)(ws + OFF_F2) + (4 * P + w) * 16 + j, tag, RLX_AGENT);
#pragma unroll
            for (int n = 38; n < 44; n++) ldV(n);
            spin(C_C, 4);
#pragma unroll
            for (int n = 29; n < 32; n++) ldV(n);
            poll_flags((const GAS u64*)(ws + OFF_F2) + 2 * tr * 16 + l31, tag);
            STAMP(5);
            f32x16 ac2[2][2];
#pragma unroll
            for (int i = 0; i < 16; i++) { ac2[0][0][i] = 0.f; ac2[0][1][i] = 0.f; ac2[1][0][i] = 0.f; ac2[1][1][i] = 0.f; }
            const u32 sh2 = (2 * tr * 43 + 11 * w) * 2048;
            const u32 sh2t = (w == 3) ? (u32)H_BYTES : sh2;
            constexpr int WIN = 8;
            v4u hf[WIN][2];
            auto ldH = [&](int s) {
                const u32 so = (s >= 20 ? sh2t : sh2) + s * 1024;
                hf[s % WIN][0] = __builtin_amdgcn_raw_buffer_load_b128(rh, lane * 16, so, 16);
                hf[s % WIN][1] = __builtin_amdgcn_raw_buffer_load_b128(rh, lane * 16, so + 43 * 2048, 16);
            };
#pragma unroll
            for (int s = 0; s < WIN; s++) ldH(s);
            v4u vb[2][2];
#pragma unroll
            for (int s = 0; s < 22; s++) {
                SB0();
                if (s + 1 >= 6 && s + 1 < 22) { vb[(s + 1) & 1][0] = rdV(s + 1, 0); vb[(s + 1) & 1][1] = rdV(s + 1, 1); }
                const v4u a0 = s < 6 ? park[s < 6 ? s : 0][0] : vb[s & 1][0], a1 = s < 6 ? park[s < 6 ? s : 0][1] : vb[s & 1][1];
                ac2[0][0] = mfma32(a0, hf[s % WIN][0], ac2[0][0]);
                ac2[1][0] = mfma32(a1, hf[s % WIN][0], ac2[1][0]);
                ac2[0][1] = mfma32(a0, hf[s % WIN][1], ac2[0][1]);
                ac2[1][1] = mfma32(a1, hf[s % WIN][1], ac2[1][1]);
                SB0();
                if (s + WIN < 22) ldH(s + WIN);
            }
            SB0();
            STAMP(7);
#pragma unroll
            for (int tt = 0; tt < 2; tt++)
#pragma unroll
                for (int dt = 0; dt < 2; dt++)
#pragma unroll
                    for (int q = 0; q < 4; q++)
                        lds_st<f32x4>(lw + (32 * tt + l31) * 272 + (32 * dt + 8 * q + 4 * h) * 4,
                                      (f32x4){ac2[dt][tt][4 * q], ac2[dt][tt][4 * q + 1], ac2[dt][tt][4 * q + 2], ac2[dt][tt][4 * q + 3]});
        }
    } else {
        const int tb = 16 * P + j;
        const int chl = w >> 1, hf = w & 1, ch = 2 * b + chl;
        {
            const v4u rxr = mkrsrc(x + (8 * tb + 2 * w) * 512, 2048);
            glds(rxr, lane * 16, 0, L_KREV + (2 * w) * 1088);
            glds(rxr, lane * 16, 1024, L_KREV + (2 * w + 1) * 1088);
        }
        const int pi = ch * 128 + hf * 64 + lane;
        const u32 pA = logA[pi], pI = Aim[pi], pR = Cre[pi], pC = Cim[pi], pT = logdt[ch], pD = Dskip[ch];
        if (w == 1) {
            const v4u rb1 = mkrsrc(b1, 1376 * 2);
            asm volatile("s_mov_b32 m0, %3\n\ts_nop 0\n\tbuffer_load_dword %0, %1, %2 offen lds" :: "v"(lane * 4), "s"(rb1), "s"(u0 * 64), "s"(L_B1) : "memory");
        }
        if (w == 0 && lane < 28) lds_st<u32>(L_CNT + lane * 4, lane < 16 ? 0u : ~0u);
        if (w == 2) {
            const v4u re = mkrsrc(ws, 16);
            asm volatile("s_mov_b32 m0, %1\n\ts_mov_b64 exec, 1\n\tbuffer_load_dwordx4 off, %0, 0 sc1 lds\n\ts_mov_b64 exec, -1" :: "s"(re), "s"(L_E) : "memory");
        }
        asm volatile("s_waitcnt vmcnt(0) lgkmcnt(0)" ::: "memory");
        __builtin_amdgcn_s_barrier();
        asm volatile("" ::: "memory");
        const u64 tag = get_tag();
        STAMP(1);
        {
            const v4u xr0 = lds_ld<v4u>(L_KREV + (2 * w) * 1088 + lane * 16), xr1 = lds_ld<v4u>(L_KREV + (2 * w + 1) * 1088 + lane * 16);
            f32x2 v[8];
#pragma unroll
            for (int i = 0; i < 4; i++) { v[2 * i] = (f32x2){bf_lo(xr0[i]), bf_lo(xr1[i])}; v[2 * i + 1] = (f32x2){bf_hi(xr0[i]), bf_hi(xr1[i])}; }
            f32x2 sm = ((v[0] + v[1]) + (v[2] + v[3])) + ((v[4] + v[5]) + (v[6] + v[7]));
            const f32x2 m = wave_sum2(sm) * (1.f / 512.f);
            f32x2 q = {0.f, 0.f};
#pragma unroll
            for (int i = 0; i < 8; i++) { f32x2 d = v[i] - m; q = __builtin_elementwise_fma(d, d, q); }
            const f32x2 var = wave_sum2(q) * (1.f / 512.f);
            const u32 vb = pk_bf(var.x, var.y), ve = pk_bf(bf_lo(vb) + 1e-5f, bf_hi(vb) + 1e-5f);
            const float sd0 = __builtin_amdgcn_sqrtf(bf_lo(ve)), sd1 = __builtin_amdgcn_sqrtf(bf_hi(ve));
            if (lane == 0) lds_st<v2u>(L_LNS + w * 8, (v2u){pk_bf(m.x, sd0) & 0x7fffffffu, pk_bf(m.y, sd1) & 0x7fffffffu});
        }
        const v4u rus = mkrsrc(ws + OFF_U, U_BYTES);
        {
            const u32 ta = opq(L_KREV + (l15 >> 2) * 1088 + (128 * w + 16 * g + 4 * (lane & 3)) * 2);
            v2u xt[2][2];
#pragma unroll
            for (int i = 0; i < 2; i++) { xt[i][0] = lds_tr(ta + i * 128); xt[i][1] = lds_tr(ta + i * 128 + 4 * 1088); }
            v4u s0, s1;
            int n = 0;
            while (true) {
                s0 = lds_ldv<v4u>(L_LNS); s1 = lds_ldv<v4u>(L_LNS + 16);
                if ((int)__builtin_amdgcn_readfirstlane(s0.x | s0.z | s1.x | s1.z) >= 0 || ++n > (SPIN_CAP << 4)) break;
            }
            float mb[8], rr[8];
#pragma unroll
            for (int t = 0; t < 8; t++) { const u32 sv = t < 4 ? s0[t & 3] : s1[t & 3]; mb[t] = bf_lo(sv); rr[t] = __builtin_amdgcn_rcpf(bf_hi(sv)); }
#pragma unroll
            for (int i = 0; i < 2; i++) {
                v4u o;
#pragma unroll
                for (int e = 0; e < 4; e++) {
                    const u32 xv = xt[i][e >> 1][e & 1];
                    const u32 d = pk_bf(bf_lo(xv) - mb[2 * e], bf_hi(xv) - mb[2 * e + 1]);
                    o[e] = pk_bf(bf_lo(d) * rr[2 * e], bf_hi(d) * rr[2 * e + 1]);
                }
                st_sc1(o, rus, (128 * w + 64 * i + lane) * U_PITCH, 128 + tb * 16);
                if (tb == 0) st_sc1((v4u){0u, 0u, 0u, 0u}, rus, (128 * w + 64 * i + lane) * U_PITCH, 112);
            }
            if (lane < 2) st_sc1(lane ? s1 : s0, mkrsrc(ws + OFF_ST, 32768), lane * 16, w * 8192 + tb * 32);
        }
        STAMP(2);
        const float dt = rbf(expf(bf_lo(pT)));
        {
            const float zr = -rbf(expf(bf_lo(pA))) * dt, zi = bf_lo(pI) * dt;
            const float lr = zr * 1.44269504088896341f;
            const float th = zi * 0.159154943091895336f, tl = __builtin_fmaf(zi, 0.159154943091895336f, -th) + zi * 6.4206383e-09f;
            const f32x2 w1c = cexp_pow(lr, -th, -tl, 1.f), qc = cexp_pow(lr, -th, -tl, 128.f);
            const f32x2 w8 = cexp_pow(lr, th, tl, 8.f), w32 = cexp_pow(lr, th, tl, 32.f);
            const float cmax = wave_max(fmaxf(fabsf(bf_lo(pR)), fabsf(bf_lo(pC))));
            int ex = (int)(__float_as_uint(cmax) >> 23); ex = ex < 12 ? 12 : (ex > 240 ? 240 : ex);
            const float sdn = __uint_as_float((u32)(262 - ex) << 23), sup = __uint_as_float((u32)(ex - 8) << 23);
            v4u selLo, selHi;
#pragma unroll
            for (int e = 0; e < 4; e++) {
                const u32 one = (h == (g & 1) && ((l15 & 7) >> 1) == e) ? ((l15 & 1) ? 0x3C000000u : 0x00003C00u) : 0u;
                selLo[e] = l15 < 8 ? one : 0u; selHi[e] = l15 < 8 ? 0u : one;
            }
            auto transpose = [&](const f32x2 (&v)[16], v4u (&f)[4]) {
                f32x16 z; for (int i = 0; i < 16; i++) z[i] = 0.f;
                f32x16 dr = mfma32h((v4u){pk_h2(v[0].x, v[1].x), pk_h2(v[2].x, v[3].x), pk_h2(v[4].x, v[5].x), pk_h2(v[6].x, v[7].x)}, selLo, z);
                f32x16 di = mfma32h((v4u){pk_h2(v[0].y, v[1].y), pk_h2(v[2].y, v[3].y), pk_h2(v[4].y, v[5].y), pk_h2(v[6].y, v[7].y)}, selLo, z);
                dr = mfma32h((v4u){pk_h2(v[8].x, v[9].x), pk_h2(v[10].x, v[11].x), pk_h2(v[12].x, v[13].x), pk_h2(v[14].x, v[15].x)}, selHi, dr);
                di = mfma32h((v4u){pk_h2(v[8].y, v[9].y), pk_h2(v[10].y, v[11].y), pk_h2(v[12].y, v[13].y), pk_h2(v[14].y, v[15].y)}, selHi, di);
#pragma unroll
                for (int e = 0; e < 4; e++) {
                    f[0][e] = pk_h2(dr[2 * e], dr[2 * e + 1]); f[1][e] = pk_h2(dr[8 + 2 * e], dr[9 + 2 * e]);
                    f[2][e] = pk_h2(di[2 * e], di[2 * e + 1]); f[3][e] = pk_h2(di[8 + 2 * e], di[9 + 2 * e]);
                }
            };
            v4u fy[4], fx[4];
            f32x2 v[16];
            v[0] = (f32x2){1.f, 0.f}; v[1] = w8; v[2] = cmul(w8, w8); v[3] = cmul(v[2], w8); v[4] = w32;
#pragma unroll
            for (int i = 5; i < 16; i++) v[i] = cmul(v[i - 4], w32);
            transpose(v, fy);
            wait_vm<0>();
            if (lane == 0) __hip_atomic_store((GAS u64*)(ws + OFF_F1) + w * 256 + tb, tag, RLX_AGENT);
            STAMP(3);
            const float fin = 2.f * dt * sup;
            f32x4 kt[3];
            auto tile = [&](int t) {
                transpose(v, fx);
                f32x4 d = {0.f, 0.f, 0.f, 0.f};
#pragma unroll
                for (int s = 0; s < 4; s++) d = mfma16h(fy[s], fx[s], d);
                kt[t] = d * fin;
            };
            f32x2 xo[8];
            xo[0] = (f32x2){bf_lo(pR) * sdn, -bf_lo(pC) * sdn};
#pragma unroll
            for (int i = 1; i < 8; i++) xo[i] = cmul(xo[i - 1], w1c);
            const f32x2 q2 = cmul(qc, qc);
            v[0] = xo[0]; v[1] = cmul(xo[0], qc);
#pragma unroll
            for (int s = 2; s < 16; s++) v[s] = cmul(v[s - 2], q2);
            const f32x2 last = cmul(v[14], q2);
            tile(0);
            v[0] = last; v[1] = xo[4]; v[9] = xo[2]; v[13] = xo[6];
#pragma unroll
            for (int s = 2; s < 9; s++) v[s] = cmul(v[s - 1], qc);
#pragma unroll
            for (int s = 10; s < 13; s++) v[s] = cmul(v[s - 1], qc);
            v[14] = cmul(v[13], qc); v[15] = cmul(v[14], qc);
            const f32x2 last2 = cmul(v[15], qc);
            tile(1);
            v[0] = last2;
#pragma unroll
            for (int i = 0; i < 4; i++) { v[1 + 2 * i] = xo[2 * i + 1]; v[2 + 2 * i] = cmul(xo[2 * i + 1], qc); }
#pragma unroll
            for (int i = 9; i < 16; i++) v[i] = (f32x2){0.f, 0.f};
            tile(2);
            spin_zero4(L_FLG + 3 * 16);
#pragma unroll
            for (int t = 0; t < 3; t++) lds_st<f32x4>(w * 32768 + (16 * t + l15) * 64 + g * 16, kt[t]);
        }
        STAMP(4);
        poll_flags((const GAS u64*)(ws + OFF_F1) + (b >> 6) * 256 + 64 * w + lane, tag);
        STAMP(5);
        if (w == (P >> 2)) {
            const v4u rst = mkrsrc(ws + OFF_ST + (b >> 6) * 8192 + P * 512, 512);
            asm volatile("s_mov_b32 m0, %2\n\ts_mov_b32 exec_hi, 0\n\tbuffer_load_dwordx4 %0, %1, 0 offen sc1 lds\n\ts_mov_b32 exec_hi, -1" :: "v"(lane * 16), "s"(rst), "s"(L_STAT) : "memory");
            flag_dma(rst, L_SF);
        }
        sig(C_A); spin(C_A, 4);
#pragma unroll
        for (int i = 0; i < 4; i++) glds_sc1(rus, lane * 16, (2 * b + (i >> 1)) * U_PITCH + 128 + w * 1024 - (i & 1) * 2, L_UB + (i >> 1) * 8704 + (i & 1) * 4288 + 32 + w * 1024);
        zero_dma(rus, L_UB + chl * 8704 + hf * 4288, 3ull);
        {
            const int m = 64 * hf + lane;
            const u32 t0 = opq((2 * chl) * 32768 + m * 4);
            const bool r1 = m < 32, r2 = m < 64;
            const int bs[8] = {0, r1 ? 528 : 0, r2 ? 400 : 0, r1 ? 560 : 272, 272, r1 ? 592 : 272, r2 ? 464 : 1, r1 ? 624 : 1};
            float k[8];
#pragma unroll
            for (int e = 0; e < 8; e++) k[e] = lds_ld<float>(t0 + bs[e] * 4) + lds_ld<float>(t0 + bs[e] * 4 + 32768);
            if (m == 0) k[0] += bf_lo(pD);
            const u32 kr = L_KREV + chl * 4608;
            const float va = lds_ld<float>(t0 + 512) + lds_ld<float>(t0 + 512 + 32768);
            const float vb = lds_ld<float>(t0 + 516) + lds_ld<float>(t0 + 516 + 32768);
            lds_st<v4u>(kr + (2040 - 8 * m) * 2, (v4u){pk_bf(k[7], k[6]), pk_bf(k[5], k[4]), pk_bf(k[3], k[2]), pk_bf(k[1], k[0])});
            const u32 pa = pk_bf(va, va), pb = pk_bf(vb, vb);
            lds_st<v4u>(kr + (1016 - 8 * m) * 2, (v4u){pb, (m & 1) ? pb : pk_bf(vb, va), pa, pa});
            zero_dma(rus, kr + 4096 + hf * 256, 0xffffull);
        }
        wait_vm<0>();
        sig(C_C); spin(C_C, 4);
        STAMP(6);
        if (b == 0 && w == 0 && lane == 0) __hip_atomic_store((GAS u64*)ws, tag, RLX_AGENT);
        f32x4 cv[8];
        {
#pragma unroll
            for (int t = 0; t < 8; t++) cv[t] = (f32x4){0.f, 0.f, 0.f, 0.f};
            u32 ua = L_UB + chl * 8704 + ((l15 & 1) ? (1 + l15 + 8 * g) * 2 : 4288 + (2 + l15 + 8 * g) * 2) + hf * 256;
            u32 ka = L_KREV + chl * 4608 + (240 - 16 * l15 + 8 * g) * 2 + hf * 256;
            v4u A[8], B[8];
            u32 ub[4];
            auto rdA = [&](int a) { const u32 p = ub[a >> 1] + (a & 1) * 512; return (v4u){lds_ld<u32>(p), lds_ld<u32>(p + 4), lds_ld<u32>(p + 8), lds_ld<u32>(p + 12)}; };
            auto rdB = [&](int d) { return lds_ld<v4u>(ka + (7 - d) * 512); };
#pragma unroll
            for (int i = 0; i < 4; i++) ub[i] = opq(ua + i * 1024);
            ka = opq(ka);
#pragma unroll
            for (int i = 0; i < 8; i++) { A[7 - i] = rdA(7 - i); B[i] = rdB(i); }
#pragma unroll 1
            for (int r = 0; r < 4; r++) {
                ka = opq(ka + 64);
#pragma unroll
                for (int i = 0; i < 4; i++) ub[i] = opq(ub[i] + 64);
#pragma unroll
                for (int a = 7; a >= 0; a--) {
#pragma unroll
                    for (int d = 0; d + a < 8; d++) {
                        cv[a + d] = mfma16(A[a], B[d], cv[a + d]);
                        if (a == 0) { SB0(); B[d] = rdB(d); SB0(); }
                    }
                    SB0(); A[a] = rdA(a); SB0();
                }
            }
        }
        STAMP(7);
        sig(C_D); spin(C_D, 4);
        {
            const u32 mine = hf ? L_UB + chl * 8704 + 4288 : L_KREV + chl * 4608, other = hf ? L_KREV + chl * 4608 : L_UB + chl * 8704 + 4288;
#pragma unroll
            for (int t = 0; t < 4; t++) lds_st<f32x4>(mine + t * 1024 + lane * 16, sel4(hf, cv[t], cv[4 + t]));
            sig(C_E); spin(C_E, 4);
            const __amdgpu_buffer_rsrc_t ra = __builtin_amdgcn_make_buffer_rsrc(ws + OFF_A, 0, 2 << 20, 0x00020000);
            u32 o[8];
#pragma unroll
            for (int t = 0; t < 4; t++) {
                const f32x4 y = sel4(hf, cv[4 + t], cv[t]) + lds_ld<f32x4>(other + t * 1024 + lane * 16);
                o[2 * t] = pk_bf(y[0], y[1]); o[2 * t + 1] = pk_bf(y[2], y[3]);
            }
            const v4u ras = mkrsrc(ws + OFF_A, 2 << 20);
            st_sc1((v4u){o[0], o[1], o[2], o[3]}, ras, lane * 16, ch * 4096 + hf * 2048);
            st_sc1((v4u){o[4], o[5], o[6], o[7]}, ras, lane * 16, ch * 4096 + hf * 2048 + 1024);
            sig(C_X);
            wait_vm<0>();
            STAMP(8);
            if (lane == 0) __hip_atomic_store((GAS u64*)(ws + OFF_F3) + hf * 512 + ch, tag, RLX_AGENT);
            const int ht = tid - 256, rt = ht >> 2, ro = (ht & 3) * 16;
            const GAS v4u* xp = (const GAS v4u*)(x + (64 * tr + rt) * 512 + tc * 64 + ro);
            const GAS v4u* bp = (const GAS v4u*)(b2 + tc * 64 + ro);
            const v4u xa = xp[0], xb = xp[1], ba = bp[0], bb = bp[1];
            poll_flags((const GAS u64*)(ws + OFF_F3) + (tr >> 4) * 512 + tc * 64 + 16 * w + l15, tag);
            STAMP(9);
            v4u av[4];
#pragma unroll
            for (int i = 0; i < 4; i++)
                av[i] = __builtin_amdgcn_raw_buffer_load_b128(ra, (tc * 64 + 16 * w + 4 * i + g) * 4096 + ((l15 >> 2) * 16 + (tr & 3) * 4 + (lane & 3)) * 16, (tr >> 3) * 1024, 16);
            spin(C_X, 4);
            const bool odd = (tr >> 2) & 1;
#pragma unroll
            for (int i = 0; i < 4; i++) {
                const u32 lo = odd ? av[i].z : av[i].x, hi = odd ? av[i].w : av[i].y;
                const u32 aa = L_KREV + (16 * (lane & 3) + 4 * (l15 >> 2)) * 144 + (16 * w + 4 * i + g) * 2;
                lds_st<u16>(aa, (u16)lo); lds_st<u16>(aa + 144, (u16)(lo >> 16)); lds_st<u16>(aa + 288, (u16)hi); lds_st<u16>(aa + 432, (u16)(hi >> 16));
            }
            sig(C_F); spin(C_F, 4);
            const v4u a0 = lds_ld<v4u>(L_KREV + rt * 144 + ro * 2), a1 = lds_ld<v4u>(L_KREV + rt * 144 + ro * 2 + 16);
#pragma unroll
            for (int i = 0; i < 4; i++) {
                const u32 xx0 = i < 2 ? xa[2 * i] : xb[2 * i - 4], xx1 = i < 2 ? xa[2 * i + 1] : xb[2 * i - 3];
                const u32 bb0 = i < 2 ? ba[2 * i] : bb[2 * i - 4], bb1 = i < 2 ? ba[2 * i + 1] : bb[2 * i - 3];
                const u32 aa0 = i < 2 ? a0[2 * i] : a1[2 * i - 4], aa1 = i < 2 ? a0[2 * i + 1] : a1[2 * i - 3];
                lds_st<f32x4>(L_UB + rt * 272 + (ro + 4 * i) * 4,
                              (f32x4){bf_lo(xx0) + bf_lo(aa0) + bf_lo(bb0), bf_hi(xx0) + bf_hi(aa0) + bf_hi(bb0), bf_lo(xx1) + bf_lo(aa1) + bf_lo(bb1), bf_hi(xx1) + bf_hi(aa1) + bf_hi(bb1)});
            }
            STAMP(10);
        }
    }
    wait_lgkm0();
    __builtin_amdgcn_s_barrier();
    asm volatile("" ::: "memory");
    STAMP(11);
    {
        const u32 ra = (tid >> 3) * 272 + (tid & 7) * 32;
        f32x4 s0 = lds_ld<f32x4>(L_UB + ra), s1 = lds_ld<f32x4>(L_UB + ra + 16);
#pragma unroll
        for (int k = 0; k < 4; k++) { s0 += lds_ld<f32x4>(k * 32768 + ra); s1 += lds_ld<f32x4>(k * 32768 + ra + 16); }
        *(GAS v4u*)(out + (64 * tr + (tid >> 3)) * 512 + tc * 64 + (tid & 7) * 8) = (v4u){pk_bf(s0[0], s0[1]), pk_bf(s0[2], s0[3]), pk_bf(s1[0], s1[1]), pk_bf(s1[2], s1[3])};
    }
}

extern "C" void kernel_launch(void* const* d_in, const int* in_sizes, int n_in, void* d_out, int out_size, void* d_ws, size_t ws_size,
                              hipStream_t stream) {
    fused_kernel<<<256, 512, 0, stream>>>((const u16*)d_in[0], (const u16*)d_in[1], (const u16*)d_in[2], (const u16*)d_in[3], (const u16*)d_in[4],
                                          (const u16*)d_in[5], (const u16*)d_in[6], (const u16*)d_in[8], (const u16*)d_in[9], (const u16*)d_in[10],
                                          (const u16*)d_in[11], (u16*)d_out, (char*)d_ws);
}
```

```cpp
#include <hip/hip_runtime.h>
#include <stdint.h>

typedef unsigned short u16;
typedef unsigned int u32;
typedef unsigned long long u64;
typedef u32 v4u __attribute__((ext_vector_type(4)));
typedef u32 v2u __attribute__((ext_vector_type(2)));
typedef float f32x2 __attribute__((ext_vector_type(2)));
typedef float f32x4 __attribute__((ext_vector_type(4)));
typedef float f32x16 __attribute__((ext_vector_type(16)));
typedef __bf16 bf16x8 __attribute__((ext_vector_type(8)));
typedef __bf16 bf16x2 __attribute__((ext_vector_type(2)));
typedef _Float16 f16x8 __attribute__((ext_vector_type(8)));
typedef _Float16 f16x2 __attribute__((ext_vector_type(2)));
typedef short s16x4 __attribute__((ext_vector_type(4)));
#define GAS __attribute__((address_space(1)))
#define LAS __attribute__((address_space(3)))
#define DI __device__ __forceinline__
#define RLX_AGENT __ATOMIC_RELAXED, __HIP_MEMORY_SCOPE_AGENT
#define RLX_WG __ATOMIC_RELAXED, __HIP_MEMORY_SCOPE_WORKGROUP
#define SB0() __builtin_amdgcn_sched_barrier(0)
#define CHST 0
#define NOCS 0
#define NOLD 0
#define NOV 0

constexpr int OFF_F1 = 4096, OFF_F2 = 16384, OFF_F3 = 32768, OFF_ST = 65536;
constexpr int OFF_U = 1 << 20, OFF_A = 4 << 20, OFF_H = 8 << 20;
constexpr int U_PITCH = 4224, U_BYTES = 512 * U_PITCH;
constexpr int H_BYTES = 64 * 43 * 2048;
constexpr int L_C = 131072;
constexpr int L_FLG = L_C, L_CNT = L_C + 128, L_LNS = L_C + 192, L_SF = L_C + 224, L_E = L_C + 240, L_STAT = L_C + 256, L_B1 = L_C + 768, L_CSP = L_C + 1024;
constexpr int L_KREV = L_C + 3072, L_UB = L_C + 12288;
constexpr int SPIN_CAP = 1 << 16;
enum { C_A = 0, C_C = 1, C_D = 2, C_E = 3, C_F = 4, C_X = 5 };

#define STAMP(i)
#define CST(i)

template <typename T> DI T lds_ld(u32 a) { return *(LAS T*)(size_t)a; }
template <typename T> DI void lds_st(u32 a, T v) { *(LAS T*)(size_t)a = v; }
DI v2u lds_tr(u32 a) { return __builtin_bit_cast(v2u, __builtin_amdgcn_ds_read_tr16_b64_v4i16((LAS s16x4*)(size_t)a)); }
DI v4u mkrsrc(const void* p, u32 bytes) { u64 a = (u64)p; v4u r; r.x = (u32)a; r.y = (u32)(a >> 32) & 0xffffu; r.z = bytes; r.w = 0x00020000u; return r; }
DI void glds(v4u rsrc, u32 voff, u32 soff, u32 lds) {
    asm volatile("s_mov_b32 m0, %3\n\ts_nop 0\n\tbuffer_load_dwordx4 %0, %1, %2 offen lds" :: "v"(voff), "s"(rsrc), "s"(soff), "s"(lds) : "memory");
}
DI void glds_sc1(v4u rsrc, u32 voff, u32 soff, u32 lds) {
    asm volatile("s_mov_b32 m0, %3\n\ts_nop 0\n\tbuffer_load_dwordx4 %0, %1, %2 offen sc1 lds" :: "v"(voff), "s"(rsrc), "s"(soff), "s"(lds) : "memory");
}
DI void flag_dma(v4u rsrc, u32 lds) {
    asm volatile("s_mov_b32 m0, %2\n\ts_mov_b64 exec, 1\n\tbuffer_load_dword off, %0, %1 lds\n\ts_mov_b64 exec, -1" :: "s"(rsrc), "s"(rsrc.z), "s"(lds) : "memory");
}
DI void zero_dma(v4u rsrc, u32 lds, u64 mask) {
    asm volatile("s_mov_b32 m0, %2\n\ts_mov_b64 exec, %3\n\tbuffer_load_dwordx4 off, %0, %1 lds\n\ts_mov_b64 exec, -1" :: "s"(rsrc), "s"(rsrc.z), "s"(lds), "s"(mask) : "memory");
}
DI void st_sc1(v4u v, v4u rsrc, u32 voff, u32 soff) {
    asm volatile("buffer_store_dwordx4 %0, %1, %2, %3 offen sc1\n\ts_nop 1" :: "v"(v), "v"(voff), "s"(rsrc), "s"(soff) : "memory");
}
DI u32 opq(u32 a) { asm volatile("" : "+v"(a)); return a; }
template <typename T> DI T lds_ldv(u32 a) { return *(volatile LAS T*)(size_t)a; }
DI void spin_zero4(u32 a) {
    int n = 0;
    while (true) { v4u f = lds_ldv<v4u>(a); if (__builtin_amdgcn_readfirstlane(f.x | f.y | f.z | f.w) == 0 || ++n > (SPIN_CAP << 4)) break; }
    asm volatile("" ::: "memory");
}
template <int N> DI void wait_vm() { asm volatile("s_waitcnt vmcnt(%0)" :: "i"(N) : "memory"); }
DI void wait_lgkm0() { asm volatile("s_waitcnt lgkmcnt(0)" ::: "memory"); }
DI u64 mix64(u64 e) { e += 0x9E3779B97F4A7C15ull; e ^= e >> 31; e *= 0xBF58476D1CE4E5B9ull; e ^= e >> 29; return e; }
DI u64 get_tag() { const v2u e = lds_ld<v2u>(L_E); return mix64(((u64)(u32)__builtin_amdgcn_readfirstlane(e.y) << 32) | (u32)__builtin_amdgcn_readfirstlane(e.x)); }
DI void sig(int c) { asm volatile("s_mov_b64 exec, 1\n\tds_add_u32 %0, %1\n\ts_mov_b64 exec, -1" :: "v"(L_CNT + 4 * c), "v"(1u) : "memory"); }
DI void spin(int c, u32 target) {
    int n = 0;
    while ((u32)__builtin_amdgcn_readfirstlane(__hip_atomic_load((LAS u32*)(size_t)(L_CNT + 4 * c), RLX_WG)) < target && ++n < (SPIN_CAP << 4)) {}
    asm volatile("" ::: "memory");
}
DI void poll_flags(const GAS u64* p, u64 tag) {
    int n = 0;
    while (true) {
        u64 f = __hip_atomic_load(p, RLX_AGENT);
        if (__all(f == tag) || ++n > SPIN_CAP) break;
        __builtin_amdgcn_s_sleep(2);
    }
    asm volatile("" ::: "memory");
}
DI float bf_lo(u32 v) { return __uint_as_float(v << 16); }
DI float bf_hi(u32 v) { return __uint_as_float(v & 0xffff0000u); }
DI u32 pk_bf(float a, float b) { f32x2 v = {a, b}; return __builtin_bit_cast(u32, __builtin_convertvector(v, bf16x2)); }
DI float rbf(float a) { return bf_lo(pk_bf(a, a)); }
DI u32 pk_h(f32x2 v) { return __builtin_bit_cast(u32, __builtin_convertvector(v, f16x2)); }
DI f32x16 mfma32(v4u a, v4u b, f32x16 c) { return __builtin_amdgcn_mfma_f32_32x32x16_bf16(__builtin_bit_cast(bf16x8, a), __builtin_bit_cast(bf16x8, b), c, 0, 0, 0); }
DI f32x4 mfma16(v4u a, v4u b, f32x4 c) { return __builtin_amdgcn_mfma_f32_16x16x32_bf16(__builtin_bit_cast(bf16x8, a), __builtin_bit_cast(bf16x8, b), c, 0, 0, 0); }
DI f32x4 mfma16h(v4u a, v4u b, f32x4 c) { return __builtin_amdgcn_mfma_f32_16x16x32_f16(__builtin_bit_cast(f16x8, a), __builtin_bit_cast(f16x8, b), c, 0, 0, 0); }
template <int CTRL> DI float dpp_add(float v) { return v + __builtin_bit_cast(float, __builtin_amdgcn_update_dpp(0, __builtin_bit_cast(int, v), CTRL, 0xf, 0xf, true)); }
template <int CTRL> DI float dpp_max(float v) { return fmaxf(v, __builtin_bit_cast(float, __builtin_amdgcn_update_dpp(0, __builtin_bit_cast(int, v), CTRL, 0xf, 0xf, true))); }
DI float rl(float v, int l) { return __builtin_bit_cast(float, __builtin_amdgcn_readlane(__builtin_bit_cast(int, v), l)); }
DI float wave_sum(float v) {
    v = dpp_add<0xB1>(v); v = dpp_add<0x4E>(v); v = dpp_add<0x141>(v); v = dpp_add<0x140>(v);
    return (rl(v, 0) + rl(v, 16)) + (rl(v, 32) + rl(v, 48));
}
DI f32x2 wave_sum2(f32x2 v) {
    float a = v.x, c = v.y;
    a = dpp_add<0xB1>(a); c = dpp_add<0xB1>(c); a = dpp_add<0x4E>(a); c = dpp_add<0x4E>(c);
    a = dpp_add<0x141>(a); c = dpp_add<0x141>(c); a = dpp_add<0x140>(a); c = dpp_add<0x140>(c);
    return ((f32x2){rl(a, 0), rl(c, 0)} + (f32x2){rl(a, 16), rl(c, 16)}) + ((f32x2){rl(a, 32), rl(c, 32)} + (f32x2){rl(a, 48), rl(c, 48)});
}
DI float wave_max(float v) {
    v = dpp_max<0xB1>(v); v = dpp_max<0x4E>(v); v = dpp_max<0x141>(v); v = dpp_max<0x140>(v);
    return fmaxf(fmaxf(rl(v, 0), rl(v, 16)), fmaxf(rl(v, 32), rl(v, 48)));
}
DI f32x2 cmul(f32x2 a, f32x2 b) { f32x2 t = (f32x2){-a.y, a.y} * (f32x2){b.y, b.x}; return __builtin_elementwise_fma((f32x2){a.x, a.x}, b, t); }
DI f32x16 mfma32h(v4u a, v4u b, f32x16 c) { return __builtin_amdgcn_mfma_f32_32x32x16_f16(__builtin_bit_cast(f16x8, a), __builtin_bit_cast(f16x8, b), c, 0, 0, 0); }
DI u32 pk_h2(float a, float b) { return pk_h((f32x2){a, b}); }
DI f32x2 cexp_pow(float lr, float th, float tl, float e) {
    float a = __builtin_amdgcn_fractf(th * e) + tl * e;
    float m = __builtin_amdgcn_exp2f(lr * e);
    f32x2 r; r.x = m * __builtin_amdgcn_cosf(a); r.y = m * __builtin_amdgcn_sinf(a); return r;
}
DI f32x4 sel4(bool c, f32x4 a, f32x4 b) { return c ? a : b; }
constexpr int w2loc(int n) { return n >= 32 ? w2loc(n - 32) : n < 9 ? 3 + n : n < 13 ? 24 + (n - 9) : n < 16 ? 12 + (n - 13) : n < 20 ? 28 + (n - 16) : n < 29 ? 15 + (n - 20) : n - 29; }

__global__ void __launch_bounds__(512, 2)
fused_kernel(const u16* __restrict__ x, const u16* __restrict__ logA, const u16* __restrict__ Aim, const u16* __restrict__ Cre,
             const u16* __restrict__ Cim, const u16* __restrict__ logdt, const u16* __restrict__ Dskip, const u16* __restrict__ w1,
             const u16* __restrict__ b1, const u16* __restrict__ w2, const u16* __restrict__ b2, u16* __restrict__ out, char* ws) {
    __shared__ __attribute__((aligned(16))) char smem[163840];
    asm volatile("" :: "v"((u32)(size_t)(LAS char*)smem));
    const int tid = threadIdx.x, lane = tid & 63, wave = __builtin_amdgcn_readfirstlane(tid >> 6), w = wave & 3;
    const int b = blockIdx.x, xcd = b & 7, slot = b >> 3;
    const int P = 4 * (xcd >> 1) + (slot >> 3), j = 8 * (xcd & 1) + (slot & 7);
    const int tr = 4 * xcd + (slot >> 3), tc = slot & 7;
    const int u0 = (j * 43) >> 4;
    STAMP(0);
    const int h = lane >> 5, l31 = lane & 31, l15 = lane & 15, g = lane >> 4;

    if (wave < 4) {
        const int rot = (2 * (slot >> 3) + (slot & 7)) & 7;
        const v4u rx = mkrsrc(x + (128 * P + 32 * w) * 512, 32 * 1024);
        const v4u rw1 = mkrsrc(w1, 512 * 1376 * 2);
        const v4u rw2 = mkrsrc(w2, 512 * 1376 * 2);
        const u32 vx0 = (lane >> 3) * 1024 + ((lane & 7) ^ g) * 16, vx1 = vx0 ^ 64;
        u32 vw[3];
#pragma unroll
        for (int i = 0; i < 3; i++) { const int p = 64 * i + lane, r = (p * 171) >> 11; vw[i] = r * 2752 + (p - 12 * r) * 16; }
        const u32 vv = (lane >> 3) * 1024 + ((lane & 7) ^ ((g & 1) << 2)) * 16;
        const u32 lw = w * 32768;
        const u32 sw1 = 16 * w * 2752 + u0 * 64, sw2 = 352 * w * 1024 + tc * 128;
        auto ldX = [&](int c, int i) { glds(rx, (i & 1) ? vx1 : vx0, ((c + rot) & 7) * 128 + i * 8192, lw + (24 + (c & 1) * 4 + i) * 1024); };
        auto ldW = [&](int c, int i) { glds(rw1, vw[i], ((c + rot) & 7) * (64 * 2752) + sw1, lw + (3 * c + i) * 1024); };
        auto ldV = [&](int n) { glds(rw2, vv, sw2 + n * 8192, lw + w2loc(n) * 1024); };
        auto flg = [&](int c) { flag_dma(rx, L_FLG + c * 16 + w * 4); };
        if (lane < 8) lds_st<u32>(L_FLG + lane * 16 + w * 4, 1u);
        if (lane < 24) lds_st<v4u>(L_CSP + w * 384 + lane * 16, (v4u){~0u, ~0u, ~0u, ~0u});
#pragma unroll
        for (int u = 0; u < 3; u++) ldW(0, u);
#pragma unroll
        for (int i = 0; i < 4; i++) ldX(0, i);
        wait_lgkm0();
        flg(0);
#pragma unroll
        for (int u = 0; u < 3; u++) ldW(1, u);
#pragma unroll
        for (int i = 0; i < 4; i++) ldX(1, i);
        flg(1);
#pragma unroll
        for (int u = 0; u < 6; u++) ldW(2 + u / 3, u % 3);
        __builtin_amdgcn_s_setprio(1);
        u32 aW[4], aX[4];
#pragma unroll
        for (int s = 0; s < 4; s++) {
            const int ks = (s + w) & 3;
            aW[s] = opq(ks * 32768 + (8 * h + (l15 >> 2)) * 192 + (g & 1) * 32 + (lane & 3) * 8);
            aX[s] = opq(lw + 24576 + l31 * 128 + (((2 * ks + h) ^ ((l31 >> 1) & 7)) * 16));
        }
        const u32 aF = opq(L_FLG);
        f32x16 acc[3]; f32x4 cs = {0.f, 0.f, 0.f, 0.f};
        v4u sel[3];
#pragma unroll
        for (int u = 0; u < 3; u++) { for (int i = 0; i < 16; i++) acc[u][i] = 0.f; const u32 sv = (l15 == 2 * u + (g & 1)) ? 0x3F803F80u : 0u; sel[u] = (v4u){sv, sv, sv, sv}; }
        __builtin_amdgcn_s_barrier();
        asm volatile("" ::: "memory");
        v4u fa[3][3], fb[2][4];
        auto rdW = [&](int n, int u) { const u32 a = aW[n & 3] + (n >> 2) * 3072 + u * 64; v2u lo = lds_tr(a), hi = lds_tr(a + 768); fa[n % 3][u] = (v4u){lo.x, lo.y, hi.x, hi.y}; };
        auto rdX = [&](int c, int s) { fb[c & 1][s] = lds_ld<v4u>(aX[s] + (c & 1) * 4096); };
        spin_zero4(aF);
        STAMP(1);
#pragma unroll
        for (int s = 0; s < 4; s++) rdX(0, s);
#pragma unroll
        for (int u = 0; u < 6; u++) rdW(u / 3, u % 3);
        asm volatile("" : "+v"(fb[0][0]), "+v"(fb[0][1]), "+v"(fb[0][2]), "+v"(fb[0][3]));
#pragma unroll
        for (int i = 0; i < 4; i++) ldX(2, i);
        flg(2);
        v4u fl;
#pragma unroll
        for (int n = 0; n < 32; n++) {
            const int c = n >> 2, s = n & 3;
            auto req = [&](int k) {
                if (s == 3) {
                    if (c < 5) { if (k < 2) { ldX(c + 3, 2 * k); ldX(c + 3, 2 * k + 1); } else flg(c + 3); }
                    else if (c < 7 && k < 2 && !NOV) { ldV(7 * c - 26 + 2 * k); ldV(7 * c - 25 + 2 * k); }
                } else if (k == 1) {
                    if (c < 4) ldW(c + 4, s); else if (!NOV) ldV(c < 7 ? 3 * (c - 4) + s : 13 + s);
                }
            };
            SB0();
            if (s == 2 && c < 7 && !NOLD) {
                if (__builtin_expect(__builtin_amdgcn_readfirstlane(fl.x | fl.y | fl.z | fl.w) != 0, 0)) spin_zero4(aF + (c + 1) * 16);
                asm volatile("" ::: "memory");
            }
            if (s == 3 && c < 7) asm volatile("" : "+v"(fb[(c + 1) & 1][0]), "+v"(fb[(c + 1) & 1][1]), "+v"(fb[(c + 1) & 1][2]), "+v"(fb[(c + 1) & 1][3]));
#pragma unroll
            for (int u = 0; u < 3; u++) {
                acc[u] = mfma32(fa[n % 3][u], fb[c & 1][s], acc[u]);
                if (s == 0 && !NOCS) cs = mfma16(fa[n % 3][u], sel[u], cs);
                SB0();
                if (s == 2 && c < 7 && u < 2) { rdX(c + 1, 2 * u); rdX(c + 1, 2 * u + 1); }
                if (n + 2 < 32) rdW(n + 2, u);
                if (s == 1 && u == 0 && c < 7) fl = lds_ldv<v4u>(aF + (c + 1) * 16);
                if (n == 29 && u == 2 && l15 < 6) lds_st<f32x4>(L_CSP + w * 384 + l15 * 64 + g * 16, cs);
                if (!NOLD) req(u);
                SB0();
            }
        }
        STAMP(2); CST(1);
        const u32 aV0 = opq(lw + (4 * h + (l15 >> 2)) * 128 + ((l15 >> 3) & 1) * 64 + (g & 1) * 32 + (lane & 3) * 8), aV1 = opq(aV0 ^ 64);
        auto rdV = [&](int s, int dt) { v2u lo = lds_tr((dt ? aV1 : aV0) + w2loc(2 * s) * 1024), hi = lds_tr((dt ? aV1 : aV0) + w2loc(2 * s + 1) * 1024); return (v4u){lo.x, lo.y, hi.x, hi.y}; };
        v4u park[6][2];
        {
            float cc[3];
            {
                int n = 0;
                const u32 ca = opq(L_CSP + l31 * 4);
                while (true) {
                    u32 all = lds_ldv<u32>(L_SF) == 0 ? 0u : ~0u, bad = 0;
#pragma unroll
                    for (int u = 0; u < 3; u++) {
                        const u32 p0 = lds_ldv<u32>(ca + u * 128), p1 = lds_ldv<u32>(ca + u * 128 + 384), p2 = lds_ldv<u32>(ca + u * 128 + 768), p3 = lds_ldv<u32>(ca + u * 128 + 1152);
                        bad |= (p0 == ~0u) | (p1 == ~0u) | (p2 == ~0u) | (p3 == ~0u);
                        cc[u] = -((__uint_as_float(p0) + __uint_as_float(p1)) + (__uint_as_float(p2) + __uint_as_float(p3)));
                    }
                    if (!__any(bad | all) || ++n > (SPIN_CAP << 2)) break;
                }
                asm volatile("" ::: "memory");
            }
            STAMP(3);
            const u64 tag = get_tag();
            const u32 stw = lds_ld<u32>(L_STAT + (32 * w + l31) * 4);
            const u32 mm2 = (stw & 0xffffu) * 0x10001u;
            const v4u fbs = {h ? 0u : mm2, h ? 0u : stw, 0u, 0u};
#pragma unroll
            for (int u = 0; u < 3; u++) {
                const float c = cc[u], c0 = rbf(c), c1 = rbf(c - c0), c2 = (c - c0) - c1;
                const u32 bb = lds_ld<u16>(L_B1 + (u * 32 + l31) * 2);
                const v4u fas = {pk_bf(c0, c1), (pk_bf(c2, c2) & 0xffffu) | (bb << 16), 0u, 0u};
                acc[u] = mfma32(fas, fbs, acc[u]);
            }
            const float rs = __builtin_amdgcn_rcpf(bf_hi(stw));
            const v4u rhs = mkrsrc(ws + OFF_H, H_BYTES);
            const __amdgpu_buffer_rsrc_t rh = __builtin_amdgcn_make_buffer_rsrc(ws + OFF_H, 0, H_BYTES, 0x00020000);
            const int nown = (((j + 1) * 43) >> 4) - u0;
            const u32 sh = ((4 * P + w) * 43 + u0) * 2048;
#pragma unroll
            for (int u = 0; u < 3; u++) {
#pragma unroll
                for (int s = 0; s < 2; s++) {
                    v4u o;
#pragma unroll
                    for (int e = 0; e < 4; e++) {
                        f32x2 r = (f32x2){acc[u][8 * s + 2 * e], acc[u][8 * s + 2 * e + 1]} * rs;
                        r.x = fmaxf(r.x, 0.f); r.y = fmaxf(r.y, 0.f); r = r * r;
                        o[e] = pk_bf(r.x, r.y);
                    }
                    st_sc1(o, rhs, lane * 16, (u < 2 || nown == 3) ? sh + u * 2048 + s * 1024 : (u32)H_BYTES);
                    if (2 * u + s < 5) ldV(20 + 2 * u + s);
                    SB0();
                }
            }
#pragma unroll
            for (int n = 25; n < 29; n++) ldV(n);
            wait_vm<22>();
#pragma unroll
            for (int s = 0; s < 6; s++) { park[s][0] = rdV(s, 0); park[s][1] = rdV(s, 1); }
#pragma unroll
            for (int s = 0; s < 6; s++) asm volatile("" : "+v"(park[s][0]), "+v"(park[s][1]));
#pragma unroll
            for (int n = 32; n < 38; n++) ldV(n);
            wait_vm<10>();
            STAMP(4);
            if (lane == 0) __hip_atomic_store((GAS u64*)(ws + OFF_F2) + (4 * P + w) * 16 + j, tag, RLX_AGENT);
#pragma unroll
            for (int n = 38; n < 44; n++) ldV(n);
            spin(C_C, 4);
#pragma unroll
            for (int n = 29; n < 32; n++) ldV(n);
            poll_flags((const GAS u64*)(ws + OFF_F2) + 2 * tr * 16 + l31, tag);
            STAMP(5);
            f32x16 ac2[2][2];
#pragma unroll
            for (int i = 0; i < 16; i++) { ac2[0][0][i] = 0.f; ac2[0][1][i] = 0.f; ac2[1][0][i] = 0.f; ac2[1][1][i] = 0.f; }
            const u32 sh2 = (2 * tr * 43 + 11 * w) * 2048;
            const u32 sh2t = (w == 3) ? (u32)H_BYTES : sh2;
            constexpr int WIN = 8;
            v4u hf[WIN][2];
            auto ldH = [&](int s) {
                const u32 so = (s >= 20 ? sh2t : sh2) + s * 1024;
                hf[s % WIN][0] = __builtin_amdgcn_raw_buffer_load_b128(rh, lane * 16, so, 16);
                hf[s % WIN][1] = __builtin_amdgcn_raw_buffer_load_b128(rh, lane * 16, so + 43 * 2048, 16);
            };
#pragma unroll
            for (int s = 0; s < WIN; s++) ldH(s);
            v4u vb[2][2];
#pragma unroll
            for (int s = 0; s < 22; s++) {
                SB0();
                if (s + 1 >= 6 && s + 1 < 22) { vb[(s + 1) & 1][0] = rdV(s + 1, 0); vb[(s + 1) & 1][1] = rdV(s + 1, 1); }
                const v4u a0 = s < 6 ? park[s < 6 ? s : 0][0] : vb[s & 1][0], a1 = s < 6 ? park[s < 6 ? s : 0][1] : vb[s & 1][1];
                ac2[0][0] = mfma32(a0, hf[s % WIN][0], ac2[0][0]);
                ac2[1][0] = mfma32(a1, hf[s % WIN][0], ac2[1][0]);
                ac2[0][1] = mfma32(a0, hf[s % WIN][1], ac2[0][1]);
                ac2[1][1] = mfma32(a1, hf[s % WIN][1], ac2[1][1]);
                SB0();
                if (s + WIN < 22) ldH(s + WIN);
            }
            SB0();
            STAMP(7); CST(3);
#pragma unroll
            for (int tt = 0; tt < 2; tt++)
#pragma unroll
                for (int dt = 0; dt < 2; dt++)
#pragma unroll
                    for (int q = 0; q < 4; q++)
                        lds_st<f32x4>(lw + (32 * tt + l31) * 272 + (32 * dt + 8 * q + 4 * h) * 4,
                                      (f32x4){ac2[dt][tt][4 * q], ac2[dt][tt][4 * q + 1], ac2[dt][tt][4 * q + 2], ac2[dt][tt][4 * q + 3]});
        }
    } else {
        const int tb = 16 * P + j;
        const int chl = w >> 1, hf = w & 1, ch = 2 * b + chl;
        {
            const v4u rxr = mkrsrc(x + (8 * tb + 2 * w) * 512, 2048);
            glds(rxr, lane * 16, 0, L_KREV + (2 * w) * 1088);
            glds(rxr, lane * 16, 1024, L_KREV + (2 * w + 1) * 1088);
        }
        const int pi = ch * 128 + hf * 64 + lane;
        const u32 pA = logA[pi], pI = Aim[pi], pR = Cre[pi], pC = Cim[pi], pT = logdt[ch], pD = Dskip[ch];
        if (w == 1) {
            const v4u rb1 = mkrsrc(b1, 1376 * 2);
            asm volatile("s_mov_b32 m0, %3\n\ts_nop 0\n\tbuffer_load_dword %0, %1, %2 offen lds" :: "v"(lane * 4), "s"(rb1), "s"(u0 * 64), "s"(L_B1) : "memory");
        }
        if (w == 0 && lane < 28) lds_st<u32>(L_CNT + lane * 4, lane < 16 ? 0u : ~0u);
        if (w == 2) {
            const v4u re = mkrsrc(ws, 16);
            asm volatile("s_mov_b32 m0, %1\n\ts_mov_b64 exec, 1\n\tbuffer_load_dwordx4 off, %0, 0 sc1 lds\n\ts_mov_b64 exec, -1" :: "s"(re), "s"(L_E) : "memory");
        }
        asm volatile("s_waitcnt vmcnt(0) lgkmcnt(0)" ::: "memory");
        __builtin_amdgcn_s_barrier();
        asm volatile("" ::: "memory");
        const u64 tag = get_tag();
        STAMP(1);
        {
            const v4u xr0 = lds_ld<v4u>(L_KREV + (2 * w) * 1088 + lane * 16), xr1 = lds_ld<v4u>(L_KREV + (2 * w + 1) * 1088 + lane * 16);
            f32x2 v[8];
#pragma unroll
            for (int i = 0; i < 4; i++) { v[2 * i] = (f32x2){bf_lo(xr0[i]), bf_lo(xr1[i])}; v[2 * i + 1] = (f32x2){bf_hi(xr0[i]), bf_hi(xr1[i])}; }
            f32x2 sm = ((v[0] + v[1]) + (v[2] + v[3])) + ((v[4] + v[5]) + (v[6] + v[7]));
            const f32x2 m = wave_sum2(sm) * (1.f / 512.f);
            f32x2 q = {0.f, 0.f};
#pragma unroll
            for (int i = 0; i < 8; i++) { f32x2 d = v[i] - m; q = __builtin_elementwise_fma(d, d, q); }
            const f32x2 var = wave_sum2(q) * (1.f / 512.f);
            const u32 vb = pk_bf(var.x, var.y), ve = pk_bf(bf_lo(vb) + 1e-5f, bf_hi(vb) + 1e-5f);
            const float sd0 = __builtin_amdgcn_sqrtf(bf_lo(ve)), sd1 = __builtin_amdgcn_sqrtf(bf_hi(ve));
            if (lane == 0) lds_st<v2u>(L_LNS + w * 8, (v2u){pk_bf(m.x, sd0) & 0x7fffffffu, pk_bf(m.y, sd1) & 0x7fffffffu});
        }
        const v4u rus = mkrsrc(ws + OFF_U, U_BYTES);
        {
            const u32 ta = opq(L_KREV + (l15 >> 2) * 1088 + (128 * w + 16 * g + 4 * (lane & 3)) * 2);
            v2u xt[2][2];
#pragma unroll
            for (int i = 0; i < 2; i++) { xt[i][0] = lds_tr(ta + i * 128); xt[i][1] = lds_tr(ta + i * 128 + 4 * 1088); }
            v4u s0, s1;
            int n = 0;
            while (true) {
                s0 = lds_ldv<v4u>(L_LNS); s1 = lds_ldv<v4u>(L_LNS + 16);
                if ((int)__builtin_amdgcn_readfirstlane(s0.x | s0.z | s1.x | s1.z) >= 0 || ++n > (SPIN_CAP << 4)) break;
            }
            float mb[8], rr[8];
#pragma unroll
            for (int t = 0; t < 8; t++) { const u32 sv = t < 4 ? s0[t & 3] : s1[t & 3]; mb[t] = bf_lo(sv); rr[t] = __builtin_amdgcn_rcpf(bf_hi(sv)); }
#pragma unroll
            for (int i = 0; i < 2; i++) {
                v4u o;
#pragma unroll
                for (int e = 0; e < 4; e++) {
                    const u32 xv = xt[i][e >> 1][e & 1];
                    const u32 d = pk_bf(bf_lo(xv) - mb[2 * e], bf_hi(xv) - mb[2 * e + 1]);
                    o[e] = pk_bf(bf_lo(d) * rr[2 * e], bf_hi(d) * rr[2 * e + 1]);
                }
                st_sc1(o, rus, (128 * w + 64 * i + lane) * U_PITCH, 128 + tb * 16);
                if (tb == 0) st_sc1((v4u){0u, 0u, 0u, 0u}, rus, (128 * w + 64 * i + lane) * U_PITCH, 112);
            }
            if (lane < 2) st_sc1(lane ? s1 : s0, mkrsrc(ws + OFF_ST, 32768), lane * 16, w * 8192 + tb * 32);
        }
        STAMP(2);
        const float dt = rbf(expf(bf_lo(pT)));
        {
            const float zr = -rbf(expf(bf_lo(pA))) * dt, zi = bf_lo(pI) * dt;
            const float lr = zr * 1.44269504088896341f;
            const float th = zi * 0.159154943091895336f, tl = __builtin_fmaf(zi, 0.159154943091895336f, -th) + zi * 6.4206383e-09f;
            const f32x2 w1c = cexp_pow(lr, -th, -tl, 1.f), qc = cexp_pow(lr, -th, -tl, 128.f);
            const f32x2 w8 = cexp_pow(lr, th, tl, 8.f), w32 = cexp_pow(lr, th, tl, 32.f);
            const float cmax = wave_max(fmaxf(fabsf(bf_lo(pR)), fabsf(bf_lo(pC))));
            int ex = (int)(__float_as_uint(cmax) >> 23); ex = ex < 12 ? 12 : (ex > 240 ? 240 : ex);
            const float sdn = __uint_as_float((u32)(262 - ex) << 23), sup = __uint_as_float((u32)(ex - 8) << 23);
            v4u selLo, selHi;
#pragma unroll
            for (int e = 0; e < 4; e++) {
                const u32 one = (h == (g & 1) && ((l15 & 7) >> 1) == e) ? ((l15 & 1) ? 0x3C000000u : 0x00003C00u) : 0u;
                selLo[e] = l15 < 8 ? one : 0u; selHi[e] = l15 < 8 ? 0u : one;
            }
            auto transpose = [&](const f32x2 (&v)[16], v4u (&f)[4]) {
                f32x16 z; for (int i = 0; i < 16; i++) z[i] = 0.f;
                f32x16 dr = mfma32h((v4u){pk_h2(v[0].x, v[1].x), pk_h2(v[2].x, v[3].x), pk_h2(v[4].x, v[5].x), pk_h2(v[6].x, v[7].x)}, selLo, z);
                f32x16 di = mfma32h((v4u){pk_h2(v[0].y, v[1].y), pk_h2(v[2].y, v[3].y), pk_h2(v[4].y, v[5].y), pk_h2(v[6].y, v[7].y)}, selLo, z);
                dr = mfma32h((v4u){pk_h2(v[8].x, v[9].x), pk_h2(v[10].x, v[11].x), pk_h2(v[12].x, v[13].x), pk_h2(v[14].x, v[15].x)}, selHi, dr);
                di = mfma32h((v4u){pk_h2(v[8].y, v[9].y), pk_h2(v[10].y, v[11].y), pk_h2(v[12].y, v[13].y), pk_h2(v[14].y, v[15].y)}, selHi, di);
#pragma unroll
                for (int e = 0; e < 4; e++) {
                    f[0][e] = pk_h2(dr[2 * e], dr[2 * e + 1]); f[1][e] = pk_h2(dr[8 + 2 * e], dr[9 + 2 * e]);
                    f[2][e] = pk_h2(di[2 * e], di[2 * e + 1]); f[3][e] = pk_h2(di[8 + 2 * e], di[9 + 2 * e]);
                }
            };
            v4u fy[4], fx[4];
            f32x2 v[16];
            v[0] = (f32x2){1.f, 0.f}; v[1] = w8; v[2] = cmul(w8, w8); v[3] = cmul(v[2], w8); v[4] = w32;
#pragma unroll
            for (int i = 5; i < 16; i++) v[i] = cmul(v[i - 4], w32);
            transpose(v, fy);
            wait_vm<0>();
            if (lane == 0) __hip_atomic_store((GAS u64*)(ws + OFF_F1) + w * 256 + tb, tag, RLX_AGENT);
            STAMP(3); CST(2);
            const float fin = 2.f * dt * sup;
            f32x4 kt[3];
            auto tile = [&](int t) {
                transpose(v, fx);
                f32x4 d = {0.f, 0.f, 0.f, 0.f};
#pragma unroll
                for (int s = 0; s < 4; s++) d = mfma16h(fy[s], fx[s], d);
                kt[t] = d * fin;
            };
            f32x2 xo[8];
            xo[0] = (f32x2){bf_lo(pR) * sdn, -bf_lo(pC) * sdn};
#pragma unroll
            for (int i = 1; i < 8; i++) xo[i] = cmul(xo[i - 1], w1c);
            const f32x2 q2 = cmul(qc, qc);
            v[0] = xo[0]; v[1] = cmul(xo[0], qc);
#pragma unroll
            for (int s = 2; s < 16; s++) v[s] = cmul(v[s - 2], q2);
            const f32x2 last = cmul(v[14], q2);
            tile(0);
            v[0] = last; v[1] = xo[4]; v[9] = xo[2]; v[13] = xo[6];
#pragma unroll
            for (int s = 2; s < 9; s++) v[s] = cmul(v[s - 1], qc);
#pragma unroll
            for (int s = 10; s < 13; s++) v[s] = cmul(v[s - 1], qc);
            v[14] = cmul(v[13], qc); v[15] = cmul(v[14], qc);
            const f32x2 last2 = cmul(v[15], qc);
            tile(1);
            v[0] = last2;
#pragma unroll
            for (int i = 0; i < 4; i++) { v[1 + 2 * i] = xo[2 * i + 1]; v[2 + 2 * i] = cmul(xo[2 * i + 1], qc); }
#pragma unroll
            for (int i = 9; i < 16; i++) v[i] = (f32x2){0.f, 0.f};
            tile(2);
            CST(3);
            spin_zero4(L_FLG + 3 * 16);
#pragma unroll
            for (int t = 0; t < 3; t++) lds_st<f32x4>(w * 32768 + (16 * t + l15) * 64 + g * 16, kt[t]);
        }
        STAMP(4);
        poll_flags((const GAS u64*)(ws + OFF_F1) + (b >> 6) * 256 + 64 * w + lane, tag);
        STAMP(5);
        if (w == (P >> 2)) {
            const v4u rst = mkrsrc(ws + OFF_ST + (b >> 6) * 8192 + P * 512, 512);
            asm volatile("s_mov_b32 m0, %2\n\ts_mov_b32 exec_hi, 0\n\tbuffer_load_dwordx4 %0, %1, 0 offen sc1 lds\n\ts_mov_b32 exec_hi, -1" :: "v"(lane * 16), "s"(rst), "s"(L_STAT) : "memory");
            flag_dma(rst, L_SF);
        }
        sig(C_A); spin(C_A, 4);
#pragma unroll
        for (int i = 0; i < 4; i++) glds_sc1(rus, lane * 16, (2 * b + (i >> 1)) * U_PITCH + 128 + w * 1024 - (i & 1) * 2, L_UB + (i >> 1) * 8704 + (i & 1) * 4288 + 32 + w * 1024);
        zero_dma(rus, L_UB + chl * 8704 + hf * 4288, 3ull);
        {
            const int m = 64 * hf + lane;
            const u32 t0 = opq((2 * chl) * 32768 + m * 4);
            const bool r1 = m < 32, r2 = m < 64;
            const int bs[8] = {0, r1 ? 528 : 0, r2 ? 400 : 0, r1 ? 560 : 272, 272, r1 ? 592 : 272, r2 ? 464 : 1, r1 ? 624 : 1};
            float k[8];
#pragma unroll
            for (int e = 0; e < 8; e++) k[e] = lds_ld<float>(t0 + bs[e] * 4) + lds_ld<float>(t0 + bs[e] * 4 + 32768);
            if (m == 0) k[0] += bf_lo(pD);
            const u32 kr = L_KREV + chl * 4608;
            const float va = lds_ld<float>(t0 + 512) + lds_ld<float>(t0 + 512 + 32768);
            const float vb = lds_ld<float>(t0 + 516) + lds_ld<float>(t0 + 516 + 32768);
            lds_st<v4u>(kr + (2040 - 8 * m) * 2, (v4u){pk_bf(k[7], k[6]), pk_bf(k[5], k[4]), pk_bf(k[3], k[2]), pk_bf(k[1], k[0])});
            const u32 pa = pk_bf(va, va), pb = pk_bf(vb, vb);
            lds_st<v4u>(kr + (1016 - 8 * m) * 2, (v4u){pb, (m & 1) ? pb : pk_bf(vb, va), pa, pa});
            zero_dma(rus, kr + 4096 + hf * 256, 0xffffull);
        }
        wait_vm<0>();
        sig(C_C); spin(C_C, 4);
        STAMP(6);
        if (b == 0 && w == 0 && lane == 0) __hip_atomic_store((GAS u64*)ws, tag, RLX_AGENT);
        f32x4 cv[8];
        {
#pragma unroll
            for (int t = 0; t < 8; t++) cv[t] = (f32x4){0.f, 0.f, 0.f, 0.f};
            u32 ua = L_UB + chl * 8704 + ((l15 & 1) ? (1 + l15 + 8 * g) * 2 : 4288 + (2 + l15 + 8 * g) * 2) + hf * 256;
            u32 ka = L_KREV + chl * 4608 + (240 - 16 * l15 + 8 * g) * 2 + hf * 256;
            v4u A[8], B[8];
            u32 ub[4];
            auto rdA = [&](int a) { const u32 p = ub[a >> 1] + (a & 1) * 512; return (v4u){lds_ld<u32>(p), lds_ld<u32>(p + 4), lds_ld<u32>(p + 8), lds_ld<u32>(p + 12)}; };
            auto rdB = [&](int d) { return lds_ld<v4u>(ka + (7 - d) * 512); };
#pragma unroll
            for (int i = 0; i < 4; i++) ub[i] = opq(ua + i * 1024);
            ka = opq(ka);
#pragma unroll
            for (int i = 0; i < 8; i++) { A[7 - i] = rdA(7 - i); B[i] = rdB(i); }
CST(0); STAMP(20);
#pragma unroll 1
            for (int r = 0; r < 4; r++) {
                ka = opq(ka + 64);
#pragma unroll
                for (int i = 0; i < 4; i++) ub[i] = opq(ub[i] + 64);
#pragma unroll
                for (int a = 7; a >= 0; a--) {
#pragma unroll
                    for (int d = 0; d + a < 8; d++) {
                        cv[a + d] = mfma16(A[a], B[d], cv[a + d]);
                        if (a == 0) { SB0(); B[d] = rdB(d); SB0(); }
                    }
                    SB0(); A[a] = rdA(a); SB0();
                }
            }
        }
        STAMP(7); CST(1);
        sig(C_D); spin(C_D, 4);
        {
            const u32 mine = hf ? L_UB + chl * 8704 + 4288 : L_KREV + chl * 4608, other = hf ? L_KREV + chl * 4608 : L_UB + chl * 8704 + 4288;
#pragma unroll
            for (int t = 0; t < 4; t++) lds_st<f32x4>(mine + t * 1024 + lane * 16, sel4(hf, cv[t], cv[4 + t]));
            sig(C_E); spin(C_E, 4);
            const __amdgpu_buffer_rsrc_t ra = __builtin_amdgcn_make_buffer_rsrc(ws + OFF_A, 0, 2 << 20, 0x00020000);
            u32 o[8];
#pragma unroll
            for (int t = 0; t < 4; t++) {
                const f32x4 y = sel4(hf, cv[4 + t], cv[t]) + lds_ld<f32x4>(other + t * 1024 + lane * 16);
                o[2 * t] = pk_bf(y[0], y[1]); o[2 * t + 1] = pk_bf(y[2], y[3]);
            }
            const v4u ras = mkrsrc(ws + OFF_A, 2 << 20);
            st_sc1((v4u){o[0], o[1], o[2], o[3]}, ras, lane * 16, ch * 4096 + hf * 2048);
            st_sc1((v4u){o[4], o[5], o[6], o[7]}, ras, lane * 16, ch * 4096 + hf * 2048 + 1024);
            sig(C_X);
            wait_vm<0>();
            STAMP(8);
            if (lane == 0) __hip_atomic_store((GAS u64*)(ws + OFF_F3) + hf * 512 + ch, tag, RLX_AGENT);
            const int ht = tid - 256, rt = ht >> 2, ro = (ht & 3) * 16;
            const GAS v4u* xp = (const GAS v4u*)(x + (64 * tr + rt) * 512 + tc * 64 + ro);
            const GAS v4u* bp = (const GAS v4u*)(b2 + tc * 64 + ro);
            const v4u xa = xp[0], xb = xp[1], ba = bp[0], bb = bp[1];
            poll_flags((const GAS u64*)(ws + OFF_F3) + (tr >> 4) * 512 + tc * 64 + 16 * w + l15, tag);
            STAMP(9);
            v4u av[4];
#pragma unroll
            for (int i = 0; i < 4; i++)
                av[i] = __builtin_amdgcn_raw_buffer_load_b128(ra, (tc * 64 + 16 * w + 4 * i + g) * 4096 + ((l15 >> 2) * 16 + (tr & 3) * 4 + (lane & 3)) * 16, (tr >> 3) * 1024, 16);
            spin(C_X, 4);
            const bool odd = (tr >> 2) & 1;
#pragma unroll
            for (int i = 0; i < 4; i++) {
                const u32 lo = odd ? av[i].z : av[i].x, hi = odd ? av[i].w : av[i].y;
                const u32 aa = L_KREV + (16 * (lane & 3) + 4 * (l15 >> 2)) * 144 + (16 * w + 4 * i + g) * 2;
                lds_st<u16>(aa, (u16)lo); lds_st<u16>(aa + 144, (u16)(lo >> 16)); lds_st<u16>(aa + 288, (u16)hi); lds_st<u16>(aa + 432, (u16)(hi >> 16));
            }
            sig(C_F); spin(C_F, 4);
            const v4u a0 = lds_ld<v4u>(L_KREV + rt * 144 + ro * 2), a1 = lds_ld<v4u>(L_KREV + rt * 144 + ro * 2 + 16);
#pragma unroll
            for (int i = 0; i < 4; i++) {
                const u32 xx0 = i < 2 ? xa[2 * i] : xb[2 * i - 4], xx1 = i < 2 ? xa[2 * i + 1] : xb[2 * i - 3];
                const u32 bb0 = i < 2 ? ba[2 * i] : bb[2 * i - 4], bb1 = i < 2 ? ba[2 * i + 1] : bb[2 * i - 3];
                const u32 aa0 = i < 2 ? a0[2 * i] : a1[2 * i - 4], aa1 = i < 2 ? a0[2 * i + 1] : a1[2 * i - 3];
                lds_st<f32x4>(L_UB + rt * 272 + (ro + 4 * i) * 4,
                              (f32x4){bf_lo(xx0) + bf_lo(aa0) + bf_lo(bb0), bf_hi(xx0) + bf_hi(aa0) + bf_hi(bb0), bf_lo(xx1) + bf_lo(aa1) + bf_lo(bb1), bf_hi(xx1) + bf_hi(aa1) + bf_hi(bb1)});
            }
            STAMP(10);
        }
    }
    wait_lgkm0();
    __builtin_amdgcn_s_barrier();
    asm volatile("" ::: "memory");
    STAMP(11);
    {
        const u32 ra = (tid >> 3) * 272 + (tid & 7) * 32;
        f32x4 s0 = lds_ld<f32x4>(L_UB + ra), s1 = lds_ld<f32x4>(L_UB + ra + 16);
#pragma unroll
        for (int k = 0; k < 4; k++) { s0 += lds_ld<f32x4>(k * 32768 + ra); s1 += lds_ld<f32x4>(k * 32768 + ra + 16); }
        *(GAS v4u*)(out + (64 * tr + (tid >> 3)) * 512 + tc * 64 + (tid & 7) * 8) = (v4u){pk_bf(s0[0], s0[1]), pk_bf(s0[2], s0[3]), pk_bf(s1[0], s1[1]), pk_bf(s1[2], s1[3])};
    }
}

extern "C" void kernel_launch(void* const* d_in, const int* in_sizes, int n_in, void* d_out, int out_size, void* d_ws, size_t ws_size,
                              hipStream_t stream) {
    fused_kernel<<<256, 512, 0, stream>>>((const u16*)d_in[0], (const u16*)d_in[1], (const u16*)d_in[2], (const u16*)d_in[3], (const u16*)d_in[4],
                                          (const u16*)d_in[5], (const u16*)d_in[6], (const u16*)d_in[8], (const u16*)d_in[9], (const u16*)d_in[10],
                                          (const u16*)d_in[11], (u16*)d_out, (char*)d_ws);
}
```

```cpp
#define EXPT 0
#include <hip/hip_runtime.h>
#include <stdint.h>

typedef unsigned short u16;
typedef unsigned int u32;
typedef unsigned long long u64;
typedef u32 v4u __attribute__((ext_vector_type(4)));
typedef u32 v2u __attribute__((ext_vector_type(2)));
typedef float f32x2 __attribute__((ext_vector_type(2)));
typedef float f32x4 __attribute__((ext_vector_type(4)));
typedef float f32x16 __attribute__((ext_vector_type(16)));
typedef __bf16 bf16x8 __attribute__((ext_vector_type(8)));
typedef __bf16 bf16x2 __attribute__((ext_vector_type(2)));
typedef _Float16 f16x8 __attribute__((ext_vector_type(8)));
typedef _Float16 f16x2 __attribute__((ext_vector_type(2)));
typedef short s16x4 __attribute__((ext_vector_type(4)));
#define GAS __attribute__((address_space(1)))
#define LAS __attribute__((address_space(3)))
#define DI __device__ __forceinline__
#define RLX_AGENT __ATOMIC_RELAXED, __HIP_MEMORY_SCOPE_AGENT
#define RLX_WG __ATOMIC_RELAXED, __HIP_MEMORY_SCOPE_WORKGROUP
#define SB0() __builtin_amdgcn_sched_barrier(0)
#define CHST 0
#define NOCS 0
#define NOLD 0
#define NOV 0

constexpr int OFF_F1 = 4096, OFF_F2 = 16384, OFF_F3 = 32768, OFF_ST = 65536;
constexpr int OFF_U = 1 << 20, OFF_A = 4 << 20, OFF_H = 8 << 20;
constexpr int U_PITCH = 4224, U_BYTES = 512 * U_PITCH;
constexpr int H_BYTES = 64 * 43 * 2048;
constexpr int L_C = 131072;
constexpr int L_FLG = L_C, L_CNT = L_C + 128, L_LNS = L_C + 192, L_SF = L_C + 224, L_E = L_C + 240, L_STAT = L_C + 256, L_B1 = L_C + 768, L_CSP = L_C + 1024;
constexpr int L_KREV = L_C + 3072, L_UB = L_C + 12288;
constexpr int SPIN_CAP = 1 << 16;
enum { C_A = 0, C_C = 1, C_D = 2, C_E = 3, C_F = 4, C_X = 5 };

#define STAMP(i)
#define CST(i)

template <typename T> DI T lds_ld(u32 a) { return *(LAS T*)(size_t)a; }
template <typename T> DI void lds_st(u32 a, T v) { *(LAS T*)(size_t)a = v; }
DI v2u lds_tr(u32 a) { return __builtin_bit_cast(v2u, __builtin_amdgcn_ds_read_tr16_b64_v4i16((LAS s16x4*)(size_t)a)); }
DI v4u mkrsrc(const void* p, u32 bytes) { u64 a = (u64)p; v4u r; r.x = (u32)a; r.y = (u32)(a >> 32) & 0xffffu; r.z = bytes; r.w = 0x00020000u; return r; }
DI void glds(v4u rsrc, u32 voff, u32 soff, u32 lds) {
    asm volatile("s_mov_b32 m0, %3\n\ts_nop 0\n\tbuffer_load_dwordx4 %0, %1, %2 offen lds" :: "v"(voff), "s"(rsrc), "s"(soff), "s"(lds) : "memory");
}
DI void glds_sc1(v4u rsrc, u32 voff, u32 soff, u32 lds) {
    asm volatile("s_mov_b32 m0, %3\n\ts_nop 0\n\tbuffer_load_dwordx4 %0, %1, %2 offen sc1 lds" :: "v"(voff), "s"(rsrc), "s"(soff), "s"(lds) : "memory");
}
DI void flag_dma(v4u rsrc, u32 lds) {
    asm volatile("s_mov_b32 m0, %2\n\ts_mov_b64 exec, 1\n\tbuffer_load_dword off, %0, %1 lds\n\ts_mov_b64 exec, -1" :: "s"(rsrc), "s"(rsrc.z), "s"(lds) : "memory");
}
DI void zero_dma(v4u rsrc, u32 lds, u64 mask) {
    asm volatile("s_mov_b32 m0, %2\n\ts_mov_b64 exec, %3\n\tbuffer_load_dwordx4 off, %0, %1 lds\n\ts_mov_b64 exec, -1" :: "s"(rsrc), "s"(rsrc.z), "s"(lds), "s"(mask) : "memory");
}
DI void st_sc1(v4u v, v4u rsrc, u32 voff, u32 soff) {
    asm volatile("buffer_store_dwordx4 %0, %1, %2, %3 offen sc1\n\ts_nop 1" :: "v"(v), "v"(voff), "s"(rsrc), "s"(soff) : "memory");
}
DI u32 opq(u32 a) { asm volatile("" : "+v"(a)); return a; }
template <typename T> DI T lds_ldv(u32 a) { return *(volatile LAS T*)(size_t)a; }
DI void spin_zero4(u32 a) {
    int n = 0;
    while (true) { v4u f = lds_ldv<v4u>(a); if (__builtin_amdgcn_readfirstlane(f.x | f.y | f.z | f.w) == 0 || ++n > (SPIN_CAP << 4)) break; }
    asm volatile("" ::: "memory");
}
template <int N> DI void wait_vm() { asm volatile("s_waitcnt vmcnt(%0)" :: "i"(N) : "memory"); }
DI void wait_lgkm0() { asm volatile("s_waitcnt lgkmcnt(0)" ::: "memory"); }
DI u64 mix64(u64 e) { e += 0x9E3779B97F4A7C15ull; e ^= e >> 31; e *= 0xBF58476D1CE4E5B9ull; e ^= e >> 29; return e; }
DI u64 get_tag() { const v2u e = lds_ld<v2u>(L_E); return mix64(((u64)(u32)__builtin_amdgcn_readfirstlane(e.y) << 32) | (u32)__builtin_amdgcn_readfirstlane(e.x)); }
DI void sig(int c) { asm volatile("s_mov_b64 exec, 1\n\tds_add_u32 %0, %1\n\ts_mov_b64 exec, -1" :: "v"(L_CNT + 4 * c), "v"(1u) : "memory"); }
DI void spin(int c, u32 target) {
    int n = 0;
    while ((u32)__builtin_amdgcn_readfirstlane(__hip_atomic_load((LAS u32*)(size_t)(L_CNT + 4 * c), RLX_WG)) < target && ++n < (SPIN_CAP << 4)) {}
    asm volatile("" ::: "memory");
}
DI void poll_flags(const GAS u64* p, u64 tag) {
    int n = 0;
    while (true) {
        u64 f = __hip_atomic_load(p, RLX_AGENT);
        if (__all(f == tag) || ++n > SPIN_CAP) break;
        __builtin_amdgcn_s_sleep(2);
    }
    asm volatile("" ::: "memory");
}
DI float bf_lo(u32 v) { return __uint_as_float(v << 16); }
DI float bf_hi(u32 v) { return __uint_as_float(v & 0xffff0000u); }
DI u32 pk_bf(float a, float b) { f32x2 v = {a, b}; return __builtin_bit_cast(u32, __builtin_convertvector(v, bf16x2)); }
DI float rbf(float a) { return bf_lo(pk_bf(a, a)); }
DI u32 pk_h(f32x2 v) { return __builtin_bit_cast(u32, __builtin_convertvector(v, f16x2)); }
DI f32x16 mfma32(v4u a, v4u b, f32x16 c) { return __builtin_amdgcn_mfma_f32_32x32x16_bf16(__builtin_bit_cast(bf16x8, a), __builtin_bit_cast(bf16x8, b), c, 0, 0, 0); }
DI f32x4 mfma16(v4u a, v4u b, f32x4 c) { return __builtin_amdgcn_mfma_f32_16x16x32_bf16(__builtin_bit_cast(bf16x8, a), __builtin_bit_cast(bf16x8, b), c, 0, 0, 0); }
DI f32x4 mfma16h(v4u a, v4u b, f32x4 c) { return __builtin_amdgcn_mfma_f32_16x16x32_f16(__builtin_bit_cast(f16x8, a), __builtin_bit_cast(f16x8, b), c, 0, 0, 0); }
template <int CTRL> DI float dpp_add(float v) { return v + __builtin_bit_cast(float, __builtin_amdgcn_update_dpp(0, __builtin_bit_cast(int, v), CTRL, 0xf, 0xf, true)); }
template <int CTRL> DI float dpp_max(float v) { return fmaxf(v, __builtin_bit_cast(float, __builtin_amdgcn_update_dpp(0, __builtin_bit_cast(int, v), CTRL, 0xf, 0xf, true))); }
DI float rl(float v, int l) { return __builtin_bit_cast(float, __builtin_amdgcn_readlane(__builtin_bit_cast(int, v), l)); }
DI float wave_sum(float v) {
    v = dpp_add<0xB1>(v); v = dpp_add<0x4E>(v); v = dpp_add<0x141>(v); v = dpp_add<0x140>(v);
    return (rl(v, 0) + rl(v, 16)) + (rl(v, 32) + rl(v, 48));
}
DI f32x2 wave_sum2(f32x2 v) {
    float a = v.x, c = v.y;
    a = dpp_add<0xB1>(a); c = dpp_add<0xB1>(c); a = dpp_add<0x4E>(a); c = dpp_add<0x4E>(c);
    a = dpp_add<0x141>(a); c = dpp_add<0x141>(c); a = dpp_add<0x140>(a); c = dpp_add<0x140>(c);
    return ((f32x2){rl(a, 0), rl(c, 0)} + (f32x2){rl(a, 16), rl(c, 16)}) + ((f32x2){rl(a, 32), rl(c, 32)} + (f32x2){rl(a, 48), rl(c, 48)});
}
DI float wave_max(float v) {
    v = dpp_max<0xB1>(v); v = dpp_max<0x4E>(v); v = dpp_max<0x141>(v); v = dpp_max<0x140>(v);
    return fmaxf(fmaxf(rl(v, 0), rl(v, 16)), fmaxf(rl(v, 32), rl(v, 48)));
}
DI f32x2 cmul(f32x2 a, f32x2 b) { f32x2 t = (f32x2){-a.y, a.y} * (f32x2){b.y, b.x}; return __builtin_elementwise_fma((f32x2){a.x, a.x}, b, t); }
DI f32x16 mfma32h(v4u a, v4u b, f32x16 c) { return __builtin_amdgcn_mfma_f32_32x32x16_f16(__builtin_bit_cast(f16x8, a), __builtin_bit_cast(f16x8, b), c, 0, 0, 0); }
DI u32 pk_h2(float a, float b) { return pk_h((f32x2){a, b}); }
DI f32x2 cexp_pow(float lr, float th, float tl, float e) {
    float a = __builtin_amdgcn_fractf(th * e) + tl * e;
    float m = __builtin_amdgcn_exp2f(lr * e);
    f32x2 r; r.x = m * __builtin_amdgcn_cosf(a); r.y = m * __builtin_amdgcn_sinf(a); return r;
}
DI f32x4 sel4(bool c, f32x4 a, f32x4 b) { return c ? a : b; }
constexpr int w2loc(int n) { return n >= 32 ? w2loc(n - 32) : n < 9 ? 3 + n : n < 13 ? 24 + (n - 9) : n < 16 ? 12 + (n - 13) : n < 20 ? 28 + (n - 16) : n < 29 ? 15 + (n - 20) : n - 29; }

__global__ void __launch_bounds__(512, 2)
fused_kernel(const u16* __restrict__ x, const u16* __restrict__ logA, const u16* __restrict__ Aim, const u16* __restrict__ Cre,
             const u16* __restrict__ Cim, const u16* __restrict__ logdt, const u16* __restrict__ Dskip, const u16* __restrict__ w1,
             const u16* __restrict__ b1, const u16* __restrict__ w2, const u16* __restrict__ b2, u16* __restrict__ out, char* ws) {
    __shared__ __attribute__((aligned(16))) char smem[163840];
    asm volatile("" :: "v"((u32)(size_t)(LAS char*)smem));
    const int tid = threadIdx.x, lane = tid & 63, wave = __builtin_amdgcn_readfirstlane(tid >> 6), w = wave & 3;
    const int b = blockIdx.x, xcd = b & 7, slot = b >> 3;
    const int P = 4 * (xcd >> 1) + (slot >> 3), j = 8 * (xcd & 1) + (slot & 7);
    const int tr = 4 * xcd + (slot >> 3), tc = slot & 7;
    const int u0 = (j * 43) >> 4;
    STAMP(0);
    const u32 t_start = (u32)__builtin_amdgcn_s_memrealtime();
    if (wave == 4 && lane == 0) __hip_atomic_store((GAS u32*)(ws + (40 << 20)) + b, t_start, RLX_AGENT);
    const int h = lane >> 5, l31 = lane & 31, l15 = lane & 15, g = lane >> 4;

    if (wave < 4) {
        const int rot = (2 * (slot >> 3) + (slot & 7)) & 7;
        const v4u rx = mkrsrc(x + (128 * P + 32 * w) * 512, 32 * 1024);
        const v4u rw1 = mkrsrc(w1, 512 * 1376 * 2);
        const v4u rw2 = mkrsrc(w2, 512 * 1376 * 2);
        const u32 vx0 = (lane >> 3) * 1024 + ((lane & 7) ^ g) * 16, vx1 = vx0 ^ 64;
        u32 vw[3];
#pragma unroll
        for (int i = 0; i < 3; i++) { const int p = 64 * i + lane, r = (p * 171) >> 11; vw[i] = r * 2752 + (p - 12 * r) * 16; }
        const u32 vv = (lane >> 3) * 1024 + ((lane & 7) ^ ((g & 1) << 2)) * 16;
        const u32 lw = w * 32768;
        const u32 sw1 = 16 * w * 2752 + u0 * 64, sw2 = 352 * w * 1024 + tc * 128;
        auto ldX = [&](int c, int i) { glds(rx, (i & 1) ? vx1 : vx0, ((c + rot) & 7) * 128 + i * 8192, lw + (24 + (c & 1) * 4 + i) * 1024); };
        auto ldW = [&](int c, int i) { glds(rw1, vw[i], ((c + rot) & 7) * (64 * 2752) + sw1, lw + (3 * c + i) * 1024); };
        auto ldV = [&](int n) { glds(rw2, vv, sw2 + n * 8192, lw + w2loc(n) * 1024); };
        auto flg = [&](int c) { flag_dma(rx, L_FLG + c * 16 + w * 4); };
        if (lane < 8) lds_st<u32>(L_FLG + lane * 16 + w * 4, 1u);
        if (lane < 24) lds_st<v4u>(L_CSP + w * 384 + lane * 16, (v4u){~0u, ~0u, ~0u, ~0u});
#pragma unroll
        for (int u = 0; u < 3; u++) ldW(0, u);
#pragma unroll
        for (int i = 0; i < 4; i++) ldX(0, i);
        wait_lgkm0();
        flg(0);
#pragma unroll
        for (int u = 0; u < 3; u++) ldW(1, u);
#pragma unroll
        for (int i = 0; i < 4; i++) ldX(1, i);
        flg(1);
#pragma unroll
        for (int u = 0; u < 6; u++) ldW(2 + u / 3, u % 3);
        __builtin_amdgcn_s_setprio(1);
        u32 aW[4], aX[4];
#pragma unroll
        for (int s = 0; s < 4; s++) {
            const int ks = (s + w) & 3;
            aW[s] = opq(ks * 32768 + (8 * h + (l15 >> 2)) * 192 + (g & 1) * 32 + (lane & 3) * 8);
            aX[s] = opq(lw + 24576 + l31 * 128 + (((2 * ks + h) ^ ((l31 >> 1) & 7)) * 16));
        }
        const u32 aF = opq(L_FLG);
        f32x16 acc[3]; f32x4 cs = {0.f, 0.f, 0.f, 0.f};
        v4u sel[3];
#pragma unroll
        for (int u = 0; u < 3; u++) { for (int i = 0; i < 16; i++) acc[u][i] = 0.f; const u32 sv = (l15 == 2 * u + (g & 1)) ? 0x3F803F80u : 0u; sel[u] = (v4u){sv, sv, sv, sv}; }
        __builtin_amdgcn_s_barrier();
        asm volatile("" ::: "memory");
        v4u fa[3][3], fb[2][4];
        auto rdW = [&](int n, int u) { const u32 a = aW[n & 3] + (n >> 2) * 3072 + u * 64; v2u lo = lds_tr(a), hi = lds_tr(a + 768); fa[n % 3][u] = (v4u){lo.x, lo.y, hi.x, hi.y}; };
        auto rdX = [&](int c, int s) { fb[c & 1][s] = lds_ld<v4u>(aX[s] + (c & 1) * 4096); };
        spin_zero4(aF);
        STAMP(1);
#pragma unroll
        for (int s = 0; s < 4; s++) rdX(0, s);
#pragma unroll
        for (int u = 0; u < 6; u++) rdW(u / 3, u % 3);
        asm volatile("" : "+v"(fb[0][0]), "+v"(fb[0][1]), "+v"(fb[0][2]), "+v"(fb[0][3]));
#pragma unroll
        for (int i = 0; i < 4; i++) ldX(2, i);
        flg(2);
        v4u fl;
#pragma unroll
        for (int n = 0; n < 32; n++) {
            const int c = n >> 2, s = n & 3;
            auto req = [&](int k) {
                if (s == 3) {
                    if (c < 5) { if (k < 2) { ldX(c + 3, 2 * k); ldX(c + 3, 2 * k + 1); } else flg(c + 3); }
                    else if (c < 7 && k < 2 && !NOV) { ldV(7 * c - 26 + 2 * k); ldV(7 * c - 25 + 2 * k); }
                } else if (k == 1) {
                    if (c < 4) ldW(c + 4, s); else if (!NOV) ldV(c < 7 ? 3 * (c - 4) + s : 13 + s);
                }
            };
            SB0();
            if (s == 2 && c < 7 && !NOLD) {
                if (__builtin_expect(__builtin_amdgcn_readfirstlane(fl.x | fl.y | fl.z | fl.w) != 0, 0)) spin_zero4(aF + (c + 1) * 16);
                asm volatile("" ::: "memory");
            }
            if (s == 3 && c < 7) asm volatile("" : "+v"(fb[(c + 1) & 1][0]), "+v"(fb[(c + 1) & 1][1]), "+v"(fb[(c + 1) & 1][2]), "+v"(fb[(c + 1) & 1][3]));
#pragma unroll
            for (int u = 0; u < 3; u++) {
                acc[u] = mfma32(fa[n % 3][u], fb[c & 1][s], acc[u]);
                if (s == 0 && !NOCS) cs = mfma16(fa[n % 3][u], sel[u], cs);
                SB0();
                if (s == 2 && c < 7 && u < 2) { rdX(c + 1, 2 * u); rdX(c + 1, 2 * u + 1); }
                if (n + 2 < 32) rdW(n + 2, u);
                if (s == 1 && u == 0 && c < 7) fl = lds_ldv<v4u>(aF + (c + 1) * 16);
                if (n == 29 && u == 2 && l15 < 6) lds_st<f32x4>(L_CSP + w * 384 + l15 * 64 + g * 16, cs);
                if (!NOLD) req(u);
                SB0();
            }
        }
        STAMP(2); CST(1);
        const u32 aV0 = opq(lw + (4 * h + (l15 >> 2)) * 128 + ((l15 >> 3) & 1) * 64 + (g & 1) * 32 + (lane & 3) * 8), aV1 = opq(aV0 ^ 64);
        auto rdV = [&](int s, int dt) { v2u lo = lds_tr((dt ? aV1 : aV0) + w2loc(2 * s) * 1024), hi = lds_tr((dt ? aV1 : aV0) + w2loc(2 * s + 1) * 1024); return (v4u){lo.x, lo.y, hi.x, hi.y}; };
        v4u park[6][2];
        {
            float cc[3];
            {
                int n = 0;
                const u32 ca = opq(L_CSP + l31 * 4);
                while (true) {
                    u32 all = lds_ldv<u32>(L_SF) == 0 ? 0u : ~0u, bad = 0;
#pragma unroll
                    for (int u = 0; u < 3; u++) {
                        const u32 p0 = lds_ldv<u32>(ca + u * 128), p1 = lds_ldv<u32>(ca + u * 128 + 384), p2 = lds_ldv<u32>(ca + u * 128 + 768), p3 = lds_ldv<u32>(ca + u * 128 + 1152);
                        bad |= (p0 == ~0u) | (p1 == ~0u) | (p2 == ~0u) | (p3 == ~0u);
                        cc[u] = -((__uint_as_float(p0) + __uint_as_float(p1)) + (__uint_as_float(p2) + __uint_as_float(p3)));
                    }
                    if (!__any(bad | all) || ++n > (SPIN_CAP << 2)) break;
                }
                asm volatile("" ::: "memory");
            }
            STAMP(3);
            const u64 tag = get_tag();
            const u32 stw = lds_ld<u32>(L_STAT + (32 * w + l31) * 4);
            const u32 mm2 = (stw & 0xffffu) * 0x10001u;
            const v4u fbs = {h ? 0u : mm2, h ? 0u : stw, 0u, 0u};
#pragma unroll
            for (int u = 0; u < 3; u++) {
                const float c = cc[u], c0 = rbf(c), c1 = rbf(c - c0), c2 = (c - c0) - c1;
                const u32 bb = lds_ld<u16>(L_B1 + (u * 32 + l31) * 2);
                const v4u fas = {pk_bf(c0, c1), (pk_bf(c2, c2) & 0xffffu) | (bb << 16), 0u, 0u};
                acc[u] = mfma32(fas, fbs, acc[u]);
            }
            const float rs = __builtin_amdgcn_rcpf(bf_hi(stw));
            const v4u rhs = mkrsrc(ws + OFF_H, H_BYTES);
            const __amdgpu_buffer_rsrc_t rh = __builtin_amdgcn_make_buffer_rsrc(ws + OFF_H, 0, H_BYTES, 0x00020000);
            const int nown = (((j + 1) * 43) >> 4) - u0;
            const u32 sh = ((4 * P + w) * 43 + u0) * 2048;
#pragma unroll
            for (int u = 0; u < 3; u++) {
#pragma unroll
                for (int s = 0; s < 2; s++) {
                    v4u o;
#pragma unroll
                    for (int e = 0; e < 4; e++) {
                        f32x2 r = (f32x2){acc[u][8 * s + 2 * e], acc[u][8 * s + 2 * e + 1]} * rs;
                        r.x = fmaxf(r.x, 0.f); r.y = fmaxf(r.y, 0.f); r = r * r;
                        o[e] = pk_bf(r.x, r.y);
                    }
                    st_sc1(o, rhs, lane * 16, (u < 2 || nown == 3) ? sh + u * 2048 + s * 1024 : (u32)H_BYTES);
                    if (2 * u + s < 5) ldV(20 + 2 * u + s);
                    SB0();
                }
            }
#pragma unroll
            for (int n = 25; n < 29; n++) ldV(n);
            wait_vm<22>();
#pragma unroll
            for (int s = 0; s < 6; s++) { park[s][0] = rdV(s, 0); park[s][1] = rdV(s, 1); }
#pragma unroll
            for (int s = 0; s < 6; s++) asm volatile("" : "+v"(park[s][0]), "+v"(park[s][1]));
#pragma unroll
            for (int n = 32; n < 38; n++) ldV(n);
            wait_vm<10>();
            STAMP(4);
            if (lane == 0) __hip_atomic_store((GAS u64*)(ws + OFF_F2) + (4 * P + w) * 16 + j, tag, RLX_AGENT);
#pragma unroll
            for (int n = 38; n < 44; n++) ldV(n);
            spin(C_C, 4);
#pragma unroll
            for (int n = 29; n < 32; n++) ldV(n);
            poll_flags((const GAS u64*)(ws + OFF_F2) + 2 * tr * 16 + l31, tag);
            STAMP(5);
            f32x16 ac2[2][2];
#pragma unroll
            for (int i = 0; i < 16; i++) { ac2[0][0][i] = 0.f; ac2[0][1][i] = 0.f; ac2[1][0][i] = 0.f; ac2[1][1][i] = 0.f; }
            const u32 sh2 = (2 * tr * 43 + 11 * w) * 2048;
            const u32 sh2t = (w == 3) ? (u32)H_BYTES : sh2;
            constexpr int WIN = 8;
            v4u hf[WIN][2];
            auto ldH = [&](int s) {
                const u32 so = (s >= 20 ? sh2t : sh2) + s * 1024;
                hf[s % WIN][0] = __builtin_amdgcn_raw_buffer_load_b128(rh, lane * 16, so, 16);
                hf[s % WIN][1] = __builtin_amdgcn_raw_buffer_load_b128(rh, lane * 16, so + 43 * 2048, 16);
            };
#pragma unroll
            for (int s = 0; s < WIN; s++) ldH(s);
            v4u vb[2][2];
#pragma unroll
            for (int s = 0; s < 22; s++) {
                SB0();
                if (s + 1 >= 6 && s + 1 < 22) { vb[(s + 1) & 1][0] = rdV(s + 1, 0); vb[(s + 1) & 1][1] = rdV(s + 1, 1); }
                const v4u a0 = s < 6 ? park[s < 6 ? s : 0][0] : vb[s & 1][0], a1 = s < 6 ? park[s < 6 ? s : 0][1] : vb[s & 1][1];
                ac2[0][0] = mfma32(a0, hf[s % WIN][0], ac2[0][0]);
                ac2[1][0] = mfma32(a1, hf[s % WIN][0], ac2[1][0]);
                ac2[0][1] = mfma32(a0, hf[s % WIN][1], ac2[0][1]);
                ac2[1][1] = mfma32(a1, hf[s % WIN][1], ac2[1][1]);
                SB0();
                if (s + WIN < 22) ldH(s + WIN);
            }
            SB0();
            STAMP(7); CST(3);
#pragma unroll
            for (int tt = 0; tt < 2; tt++)
#pragma unroll
                for (int dt = 0; dt < 2; dt++)
#pragma unroll
                    for (int q = 0; q < 4; q++)
                        lds_st<f32x4>(lw + (32 * tt + l31) * 272 + (32 * dt + 8 * q + 4 * h) * 4,
                                      (f32x4){ac2[dt][tt][4 * q], ac2[dt][tt][4 * q + 1], ac2[dt][tt][4 * q + 2], ac2[dt][tt][4 * q + 3]});
        }
    } else {
        const int tb = 16 * P + j;
        const int chl = w >> 1, hf = w & 1, ch = 2 * b + chl;
        {
            const v4u rxr = mkrsrc(x + (8 * tb + 2 * w) * 512, 2048);
            glds(rxr, lane * 16, 0, L_KREV + (2 * w) * 1088);
            glds(rxr, lane * 16, 1024, L_KREV + (2 * w + 1) * 1088);
        }
        const int pi = ch * 128 + hf * 64 + lane;
        const u32 pA = logA[pi], pI = Aim[pi], pR = Cre[pi], pC = Cim[pi], pT = logdt[ch], pD = Dskip[ch];
        if (w == 1) {
            const v4u rb1 = mkrsrc(b1, 1376 * 2);
            asm volatile("s_mov_b32 m0, %3\n\ts_nop 0\n\tbuffer_load_dword %0, %1, %2 offen lds" :: "v"(lane * 4), "s"(rb1), "s"(u0 * 64), "s"(L_B1) : "memory");
        }
        if (w == 0 && lane < 28) lds_st<u32>(L_CNT + lane * 4, lane < 16 ? 0u : ~0u);
        if (w == 2) {
            const v4u re = mkrsrc(ws, 16);
            asm volatile("s_mov_b32 m0, %1\n\ts_mov_b64 exec, 1\n\tbuffer_load_dwordx4 off, %0, 0 sc1 lds\n\ts_mov_b64 exec, -1" :: "s"(re), "s"(L_E) : "memory");
        }
        asm volatile("s_waitcnt vmcnt(0) lgkmcnt(0)" ::: "memory");
        __builtin_amdgcn_s_barrier();
        asm volatile("" ::: "memory");
        const u64 tag = get_tag();
        STAMP(1);
        {
            const v4u xr0 = lds_ld<v4u>(L_KREV + (2 * w) * 1088 + lane * 16), xr1 = lds_ld<v4u>(L_KREV + (2 * w + 1) * 1088 + lane * 16);
            f32x2 v[8];
#pragma unroll
            for (int i = 0; i < 4; i++) { v[2 * i] = (f32x2){bf_lo(xr0[i]), bf_lo(xr1[i])}; v[2 * i + 1] = (f32x2){bf_hi(xr0[i]), bf_hi(xr1[i])}; }
            f32x2 sm = ((v[0] + v[1]) + (v[2] + v[3])) + ((v[4] + v[5]) + (v[6] + v[7]));
            const f32x2 m = wave_sum2(sm) * (1.f / 512.f);
            f32x2 q = {0.f, 0.f};
#pragma unroll
            for (int i = 0; i < 8; i++) { f32x2 d = v[i] - m; q = __builtin_elementwise_fma(d, d, q); }
            const f32x2 var = wave_sum2(q) * (1.f / 512.f);
            const u32 vb = pk_bf(var.x, var.y), ve = pk_bf(bf_lo(vb) + 1e-5f, bf_hi(vb) + 1e-5f);
            const float sd0 = __builtin_amdgcn_sqrtf(bf_lo(ve)), sd1 = __builtin_amdgcn_sqrtf(bf_hi(ve));
            if (lane == 0) lds_st<v2u>(L_LNS + w * 8, (v2u){pk_bf(m.x, sd0) & 0x7fffffffu, pk_bf(m.y, sd1) & 0x7fffffffu});
        }
        const v4u rus = mkrsrc(ws + OFF_U, U_BYTES);
        {
            const u32 ta = opq(L_KREV + (l15 >> 2) * 1088 + (128 * w + 16 * g + 4 * (lane & 3)) * 2);
            v2u xt[2][2];
#pragma unroll
            for (int i = 0; i < 2; i++) { xt[i][0] = lds_tr(ta + i * 128); xt[i][1] = lds_tr(ta + i * 128 + 4 * 1088); }
            v4u s0, s1;
            int n = 0;
            while (true) {
                s0 = lds_ldv<v4u>(L_LNS); s1 = lds_ldv<v4u>(L_LNS + 16);
                if ((int)__builtin_amdgcn_readfirstlane(s0.x | s0.z | s1.x | s1.z) >= 0 || ++n > (SPIN_CAP << 4)) break;
            }
            float mb[8], rr[8];
#pragma unroll
            for (int t = 0; t < 8; t++) { const u32 sv = t < 4 ? s0[t & 3] : s1[t & 3]; mb[t] = bf_lo(sv); rr[t] = __builtin_amdgcn_rcpf(bf_hi(sv)); }
#pragma unroll
            for (int i = 0; i < 2; i++) {
                v4u o;
#pragma unroll
                for (int e = 0; e < 4; e++) {
                    const u32 xv = xt[i][e >> 1][e & 1];
                    const u32 d = pk_bf(bf_lo(xv) - mb[2 * e], bf_hi(xv) - mb[2 * e + 1]);
                    o[e] = pk_bf(bf_lo(d) * rr[2 * e], bf_hi(d) * rr[2 * e + 1]);
                }
                st_sc1(o, rus, (128 * w + 64 * i + lane) * U_PITCH, 128 + tb * 16);
                if (tb == 0) st_sc1((v4u){0u, 0u, 0u, 0u}, rus, (128 * w + 64 * i + lane) * U_PITCH, 112);
            }
            if (lane < 2) st_sc1(lane ? s1 : s0, mkrsrc(ws + OFF_ST, 32768), lane * 16, w * 8192 + tb * 32);
        }
        STAMP(2);
        const float dt = rbf(expf(bf_lo(pT)));
        {
            const float zr = -rbf(expf(bf_lo(pA))) * dt, zi = bf_lo(pI) * dt;
            const float lr = zr * 1.44269504088896341f;
            const float th = zi * 0.159154943091895336f, tl = __builtin_fmaf(zi, 0.159154943091895336f, -th) + zi * 6.4206383e-09f;
            const f32x2 w1c = cexp_pow(lr, -th, -tl, 1.f), qc = cexp_pow(lr, -th, -tl, 128.f);
            const f32x2 w8 = cexp_pow(lr, th, tl, 8.f), w32 = cexp_pow(lr, th, tl, 32.f);
            const float cmax = wave_max(fmaxf(fabsf(bf_lo(pR)), fabsf(bf_lo(pC))));
            int ex = (int)(__float_as_uint(cmax) >> 23); ex = ex < 12 ? 12 : (ex > 240 ? 240 : ex);
            const float sdn = __uint_as_float((u32)(262 - ex) << 23), sup = __uint_as_float((u32)(ex - 8) << 23);
            v4u selLo, selHi;
#pragma unroll
            for (int e = 0; e < 4; e++) {
                const u32 one = (h == (g & 1) && ((l15 & 7) >> 1) == e) ? ((l15 & 1) ? 0x3C000000u : 0x00003C00u) : 0u;
                selLo[e] = l15 < 8 ? one : 0u; selHi[e] = l15 < 8 ? 0u : one;
            }
            auto transpose = [&](const f32x2 (&v)[16], v4u (&f)[4]) {
                f32x16 z; for (int i = 0; i < 16; i++) z[i] = 0.f;
                f32x16 dr = mfma32h((v4u){pk_h2(v[0].x, v[1].x), pk_h2(v[2].x, v[3].x), pk_h2(v[4].x, v[5].x), pk_h2(v[6].x, v[7].x)}, selLo, z);
                f32x16 di = mfma32h((v4u){pk_h2(v[0].y, v[1].y), pk_h2(v[2].y, v[3].y), pk_h2(v[4].y, v[5].y), pk_h2(v[6].y, v[7].y)}, selLo, z);
                dr = mfma32h((v4u){pk_h2(v[8].x, v[9].x), pk_h2(v[10].x, v[11].x), pk_h2(v[12].x, v[13].x), pk_h2(v[14].x, v[15].x)}, selHi, dr);
                di = mfma32h((v4u){pk_h2(v[8].y, v[9].y), pk_h2(v[10].y, v[11].y), pk_h2(v[12].y, v[13].y), pk_h2(v[14].y, v[15].y)}, selHi, di);
#pragma unroll
                for (int e = 0; e < 4; e++) {
                    f[0][e] = pk_h2(dr[2 * e], dr[2 * e + 1]); f[1][e] = pk_h2(dr[8 + 2 * e], dr[9 + 2 * e]);
                    f[2][e] = pk_h2(di[2 * e], di[2 * e + 1]); f[3][e] = pk_h2(di[8 + 2 * e], di[9 + 2 * e]);
                }
            };
            v4u fy[4], fx[4];
            f32x2 v[16];
            v[0] = (f32x2){1.f, 0.f}; v[1] = w8; v[2] = cmul(w8, w8); v[3] = cmul(v[2], w8); v[4] = w32;
#pragma unroll
            for (int i = 5; i < 16; i++) v[i] = cmul(v[i - 4], w32);
            transpose(v, fy);
            wait_vm<0>();
            if (lane == 0) __hip_atomic_store((GAS u64*)(ws + OFF_F1) + w * 256 + tb, tag, RLX_AGENT);
            STAMP(3); CST(2);
            const float fin = 2.f * dt * sup;
            f32x4 kt[3];
            auto tile = [&](int t) {
                transpose(v, fx);
                f32x4 d = {0.f, 0.f, 0.f, 0.f};
#pragma unroll
                for (int s = 0; s < 4; s++) d = mfma16h(fy[s], fx[s], d);
                kt[t] = d * fin;
            };
            f32x2 xo[8];
            xo[0] = (f32x2){bf_lo(pR) * sdn, -bf_lo(pC) * sdn};
#pragma unroll
            for (int i = 1; i < 8; i++) xo[i] = cmul(xo[i - 1], w1c);
            const f32x2 q2 = cmul(qc, qc);
            v[0] = xo[0]; v[1] = cmul(xo[0], qc);
#pragma unroll
            for (int s = 2; s < 16; s++) v[s] = cmul(v[s - 2], q2);
            const f32x2 last = cmul(v[14], q2);
            tile(0);
            v[0] = last; v[1] = xo[4]; v[9] = xo[2]; v[13] = xo[6];
#pragma unroll
            for (int s = 2; s < 9; s++) v[s] = cmul(v[s - 1], qc);
#pragma unroll
            for (int s = 10; s < 13; s++) v[s] = cmul(v[s - 1], qc);
            v[14] = cmul(v[13], qc); v[15] = cmul(v[14], qc);
            const f32x2 last2 = cmul(v[15], qc);
            tile(1);
            v[0] = last2;
#pragma unroll
            for (int i = 0; i < 4; i++) { v[1 + 2 * i] = xo[2 * i + 1]; v[2 + 2 * i] = cmul(xo[2 * i + 1], qc); }
#pragma unroll
            for (int i = 9; i < 16; i++) v[i] = (f32x2){0.f, 0.f};
            tile(2);
            CST(3);
            spin_zero4(L_FLG + 3 * 16);
#pragma unroll
            for (int t = 0; t < 3; t++) lds_st<f32x4>(w * 32768 + (16 * t + l15) * 64 + g * 16, kt[t]);
        }
        STAMP(4);
        poll_flags((const GAS u64*)(ws + OFF_F1) + (b >> 6) * 256 + 64 * w + lane, tag);
        STAMP(5);
        if (w == (P >> 2)) {
            const v4u rst = mkrsrc(ws + OFF_ST + (b >> 6) * 8192 + P * 512, 512);
            asm volatile("s_mov_b32 m0, %2\n\ts_mov_b32 exec_hi, 0\n\tbuffer_load_dwordx4 %0, %1, 0 offen sc1 lds\n\ts_mov_b32 exec_hi, -1" :: "v"(lane * 16), "s"(rst), "s"(L_STAT) : "memory");
            flag_dma(rst, L_SF);
        }
        sig(C_A); spin(C_A, 4);
#pragma unroll
        for (int i = 0; i < 4; i++) glds_sc1(rus, lane * 16, (2 * b + (i >> 1)) * U_PITCH + 128 + w * 1024 - (i & 1) * 2, L_UB + (i >> 1) * 8704 + (i & 1) * 4288 + 32 + w * 1024);
        zero_dma(rus, L_UB + chl * 8704 + hf * 4288, 3ull);
        {
            const int m = 64 * hf + lane;
            const u32 t0 = opq((2 * chl) * 32768 + m * 4);
            const bool r1 = m < 32, r2 = m < 64;
            const int bs[8] = {0, r1 ? 528 : 0, r2 ? 400 : 0, r1 ? 560 : 272, 272, r1 ? 592 : 272, r2 ? 464 : 1, r1 ? 624 : 1};
            float k[8];
#pragma unroll
            for (int e = 0; e < 8; e++) k[e] = lds_ld<float>(t0 + bs[e] * 4) + lds_ld<float>(t0 + bs[e] * 4 + 32768);
            if (m == 0) k[0] += bf_lo(pD);
            const u32 kr = L_KREV + chl * 4608;
            const float va = lds_ld<float>(t0 + 512) + lds_ld<float>(t0 + 512 + 32768);
            const float vb = lds_ld<float>(t0 + 516) + lds_ld<float>(t0 + 516 + 32768);
            lds_st<v4u>(kr + (2040 - 8 * m) * 2, (v4u){pk_bf(k[7], k[6]), pk_bf(k[5], k[4]), pk_bf(k[3], k[2]), pk_bf(k[1], k[0])});
            const u32 pa = pk_bf(va, va), pb = pk_bf(vb, vb);
            lds_st<v4u>(kr + (1016 - 8 * m) * 2, (v4u){pb, (m & 1) ? pb : pk_bf(vb, va), pa, pa});
            zero_dma(rus, kr + 4096 + hf * 256, 0xffffull);
        }
        wait_vm<0>();
        sig(C_C); spin(C_C, 4);
        STAMP(6);
        if (b == 0 && w == 0 && lane == 0) __hip_atomic_store((GAS u64*)ws, tag, RLX_AGENT);
        f32x4 cv[8];
        {
#pragma unroll
            for (int t = 0; t < 8; t++) cv[t] = (f32x4){0.f, 0.f, 0.f, 0.f};
            u32 ua = L_UB + chl * 8704 + ((l15 & 1) ? (1 + l15 + 8 * g) * 2 : 4288 + (2 + l15 + 8 * g) * 2) + hf * 256;
            u32 ka = L_KREV + chl * 4608 + (240 - 16 * l15 + 8 * g) * 2 + hf * 256;
            v4u A[8], B[8];
            u32 ub[4];
            auto rdA = [&](int a) { const u32 p = ub[a >> 1] + (a & 1) * 512; return (v4u){lds_ld<u32>(p), lds_ld<u32>(p + 4), lds_ld<u32>(p + 8), lds_ld<u32>(p + 12)}; };
            auto rdB = [&](int d) { return lds_ld<v4u>(ka + (7 - d) * 512); };
#pragma unroll
            for (int i = 0; i < 4; i++) ub[i] = opq(ua + i * 1024);
            ka = opq(ka);
#pragma unroll
            for (int i = 0; i < 8; i++) { A[7 - i] = rdA(7 - i); B[i] = rdB(i); }
CST(0); STAMP(20);
#pragma unroll 1
            for (int r = 0; r < 4; r++) {
                ka = opq(ka + 64);
#pragma unroll
                for (int i = 0; i < 4; i++) ub[i] = opq(ub[i] + 64);
#pragma unroll
                for (int a = 7; a >= 0; a--) {
#pragma unroll
                    for (int d = 0; d + a < 8; d++) {
                        cv[a + d] = mfma16(A[a], B[d], cv[a + d]);
                        if (a == 0) { SB0(); B[d] = rdB(d); SB0(); }
                    }
                    SB0(); A[a] = rdA(a); SB0();
                }
            }
        }
        STAMP(7); CST(1);
        sig(C_D); spin(C_D, 4);
        {
            const u32 mine = hf ? L_UB + chl * 8704 + 4288 : L_KREV + chl * 4608, other = hf ? L_KREV + chl * 4608 : L_UB + chl * 8704 + 4288;
#pragma unroll
            for (int t = 0; t < 4; t++) lds_st<f32x4>(mine + t * 1024 + lane * 16, sel4(hf, cv[t], cv[4 + t]));
            sig(C_E); spin(C_E, 4);
            const __amdgpu_buffer_rsrc_t ra = __builtin_amdgcn_make_buffer_rsrc(ws + OFF_A, 0, 2 << 20, 0x00020000);
            u32 o[8];
#pragma unroll
            for (int t = 0; t < 4; t++) {
                const f32x4 y = sel4(hf, cv[4 + t], cv[t]) + lds_ld<f32x4>(other + t * 1024 + lane * 16);
                o[2 * t] = pk_bf(y[0], y[1]); o[2 * t + 1] = pk_bf(y[2], y[3]);
            }
            const v4u ras = mkrsrc(ws + OFF_A, 2 << 20);
            st_sc1((v4u){o[0], o[1], o[2], o[3]}, ras, lane * 16, ch * 4096 + hf * 2048);
            st_sc1((v4u){o[4], o[5], o[6], o[7]}, ras, lane * 16, ch * 4096 + hf * 2048 + 1024);
            sig(C_X);
            wait_vm<0>();
            STAMP(8);
            if (lane == 0) __hip_atomic_store((GAS u64*)(ws + OFF_F3) + hf * 512 + ch, tag, RLX_AGENT);
            const int ht = tid - 256, rt = ht >> 2, ro = (ht & 3) * 16;
            const GAS v4u* xp = (const GAS v4u*)(x + (64 * tr + rt) * 512 + tc * 64 + ro);
            const GAS v4u* bp = (const GAS v4u*)(b2 + tc * 64 + ro);
            const v4u xa = xp[0], xb = xp[1], ba = bp[0], bb = bp[1];
            poll_flags((const GAS u64*)(ws + OFF_F3) + (tr >> 4) * 512 + tc * 64 + 16 * w + l15, tag);
            STAMP(9);
            v4u av[4];
#pragma unroll
            for (int i = 0; i < 4; i++)
                av[i] = __builtin_amdgcn_raw_buffer_load_b128(ra, (tc * 64 + 16 * w + 4 * i + g) * 4096 + ((l15 >> 2) * 16 + (tr & 3) * 4 + (lane & 3)) * 16, (tr >> 3) * 1024, 16);
            spin(C_X, 4);
            const bool odd = (tr >> 2) & 1;
#pragma unroll
            for (int i = 0; i < 4; i++) {
                const u32 lo = odd ? av[i].z : av[i].x, hi = odd ? av[i].w : av[i].y;
                const u32 aa = L_KREV + (16 * (lane & 3) + 4 * (l15 >> 2)) * 144 + (16 * w + 4 * i + g) * 2;
                lds_st<u16>(aa, (u16)lo); lds_st<u16>(aa + 144, (u16)(lo >> 16)); lds_st<u16>(aa + 288, (u16)hi); lds_st<u16>(aa + 432, (u16)(hi >> 16));
            }
            sig(C_F); spin(C_F, 4);
            const v4u a0 = lds_ld<v4u>(L_KREV + rt * 144 + ro * 2), a1 = lds_ld<v4u>(L_KREV + rt * 144 + ro * 2 + 16);
#pragma unroll
            for (int i = 0; i < 4; i++) {
                const u32 xx0 = i < 2 ? xa[2 * i] : xb[2 * i - 4], xx1 = i < 2 ? xa[2 * i + 1] : xb[2 * i - 3];
                const u32 bb0 = i < 2 ? ba[2 * i] : bb[2 * i - 4], bb1 = i < 2 ? ba[2 * i + 1] : bb[2 * i - 3];
                const u32 aa0 = i < 2 ? a0[2 * i] : a1[2 * i - 4], aa1 = i < 2 ? a0[2 * i + 1] : a1[2 * i - 3];
                lds_st<f32x4>(L_UB + rt * 272 + (ro + 4 * i) * 4,
                              (f32x4){bf_lo(xx0) + bf_lo(aa0) + bf_lo(bb0), bf_hi(xx0) + bf_hi(aa0) + bf_hi(bb0), bf_lo(xx1) + bf_lo(aa1) + bf_lo(bb1), bf_hi(xx1) + bf_hi(aa1) + bf_hi(bb1)});
            }
            STAMP(10);
        }
    }
    wait_lgkm0();
    __builtin_amdgcn_s_barrier();
    asm volatile("" ::: "memory");
    STAMP(11);
    {
        const u32 ra = (tid >> 3) * 272 + (tid & 7) * 32;
        f32x4 s0 = lds_ld<f32x4>(L_UB + ra), s1 = lds_ld<f32x4>(L_UB + ra + 16);
#pragma unroll
        for (int k = 0; k < 4; k++) { s0 += lds_ld<f32x4>(k * 32768 + ra); s1 += lds_ld<f32x4>(k * 32768 + ra + 16); }
        *(GAS v4u*)(out + (64 * tr + (tid >> 3)) * 512 + tc * 64 + (tid & 7) * 8) = (v4u){pk_bf(s0[0], s0[1]), pk_bf(s0[2], s0[3]), pk_bf(s1[0], s1[1]), pk_bf(s1[2], s1[3])};
    }
    if (b == 0 && wave == 4) {
        wait_vm<0>();
        const u32 t_end = (u32)__builtin_amdgcn_s_memrealtime();
        int mn = 1 << 30, mx = -(1 << 30);
        for (int i = 0; i < 4; i++) { int v = (int)(__hip_atomic_load((GAS u32*)(ws + (40 << 20)) + i * 64 + lane, RLX_AGENT) - t_start); mn = v < mn ? v : mn; mx = v > mx ? v : mx; }
        for (int m = 32; m >= 1; m >>= 1) { int a = __shfl_xor(mn, m), c = __shfl_xor(mx, m); mn = a < mn ? a : mn; mx = c > mx ? c : mx; }
        int extra = EXPT == 1 ? 4 * (mx - mn) : EXPT == 2 ? (int)(t_end - t_start) - mx : 0;
        extra = __builtin_amdgcn_readfirstlane(extra);
        if (extra > 20000) extra = 20000;
        while ((int)((u32)__builtin_amdgcn_s_memrealtime() - t_end) < extra + 300) {}
    }
}

extern "C" void kernel_launch(void* const* d_in, const int* in_sizes, int n_in, void* d_out, int out_size, void* d_ws, size_t ws_size,
                              hipStream_t stream) {
    fused_kernel<<<256, 512, 0, stream>>>((const u16*)d_in[0], (const u16*)d_in[1], (const u16*)d_in[2], (const u16*)d_in[3], (const u16*)d_in[4],
                                          (const u16*)d_in[5], (const u16*)d_in[6], (const u16*)d_in[8], (const u16*)d_in[9], (const u16*)d_in[10],
                                          (const u16*)d_in[11], (u16*)d_out, (char*)d_ws);
}
```

```cpp
#include <hip/hip_runtime.h>
#include <stdint.h>
#include <utility>

typedef unsigned short u16;
typedef unsigned int u32;
typedef unsigned long long u64;
typedef u32 v4u __attribute__((ext_vector_type(4)));
typedef u32 v2u __attribute__((ext_vector_type(2)));
typedef float f32x2 __attribute__((ext_vector_type(2)));
typedef float f32x4 __attribute__((ext_vector_type(4)));
typedef float f32x16 __attribute__((ext_vector_type(16)));
typedef __bf16 bf16x8 __attribute__((ext_vector_type(8)));
typedef __bf16 bf16x2 __attribute__((ext_vector_type(2)));
typedef _Float16 f16x8 __attribute__((ext_vector_type(8)));
typedef _Float16 f16x2 __attribute__((ext_vector_type(2)));
typedef short s16x4 __attribute__((ext_vector_type(4)));
#define GAS __attribute__((address_space(1)))
#define LAS __attribute__((address_space(3)))
#define DI __device__ __forceinline__
#define RLX_AGENT __ATOMIC_RELAXED, __HIP_MEMORY_SCOPE_AGENT
#define RLX_WG __ATOMIC_RELAXED, __HIP_MEMORY_SCOPE_WORKGROUP
#define SB0() __builtin_amdgcn_sched_barrier(0)
#define CHST 0
#define NOCS 0
#define NOLD 0
#define PRIO 0
#define PSLEEP 2
#ifndef NV1
#define NV1 5
#endif
#define H_STOP 0
#define ROT 2
#define NOV 0

constexpr int OFF_F1 = 4096, OFF_F2 = 16384, OFF_F3 = 32768, OFF_ST = 65536;
constexpr int OFF_U = 1 << 20, OFF_A = 4 << 20, OFF_H = 8 << 20;
constexpr int H_BYTES = 64 * 43 * 2048;
constexpr int L_C = 131072;
constexpr int L_FLG = L_C, L_CNT = L_C + 128, L_UF = L_C + 160, L_E = L_C + 176, L_LNS = L_C + 192, L_B1 = L_C + 256, L_CSP = L_C + 512, L_STAT = L_C + 2048;
constexpr int L_U = L_C + 3072, L_KB = L_C + 12288;
constexpr int SPIN_CAP = 1 << 16;
enum { C_A = 0, C_K = 2, C_E = 4, C_D = 6 };

#define STAMP(i)
#define CST(i)

#if defined(__HIP_DEVICE_COMPILE__) && __HIP_DEVICE_COMPILE__
#pragma clang attribute push(__attribute__((target("no-packed-fp32-ops"))), apply_to = function)
#endif
DI float u2f(u32 v) { return __builtin_bit_cast(float, v); }
DI u32 f2u(float v) { return __builtin_bit_cast(u32, v); }
DI bool any_(bool c) { return __builtin_amdgcn_ballot_w64(c) != 0; }
DI bool all_(bool c) { return __builtin_amdgcn_ballot_w64(!c) == 0; }
template <typename T> DI T lds_ld(u32 a) { return *(LAS T*)(size_t)a; }
template <typename T> DI void lds_st(u32 a, T v) { *(LAS T*)(size_t)a = v; }
DI v2u lds_tr(u32 a) { return __builtin_bit_cast(v2u, __builtin_amdgcn_ds_read_tr16_b64_v4i16((LAS s16x4*)(size_t)a)); }
DI v4u mkrsrc(const void* p, u32 bytes) { u64 a = (u64)p; v4u r; r.x = (u32)a; r.y = (u32)(a >> 32) & 0xffffu; r.z = bytes; r.w = 0x00020000u; return r; }
DI void glds(v4u rsrc, u32 voff, u32 soff, u32 lds) {
    asm volatile("s_mov_b32 m0, %3\n\ts_nop 0\n\tbuffer_load_dwordx4 %0, %1, %2 offen lds" :: "v"(voff), "s"(rsrc), "s"(soff), "s"(lds) : "memory");
}
template <int LOFF> DI u32 glds_first(v4u rsrc, u32 voff, u32 sa, u32 sb, u32 lbase) {
    u32 so;
    asm volatile("s_add_u32 m0, %5, %6\n\ts_add_u32 %0, %3, %4\n\tbuffer_load_dwordx4 %1, %2, %0 offen lds" : "=&s"(so) : "v"(voff), "s"(rsrc), "s"(sa), "s"(sb), "s"(lbase), "i"(LOFF) : "memory", "scc");
    return so;
}
template <int LOFF, int SOFF> DI void glds_imm(v4u rsrc, u32 voff, u32 sa, u32 lbase) {
    u32 so;
    asm volatile("s_add_u32 m0, %4, %5\n\ts_add_u32 %0, %3, %6\n\tbuffer_load_dwordx4 %1, %2, %0 offen lds" : "=&s"(so) : "v"(voff), "s"(rsrc), "s"(sa), "s"(lbase), "i"(LOFF), "i"(SOFF) : "memory", "scc");
}
template <int OFF> DI void glds_next(v4u rsrc, u32 voff, u32 so) {
    asm volatile("buffer_load_dwordx4 %0, %1, %2 offen offset:%3 lds" :: "v"(voff), "s"(rsrc), "s"(so), "i"(OFF) : "memory");
}
template <int LOFF> DI void flag_imm(v4u rsrc, u32 lbase) {
    asm volatile("s_add_u32 m0, %2, %3\n\ts_mov_b64 exec, 1\n\tbuffer_load_dword off, %0, %1 lds\n\ts_mov_b64 exec, -1" :: "s"(rsrc), "s"(rsrc.z), "s"(lbase), "i"(LOFF) : "memory", "scc");
}
DI void glds_sc1(v4u rsrc, u32 voff, u32 soff, u32 lds) {
    asm volatile("s_mov_b32 m0, %3\n\ts_nop 0\n\tbuffer_load_dwordx4 %0, %1, %2 offen sc1 lds" :: "v"(voff), "s"(rsrc), "s"(soff), "s"(lds) : "memory");
}
DI void flag_dma(v4u rsrc, u32 lds) {
    asm volatile("s_mov_b32 m0, %2\n\ts_mov_b64 exec, 1\n\tbuffer_load_dword off, %0, %1 lds\n\ts_mov_b64 exec, -1" :: "s"(rsrc), "s"(rsrc.z), "s"(lds) : "memory");
}
DI void zero_dma(v4u rsrc, u32 lds, u64 mask) {
    asm volatile("s_mov_b32 m0, %2\n\ts_mov_b64 exec, %3\n\tbuffer_load_dwordx4 off, %0, %1 lds\n\ts_mov_b64 exec, -1" :: "s"(rsrc), "s"(rsrc.z), "s"(lds), "s"(mask) : "memory");
}
DI void st_sc1(v4u v, v4u rsrc, u32 voff, u32 soff) {
    asm volatile("buffer_store_dwordx4 %0, %1, %2, %3 offen sc1\n\ts_nop 1" :: "v"(v), "v"(voff), "s"(rsrc), "s"(soff) : "memory");
}
DI u32 opq(u32 a) { asm volatile("" : "+v"(a)); return a; }
template <typename T> DI T lds_ldv(u32 a) { return *(volatile LAS T*)(size_t)a; }
DI void spin_zero4(u32 a) {
    int n = 0;
    while (any_(lds_ldv<u32>(a) != 0) && ++n < (SPIN_CAP << 4)) {}
    asm volatile("" ::: "memory");
}
template <int N> DI void wait_vm() { asm volatile("s_waitcnt vmcnt(%0)" :: "i"(N) : "memory"); }
DI void wait_lgkm0() { asm volatile("s_waitcnt lgkmcnt(0)" ::: "memory"); }
DI u64 mix64(u64 e) { e += 0x9E3779B97F4A7C15ull; e ^= e >> 31; e *= 0xBF58476D1CE4E5B9ull; e ^= e >> 29; return e; }
DI u64 get_tag() { const v2u e = lds_ld<v2u>(L_E); return mix64(((u64)(u32)__builtin_amdgcn_readfirstlane(e.y) << 32) | (u32)__builtin_amdgcn_readfirstlane(e.x)); }
template <int C> DI void sig(u32 a, u32 one) { asm volatile("s_mov_b64 exec, 1\n\tds_add_u32 %0, %1 offset:%2\n\ts_mov_b64 exec, -1" :: "v"(a), "v"(one), "i"(4 * C) : "memory"); }
DI void spin(u32 a, u32 target) {
    int n = 0;
    while ((u32)__builtin_amdgcn_readfirstlane(lds_ldv<u32>(a)) < target && ++n < (SPIN_CAP << 4)) {}
    asm volatile("" ::: "memory");
}
DI void poll_flags(const GAS u64* p, u64 tag) {
    int n = 0;
    while (true) {
        u64 f = __hip_atomic_load(p, RLX_AGENT);
        if (all_(f == tag) || ++n > SPIN_CAP) break;
        __builtin_amdgcn_s_sleep(PSLEEP);
    }
    asm volatile("" ::: "memory");
}
DI float bf_lo(u32 v) { return u2f(v << 16); }
DI float bf_hi(u32 v) { return u2f(v & 0xffff0000u); }
DI u32 pk_bf(float a, float b) { f32x2 v = {a, b}; return __builtin_bit_cast(u32, __builtin_convertvector(v, bf16x2)); }
DI float rbf(float a) { return bf_lo(pk_bf(a, a)); }
DI u32 pk_h(f32x2 v) { return __builtin_bit_cast(u32, __builtin_convertvector(v, f16x2)); }
DI f32x16 mfma32(v4u a, v4u b, f32x16 c) { return __builtin_amdgcn_mfma_f32_32x32x16_bf16(__builtin_bit_cast(bf16x8, a), __builtin_bit_cast(bf16x8, b), c, 0, 0, 0); }
DI f32x4 mfma16(v4u a, v4u b, f32x4 c) { return __builtin_amdgcn_mfma_f32_16x16x32_bf16(__builtin_bit_cast(bf16x8, a), __builtin_bit_cast(bf16x8, b), c, 0, 0, 0); }
DI f32x4 mfma16h(v4u a, v4u b, f32x4 c) { return __builtin_amdgcn_mfma_f32_16x16x32_f16(__builtin_bit_cast(f16x8, a), __builtin_bit_cast(f16x8, b), c, 0, 0, 0); }
template <int CTRL> DI float dpp_add(float v) { return v + __builtin_bit_cast(float, __builtin_amdgcn_update_dpp(0, __builtin_bit_cast(int, v), CTRL, 0xf, 0xf, true)); }
template <int CTRL> DI u32 dpp_max(u32 v) { const u32 o = (u32)__builtin_amdgcn_update_dpp(0, (int)v, CTRL, 0xf, 0xf, true); return v > o ? v : o; }
DI float rl(float v, int l) { return __builtin_bit_cast(float, __builtin_amdgcn_readlane(__builtin_bit_cast(int, v), l)); }
DI float wave_sum(float v) {
    v = dpp_add<0xB1>(v); v = dpp_add<0x4E>(v); v = dpp_add<0x141>(v); v = dpp_add<0x140>(v);
    return (rl(v, 0) + rl(v, 16)) + (rl(v, 32) + rl(v, 48));
}
DI f32x2 wave_sum2(f32x2 v) {
    float a = v.x, c = v.y;
    a = dpp_add<0xB1>(a); c = dpp_add<0xB1>(c); a = dpp_add<0x4E>(a); c = dpp_add<0x4E>(c);
    a = dpp_add<0x141>(a); c = dpp_add<0x141>(c); a = dpp_add<0x140>(a); c = dpp_add<0x140>(c);
    return ((f32x2){rl(a, 0), rl(c, 0)} + (f32x2){rl(a, 16), rl(c, 16)}) + ((f32x2){rl(a, 32), rl(c, 32)} + (f32x2){rl(a, 48), rl(c, 48)});
}
DI u32 wave_max(u32 v) {
    v = dpp_max<0xB1>(v); v = dpp_max<0x4E>(v); v = dpp_max<0x141>(v); v = dpp_max<0x140>(v);
    const u32 a = __builtin_amdgcn_readlane(v, 0), c = __builtin_amdgcn_readlane(v, 16), d = __builtin_amdgcn_readlane(v, 32), e = __builtin_amdgcn_readlane(v, 48);
    const u32 p = a > c ? a : c, q = d > e ? d : e;
    return p > q ? p : q;
}
struct cmult { f32x2 b, n; };
DI cmult mkc(f32x2 b) { cmult c; c.b = b; c.n = (f32x2){-b.y, b.x}; return c; }
DI f32x2 cmul(f32x2 a, const cmult& c) { return __builtin_elementwise_fma((f32x2){a.x, a.x}, c.b, (f32x2){a.y, a.y} * c.n); }
DI f32x16 mfma32h(v4u a, v4u b, f32x16 c) { return __builtin_amdgcn_mfma_f32_32x32x16_f16(__builtin_bit_cast(f16x8, a), __builtin_bit_cast(f16x8, b), c, 0, 0, 0); }
DI u32 pk_h2(float a, float b) { return pk_h((f32x2){a, b}); }
DI f32x2 cexp_pow(float lr, float th, float tl, float e) {
    float a = __builtin_amdgcn_fractf(th * e) + tl * e;
    float m = __builtin_amdgcn_exp2f(lr * e);
    f32x2 r; r.x = m * __builtin_amdgcn_cosf(a); r.y = m * __builtin_amdgcn_sinf(a); return r;
}
DI f32x4 sel4(bool c, f32x4 a, f32x4 b) { return c ? a : b; }
constexpr int w2loc(int n) { return n >= 28 ? w2loc(n - 28) : n < 6 ? 4 + n : n < 10 ? 18 + n : n < 13 ? n : n < 17 ? 15 + n : n - 4; }
template <class F, int... I> DI void sfor_(F&& f, std::integer_sequence<int, I...>) { (f(std::integral_constant<int, I>{}), ...); }
template <int N, class F> DI void sfor(F&& f) { sfor_(f, std::make_integer_sequence<int, N>{}); }
template <int I> using IC = std::integral_constant<int, I>;
template <typename T> DI void pin(T& v) { asm volatile("" : "+v"(v)); }

__global__ void __launch_bounds__(512, 2)
fused_kernel(const u16* __restrict__ x, const u16* __restrict__ logA, const u16* __restrict__ Aim, const u16* __restrict__ Cre,
             const u16* __restrict__ Cim, const u16* __restrict__ logdt, const u16* __restrict__ Dskip, const u16* __restrict__ w1,
             const u16* __restrict__ b1, const u16* __restrict__ w2, const u16* __restrict__ b2, u16* __restrict__ out, char* ws) {
    __shared__ __attribute__((aligned(16))) char smem[163840];
    asm volatile("" :: "v"((u32)(size_t)(LAS char*)smem));
    const int tid = threadIdx.x, lane = tid & 63, wave = __builtin_amdgcn_readfirstlane(tid >> 6), w = wave & 3;
    const int b = blockIdx.x, xcd = b & 7, slot = b >> 3;
    const int P = 4 * (xcd >> 1) + (slot >> 3), j = 8 * (xcd & 1) + (slot & 7);
    const int tr = 4 * xcd + (slot >> 3), tc = slot & 7;
    const int u0 = (j * 43) >> 4;
    STAMP(0);
    const int h = lane >> 5, l31 = lane & 31, l15 = lane & 15, g = lane >> 4;
    const u32 l16 = lane * 16;
    v4u fin_x, fin_b;
    const int fr = tid >> 3;
    const u32 fin_off = (512 * (tr >> 3) + 256 * ((fr >> 2) & 1) + 128 * ((tr >> 2) & 1) + 16 * (fr >> 3) + 4 * (tr & 3) + (fr & 3)) * 512 + tc * 64 + (tid & 7) * 8;

    asm volatile("" :: "s"(x), "s"(logA), "s"(Aim), "s"(Cre), "s"(Cim), "s"(logdt), "s"(Dskip), "s"(w1), "s"(b1), "s"(w2), "s"(b2), "s"(out), "s"(ws));
    if (wave < 4) {
        const int rot = ROT == 0 ? 0 : ROT == 1 ? (((slot >> 3) + slot) & 1) * 4 : ROT == 2 ? (((slot >> 3) + slot) & 3) * 2 : (2 * (slot >> 3) + (slot & 7)) & 7;
        const v4u rx = mkrsrc(x, 2 << 20);
        const u32 sx = (512 * (P >> 2) + 128 * ((P >> 1) & 1) + 64 * (w & 1) + 8 * (P & 1) + 4 * (w >> 1)) * 1024;
        const v4u rw1 = mkrsrc(w1, 512 * 1376 * 2);
        const v4u rw2 = mkrsrc(w2, 512 * 1376 * 2);
        u32 vx[4], vw[3];
#pragma unroll
        for (int i = 0; i < 4; i++) vx[i] = (((h * 256 + ((lane >> 3) & 3)) * 1024 + ((lane & 7) ^ g) * 16) ^ ((i & 1) * 64)) + i * (16384 - 1024);
#pragma unroll
        for (int i = 0; i < 3; i++) { const int p = 64 * i + lane, r = (p * 171) >> 11; vw[i] = r * 2752 + (p - 12 * r) * 16 - i * 1024; }
        const u32 lw = w * 32768, w4 = w * 4;
        const u32 sw1 = 16 * w * 2752 + u0 * 64, sw2 = 352 * w * 1024 + tc * 128;
        u32 sox, sow;
        auto ldX = [&](auto C_, auto I_) { constexpr int c = decltype(C_)::value, i = decltype(I_)::value;
            if constexpr (i == 0) sox = glds_first<(24 + (c & 1) * 4) * 1024>(rx, vx[0], sx, ((c + rot) & 7) * 128, lw); else glds_next<i * 1024>(rx, vx[i], sox); };
        auto ldW = [&](auto C_, auto I_) { constexpr int c = decltype(C_)::value, i = decltype(I_)::value;
            if constexpr (i == 0) sow = glds_first<3 * c * 1024>(rw1, vw[0], sw1, ((c + rot) & 7) * (64 * 2752), lw); else glds_next<i * 1024>(rw1, vw[i], sow); };
        auto flg = [&](auto C_) { flag_imm<L_FLG + decltype(C_)::value * 16>(rx, w4); };
        if (lane < 8) lds_st<u32>(L_FLG + l16 + w * 4, 1u);
        if (lane < 24) lds_st<v4u>(L_CSP + w * 384 + l16, (v4u){~0u, ~0u, ~0u, ~0u});
        sfor<3>([&](auto I) { ldW(IC<0>{}, I); });
        sfor<4>([&](auto I) { ldX(IC<0>{}, I); });
        wait_lgkm0();
        flg(IC<0>{});
        __builtin_amdgcn_s_barrier();
        asm volatile("" ::: "memory");
        sfor<3>([&](auto I) { ldW(IC<1>{}, I); });
        sfor<4>([&](auto I) { ldX(IC<1>{}, I); });
        flg(IC<1>{});
        const u32 vv = opq((lane >> 3) * 1024 + ((lane & 7) ^ ((g & 1) << 2)) * 16);
        auto ldV = [&](auto N_) { constexpr int n = decltype(N_)::value; glds_imm<w2loc(n) * 1024, n * 8192>(rw2, vv, sw2, lw); };
        u32 aW[4], aX[4];
#pragma unroll
        for (int s = 0; s < 4; s++) {
            const int ks = (s + w) & 3;
            aW[s] = opq(ks * 32768 + (8 * h + (l15 >> 2)) * 192 + (g & 1) * 32 + (lane & 3) * 8);
            aX[s] = opq(lw + 24576 + l31 * 128 + (((2 * ks + h) ^ ((l31 >> 1) & 7)) * 16));
        }
        const u32 aF = opq(L_FLG + (lane & 3) * 4), aE = opq(L_E);
        const u32 aC = opq(L_CSP + l31 * 4), aCw = opq(L_CSP + w * 384 + l15 * 64 + g * 16), aS = opq(L_STAT + w * 256 + l31 * 8), aB = opq(L_B1 + l31 * 2);
        const u32 vS = opq(((lane >> 1) * 4 + (lane & 1)) * 16);
        const v4u rst = mkrsrc(ws + OFF_ST, 16384);
        const u32 sS = P * 1024 + (w & 1) * 512 + (w >> 1) * 32;
        auto ldS = [&]() { asm volatile("s_mov_b32 m0, %3\n\ts_mov_b64 exec, 0xffff\n\tbuffer_load_dwordx4 %0, %1, %2 offen sc1 lds\n\ts_mov_b64 exec, -1" :: "v"(vS), "s"(rst), "s"(sS), "s"(L_STAT + w * 256) : "memory"); };
        const u32 aV0 = opq(lw + (4 * h + (l15 >> 2)) * 128 + ((l15 >> 3) & 1) * 64 + (g & 1) * 32 + (lane & 3) * 8), aV1 = opq(aV0 ^ 64);
        f32x16 acc[3]; f32x4 cs = {0.f, 0.f, 0.f, 0.f};
        v4u sel[3];
#pragma unroll
        for (int u = 0; u < 3; u++) { for (int i = 0; i < 16; i++) acc[u][i] = 0.f; const u32 sv = (l15 == 2 * u + (g & 1)) ? 0x3F803F80u : 0u; sel[u] = (v4u){sv, sv, sv, sv}; pin(sel[u]); pin(acc[u]); }
        sfor<3>([&](auto I) { ldW(IC<2>{}, I); });
        sfor<3>([&](auto I) { ldW(IC<3>{}, I); });
        v4u fa[3][3], fb[2][4];
        auto rdW = [&](int n, int u) { const u32 a = aW[n & 3] + (n >> 2) * 3072 + u * 64; v2u lo = lds_tr(a), hi = lds_tr(a + 768); fa[n % 3][u] = (v4u){lo.x, lo.y, hi.x, hi.y}; };
        auto rdX = [&](int c, int s) { fb[c & 1][s] = lds_ld<v4u>(aX[s] + (c & 1) * 4096); };
        spin_zero4(aF);
        STAMP(1);
#pragma unroll
        for (int s = 0; s < 4; s++) rdX(0, s);
#pragma unroll
        for (int u = 0; u < 6; u++) rdW(u / 3, u % 3);
        asm volatile("" : "+v"(fb[0][0]), "+v"(fb[0][1]), "+v"(fb[0][2]), "+v"(fb[0][3]));
        sfor<4>([&](auto I) { ldX(IC<2>{}, I); });
        flg(IC<2>{});
        u32 fl;
        u32 cp[12], bb[3]; v2u stg; v4u tg4;
        sfor<32>([&](auto N_) __attribute__((always_inline)) {
            constexpr int n = decltype(N_)::value, c = n >> 2, s = n & 3;
            auto req = [&](auto U_) {
                constexpr int q = 3 * s + decltype(U_)::value;
                if constexpr (q >= 9) {
                    if constexpr (c < 5) ldX(IC<c + 3>{}, IC<q - 9>{}); else if constexpr (c < 7) ldV(IC<7 * c - 29 + q - 9>{});
                } else if constexpr (q == 0) {
                    if constexpr (c > 0 && c < 6) ldX(IC<c + 2>{}, IC<3>{}); else if constexpr (c >= 6) ldV(IC<7 * c - 33>{});
                } else if constexpr (q == 1) {
                    if constexpr (c > 0 && c < 6) flg(IC<c + 2>{}); else if constexpr (c == 6) ldS();
                } else if constexpr (q == 3 || q == 5 || q == 8) {
                    constexpr int i = q == 3 ? 0 : q == 5 ? 1 : 2;
                    if constexpr (c < 4) ldW(IC<c + 4>{}, IC<i>{}); else if constexpr (c < 6) ldV(IC<3 * (c - 4) + i>{}); else if constexpr (c == 6 || i < 2) ldV(IC<7 * c - 32 + i>{});
                }
            };
            SB0();
            if (s == 2 && c < 7) {
                if (__builtin_expect(any_(fl != 0), 0)) spin_zero4(aF + (c + 1) * 16);
                asm volatile("" ::: "memory");
            }
            if (s == 3 && c < 7) asm volatile("" : "+v"(fb[(c + 1) & 1][0]), "+v"(fb[(c + 1) & 1][1]), "+v"(fb[(c + 1) & 1][2]), "+v"(fb[(c + 1) & 1][3]));
            sfor<3>([&](auto U_) __attribute__((always_inline)) {
                constexpr int u = decltype(U_)::value;
                acc[u] = mfma32(fa[n % 3][u], fb[c & 1][s], acc[u]);
                if (s == 0) cs = mfma16(fa[n % 3][u], sel[u], cs);
                SB0();
                if (s == 2 && c < 7 && u < 2) { rdX(c + 1, 2 * u); rdX(c + 1, 2 * u + 1); }
                if (n + 2 < 32) rdW(n + 2, u);
                if (s == 1 && u == 1 && c < 7) fl = lds_ldv<u32>(aF + (c + 1) * 16);
                if (n == 29 && u == 2 && l15 < 6) lds_st<f32x4>(aCw, cs);
                if (n == 30) { if (u == 0) tg4 = lds_ldv<v4u>(aE); bb[u] = lds_ldv<u16>(aB + u * 64); }
                if (n == 31) {
                    if (u == 0) { wait_vm<9>(); stg = lds_ldv<v2u>(aS); }
#pragma unroll
                    for (int k = 0; k < 4; k++) cp[4 * u + k] = lds_ldv<u32>(aC + u * 128 + k * 384);
                }
                req(U_);
                SB0();
            });
        });
        STAMP(2); CST(1);
        {
            int n = 0;
            while (__builtin_expect(__builtin_amdgcn_readfirstlane(tg4.z) != 0, 0) && ++n < (SPIN_CAP << 4)) {
                tg4 = lds_ldv<v4u>(aE);
#pragma unroll
                for (int u = 0; u < 3; u++) bb[u] = lds_ldv<u16>(aB + u * 64);
            }
            const u32 want = (lane & 1) ? tg4.y : tg4.x;
            n = 0;
            while (true) {
                bool bad = false;
#pragma unroll
                for (int k = 0; k < 12; k++) bad |= cp[k] == ~0u;
                if (__builtin_expect(!any_(bad), 1) || ++n > (SPIN_CAP << 2)) break;
#pragma unroll
                for (int k = 0; k < 12; k++) cp[k] = lds_ldv<u32>(aC + (k >> 2) * 128 + (k & 3) * 384);
            }
            n = 0;
            while (__builtin_expect(any_(stg.y != want), 0) && ++n < SPIN_CAP) { __builtin_amdgcn_s_sleep(2); ldS(); wait_vm<0>(); stg = lds_ldv<v2u>(aS); }
            asm volatile("" ::: "memory");
        }
        STAMP(3);
        const u64 tag = ((u64)tg4.y << 32) | tg4.x;
        auto rdV = [&](int s, int dt) { v2u lo = lds_tr((dt ? aV1 : aV0) + w2loc(2 * s) * 1024), hi = lds_tr((dt ? aV1 : aV0) + w2loc(2 * s + 1) * 1024); return (v4u){lo.x, lo.y, hi.x, hi.y}; };
        constexpr int NPK = 8;
        v4u park[NPK][2];
        {
            const u32 stw = stg.x, mm2 = (stw & 0xffffu) * 0x10001u;
            const v4u fbs = {h ? 0u : mm2, h ? 0u : stw, 0u, 0u};
#pragma unroll
            for (int u = 0; u < 3; u++) {
                const float c = -((u2f(cp[4 * u]) + u2f(cp[4 * u + 1])) + (u2f(cp[4 * u + 2]) + u2f(cp[4 * u + 3])));
                const float c0 = bf_hi(f2u(c)), d = c - c0, c1 = bf_hi(f2u(d));
                const v4u fas = {__builtin_amdgcn_perm(f2u(d), f2u(c), 0x07060302u), pk_bf(d - c1, bf_lo(bb[u])), 0u, 0u};
                acc[u] = mfma32(fas, fbs, acc[u]);
            }
            SB0();
            sfor<4>([&](auto I) { ldV(IC<19 + decltype(I)::value>{}); });
            const float rs = __builtin_amdgcn_rcpf(bf_hi(stw));
            const v4u rhs = mkrsrc(ws + OFF_H, H_BYTES);
            const __amdgpu_buffer_rsrc_t rh = __builtin_amdgcn_make_buffer_rsrc(ws + OFF_H, 0, H_BYTES, 0x00020000);
            const int nown = (((j + 1) * 43) >> 4) - u0;
            const u32 sh = ((4 * P + w) * 43 + u0) * 2048;
            sfor<6>([&](auto K_) __attribute__((always_inline)) {
                constexpr int u = decltype(K_)::value >> 1, s = decltype(K_)::value & 1;
                v4u o;
#pragma unroll
                for (int e = 0; e < 4; e++) {
                    const float r0 = fmaxf(acc[u][8 * s + 2 * e] * rs, 0.f), r1 = fmaxf(acc[u][8 * s + 2 * e + 1] * rs, 0.f);
                    o[e] = pk_bf(r0 * r0, r1 * r1);
                }
                st_sc1(o, rhs, l16, (u < 2 || nown == 3) ? sh + u * 2048 + s * 1024 : (u32)H_BYTES);
                if constexpr (2 * u + s < 5) ldV(IC<23 + 2 * u + s>{});
                SB0();
            });
            STAMP(8);
            wait_vm<18>();
#pragma unroll
            for (int s = 0; s < NPK; s++) { park[s][0] = rdV(s, 0); park[s][1] = rdV(s, 1); }
            sfor<NPK>([&](auto S_) {
                constexpr int s = decltype(S_)::value;
                asm volatile("" : "+v"(park[s][0]), "+v"(park[s][1]));
                if constexpr (s < NV1) { ldV(IC<28 + 2 * s>{}); ldV(IC<29 + 2 * s>{}); }
            });
            STAMP(9);
            wait_vm<2 * NV1>();
            STAMP(4);
            if (lane == 0) __hip_atomic_store((GAS u64*)(ws + OFF_F2) + (4 * P + w) * 16 + j, tag, RLX_AGENT);
            sfor<16 - 2 * NV1>([&](auto I) { ldV(IC<28 + 2 * NV1 + decltype(I)::value>{}); });
            fin_x = *(const GAS v4u*)(x + fin_off); fin_b = *(const GAS v4u*)(b2 + tc * 64 + (tid & 7) * 8);
            f32x16 ac2[2][2];
#pragma unroll
            for (int i = 0; i < 16; i++) { ac2[0][0][i] = 0.f; ac2[0][1][i] = 0.f; ac2[1][0][i] = 0.f; ac2[1][1][i] = 0.f; }
            pin(fin_x); pin(fin_b);
            STAMP(10);
            {
                const int k = (lane >> 1) & 7, jj = 4 * w + (k < 4 ? k : 4);
                poll_flags((const GAS u64*)(ws + OFF_F2) + (2 * tr + (lane & 1)) * 16 + (jj < 15 ? jj : 15), tag);
            }
            STAMP(5);
            const u32 sh2 = (2 * tr * 43 + 11 * w) * 2048;
            const u32 sh2t = (w == 3) ? (u32)H_BYTES : sh2;
            constexpr int WIN = 8;
            v4u hf[WIN][2];
            auto ldH = [&](int s) {
                const u32 so = (s >= 20 ? sh2t : sh2) + s * 1024;
                hf[s % WIN][0] = __builtin_amdgcn_raw_buffer_load_b128(rh, l16, so, 16);
                hf[s % WIN][1] = __builtin_amdgcn_raw_buffer_load_b128(rh, l16, so + 43 * 2048, 16);
            };
#pragma unroll
            for (int s = 0; s < WIN; s++) ldH(s);
            v4u vb[2][2];
#pragma unroll
            for (int s = 0; s < 22; s++) {
                SB0();
                if (s + 1 >= NPK && s + 1 < 22) { vb[(s + 1) & 1][0] = rdV(s + 1, 0); vb[(s + 1) & 1][1] = rdV(s + 1, 1); }
                const v4u a0 = s < NPK ? park[s < NPK ? s : 0][0] : vb[s & 1][0], a1 = s < NPK ? park[s < NPK ? s : 0][1] : vb[s & 1][1];
                ac2[0][0] = mfma32(a0, hf[s % WIN][0], ac2[0][0]);
                ac2[1][0] = mfma32(a1, hf[s % WIN][0], ac2[1][0]);
                ac2[0][1] = mfma32(a0, hf[s % WIN][1], ac2[0][1]);
                ac2[1][1] = mfma32(a1, hf[s % WIN][1], ac2[1][1]);
                SB0();
                if (s + WIN < 22) ldH(s + WIN);
            }
            SB0();
            STAMP(7); CST(3);
            const u32 da = opq(lw + 4096 + l31 * 272 + h * 16);
#pragma unroll
            for (int tt = 0; tt < 2; tt++)
#pragma unroll
                for (int dt = 0; dt < 2; dt++)
#pragma unroll
                    for (int q = 0; q < 4; q++)
                        lds_st<f32x4>(da + 32 * tt * 272 + (32 * dt + 8 * q) * 4,
                                      (f32x4){ac2[dt][tt][4 * q], ac2[dt][tt][4 * q + 1], ac2[dt][tt][4 * q + 2], ac2[dt][tt][4 * q + 3]});
        }
    } else {
        const int tb = 64 * (P >> 2) + 32 * (j & 1) + 16 * ((P >> 1) & 1) + 2 * (j >> 1) + (P & 1);
        const int cp = 32 * xcd + slot, chl = w >> 1, hf = w & 1, ch = 2 * cp + chl;
        {
            const v4u rxr = mkrsrc(x + (8 * tb + 2 * w) * 512, 2048);
            glds(rxr, l16, 0, L_KB + (2 * w) * 1088);
            glds(rxr, l16, 1024, L_KB + (2 * w + 1) * 1088);
        }
        if (w == 0) {
            const v4u rb1 = mkrsrc(b1, 1376 * 2);
            asm volatile("s_mov_b32 m0, %3\n\ts_nop 0\n\tbuffer_load_dword %0, %1, %2 offen lds" :: "v"(lane * 4), "s"(rb1), "s"(u0 * 64), "s"(L_B1) : "memory");
        }
        const int pi = ch * 128 + hf * 64 + lane;
        u32 pA = logA[pi], pI = Aim[pi], pR = Cre[pi], pC = Cim[pi], pT = logdt[ch], pD = Dskip[ch];
        u64 E = __hip_atomic_load((GAS u64*)(ws + opq(0u)), RLX_AGENT);
        if (w == 1 && lane < 32) lds_st<u32>(L_CNT + lane * 4, lane < 8 ? 0u : ~0u);
        if (w == 3) lds_st<v4u>(L_U + l16, (v4u){0u, 0u, 0u, 0u});
        wait_lgkm0();
        __builtin_amdgcn_s_barrier();
        asm volatile("" ::: "memory");
        const u32 one = opq(1u), aCnt = opq(L_CNT + chl * 4), aCtl = opq(L_C);
        const u32 aLns = opq(L_LNS + w * 16 - l16);
        const u32 aRow = opq(L_KB + 2 * w * 1088 + l16), aTr = opq(L_KB + (l15 >> 2) * 1088 + (128 * w + 16 * g + 4 * (lane & 3)) * 2);
        pin(pA); pin(pI); pin(pR); pin(pC); pin(pT); pin(pD);
        pin(E);
        wait_vm<0>();
        STAMP(1);
        const v4u xr0 = lds_ld<v4u>(aRow), xr1 = lds_ld<v4u>(aRow + 1088);
        const u64 tag = mix64(((u64)(u32)__builtin_amdgcn_readfirstlane((u32)(E >> 32)) << 32) | (u32)__builtin_amdgcn_readfirstlane((u32)E));
        {
            f32x2 v[8];
#pragma unroll
            for (int i = 0; i < 4; i++) { v[2 * i] = (f32x2){bf_lo(xr0[i]), bf_lo(xr1[i])}; v[2 * i + 1] = (f32x2){bf_hi(xr0[i]), bf_hi(xr1[i])}; }
            f32x2 sm = ((v[0] + v[1]) + (v[2] + v[3])) + ((v[4] + v[5]) + (v[6] + v[7]));
            const f32x2 mn = wave_sum2(sm) * (1.f / 512.f);
            f32x2 q[4];
#pragma unroll
            for (int i = 0; i < 4; i++) { f32x2 d = v[2 * i] - mn, e = v[2 * i + 1] - mn; q[i] = __builtin_elementwise_fma(d, d, e * e); }
            const f32x2 var = wave_sum2((q[0] + q[1]) + (q[2] + q[3])) * (1.f / 512.f);
            const u32 vb = pk_bf(var.x, var.y), ve = pk_bf(bf_lo(vb) + 1e-5f, bf_hi(vb) + 1e-5f);
            const u32 p0 = pk_bf(mn.x, __builtin_amdgcn_sqrtf(bf_lo(ve))), p1 = pk_bf(mn.y, __builtin_amdgcn_sqrtf(bf_hi(ve)));
            const u32 r0 = f2u(__builtin_amdgcn_rcpf(bf_hi(p0))) & 0x7fffffffu, r1 = f2u(__builtin_amdgcn_rcpf(bf_hi(p1)));
            STAMP(13);
            if (lane < (w == 0 ? 2 : 1)) lds_st<v4u>(aLns, lane ? (v4u){(u32)tag, (u32)(tag >> 32), 0u, 0u} : (v4u){p0 << 16, p1 << 16, r0, r1});
            if (lane == 0) {
                st_sc1((v4u){p0, (u32)tag, p1, (u32)(tag >> 32)}, mkrsrc(ws + OFF_ST, 16384), l16, (P * 64 + j * 4 + w) * 16);
            }
        }
        const v4u rus = mkrsrc(ws + OFF_U, 2 << 20);
        {
            v4u sr[4]; v2u xt[2][2];
            int n = 0;
            while (true) {
#pragma unroll
                for (int i = 0; i < 4; i++) sr[i] = lds_ldv<v4u>(aCtl + (L_LNS - L_C) + i * 16);
                asm volatile("" ::: "memory");
#pragma unroll
                for (int i = 0; i < 2; i++) { xt[i][0] = lds_tr(aTr + i * 128); xt[i][1] = lds_tr(aTr + i * 128 + 4 * 1088); }
                asm volatile("" ::: "memory");
                if ((int)__builtin_amdgcn_readfirstlane((sr[0].z | sr[1].z) | (sr[2].z | sr[3].z)) >= 0 || ++n > (SPIN_CAP << 4)) break;
            }
            STAMP(14);
#pragma unroll
            for (int i = 0; i < 2; i++) {
                v4u o;
#pragma unroll
                for (int e = 0; e < 4; e++) {
                    const u32 xv = xt[i][e >> 1][e & 1];
                    const u32 d = pk_bf(bf_lo(xv) - u2f(sr[e].x), bf_hi(xv) - u2f(sr[e].y));
                    o[e] = pk_bf(bf_lo(d) * u2f(sr[e].z), bf_hi(d) * u2f(sr[e].w));
                }
                st_sc1(o, rus, l16, tb * 8192 + w * 2048 + i * 1024);
            }
        }
        STAMP(2);
        v4u selLo, selHi;
#pragma unroll
        for (int e = 0; e < 4; e++) {
            const u32 o1 = (h == (g & 1) && ((l15 & 7) >> 1) == e) ? ((l15 & 1) ? 0x3C000000u : 0x00003C00u) : 0u;
            selLo[e] = l15 < 8 ? o1 : 0u; selHi[e] = l15 < 8 ? 0u : o1;
        }
        pin(selLo); pin(selHi);
        const int m = 64 * hf + lane;
        const u32 aT = opq(w * 32768 + l15 * 64 + g * 16), t0 = opq((2 * chl) * 32768 + m * 4), kr = opq(L_KB + chl * 8320 + (1016 - 8 * m) * 2);
        u32 tg[6];
        {
            const bool r1 = m < 32, r2 = m < 64;
            tg[0] = opq(t0 + (r1 ? 528 : 0) * 4); tg[1] = opq(t0 + (r2 ? 400 : 0) * 4); tg[2] = opq(t0 + (r1 ? 560 : 272) * 4);
            tg[3] = opq(t0 + (r1 ? 592 : 272) * 4); tg[4] = opq(t0 + (r2 ? 464 : 1) * 4); tg[5] = opq(t0 + (r1 ? 624 : 1) * 4);
        }
        u32 kb[4], ua[4];
#pragma unroll
        for (int i = 0; i < 4; i++) {
            kb[i] = opq(L_KB + chl * 8320 + ((l15 & 1) ? (239 - l15 + 8 * g) * 2 : 4160 + (240 - l15 + 8 * g) * 2) - hf * 256 - 192 + i * 1024);
            const int os = 30 + 2 * l15 + g - 16 * hf - 4 * i;
            ua[i] = opq(L_U + os * 32 + ((chl ^ (os >> 3)) & 1) * 16);
        }
        const u32 aZ = opq(L_C + (lane & 3) * 4);
        const u32 vU = opq((lane >> 1) * 8192 + ((lane ^ g) & 1) * 16), vA = opq((lane >> 3) * 4096 + (lane & 7) * 16);
        f32x4 kt[3];
        {
            const float dt = rbf(expf(bf_lo(pT)));
            const float zr = -rbf(expf(bf_lo(pA))) * dt, zi = bf_lo(pI) * dt;
            const float lr = zr * 1.44269504088896341f;
            const float th = zi * 0.159154943091895336f, tl = __builtin_fmaf(zi, 0.159154943091895336f, -th) + zi * 6.4206383e-09f;
            const cmult w1c = mkc(cexp_pow(lr, -th, -tl, 1.f)), qc = mkc(cexp_pow(lr, -th, -tl, 128.f)), q2 = mkc(cexp_pow(lr, -th, -tl, 256.f));
            const cmult w8 = mkc(cexp_pow(lr, th, tl, 8.f)), w32 = mkc(cexp_pow(lr, th, tl, 32.f));
            const u32 aR = pR & 0x7fffu, aI = pC & 0x7fffu;
            int ex = (int)(wave_max(aR > aI ? aR : aI) >> 7); ex = ex < 12 ? 12 : (ex > 240 ? 240 : ex);
            const float sdn = u2f((u32)(262 - ex) << 23), sup = u2f((u32)(ex - 8) << 23);
            auto transpose = [&](const f32x2 (&v)[16], v4u (&f)[4]) {
                f32x16 z; for (int i = 0; i < 16; i++) z[i] = 0.f;
                f32x16 dr = mfma32h((v4u){pk_h2(v[0].x, v[1].x), pk_h2(v[2].x, v[3].x), pk_h2(v[4].x, v[5].x), pk_h2(v[6].x, v[7].x)}, selLo, z);
                f32x16 di = mfma32h((v4u){pk_h2(v[0].y, v[1].y), pk_h2(v[2].y, v[3].y), pk_h2(v[4].y, v[5].y), pk_h2(v[6].y, v[7].y)}, selLo, z);
                dr = mfma32h((v4u){pk_h2(v[8].x, v[9].x), pk_h2(v[10].x, v[11].x), pk_h2(v[12].x, v[13].x), pk_h2(v[14].x, v[15].x)}, selHi, dr);
                di = mfma32h((v4u){pk_h2(v[8].y, v[9].y), pk_h2(v[10].y, v[11].y), pk_h2(v[12].y, v[13].y), pk_h2(v[14].y, v[15].y)}, selHi, di);
#pragma unroll
                for (int e = 0; e < 4; e++) {
                    f[0][e] = pk_h2(dr[2 * e], dr[2 * e + 1]); f[1][e] = pk_h2(dr[8 + 2 * e], dr[9 + 2 * e]);
                    f[2][e] = pk_h2(di[2 * e], di[2 * e + 1]); f[3][e] = pk_h2(di[8 + 2 * e], di[9 + 2 * e]);
                }
            };
            v4u fy[4], fx[4];
            f32x2 v[16], xo[8];
            v[0] = (f32x2){1.f, 0.f}; v[1] = w8.b; v[2] = cmul(w8.b, w8); v[3] = cmul(v[2], w8); v[4] = w32.b;
            xo[0] = (f32x2){bf_lo(pR) * sdn, -bf_lo(pC) * sdn};
#pragma unroll
            for (int i = 5; i < 16; i++) { v[i] = cmul(v[i - 4], w32); if (i < 12) xo[i - 4] = cmul(xo[i - 5], w1c); }
            pin(v[15]); pin(xo[7]);
            STAMP(16);
            wait_vm<0>();
            if (lane == 0) __hip_atomic_store((GAS u64*)(ws + OFF_F1) + w * 256 + tb, tag, RLX_AGENT);
            STAMP(3); CST(2);
            transpose(v, fy);
            const float fin = 2.f * dt * sup;
            auto tile = [&](int t) {
                transpose(v, fx);
                f32x4 d = {0.f, 0.f, 0.f, 0.f};
#pragma unroll
                for (int s = 0; s < 4; s++) d = mfma16h(fy[s], fx[s], d);
                kt[t] = d * fin;
            };
            v[0] = xo[0]; v[1] = cmul(xo[0], qc);
#pragma unroll
            for (int s = 2; s < 16; s++) v[s] = cmul(v[s - 2], q2);
            const f32x2 last = cmul(v[14], q2);
            tile(0);
            v[0] = last; v[1] = xo[4]; v[9] = xo[2]; v[13] = xo[6];
            v[2] = cmul(v[1], qc); v[10] = cmul(v[9], qc); v[14] = cmul(v[13], qc);
#pragma unroll
            for (int s = 3; s < 9; s++) v[s] = cmul(v[s - 2], q2);
            v[11] = cmul(v[9], q2); v[12] = cmul(v[10], q2); v[15] = cmul(v[13], q2);
            const f32x2 last2 = cmul(v[14], q2);
            tile(1);
            v[0] = last2;
#pragma unroll
            for (int i = 0; i < 4; i++) { v[1 + 2 * i] = xo[2 * i + 1]; v[2 + 2 * i] = cmul(xo[2 * i + 1], qc); }
#pragma unroll
            for (int i = 9; i < 16; i++) v[i] = (f32x2){0.f, 0.f};
            tile(2);
        }
        CST(3); STAMP(17);
        pin(kt[0]); pin(kt[1]); pin(kt[2]);
        const GAS u64* f1p = (const GAS u64*)(ws + OFF_F1) + (xcd >> 1) * 256 + 64 * w + lane;
        u64 f1 = __hip_atomic_load(f1p, RLX_AGENT);
        spin_zero4(aZ + 4 * 16);
#pragma unroll
        for (int t = 0; t < 3; t++) lds_st<f32x4>(aT + t * 1024, kt[t]);
        sig<C_A>(aCtl + (L_CNT - L_C), one);
        STAMP(4);
        if (!all_(f1 == tag)) poll_flags(f1p, tag);
        STAMP(5);
#pragma unroll
        for (int i = 0; i < 2; i++) glds_sc1(rus, vU, (64 * w + 32 * i) * 8192 + cp * 32, L_U + 1024 + w * 2048 + i * 1024);
        flag_dma(rus, L_UF + w * 4);
        spin(aCtl + (L_CNT - L_C) + 4 * C_A, 4);
        {
            auto tv = [&](u32 a) { return lds_ld<float>(a) + lds_ld<float>(a + 32768); };
            float k[9];
            k[0] = tv(t0); k[8] = tv(t0 + 4); k[4] = tv(t0 + 272 * 4);
            k[1] = tv(tg[0]); k[2] = tv(tg[1]); k[3] = tv(tg[2]); k[5] = tv(tg[3]); k[6] = tv(tg[4]); k[7] = tv(tg[5]);
            const float va = tv(t0 + 512), vb = tv(t0 + 516);
            if (m == 0) k[0] += bf_lo(pD);
            const u32 k10 = pk_bf(k[1], k[0]);
            lds_st<v4u>(kr + 2048, (v4u){pk_bf(k[7], k[6]), pk_bf(k[5], k[4]), pk_bf(k[3], k[2]), k10});
            lds_st<v4u>(kr + 2048 + 4160, (v4u){pk_bf(k[8], k[7]), pk_bf(k[6], k[5]), pk_bf(k[4], k[3]), pk_bf(k[2], k[1])});
            const u32 pa = pk_bf(va, va), pb = pk_bf(vb, vb), pba = pk_bf(vb, va);
            lds_st<v4u>(kr, (v4u){pb, (m & 1) ? pb : pba, pa, pa});
            lds_st<v4u>(kr + 4160, (v4u){pb, pb, (m & 1) ? pba : pa, pa});
            if (m < 4) lds_st<v4u>(L_KB + chl * 8320 + 4096 + (m & 1) * 16 + (m < 2 ? 4160 : 0), (v4u){m == 0 ? k10 >> 16 : 0u, 0u, 0u, 0u});
        }
        sig<C_K>(aCnt, one);
        STAMP(18);
        f32x4 cv[8];
        {
#pragma unroll
            for (int t = 0; t < 8; t++) { cv[t] = (f32x4){0.f, 0.f, 0.f, 0.f}; pin(cv[t]); }
            v4u A[8], B[8];
            auto rdA = [&](int r, int a) { const u32 p = kb[(7 - a) >> 1] + ((7 - a) & 1) * 512 + (3 - r) * 64; return (v4u){lds_ld<u32>(p), lds_ld<u32>(p + 4), lds_ld<u32>(p + 8), lds_ld<u32>(p + 12)}; };
            auto rdB = [&](int r, int d) { return lds_ld<v4u>(ua[r] + d * 1024); };
            spin(aCnt + 4 * C_K, 2);
#pragma unroll
            for (int i = 0; i < 8; i++) A[7 - i] = rdA(0, 7 - i);
            STAMP(19);
            spin_zero4(aZ + (L_UF - L_C));
            STAMP(6);
#pragma unroll
            for (int i = 0; i < 8; i++) B[i] = rdB(0, i);
            if (b == 0 && w == 0 && lane == 0) __hip_atomic_store((GAS u64*)ws, tag, RLX_AGENT);
            CST(0);
#pragma unroll
            for (int r = 0; r < 4; r++) {
#pragma unroll
                for (int a = 7; a >= 0; a--) {
#pragma unroll
                    for (int d = 0; d + a < 8; d++) {
                        cv[a + d] = mfma16(A[a], B[d], cv[a + d]);
                        if (a == 0 && r < 3) { SB0(); B[d] = rdB(r + 1, d); SB0(); }
                    }
                    if (r < 3) { SB0(); A[a] = rdA(r + 1, a); SB0(); }
                }
            }
        }
        STAMP(7); CST(1);
        {
            const u32 mine = opq(w * 32768 + l16), other = opq((w ^ 1) * 32768 + l16);
#pragma unroll
            for (int t = 0; t < 4; t++) lds_st<f32x4>(mine + t * 1024, sel4(hf, cv[t], cv[4 + t]));
            sig<C_E>(aCnt, one);
            sig<C_D>(aCtl + (L_CNT - L_C), one);
            spin(aCnt + 4 * C_E, 2);
            u32 o[8];
#pragma unroll
            for (int t = 0; t < 4; t++) {
                const f32x4 y = sel4(hf, cv[4 + t], cv[t]) + lds_ld<f32x4>(other + t * 1024);
                o[2 * t] = pk_bf(y[0], y[1]); o[2 * t + 1] = pk_bf(y[2], y[3]);
            }
            const v4u ras = mkrsrc(ws + OFF_A, 2 << 20);
            st_sc1((v4u){o[0], o[1], o[2], o[3]}, ras, l16, ch * 4096 + hf * 2048);
            st_sc1((v4u){o[4], o[5], o[6], o[7]}, ras, l16, ch * 4096 + hf * 2048 + 1024);
            STAMP(15);
            wait_vm<0>();
            STAMP(8);
            if (lane == 0) __hip_atomic_store((GAS u64*)(ws + OFF_F3) + hf * 512 + ch, tag, RLX_AGENT);
            fin_x = *(const GAS v4u*)(x + fin_off); fin_b = *(const GAS v4u*)(b2 + tc * 64 + (tid & 7) * 8);
            pin(fin_x); pin(fin_b);
            spin(aCtl + (L_CNT - L_C) + 4 * C_D, 4);
            poll_flags((const GAS u64*)(ws + OFF_F3) + (tr >> 4) * 512 + tc * 64 + 16 * w + l15, tag);
            STAMP(9);
#pragma unroll
            for (int i = 0; i < 2; i++)
                glds_sc1(ras, vA, (tc * 64 + 16 * w + 8 * i) * 4096 + (tr >> 3) * 1024 + (tr & 3) * 256 + ((tr >> 2) & 1) * 128, L_U + w * 2048 + i * 1024);
            const u32 ta = opq(L_U + w * 2048 + (l15 >> 2) * 128 + g * 32 + (lane & 3) * 8), aw = opq(L_KB + lane * 144 + w * 32);
            wait_vm<0>();
            const v2u a0 = lds_tr(ta), a1 = lds_tr(ta + 512), a2 = lds_tr(ta + 1024), a3 = lds_tr(ta + 1536);
            lds_st<v4u>(aw, (v4u){a0.x, a0.y, a1.x, a1.y});
            lds_st<v4u>(aw + 16, (v4u){a2.x, a2.y, a3.x, a3.y});
            STAMP(10);
        }
    }
    f32x4 s0 = {bf_lo(fin_x.x) + bf_lo(fin_b.x), bf_hi(fin_x.x) + bf_hi(fin_b.x), bf_lo(fin_x.y) + bf_lo(fin_b.y), bf_hi(fin_x.y) + bf_hi(fin_b.y)};
    f32x4 s1 = {bf_lo(fin_x.z) + bf_lo(fin_b.z), bf_hi(fin_x.z) + bf_hi(fin_b.z), bf_lo(fin_x.w) + bf_lo(fin_b.w), bf_hi(fin_x.w) + bf_hi(fin_b.w)};
    const u32 ra = opq(4096 + fr * 272 + (tid & 7) * 32), rb = opq(ra + 65536), rc = opq(L_KB + fr * 144 + (tid & 7) * 16);
    asm volatile("" : "+v"(s0), "+v"(s1));
    wait_lgkm0();
    __builtin_amdgcn_s_barrier();
    asm volatile("" ::: "memory");
    STAMP(11);
    {
        const v4u av = lds_ld<v4u>(rc);
        s0 += (lds_ld<f32x4>(ra) + lds_ld<f32x4>(ra + 32768)) + (lds_ld<f32x4>(rb) + lds_ld<f32x4>(rb + 32768));
        s1 += (lds_ld<f32x4>(ra + 16) + lds_ld<f32x4>(ra + 32768 + 16)) + (lds_ld<f32x4>(rb + 16) + lds_ld<f32x4>(rb + 32768 + 16));
        s0 += (f32x4){bf_lo(av.x), bf_hi(av.x), bf_lo(av.y), bf_hi(av.y)};
        s1 += (f32x4){bf_lo(av.z), bf_hi(av.z), bf_lo(av.w), bf_hi(av.w)};
        *(GAS v4u*)(out + fin_off) = (v4u){pk_bf(s0[0], s0[1]), pk_bf(s0[2], s0[3]), pk_bf(s1[0], s1[1]), pk_bf(s1[2], s1[3])};
    }
}

#if defined(__HIP_DEVICE_COMPILE__) && __HIP_DEVICE_COMPILE__
#pragma clang attribute pop
#endif

extern "C" void kernel_launch(void* const* d_in, const int* in_sizes, int n_in, void* d_out, int out_size, void* d_ws, size_t ws_size,
                              hipStream_t stream) {
    fused_kernel<<<256, 512, 0, stream>>>((const u16*)d_in[0], (const u16*)d_in[1], (const u16*)d_in[2], (const u16*)d_in[3], (const u16*)d_in[4],
                                          (const u16*)d_in[5], (const u16*)d_in[6], (const u16*)d_in[8], (const u16*)d_in[9], (const u16*)d_in[10],
                                          (const u16*)d_in[11], (u16*)d_out, (char*)d_ws);
}
```

```cpp
#include <hip/hip_runtime.h>
#include <stdint.h>
#include <utility>

typedef unsigned short u16;
typedef unsigned int u32;
typedef unsigned long long u64;
typedef u32 v4u __attribute__((ext_vector_type(4)));
typedef u32 v2u __attribute__((ext_vector_type(2)));
typedef float f32x2 __attribute__((ext_vector_type(2)));
typedef float f32x4 __attribute__((ext_vector_type(4)));
typedef float f32x16 __attribute__((ext_vector_type(16)));
typedef __bf16 bf16x8 __attribute__((ext_vector_type(8)));
typedef __bf16 bf16x2 __attribute__((ext_vector_type(2)));
typedef _Float16 f16x8 __attribute__((ext_vector_type(8)));
typedef _Float16 f16x2 __attribute__((ext_vector_type(2)));
typedef short s16x4 __attribute__((ext_vector_type(4)));
#define GAS __attribute__((address_space(1)))
#define LAS __attribute__((address_space(3)))
#define DI __device__ __forceinline__
#define RLX_AGENT __ATOMIC_RELAXED, __HIP_MEMORY_SCOPE_AGENT
#define RLX_WG __ATOMIC_RELAXED, __HIP_MEMORY_SCOPE_WORKGROUP
#define SB0() __builtin_amdgcn_sched_barrier(0)
#define CHST 0
#define PSLEEP 2
#define ROT 2
#define POL 1
#define PDS 0
#define THR 0
#define HS 0
#define AB 0
#ifndef NV2
#define NV2 8
#endif
#define WING 10
#define HROT 0
#define GP2 0
#define NE 3
#define PRIO 21

#define NOH_OK
#define NWV 12
constexpr int OFF_ST = 16384, OFF_F1 = 128 << 10, OFF_F2 = 256 << 10, OFF_F3 = 512 << 10;
constexpr int FS1 = 8, FS2 = 16, FS3 = 16;
constexpr int OFF_U = 1 << 20, OFF_A = 4 << 20, OFF_H = 8 << 20;
constexpr int H_BYTES = 64 * 43 * 2048;
constexpr int L_C = 131072;
constexpr int L_FLG = L_C, L_UF = L_C + 320, L_E = L_C + 768, L_LNS = L_C + 352, L_CNT = L_C + 416, L_B1 = L_C + 512, L_STAT = L_C + 1024, L_CSP = L_C + 29696;
enum { R_F = 0, R_P = 8, R_P7 = 13, R_PK = 15, R_X = 14, R_V1 = 16, R_V2 = 17, R_V3 = 18 };
constexpr int L_U = L_C + 3072, L_KB = L_C + 12288;
constexpr int SPIN_CAP = 1 << 16;
enum { C_A = 0, C_D = 1, C_K = 2, C_E = 4, C_PK = 8, C_RD = 12 };

#define STAMP(i)
#define CST(i)
#define TIMED(x) x

#if defined(__HIP_DEVICE_COMPILE__) && __HIP_DEVICE_COMPILE__
#pragma clang attribute push(__attribute__((target("no-packed-fp32-ops"))), apply_to = function)
#endif
DI float u2f(u32 v) { return __builtin_bit_cast(float, v); }
DI u32 f2u(float v) { return __builtin_bit_cast(u32, v); }
DI bool any_(bool c) { return __builtin_amdgcn_ballot_w64(c) != 0; }
DI bool all_(bool c) { return __builtin_amdgcn_ballot_w64(!c) == 0; }
template <typename T> DI T lds_ld(u32 a) { return *(LAS T*)(size_t)a; }
template <typename T> DI void lds_st(u32 a, T v) { *(LAS T*)(size_t)a = v; }
DI v2u lds_tr(u32 a) { return __builtin_bit_cast(v2u, __builtin_amdgcn_ds_read_tr16_b64_v4i16((LAS s16x4*)(size_t)a)); }
DI v4u mkrsrc(const void* p, u32 bytes) { u64 a = (u64)p; v4u r; r.x = (u32)a; r.y = (u32)(a >> 32) & 0xffffu; r.z = bytes; r.w = 0x00020000u; return r; }
DI void glds(v4u rsrc, u32 voff, u32 soff, u32 lds) {
    asm volatile("s_mov_b32 m0, %3\n\ts_nop 0\n\tbuffer_load_dwordx4 %0, %1, %2 offen lds" :: "v"(voff), "s"(rsrc), "s"(soff), "s"(lds) : "memory");
}
template <int LOFF> DI u32 glds_first(v4u rsrc, u32 voff, u32 sa, u32 sb, u32 lbase) {
    u32 so;
    asm volatile("s_add_u32 m0, %5, %6\n\ts_add_u32 %0, %3, %4\n\tbuffer_load_dwordx4 %1, %2, %0 offen lds" : "=&s"(so) : "v"(voff), "s"(rsrc), "s"(sa), "s"(sb), "s"(lbase), "i"(LOFF) : "memory", "scc");
    return so;
}
template <int LOFF, int SOFF> DI void glds_imm(v4u rsrc, u32 voff, u32 sa, u32 lbase) {
    u32 so;
    asm volatile("s_add_u32 m0, %4, %5\n\ts_add_u32 %0, %3, %6\n\tbuffer_load_dwordx4 %1, %2, %0 offen lds" : "=&s"(so) : "v"(voff), "s"(rsrc), "s"(sa), "s"(lbase), "i"(LOFF), "i"(SOFF) : "memory", "scc");
}
template <int OFF> DI void glds_next(v4u rsrc, u32 voff, u32 so) {
    asm volatile("buffer_load_dwordx4 %0, %1, %2 offen offset:%3 lds" :: "v"(voff), "s"(rsrc), "s"(so), "i"(OFF) : "memory");
}
template <int LOFF> DI void flag_imm(v4u rsrc, u32 lbase) {
    asm volatile("s_add_u32 m0, %2, %3\n\ts_mov_b64 exec, 1\n\tbuffer_load_dword off, %0, %1 lds\n\ts_mov_b64 exec, -1" :: "s"(rsrc), "s"(rsrc.z), "s"(lbase), "i"(LOFF) : "memory", "scc");
}
DI void glds_sc1(v4u rsrc, u32 voff, u32 soff, u32 lds) {
    asm volatile("s_mov_b32 m0, %3\n\ts_nop 0\n\tbuffer_load_dwordx4 %0, %1, %2 offen sc1 lds" :: "v"(voff), "s"(rsrc), "s"(soff), "s"(lds) : "memory");
}
DI void flag_dma(v4u rsrc, u32 lds) {
    asm volatile("s_mov_b32 m0, %2\n\ts_mov_b64 exec, 1\n\tbuffer_load_dword off, %0, %1 lds\n\ts_mov_b64 exec, -1" :: "s"(rsrc), "s"(rsrc.z), "s"(lds) : "memory");
}
DI void st_sc1(v4u v, v4u rsrc, u32 voff, u32 soff) {
    asm volatile("buffer_store_dwordx4 %0, %1, %2, %3 offen sc1\n\ts_nop 1" :: "v"(v), "v"(voff), "s"(rsrc), "s"(soff) : "memory");
}
DI u32 opq(u32 a) { asm volatile("" : "+v"(a)); return a; }
template <typename T> DI T lds_ldv(u32 a) { return *(volatile LAS T*)(size_t)a; }
template <int SLEEP = 0> DI void spin_zero4(u32 a) {
    int n = 0;
    while (any_(lds_ldv<u32>(a) != 0) && ++n < (SPIN_CAP << 4)) { if (SLEEP) __builtin_amdgcn_s_sleep(1); }
    asm volatile("" ::: "memory");
}
template <int N> DI void wait_vm() { asm volatile("s_waitcnt vmcnt(%0)" :: "i"(N) : "memory"); }
DI void wait_lgkm0() { asm volatile("s_waitcnt lgkmcnt(0)" ::: "memory"); }
DI u64 mix64(u64 e) { e += 0x9E3779B97F4A7C15ull; e ^= e >> 31; e *= 0xBF58476D1CE4E5B9ull; e ^= e >> 29; return e; }
DI u64 get_tag() { const v2u e = lds_ldv<v2u>(L_E); return mix64(((u64)(u32)__builtin_amdgcn_readfirstlane(e.y) << 32) | (u32)__builtin_amdgcn_readfirstlane(e.x)); }
template <int C> DI void sig(u32 a, u32 one) { asm volatile("s_mov_b64 exec, 1\n\tds_add_u32 %0, %1 offset:%2\n\ts_mov_b64 exec, -1" :: "v"(a), "v"(one), "i"(4 * C) : "memory"); }
template <int OFF> DI void clr(u32 a, u32 zero) { asm volatile("s_mov_b64 exec, 1\n\tds_write_b32 %0, %1 offset:%2\n\ts_mov_b64 exec, -1" :: "v"(a), "v"(zero), "i"(OFF) : "memory"); }
DI void spin(u32 a, u32 target) {
    int n = 0;
    while ((u32)__builtin_amdgcn_readfirstlane(lds_ldv<u32>(a)) < target && ++n < (SPIN_CAP << 4)) {}
    asm volatile("" ::: "memory");
}
DI void poll_flags(const GAS u64* p, u64 tag) {
    int n = 0;
    while (true) {
        u64 f = __hip_atomic_load(p, RLX_AGENT);
        if (all_(f == tag) || ++n > SPIN_CAP) break;
        __builtin_amdgcn_s_sleep(PSLEEP);
    }
    asm volatile("" ::: "memory");
}
DI float bf_lo(u32 v) { return u2f(v << 16); }
DI float bf_hi(u32 v) { return u2f(v & 0xffff0000u); }
DI u32 pk_bf(float a, float b) { f32x2 v = {a, b}; return __builtin_bit_cast(u32, __builtin_convertvector(v, bf16x2)); }
typedef short s16x2 __attribute__((ext_vector_type(2)));
DI u32 relu_pk(u32 v) { return __builtin_bit_cast(u32, __builtin_elementwise_max(__builtin_bit_cast(s16x2, v), (s16x2){0, 0})); }
DI u32 umax(u32 a, u32 b) { return a > b ? a : b; }
DI float rbf(float a) { return bf_lo(pk_bf(a, a)); }
DI u32 pk_h(f32x2 v) { return __builtin_bit_cast(u32, __builtin_convertvector(v, f16x2)); }
DI f32x16 mfma32(v4u a, v4u b, f32x16 c) { return __builtin_amdgcn_mfma_f32_32x32x16_bf16(__builtin_bit_cast(bf16x8, a), __builtin_bit_cast(bf16x8, b), c, 0, 0, 0); }
DI f32x4 mfma16(v4u a, v4u b, f32x4 c) { return __builtin_amdgcn_mfma_f32_16x16x32_bf16(__builtin_bit_cast(bf16x8, a), __builtin_bit_cast(bf16x8, b), c, 0, 0, 0); }
DI f32x4 mfma16h(v4u a, v4u b, f32x4 c) { return __builtin_amdgcn_mfma_f32_16x16x32_f16(__builtin_bit_cast(f16x8, a), __builtin_bit_cast(f16x8, b), c, 0, 0, 0); }
template <int CTRL> DI float dpp_add(float v) { return v + __builtin_bit_cast(float, __builtin_amdgcn_update_dpp(0, __builtin_bit_cast(int, v), CTRL, 0xf, 0xf, true)); }
template <int CTRL> DI u32 dpp_max(u32 v) { const u32 o = (u32)__builtin_amdgcn_update_dpp(0, (int)v, CTRL, 0xf, 0xf, true); return v > o ? v : o; }
template <int T> DI float bcast(float v) { return __builtin_bit_cast(float, __builtin_amdgcn_update_dpp(0, __builtin_bit_cast(int, v), 0x150 + T, 0xf, 0xf, true)); }
DI float rl(float v, int l) { return __builtin_bit_cast(float, __builtin_amdgcn_readlane(__builtin_bit_cast(int, v), l)); }
DI f32x2 wave_sum2(f32x2 v) {
    float a = v.x, c = v.y;
    a = dpp_add<0xB1>(a); c = dpp_add<0xB1>(c); a = dpp_add<0x4E>(a); c = dpp_add<0x4E>(c);
    a = dpp_add<0x141>(a); c = dpp_add<0x141>(c); a = dpp_add<0x140>(a); c = dpp_add<0x140>(c);
    return ((f32x2){rl(a, 0), rl(c, 0)} + (f32x2){rl(a, 16), rl(c, 16)}) + ((f32x2){rl(a, 32), rl(c, 32)} + (f32x2){rl(a, 48), rl(c, 48)});
}
DI u32 wave_max(u32 v) {
    v = dpp_max<0xB1>(v); v = dpp_max<0x4E>(v); v = dpp_max<0x141>(v); v = dpp_max<0x140>(v);
    const u32 a = __builtin_amdgcn_readlane(v, 0), c = __builtin_amdgcn_readlane(v, 16), d = __builtin_amdgcn_readlane(v, 32), e = __builtin_amdgcn_readlane(v, 48);
    const u32 p = a > c ? a : c, q = d > e ? d : e;
    return p > q ? p : q;
}
struct cmult { f32x2 b, n; };
DI cmult mkc(f32x2 b) { cmult c; c.b = b; c.n = (f32x2){-b.y, b.x}; return c; }
DI f32x2 cmul(f32x2 a, const cmult& c) { return __builtin_elementwise_fma((f32x2){a.x, a.x}, c.b, (f32x2){a.y, a.y} * c.n); }
DI f32x16 mfma32h(v4u a, v4u b, f32x16 c) { return __builtin_amdgcn_mfma_f32_32x32x16_f16(__builtin_bit_cast(f16x8, a), __builtin_bit_cast(f16x8, b), c, 0, 0, 0); }
DI u32 pk_h2(float a, float b) { return pk_h((f32x2){a, b}); }
DI f32x2 cexp_pow(float lr, float th, float tl, float e) {
    float a = __builtin_amdgcn_fractf(th * e) + tl * e;
    float m = __builtin_amdgcn_exp2f(lr * e);
    f32x2 r; r.x = m * __builtin_amdgcn_cosf(a); r.y = m * __builtin_amdgcn_sinf(a); return r;
}
DI f32x4 sel4(bool c, f32x4 a, f32x4 b) { return c ? a : b; }
constexpr int w2loc(int n) { return n < 16 ? n : n < 28 ? n + 4 : n - 28; }
template <class F, int... I> DI void sfor_(F&& f, std::integer_sequence<int, I...>) { (f(std::integral_constant<int, I>{}), ...); }
template <int N, class F> DI void sfor(F&& f) { sfor_(f, std::make_integer_sequence<int, N>{}); }
template <int I> using IC = std::integral_constant<int, I>;
template <typename T> DI void pin(T& v) { asm volatile("" : "+v"(v)); }

__global__ void __launch_bounds__(768, 3)
fused_kernel(const u16* __restrict__ x, const u16* __restrict__ logA, const u16* __restrict__ Aim, const u16* __restrict__ Cre,
             const u16* __restrict__ Cim, const u16* __restrict__ logdt, const u16* __restrict__ Dskip, const u16* __restrict__ w1,
             const u16* __restrict__ b1, const u16* __restrict__ w2, const u16* __restrict__ b2, u16* __restrict__ out, char* ws) {
    __shared__ __attribute__((aligned(16))) char smem[163840];
    asm volatile("" :: "v"((u32)(size_t)(LAS char*)smem));
    const int tid = threadIdx.x, lane = tid & 63, wave = __builtin_amdgcn_readfirstlane(tid >> 6), w = wave & 3, role = wave >> 2;
    const int b = blockIdx.x, xcd = b & 7, slot = b >> 3;
    const int P = 4 * (xcd >> 1) + (slot >> 3), j = 8 * (xcd & 1) + (slot & 7);
    const int tr = 4 * xcd + (slot >> 3), tc = slot & 7;
    const int u0 = (j * 43) >> 4;
    STAMP(0);
    const int h = lane >> 5, l31 = lane & 31, l15 = lane & 15, g = lane >> 4;
    const u32 l16 = lane * 16, lw = w * 32768, w4 = w * 4;
    const int rho = HROT ? ((slot >> 3) + slot) & 3 : (slot >> 3);
    const int tb = 64 * (P >> 2) + 32 * (j & 1) + 16 * ((P >> 1) & 1) + 2 * (j >> 1) + (P & 1);
    const int cp = 32 * xcd + slot, chl = w >> 1, hf = w & 1, ch = 2 * cp + chl;
    asm volatile("" :: "s"(x), "s"(logA), "s"(Aim), "s"(Cre), "s"(Cim), "s"(logdt), "s"(Dskip), "s"(w1), "s"(b1), "s"(w2), "s"(b2), "s"(out), "s"(ws));
    const v4u rx = mkrsrc(x, 2 << 20);
    const u32 aF = opq(L_FLG + (lane & 3) * 4);
    auto flg = [&](auto R_) { flag_imm<L_FLG + decltype(R_)::value * 16>(rx, w4); };
    u32 aV0, aV1, aFw, aCn, one, f2o;
    auto pre2 = [&]() {
        aV0 = opq(lw + (4 * h + (l15 >> 2)) * 128 + ((l15 >> 3) & 1) * 64 + (g & 1) * 32 + (lane & 3) * 8); aV1 = opq(aV0 ^ 64);
        aFw = opq(L_FLG + w4); aCn = opq(L_CNT + w4); one = opq(1u);
        const int jj = 4 * w + (lane < 4 ? lane : 4);
        f2o = opq(((2 * tr + role) * 16 + (jj < 15 ? jj : 15)) * FS2 * 8);
    };

    auto rdV = [&](int s, int dt) { v2u lo = lds_tr((dt ? aV1 : aV0) + w2loc(2 * s) * 1024), hi = lds_tr((dt ? aV1 : aV0) + w2loc(2 * s + 1) * 1024); return (v4u){lo.x, lo.y, hi.x, hi.y}; };
    auto gemm2 = [&](auto ROLE_, v4u (&stage)[16], u64 f2, u64 tag) __attribute__((always_inline)) {
        constexpr int role = decltype(ROLE_)::value;
        const GAS u64* f2p = (const GAS u64*)(ws + OFF_F2 + f2o);
        __builtin_amdgcn_s_setprio(role ? GP2 : 0);
        v4u park[8][2];
        if (role) {
            SB0();
            spin_zero4<1>(aFw + R_V1 * 16);
            STAMP(9);
            f2 = __hip_atomic_load(f2p, RLX_AGENT);
            SB0();
#pragma unroll
            for (int s = 0; s < 8; s++) { park[s][0] = rdV(s, 0); park[s][1] = rdV(s, 1); }
            clr<R_PK * 16>(aFw, opq(0u));
            SB0();
        }
        STAMP(10);
        if (!all_(f2 == tag)) poll_flags(f2p, tag);
        STAMP(5);
        const __amdgpu_buffer_rsrc_t rh = __builtin_amdgcn_make_buffer_rsrc(ws + OFF_H, 0, H_BYTES, 0x00020000);
        u32 sh2[4], sh2t[4];
#pragma unroll
        for (int k = 0; k < 4; k++) { sh2[k] = ((2 * tr + role) * 43 + 11 * w) * 2048 + (6 * rho - (k + rho >= 4 ? 22 : 0)) * 1024; sh2t[k] = (w == 3 && k + rho == 3) ? (u32)H_BYTES : sh2[k]; }
        constexpr int WIN = WING;
        v4u hq[WIN], vb[3][2];
        auto ldH = [&](int s) { hq[s % WIN] = __builtin_amdgcn_raw_buffer_load_b128(rh, l16, ((s + 2) % 6 >= 4 ? sh2t[(s + 2) / 6] : sh2[(s + 2) / 6]) + s * 1024, 16); };
#pragma unroll
        for (int s = 0; s < WIN; s++) ldH(s);
        const int fr = tid >> 3;
        const u32 fin_off = (512 * (tr >> 3) + 256 * ((fr >> 2) & 1) + 128 * ((tr >> 2) & 1) + 16 * (fr >> 3) + 4 * (tr & 3) + (fr & 3)) * 512 + tc * 64 + (tid & 7) * 8;
        v4u fin_x, fin_b; u32 fin_s, fv;
        f32x16 ac2[2];
#pragma unroll
        for (int i = 0; i < 16; i++) { ac2[0][i] = 0.f; ac2[1][i] = 0.f; }
        if (!role) {
            const u32 aSt = opq(lw + l16), zero = opq(0u);
            SB0();
            spin_zero4(opq(aFw + ((lane & 1) ? R_PK : R_V2) * 16));
            STAMP(9);
#pragma unroll
            for (int s = 0; s < 8; s++) {
                park[s][0] = rdV(s, 0); park[s][1] = rdV(s, 1);
                asm volatile("" ::: "memory");
                lds_st<v4u>(aSt + 2 * s * 1024, stage[2 * s]); lds_st<v4u>(aSt + (2 * s + 1) * 1024, stage[2 * s + 1]);
                SB0();
            }
            clr<R_V3 * 16>(aFw, zero);
            STAMP(13);
        }
#pragma unroll
        for (int s = 0; s < 22; s++) {
            SB0();
            if (role && (s == 4 || s == 10)) fv = lds_ldv<u32>(aFw + (s == 4 ? R_V2 : R_V3) * 16);
            if (role && (s == 6 || s == 12)) { if (__builtin_expect(any_(fv != 0), 0)) spin_zero4(aFw + (s == 6 ? R_V2 : R_V3) * 16); asm volatile("" ::: "memory"); }
            if (s + 2 >= 8 && s + 2 < 22) { vb[(s + 2) % 3][0] = rdV(s + 2, 0); vb[(s + 2) % 3][1] = rdV(s + 2, 1); }
            if (s == 19) sig<C_RD>(aCn, one);
            if (s == 21) fv = lds_ldv<u32>(aCn + 4 * C_RD);
            ac2[0] = mfma32(s < 8 ? park[s & 7][0] : vb[s % 3][0], hq[s % WIN], ac2[0]);
            ac2[1] = mfma32(s < 8 ? park[s & 7][1] : vb[s % 3][1], hq[s % WIN], ac2[1]);
            SB0();
            if (s + WIN < 22) ldH(s + WIN);
            if (s == 14) {
                fin_x = *(const GAS v4u*)(x + fin_off); fin_b = *(const GAS v4u*)(b2 + tc * 64 + (tid & 7) * 8);
                fin_s = __hip_atomic_load((GAS u32*)(ws + OFF_ST + (tr >> 1) * 1024 + (fr >> 2) * 64 + (tr & 1) * 32 + (fr & 3) * 8), RLX_AGENT);
            }
        }
        SB0();
        STAMP(7); CST(3);
        if (__builtin_expect(any_(fv < 2), 0)) spin(aCn + 4 * C_RD, 2);
        asm volatile("" ::: "memory");
        const u32 da = opq(lw + role * 20480 + l31 * 272 + h * 16);
#pragma unroll
        for (int dt = 0; dt < 2; dt++)
#pragma unroll
            for (int q = 0; q < 4; q++)
                lds_st<f32x4>(da + (32 * dt + 8 * q) * 4, (f32x4){ac2[dt][4 * q], ac2[dt][4 * q + 1], ac2[dt][4 * q + 2], ac2[dt][4 * q + 3]});
        f32x4 s0 = {bf_lo(fin_x.x) + bf_lo(fin_b.x), bf_hi(fin_x.x) + bf_hi(fin_b.x), bf_lo(fin_x.y) + bf_lo(fin_b.y), bf_hi(fin_x.y) + bf_hi(fin_b.y)};
        f32x4 s1 = {bf_lo(fin_x.z) + bf_lo(fin_b.z), bf_hi(fin_x.z) + bf_hi(fin_b.z), bf_lo(fin_x.w) + bf_lo(fin_b.w), bf_hi(fin_x.w) + bf_hi(fin_b.w)};
        const u32 ra = opq(role * 20480 + (fr & 31) * 272 + (tid & 7) * 32), rb = opq(ra + 65536), rc = opq(L_KB + fr * 144 + (tid & 7) * 16);
        float rs2 = __builtin_amdgcn_rcpf(bf_hi(fin_s)); rs2 *= rs2;
        asm volatile("" : "+v"(s0), "+v"(s1), "+v"(rs2));
        wait_lgkm0();
        __builtin_amdgcn_s_barrier();
        asm volatile("" ::: "memory");
        STAMP(11);
        const v4u av = lds_ld<v4u>(rc);
        s0 += ((lds_ld<f32x4>(ra) + lds_ld<f32x4>(ra + 32768)) + (lds_ld<f32x4>(rb) + lds_ld<f32x4>(rb + 32768))) * rs2;
        s1 += ((lds_ld<f32x4>(ra + 16) + lds_ld<f32x4>(ra + 32768 + 16)) + (lds_ld<f32x4>(rb + 16) + lds_ld<f32x4>(rb + 32768 + 16))) * rs2;
        s0 += (f32x4){bf_lo(av.x), bf_hi(av.x), bf_lo(av.y), bf_hi(av.y)};
        s1 += (f32x4){bf_lo(av.z), bf_hi(av.z), bf_lo(av.w), bf_hi(av.w)};
        *(GAS v4u*)(out + fin_off) = (v4u){pk_bf(s0[0], s0[1]), pk_bf(s0[2], s0[3]), pk_bf(s1[0], s1[1]), pk_bf(s1[2], s1[3])};
    };

    if (role == 0) {
        v4u stage[16]; u64 f2, tag;
        const int rot = ROT == 0 ? 0 : ROT == 1 ? (((slot >> 3) + slot) & 1) * 4 : ROT == 2 ? (((slot >> 3) + slot) & 3) * 2 : (2 * (slot >> 3) + (slot & 7)) & 7;
        const v4u rw1 = mkrsrc(w1, 512 * 1376 * 2);
        const u32 sx = (512 * (P >> 2) + 128 * ((P >> 1) & 1) + 64 * (w & 1) + 8 * (P & 1) + 4 * (w >> 1)) * 1024;
        const u32 sw1 = 16 * w * 2752 + u0 * 64, sw2 = 352 * w * 1024 + tc * 128;
        u32 vx[4], vw[3];
#pragma unroll
        for (int i = 0; i < 3; i++) { const int p = 64 * i + lane, r = (p * 171) >> 11; vw[i] = r * (2752 - 192) + l16; }
#pragma unroll
        for (int i = 0; i < 4; i++) vx[i] = (((h * 256 + ((lane >> 3) & 3)) * 1024 + ((lane & 7) ^ g) * 16) ^ ((i & 1) * 64)) + i * (16384 - 1024);
        auto ldX = [&](auto C_) { constexpr int c = decltype(C_)::value;
            const u32 so = glds_first<(16 + (c % 4) * 4) * 1024>(rx, vx[0], sx, ((c + rot) & 7) * 128, lw);
            glds_next<1024>(rx, vx[1], so); glds_next<2048>(rx, vx[2], so); glds_next<3072>(rx, vx[3], so); };
        auto ldW = [&](auto C_) { constexpr int c = decltype(C_)::value;
            const u32 so = glds_first<3 * (c % 5) * 1024>(rw1, vw[0], sw1, ((c + rot) & 7) * (64 * 2752), lw);
            glds_next<1024>(rw1, vw[1], so); glds_next<2048>(rw1, vw[2], so); };
        ldW(IC<0>{}); ldX(IC<0>{});
        __builtin_amdgcn_s_barrier();
        asm volatile("" ::: "memory");
        flg(IC<R_F>{});
        const u32 vv = (lane >> 3) * 1024 + ((lane & 7) ^ ((g & 1) << 2)) * 16;
        u32 sv[4];
#pragma unroll
        for (int k = 0; k < 4; k++) sv[k] = sw2 + (12 * rho - (k + rho >= 4 ? 44 : 0)) * 8192;
        const __amdgpu_buffer_rsrc_t rv = __builtin_amdgcn_make_buffer_rsrc((void*)w2, 0, 512 * 1376 * 2, 0x00020000);
        auto ldV = [&](int r) { stage[r] = __builtin_amdgcn_raw_buffer_load_b128(rv, vv, sv[(r + 32) / 12] + (r + 28) * 8192, 0); };
        sfor<7>([&](auto I) {
            constexpr int c = decltype(I)::value + 1;
            if constexpr (THR && c >= 2) wait_vm<THR>();
            if (!(AB & 4)) ldW(IC<c>{});
            if constexpr (c >= 4) {
                SB0();
#pragma unroll
                for (int k = 0; k < NE; k++) ldV(NE * (c - 4) + k);
                SB0();
            }
            if constexpr (c >= 4) TIMED(spin_zero4<1>(aF + (R_P + c - 4) * 16));
            if constexpr (THR && c >= 2) wait_vm<THR>();
            if (!(AB & 4)) ldX(IC<c>{});
            flg(IC<R_F + c>{});
        });
        STAMP(1);
        {
            const u32 vS = ((lane >> 1) * 4 + (lane & 1)) * 16;
            const v4u rst = mkrsrc(ws + OFF_ST, 16384);
            asm volatile("s_mov_b32 m0, %3\n\ts_mov_b64 exec, 0xffff\n\tbuffer_load_dwordx4 %0, %1, %2 offen sc1 lds\n\ts_mov_b64 exec, -1" :: "v"(vS), "s"(rst), "s"(P * 1024 + (w & 1) * 512 + (w >> 1) * 32), "s"(L_STAT + w * 256) : "memory");
        }
        spin_zero4<1>(aF + R_X * 16);
        tag = get_tag();
        const GAS u64* f1p = (const GAS u64*)(ws + OFF_F1) + ((xcd >> 1) * 256 + 64 * w + lane) * FS1;
        const u64 f1 = __hip_atomic_load(f1p, RLX_AGENT);
#pragma unroll
        for (int n = 4 * NE; n < 16; n++) ldV(n);
        SB0();
        if (!all_(f1 == tag)) poll_flags(f1p, tag);
        STAMP(16);
        {
            const v4u rus = mkrsrc(ws + OFF_U, 2 << 20);
            const u32 vU = (lane >> 1) * 8192 + ((lane ^ g) & 1) * 16;
#pragma unroll
            for (int i = 0; i < 2; i++) glds_sc1(rus, vU, (64 * w + 32 * i) * 8192 + cp * 32, L_U + 1024 + w * 2048 + i * 1024);
            flag_dma(rus, L_UF + w * 4);
        }
        STAMP(2);
        pre2();
        spin_zero4<1>(aF + R_P7 * 16);
        STAMP(3);
#pragma unroll
        for (int n = 0; n < 16; n++) pin(stage[n]);
        {
            const u32 aK = L_CNT + 4 * C_K + (lane & 1) * 4;
            int k = 0;
            while (any_(lds_ldv<u32>(aK) < 2) && ++k < (SPIN_CAP << 4)) { __builtin_amdgcn_s_sleep(1); }
        }
        STAMP(14);
        const v4u rw2 = mkrsrc(w2, 512 * 1376 * 2);
        sfor<28>([&](auto I) {
            constexpr int n = decltype(I)::value;
            if constexpr (n == 28 - NV2) asm volatile("global_load_dwordx2 %0, %1, %2 sc1" : "=v"(f2) : "v"(f2o), "s"(ws + OFF_F2) : "memory");
            glds_imm<w2loc(n) * 1024, n * 8192>(rw2, vv, sv[(n + 4) / 12], lw);
            if constexpr (n == 15) flg(IC<R_V1>{});
        });
        flg(IC<R_V2>{});
        asm volatile("s_waitcnt vmcnt(%1)" : "+v"(f2) : "i"(NV2 + 1) : "memory");
        STAMP(8);
        gemm2(IC<0>{}, stage, f2, tag);
    } else if (role == 1) {
        u64 tag;
        __builtin_amdgcn_s_setprio(PRIO / 10);
        {
            const int t = tid - 256;
            const u32 v1 = t < 192 ? ~0u : 0u, v2 = (t >= 26 && t < 30) ? 0u : ~0u;
            lds_st<v4u>((t < 192 ? L_CSP : L_U - 192 * 16) + t * 16, (v4u){v1, v1, v1, v1});
            if (t < 32) lds_st<v4u>(L_C + t * 16, (v4u){v2, v2, v2, v2});
            wait_lgkm0();
            __builtin_amdgcn_s_barrier();
            asm volatile("" ::: "memory");
        }
        u32 aW[4], aX[4];
#pragma unroll
        for (int s = 0; s < 4; s++) {
            const int ks = (s + w) & 3;
            aW[s] = opq(ks * 32768 + (8 * h + (l15 >> 2)) * 192 + (g & 1) * 32 + (lane & 3) * 8);
            aX[s] = opq(lw + 16384 + l31 * 128 + (((2 * ks + h) ^ ((l31 >> 1) & 7)) * 16));
        }
        const u32 aC = opq(L_CSP + l31 * 32 + h * 16), aCw = opq(L_CSP + lane * 32 + w * 8), aS = opq(L_STAT + w * 256 + l31 * 8), aB = opq(L_B1 + lane * 2);
        pre2();
        const u32 zero = opq(0u);
        f32x16 acc[3]; f32x4 cs = {0.f, 0.f, 0.f, 0.f};
        v4u sel[3];
#pragma unroll
        for (int u = 0; u < 3; u++) {
            for (int i = 0; i < 16; i++) acc[u][i] = 0.f;
            const u32 sv = (l15 == (u < 2 ? 8 * u : 1) + 4 * (g & 1)) ? 0xBF80BF80u : 0u; sel[u] = (v4u){sv, sv, sv, sv}; pin(sel[u]);
        }
        v4u fa[3][3], fb[2][4];
        auto rdW = [&](int n, int u) { const u32 a = aW[n & 3] + ((n >> 2) % 5) * 3072 + u * 64; v2u lo = lds_tr(a), hi = lds_tr(a + 768); fa[n % 3][u] = (v4u){lo.x, lo.y, hi.x, hi.y}; };
        auto rdX = [&](int c, int s) { fb[c & 1][s] = lds_ld<v4u>(aX[s] + (c % 4) * 4096); };
        const v4u rhs = mkrsrc(ws + OFF_H, H_BYTES);
        const int nown = (((j + 1) * 43) >> 4) - u0;
        const u32 sh = ((4 * P + w) * 43 + u0) * 2048, sh2 = nown == 3 ? sh + 4096 : (u32)H_BYTES;
        spin_zero4(aF);
        STAMP(1);
        rdX(0, 0);
#pragma unroll
        for (int u = 0; u < 3; u++) rdW(0, u);
        rdX(0, 1);
#pragma unroll
        for (int u = 0; u < 3; u++) rdW(1, u);
        rdX(0, 2); rdX(0, 3);
        u32 fl, fx, bb[2]; v2u ee; v2u stg; v4u fas[3], fbs;
        sfor<32>([&](auto N_) __attribute__((always_inline)) {
            constexpr int n = decltype(N_)::value, c = n >> 2, s = n & 3;
            SB0();
            if constexpr (s == 2 && c < 5) {
                pin(fa[(n + 1) % 3][2]);
                flg(IC<R_P + c>{});
            }
            if (s == 2 && c < 7 && !(AB & 2)) {
                if (__builtin_expect(any_(fl != 0), 0)) TIMED(spin_zero4(aF + (R_F + c + 1) * 16));
                asm volatile("" ::: "memory");
            }
            sfor<3>([&](auto U_) __attribute__((always_inline)) {
                constexpr int u = decltype(U_)::value;
                acc[u] = mfma32(fa[n % 3][u], fb[c & 1][s], acc[u]);
                if (s == 0 && c < 7) cs = mfma16(sel[u], fa[n % 3][u], cs);
                if (n == 27) cs = mfma16(sel[u], fa[28 % 3][u], cs);
                SB0();
                if (s >= 2 && c < 7 && u < 2) rdX(c + 1, 2 * (s - 2) + u);
                if (n + 2 < 32) rdW(n + 2, u);
                if (s == 1 && u == 0 && c < 7) fl = lds_ldv<u32>(aF + (R_F + c + 1) * 16);
                if constexpr (n == 29 && u == 2) clr<R_P7 * 16>(aFw, zero);
                if (n == 25 && u == 2) { bb[0] = lds_ld<u16>(aB); bb[1] = lds_ld<u16>(aB + 128); }
                if (n == 21 && u == 1) fx = lds_ldv<u32>(aF + R_X * 16);
                if (n == 23 && u == 1) { if (__builtin_expect(any_(fx != 0), 0)) spin_zero4(aF + R_X * 16); asm volatile("" ::: "memory"); ee = lds_ldv<v2u>(L_E); }
                if (n == 28 && u == 1) {
                    v2u t[2];
#pragma unroll
                    for (int k = 0; k < 2; k++) {
                        const float d = cs[k] - bf_hi(f2u(cs[k]));
                        t[k] = (v2u){__builtin_amdgcn_perm(f2u(d), f2u(cs[k]), 0x07060302u), pk_bf(d - bf_hi(f2u(d)), bf_lo(bb[k]))};
                    }
                    asm volatile("ds_write2st64_b64 %0, %1, %2 offset1:4" :: "v"(aCw), "v"(t[0]), "v"(t[1]) : "memory");
                }
                if (n == 31 && u == 0) { fas[0] = lds_ldv<v4u>(aC); fas[1] = lds_ldv<v4u>(aC + 1024); fas[2] = lds_ldv<v4u>(aC + 2048); }
                if (n == 30 && u == 0) stg = lds_ldv<v2u>(aS);
                if (n == 30 && u == 1) { tag = mix64(((u64)(u32)__builtin_amdgcn_readfirstlane(ee.y) << 32) | (u32)__builtin_amdgcn_readfirstlane(ee.x)); asm volatile("" : "+s"(tag)); }
                SB0();
            });
        });
        {
            auto csbad = [&]() { return umax(umax(umax(fas[0].x, fas[0].z), umax(fas[1].x, fas[1].z)), umax(fas[2].x, fas[2].z)) == ~0u; };
            const u32 want = (lane & 1) ? (u32)(tag >> 32) : (u32)tag;
            if (__builtin_expect(any_(csbad() || stg.y != want), 0)) {
                int k = 0;
                while (any_(csbad()) && ++k < (SPIN_CAP << 2)) { fas[0] = lds_ldv<v4u>(aC); fas[1] = lds_ldv<v4u>(aC + 1024); fas[2] = lds_ldv<v4u>(aC + 2048); }
                k = 0;
                const GAS u64* sp = (const GAS u64*)(ws + OFF_ST + P * 1024 + (w & 1) * 512 + (w >> 1) * 32 + (l31 >> 2) * 64 + (lane & 3) * 8);
                while (any_(stg.y != want) && ++k < SPIN_CAP) { const u64 gr = __hip_atomic_load(sp, RLX_AGENT); stg = (v2u){(u32)gr, (u32)(gr >> 32)}; }
            }
            const u32 m1 = stg.x & 0xffffu, m2 = m1 * 0x10001u;
            fbs = (v4u){m2, h ? m1 : stg.x, m2, m1};
        }
#pragma unroll
        for (int u = 0; u < 3; u++) acc[u] = mfma32(fas[u], fbs, acc[u]);
        STAMP(2); CST(1);
        sfor<6>([&](auto K_) __attribute__((always_inline)) {
            constexpr int u = decltype(K_)::value >> 1, s = decltype(K_)::value & 1;
            v4u o;
#pragma unroll
            for (int e = 0; e < 4; e++) {
                const float a0 = acc[u][8 * s + 2 * e], a1 = acc[u][8 * s + 2 * e + 1];
                o[e] = relu_pk(pk_bf(a0 * __builtin_fabsf(a0), a1 * __builtin_fabsf(a1)));
            }
            st_sc1(o, rhs, l16, (u < 2 ? sh + u * 2048 : sh2) + s * 1024);
            SB0();
        });
        STAMP(8);
        wait_vm<0>();
        STAMP(4);
        if (lane == 0) __hip_atomic_store((GAS u64*)(ws + OFF_F2) + ((4 * P + w) * 16 + j) * FS2, tag, RLX_AGENT);
        v4u none[16];
        gemm2(IC<1>{}, none, ~tag, tag);
    } else {
        u64 tag;
        __builtin_amdgcn_s_setprio(3);
        glds(rx, l16, (8 * tb + 2 * w) * 1024, L_KB + (2 * w) * 1088);
        glds(rx, l16, (8 * tb + 2 * w + 1) * 1024, L_KB + (2 * w + 1) * 1088);
        if (w == 0) {
            const v4u re = mkrsrc(ws, 16);
            asm volatile("s_mov_b32 m0, %1\n\ts_mov_b64 exec, 1\n\tbuffer_load_dwordx4 off, %0, 0 sc1 lds\n\ts_mov_b64 exec, -1" :: "s"(re), "s"(L_E) : "memory");
        } else if (w == 1) {
            const v4u rb1 = mkrsrc(b1, 1376 * 2);
            asm volatile("s_mov_b32 m0, %3\n\ts_nop 0\n\tbuffer_load_dword %0, %1, %2 offen lds" :: "v"(lane * 4), "s"(rb1), "s"(u0 * 64), "s"(L_B1) : "memory");
        }
        const u32 pi = opq((hf * 64 + lane) * 2);
        u32 pA = *(const GAS u16*)((const GAS char*)(logA + ch * 128) + pi), pI = *(const GAS u16*)((const GAS char*)(Aim + ch * 128) + pi);
        u32 pR = *(const GAS u16*)((const GAS char*)(Cre + ch * 128) + pi), pC = *(const GAS u16*)((const GAS char*)(Cim + ch * 128) + pi);
        u32 pT = logdt[ch], pD = Dskip[ch];
        __builtin_amdgcn_s_barrier();
        asm volatile("" ::: "memory");
        flg(IC<R_X>{});
        one = opq(1u);
        const u32 aCnt = opq(L_CNT + chl * 4), aCtl = opq(L_C);
        const v4u rus = mkrsrc(ws + OFF_U, 2 << 20);
        v4u selLo, selHi;
#pragma unroll
        for (int e = 0; e < 4; e++) {
            const u32 o1 = (h == (g & 1) && ((l15 & 7) >> 1) == e) ? ((l15 & 1) ? 0x3C000000u : 0x00003C00u) : 0u;
            selLo[e] = l15 < 8 ? o1 : 0u; selHi[e] = l15 < 8 ? 0u : o1;
        }
        pin(selLo); pin(selHi);
        const int m = 64 * hf + lane;
        const u32 aT = opq(w * 32768 + 16384 + l15 * 64 + g * 16), t0 = opq((2 * chl) * 32768 + 16384 + m * 4), kr = opq(L_KB + chl * 8320 + (1016 - 8 * m) * 2);
        u32 tg[6];
        {
            const bool r1 = m < 32, r2 = m < 64;
            tg[0] = opq(t0 + (r1 ? 528 : 0) * 4); tg[1] = opq(t0 + (r2 ? 400 : 0) * 4); tg[2] = opq(t0 + (r1 ? 560 : 272) * 4);
            tg[3] = opq(t0 + (r1 ? 592 : 272) * 4); tg[4] = opq(t0 + (r2 ? 464 : 1) * 4); tg[5] = opq(t0 + (r1 ? 624 : 1) * 4);
        }
        u32 kb[4], ua[4];
#pragma unroll
        for (int i = 0; i < 4; i++) {
            kb[i] = opq(L_KB + chl * 8320 + ((l15 & 1) ? (239 - l15 + 8 * g) * 2 : 4160 + (240 - l15 + 8 * g) * 2) - hf * 256 - 192 + i * 1024);
            const int os = 30 + 2 * l15 + g - 16 * hf - 4 * i;
            ua[i] = opq(L_U + os * 32 + ((chl ^ (os >> 3)) & 1) * 16);
        }
        const u32 aZ = opq(L_C + (lane & 3) * 4), aZ2 = opq(L_C + (lane & 3) * 4 + ((lane & 4) ? R_P + 4 : R_F + 7) * 16);
        const u32 vA = opq((lane >> 3) * 4096 + (lane & 7) * 16);
        pin(pA); pin(pI); pin(pR); pin(pC); pin(pT); pin(pD);
        {
            const u32 aRow = opq(L_KB + (l15 & 7) * 1088 + (l15 >> 3) * 512 + g * 16), aTr = opq(L_KB + (l15 >> 2) * 1088 + (128 * w + 16 * g + 4 * (lane & 3)) * 2);
            v4u ones = {0x3F803F80u, 0x3F803F80u, 0x3F803F80u, 0x3F803F80u}; pin(ones);
            spin_zero4(aF + R_X * 16);
            STAMP(1);
            v4u xb[8]; v2u xt[2][2];
#pragma unroll
            for (int s = 0; s < 8; s++) xb[s] = lds_ld<v4u>(aRow + s * 64);
#pragma unroll
            for (int i = 0; i < 2; i++) { xt[i][0] = lds_tr(aTr + i * 128); xt[i][1] = lds_tr(aTr + i * 128 + 4 * 1088); }
            tag = get_tag();
            f32x4 sm = {0.f, 0.f, 0.f, 0.f}, sq = {0.f, 0.f, 0.f, 0.f};
#pragma unroll
            for (int s = 0; s < 8; s++) { sq = mfma16(xb[s], xb[s], sq); sm = mfma16(ones, xb[s], sm); }
            float q = (l15 & 2) ? ((l15 & 1) ? sq[3] : sq[2]) : ((l15 & 1) ? sq[1] : sq[0]);
            q = g == (l15 >> 2) ? q : 0.f;
            { const auto r = __builtin_amdgcn_permlane32_swap(f2u(q), f2u(q), false, false); q = u2f(r[0]) + u2f(r[1]); }
            { const auto r = __builtin_amdgcn_permlane16_swap(f2u(q), f2u(q), false, false); q = u2f(r[0]) + u2f(r[1]); }
            const float mean = dpp_add<0x128>(sm[0]) * (1.f / 512.f);
            const float var = fmaxf(__builtin_fmaf(-mean, mean, dpp_add<0x128>(q) * (1.f / 512.f)), 0.f);
            const u32 p = pk_bf(mean, __builtin_amdgcn_sqrtf(rbf(rbf(var) + 1e-5f)));
            const float mb = bf_lo(p), rr = __builtin_amdgcn_rcpf(bf_hi(p));
            if (w == 0 && lane < 8) __hip_atomic_store((GAS u64*)(ws + OFF_ST + (P * 16 + j) * 64) + lane, ((u64)((lane & 1) ? (u32)(tag >> 32) : (u32)tag) << 32) | p, RLX_AGENT);
            sfor<2>([&](auto I_) {
                constexpr int i = decltype(I_)::value;
                v4u o;
                sfor<4>([&](auto E_) {
                    constexpr int e = decltype(E_)::value;
                    const u32 xv = xt[i][e >> 1][e & 1];
                    const u32 d = pk_bf(bf_lo(xv) - bcast<2 * e>(mb), bf_hi(xv) - bcast<2 * e + 1>(mb));
                    o[e] = pk_bf(bf_lo(d) * bcast<2 * e>(rr), bf_hi(d) * bcast<2 * e + 1>(rr));
                });
                st_sc1(o, rus, l16, tb * 8192 + w * 2048 + i * 1024);
            });
        }
        STAMP(2);
        __builtin_amdgcn_s_setprio(PRIO % 10);
        f32x4 kt[3];
        {
            const float dt = rbf(expf(bf_lo(pT)));
            const float zr = -rbf(expf(bf_lo(pA))) * dt, zi = bf_lo(pI) * dt;
            const float lr = zr * 1.44269504088896341f;
            const float th = zi * 0.159154943091895336f, tl = __builtin_fmaf(zi, 0.159154943091895336f, -th) + zi * 6.4206383e-09f;
            const cmult w1c = mkc(cexp_pow(lr, -th, -tl, 1.f)), qc = mkc(cexp_pow(lr, -th, -tl, 128.f)), q2 = mkc(cexp_pow(lr, -th, -tl, 256.f));
            const cmult w8 = mkc(cexp_pow(lr, th, tl, 8.f)), w32 = mkc(cexp_pow(lr, th, tl, 32.f));
            const u32 aR = pR & 0x7fffu, aI = pC & 0x7fffu;
            int ex = (int)(wave_max(aR > aI ? aR : aI) >> 7); ex = ex < 12 ? 12 : (ex > 240 ? 240 : ex);
            const float sdn = u2f((u32)(262 - ex) << 23), sup = u2f((u32)(ex - 8) << 23);
            auto transpose = [&](const f32x2 (&v)[16], v4u (&f)[4]) {
                f32x16 z; for (int i = 0; i < 16; i++) z[i] = 0.f;
                f32x16 dr = mfma32h((v4u){pk_h2(v[0].x, v[1].x), pk_h2(v[2].x, v[3].x), pk_h2(v[4].x, v[5].x), pk_h2(v[6].x, v[7].x)}, selLo, z);
                f32x16 di = mfma32h((v4u){pk_h2(v[0].y, v[1].y), pk_h2(v[2].y, v[3].y), pk_h2(v[4].y, v[5].y), pk_h2(v[6].y, v[7].y)}, selLo, z);
                dr = mfma32h((v4u){pk_h2(v[8].x, v[9].x), pk_h2(v[10].x, v[11].x), pk_h2(v[12].x, v[13].x), pk_h2(v[14].x, v[15].x)}, selHi, dr);
                di = mfma32h((v4u){pk_h2(v[8].y, v[9].y), pk_h2(v[10].y, v[11].y), pk_h2(v[12].y, v[13].y), pk_h2(v[14].y, v[15].y)}, selHi, di);
#pragma unroll
                for (int e = 0; e < 4; e++) {
                    f[0][e] = pk_h2(dr[2 * e], dr[2 * e + 1]); f[1][e] = pk_h2(dr[8 + 2 * e], dr[9 + 2 * e]);
                    f[2][e] = pk_h2(di[2 * e], di[2 * e + 1]); f[3][e] = pk_h2(di[8 + 2 * e], di[9 + 2 * e]);
                }
            };
            { f32x2 t0_ = w1c.b, t1_ = qc.b, t2_ = q2.b, t3_ = w8.b, t4_ = w32.b; pin(t0_); pin(t1_); pin(t2_); pin(t3_); pin(t4_); }
            STAMP(13);
            wait_vm<0>();
            if (lane == 0) __hip_atomic_store((GAS u64*)(ws + OFF_F1) + (w * 256 + tb) * FS1, tag, RLX_AGENT);
            STAMP(14);
            v4u fy[4], fx[4];
            f32x2 v[16], xo[8];
            v[0] = (f32x2){1.f, 0.f}; v[1] = w8.b; v[2] = cmul(w8.b, w8); v[3] = cmul(v[2], w8); v[4] = w32.b;
            xo[0] = (f32x2){bf_lo(pR) * sdn, -bf_lo(pC) * sdn};
#pragma unroll
            for (int i = 5; i < 16; i++) { v[i] = cmul(v[i - 4], w32); if (i < 12) xo[i - 4] = cmul(xo[i - 5], w1c); }
            STAMP(3); CST(2);
            transpose(v, fy);
            const float fin = 2.f * dt * sup;
            auto tile = [&](int t) {
                transpose(v, fx);
                f32x4 d = {0.f, 0.f, 0.f, 0.f};
#pragma unroll
                for (int s = 0; s < 4; s++) d = mfma16h(fy[s], fx[s], d);
                kt[t] = d * fin;
            };
            v[0] = xo[0]; v[1] = cmul(xo[0], qc);
#pragma unroll
            for (int s = 2; s < 16; s++) v[s] = cmul(v[s - 2], q2);
            const f32x2 last = cmul(v[14], q2);
            tile(0);
            v[0] = last; v[1] = xo[4]; v[9] = xo[2]; v[13] = xo[6];
            v[2] = cmul(v[1], qc); v[10] = cmul(v[9], qc); v[14] = cmul(v[13], qc);
#pragma unroll
            for (int s = 3; s < 9; s++) v[s] = cmul(v[s - 2], q2);
            v[11] = cmul(v[9], q2); v[12] = cmul(v[10], q2); v[15] = cmul(v[13], q2);
            const f32x2 last2 = cmul(v[14], q2);
            tile(1);
            v[0] = last2;
#pragma unroll
            for (int i = 0; i < 4; i++) { v[1 + 2 * i] = xo[2 * i + 1]; v[2 + 2 * i] = cmul(xo[2 * i + 1], qc); }
#pragma unroll
            for (int i = 9; i < 16; i++) v[i] = (f32x2){0.f, 0.f};
            tile(2);
        }
        CST(3); STAMP(17);
        pin(kt[0]); pin(kt[1]); pin(kt[2]);
        spin_zero4<1>(aZ2);
        STAMP(11);
#pragma unroll
        for (int t = 0; t < 3; t++) lds_st<f32x4>(aT + t * 1024, kt[t]);
        sig<C_A>(aCtl + (L_CNT - L_C), one);
        STAMP(4);
        spin(aCtl + (L_CNT - L_C) + 4 * C_A, 4);
        {
            auto tv = [&](u32 a) { return lds_ld<float>(a) + lds_ld<float>(a + 32768); };
            float k[9];
            k[0] = tv(t0); k[8] = tv(t0 + 4); k[4] = tv(t0 + 272 * 4);
            k[1] = tv(tg[0]); k[2] = tv(tg[1]); k[3] = tv(tg[2]); k[5] = tv(tg[3]); k[6] = tv(tg[4]); k[7] = tv(tg[5]);
            const float va = tv(t0 + 512), vb = tv(t0 + 516);
            if (m == 0) k[0] += bf_lo(pD);
            const u32 k10 = pk_bf(k[1], k[0]);
            lds_st<v4u>(kr + 2048, (v4u){pk_bf(k[7], k[6]), pk_bf(k[5], k[4]), pk_bf(k[3], k[2]), k10});
            lds_st<v4u>(kr + 2048 + 4160, (v4u){pk_bf(k[8], k[7]), pk_bf(k[6], k[5]), pk_bf(k[4], k[3]), pk_bf(k[2], k[1])});
            const u32 pa = pk_bf(va, va), pb = pk_bf(vb, vb), pba = pk_bf(vb, va);
            lds_st<v4u>(kr, (v4u){pb, (m & 1) ? pb : pba, pa, pa});
            lds_st<v4u>(kr + 4160, (v4u){pb, pb, (m & 1) ? pba : pa, pa});
            if (m < 4) lds_st<v4u>(L_KB + chl * 8320 + 4096 + (m & 1) * 16 + (m < 2 ? 4160 : 0), (v4u){m == 0 ? k10 >> 16 : 0u, 0u, 0u, 0u});
        }
        sig<C_K>(aCnt, one);
        STAMP(18);
        f32x4 cv[8];
        {
#pragma unroll
            for (int t = 0; t < 8; t++) cv[t] = (f32x4){0.f, 0.f, 0.f, 0.f};
            v4u A[8], B[8];
            auto rdA = [&](int r, int a) { const u32 p = kb[(7 - a) >> 1] + ((7 - a) & 1) * 512 + (3 - r) * 64; return (v4u){lds_ld<u32>(p), lds_ld<u32>(p + 4), lds_ld<u32>(p + 8), lds_ld<u32>(p + 12)}; };
            auto rdB = [&](int r, int d) { return lds_ld<v4u>(ua[r] + d * 1024); };
            spin(aCnt + 4 * C_K, 2);
#pragma unroll
            for (int i = 0; i < 8; i++) A[7 - i] = rdA(0, 7 - i);
            STAMP(19);
            spin_zero4(aZ + (L_UF - L_C));
            STAMP(6);
#pragma unroll
            for (int i = 0; i < 8; i++) B[i] = rdB(0, i);
            if (b == 0 && w == 0 && lane == 0) __hip_atomic_store((GAS u64*)ws, tag, RLX_AGENT);
            CST(0);
#pragma unroll
            for (int r = 0; r < 4; r++) {
#pragma unroll
                for (int a = 7; a >= 0; a--) {
#pragma unroll
                    for (int d = 0; d + a < 8; d++) {
                        cv[a + d] = mfma16(A[a], B[d], cv[a + d]);
                        if (a == 0 && r < 3) { SB0(); B[d] = rdB(r + 1, d); SB0(); }
                    }
                    if (r < 3) { SB0(); A[a] = rdA(r + 1, a); SB0(); }
                }
            }
        }
        STAMP(7); CST(1);
        {
            const u32 mine = opq(w * 32768 + 16384 + l16), other = opq((w ^ 1) * 32768 + 16384 + l16);
#pragma unroll
            for (int t = 0; t < 4; t++) lds_st<f32x4>(mine + t * 1024, sel4(hf, cv[t], cv[4 + t]));
            sig<C_E>(aCnt, one);
            sig<C_D>(aCtl + (L_CNT - L_C), one);
            spin(aCnt + 4 * C_E, 2);
            u32 o[8];
#pragma unroll
            for (int t = 0; t < 4; t++) {
                const f32x4 y = sel4(hf, cv[4 + t], cv[t]) + lds_ld<f32x4>(other + t * 1024);
                o[2 * t] = pk_bf(y[0], y[1]); o[2 * t + 1] = pk_bf(y[2], y[3]);
            }
            const v4u ras = mkrsrc(ws + OFF_A, 2 << 20);
            st_sc1((v4u){o[0], o[1], o[2], o[3]}, ras, l16, ch * 4096 + hf * 2048);
            st_sc1((v4u){o[4], o[5], o[6], o[7]}, ras, l16, ch * 4096 + hf * 2048 + 1024);
            STAMP(15);
            wait_vm<0>();
            STAMP(8);
            if (lane == 0) __hip_atomic_store((GAS u64*)(ws + OFF_F3) + (hf * 512 + ch) * FS3, tag, RLX_AGENT);
            spin(aCtl + (L_CNT - L_C) + 4 * C_D, 4);
            poll_flags((const GAS u64*)(ws + OFF_F3) + ((tr >> 4) * 512 + tc * 64 + 16 * w + l15) * FS3, tag);
            STAMP(9);
#pragma unroll
            for (int i = 0; i < 2; i++)
                glds_sc1(ras, vA, (tc * 64 + 16 * w + 8 * i) * 4096 + (tr >> 3) * 1024 + (tr & 3) * 256 + ((tr >> 2) & 1) * 128, L_U + w * 2048 + i * 1024);
            const u32 ta = opq(L_U + w * 2048 + (l15 >> 2) * 128 + g * 32 + (lane & 3) * 8), aw = opq(L_KB + lane * 144 + w * 32);
            wait_vm<0>();
            const v2u a0 = lds_tr(ta), a1 = lds_tr(ta + 512), a2 = lds_tr(ta + 1024), a3 = lds_tr(ta + 1536);
            lds_st<v4u>(aw, (v4u){a0.x, a0.y, a1.x, a1.y});
            lds_st<v4u>(aw + 16, (v4u){a2.x, a2.y, a3.x, a3.y});
            STAMP(10);
        }
        wait_lgkm0();
        __builtin_amdgcn_s_barrier();
    }
}

#if defined(__HIP_DEVICE_COMPILE__) && __HIP_DEVICE_COMPILE__
#pragma clang attribute pop
#endif

extern "C" void kernel_launch(void* const* d_in, const int* in_sizes, int n_in, void* d_out, int out_size, void* d_ws, size_t ws_size,
                              hipStream_t stream) {
    fused_kernel<<<256, 768, 0, stream>>>((const u16*)d_in[0], (const u16*)d_in[1], (const u16*)d_in[2], (const u16*)d_in[3], (const u16*)d_in[4],
                                          (const u16*)d_in[5], (const u16*)d_in[6], (const u16*)d_in[8], (const u16*)d_in[9], (const u16*)d_in[10],
                                          (const u16*)d_in[11], (u16*)d_out, (char*)d_ws);
}
```

```cpp
#define QTY 0
#define PROLE 1
#define PROBE 1
#include <hip/hip_runtime.h>
#include <stdint.h>
#include <utility>

typedef unsigned short u16;
typedef unsigned int u32;
typedef unsigned long long u64;
typedef u32 v4u __attribute__((ext_vector_type(4)));
typedef u32 v2u __attribute__((ext_vector_type(2)));
typedef float f32x2 __attribute__((ext_vector_type(2)));
typedef float f32x4 __attribute__((ext_vector_type(4)));
typedef float f32x16 __attribute__((ext_vector_type(16)));
typedef __bf16 bf16x8 __attribute__((ext_vector_type(8)));
typedef __bf16 bf16x2 __attribute__((ext_vector_type(2)));
typedef _Float16 f16x8 __attribute__((ext_vector_type(8)));
typedef _Float16 f16x2 __attribute__((ext_vector_type(2)));
typedef short s16x4 __attribute__((ext_vector_type(4)));
#define GAS __attribute__((address_space(1)))
#define LAS __attribute__((address_space(3)))
#define DI __device__ __forceinline__
#define RLX_AGENT __ATOMIC_RELAXED, __HIP_MEMORY_SCOPE_AGENT
#define RLX_WG __ATOMIC_RELAXED, __HIP_MEMORY_SCOPE_WORKGROUP
#define SB0() __builtin_amdgcn_sched_barrier(0)
#define CHST 0
#define PSLEEP 2
#define ROT 2
#define POL 1
#define PDS 0
#define THR 0
#define HS 0
#define AB 0
#ifndef NV2
#define NV2 8
#endif
#define WING 10
#define HROT 0
#define GP2 0
#define NE 3
#define PRIO 21

#define NOH_OK
#define NWV 12
constexpr int OFF_ST = 16384, OFF_F1 = 128 << 10, OFF_F2 = 256 << 10, OFF_F3 = 512 << 10;
constexpr int FS1 = 8, FS2 = 16, FS3 = 16;
constexpr int OFF_U = 1 << 20, OFF_A = 4 << 20, OFF_H = 8 << 20;
constexpr int H_BYTES = 64 * 43 * 2048;
constexpr int L_C = 131072;
constexpr int L_FLG = L_C, L_UF = L_C + 320, L_E = L_C + 768, L_LNS = L_C + 352, L_CNT = L_C + 416, L_B1 = L_C + 512, L_STAT = L_C + 1024, L_CSP = L_C + 29696;
enum { R_F = 0, R_P = 8, R_P7 = 13, R_PK = 15, R_X = 14, R_V1 = 16, R_V2 = 17, R_V3 = 18 };
constexpr int L_U = L_C + 3072, L_KB = L_C + 12288;
constexpr int SPIN_CAP = 1 << 16;
enum { C_A = 0, C_D = 1, C_K = 2, C_E = 4, C_PK = 8, C_RD = 12 };

#define STAMP(i) do { if ((i) == PROBE && prole == PROLE) { probe_t = (u32)__builtin_amdgcn_s_memrealtime(); } } while (0)
#define CST(i)
#define TIMED(x) x

#if defined(__HIP_DEVICE_COMPILE__) && __HIP_DEVICE_COMPILE__
#pragma clang attribute push(__attribute__((target("no-packed-fp32-ops"))), apply_to = function)
#endif
DI float u2f(u32 v) { return __builtin_bit_cast(float, v); }
DI u32 f2u(float v) { return __builtin_bit_cast(u32, v); }
DI bool any_(bool c) { return __builtin_amdgcn_ballot_w64(c) != 0; }
DI bool all_(bool c) { return __builtin_amdgcn_ballot_w64(!c) == 0; }
template <typename T> DI T lds_ld(u32 a) { return *(LAS T*)(size_t)a; }
template <typename T> DI void lds_st(u32 a, T v) { *(LAS T*)(size_t)a = v; }
DI v2u lds_tr(u32 a) { return __builtin_bit_cast(v2u, __builtin_amdgcn_ds_read_tr16_b64_v4i16((LAS s16x4*)(size_t)a)); }
DI v4u mkrsrc(const void* p, u32 bytes) { u64 a = (u64)p; v4u r; r.x = (u32)a; r.y = (u32)(a >> 32) & 0xffffu; r.z = bytes; r.w = 0x00020000u; return r; }
DI void glds(v4u rsrc, u32 voff, u32 soff, u32 lds) {
    asm volatile("s_mov_b32 m0, %3\n\ts_nop 0\n\tbuffer_load_dwordx4 %0, %1, %2 offen lds" :: "v"(voff), "s"(rsrc), "s"(soff), "s"(lds) : "memory");
}
template <int LOFF> DI u32 glds_first(v4u rsrc, u32 voff, u32 sa, u32 sb, u32 lbase) {
    u32 so;
    asm volatile("s_add_u32 m0, %5, %6\n\ts_add_u32 %0, %3, %4\n\tbuffer_load_dwordx4 %1, %2, %0 offen lds" : "=&s"(so) : "v"(voff), "s"(rsrc), "s"(sa), "s"(sb), "s"(lbase), "i"(LOFF) : "memory", "scc");
    return so;
}
template <int LOFF, int SOFF> DI void glds_imm(v4u rsrc, u32 voff, u32 sa, u32 lbase) {
    u32 so;
    asm volatile("s_add_u32 m0, %4, %5\n\ts_add_u32 %0, %3, %6\n\tbuffer_load_dwordx4 %1, %2, %0 offen lds" : "=&s"(so) : "v"(voff), "s"(rsrc), "s"(sa), "s"(lbase), "i"(LOFF), "i"(SOFF) : "memory", "scc");
}
template <int OFF> DI void glds_next(v4u rsrc, u32 voff, u32 so) {
    asm volatile("buffer_load_dwordx4 %0, %1, %2 offen offset:%3 lds" :: "v"(voff), "s"(rsrc), "s"(so), "i"(OFF) : "memory");
}
template <int LOFF> DI void flag_imm(v4u rsrc, u32 lbase) {
    asm volatile("s_add_u32 m0, %2, %3\n\ts_mov_b64 exec, 1\n\tbuffer_load_dword off, %0, %1 lds\n\ts_mov_b64 exec, -1" :: "s"(rsrc), "s"(rsrc.z), "s"(lbase), "i"(LOFF) : "memory", "scc");
}
DI void glds_sc1(v4u rsrc, u32 voff, u32 soff, u32 lds) {
    asm volatile("s_mov_b32 m0, %3\n\ts_nop 0\n\tbuffer_load_dwordx4 %0, %1, %2 offen sc1 lds" :: "v"(voff), "s"(rsrc), "s"(soff), "s"(lds) : "memory");
}
DI void flag_dma(v4u rsrc, u32 lds) {
    asm volatile("s_mov_b32 m0, %2\n\ts_mov_b64 exec, 1\n\tbuffer_load_dword off, %0, %1 lds\n\ts_mov_b64 exec, -1" :: "s"(rsrc), "s"(rsrc.z), "s"(lds) : "memory");
}
DI void st_sc1(v4u v, v4u rsrc, u32 voff, u32 soff) {
    asm volatile("buffer_store_dwordx4 %0, %1, %2, %3 offen sc1\n\ts_nop 1" :: "v"(v), "v"(voff), "s"(rsrc), "s"(soff) : "memory");
}
DI u32 opq(u32 a) { asm volatile("" : "+v"(a)); return a; }
template <typename T> DI T lds_ldv(u32 a) { return *(volatile LAS T*)(size_t)a; }
template <int SLEEP = 0> DI void spin_zero4(u32 a) {
    int n = 0;
    while (any_(lds_ldv<u32>(a) != 0) && ++n < (SPIN_CAP << 4)) { if (SLEEP) __builtin_amdgcn_s_sleep(1); }
    asm volatile("" ::: "memory");
}
template <int N> DI void wait_vm() { asm volatile("s_waitcnt vmcnt(%0)" :: "i"(N) : "memory"); }
DI void wait_lgkm0() { asm volatile("s_waitcnt lgkmcnt(0)" ::: "memory"); }
DI u64 mix64(u64 e) { e += 0x9E3779B97F4A7C15ull; e ^= e >> 31; e *= 0xBF58476D1CE4E5B9ull; e ^= e >> 29; return e; }
DI u64 get_tag() { const v2u e = lds_ldv<v2u>(L_E); return mix64(((u64)(u32)__builtin_amdgcn_readfirstlane(e.y) << 32) | (u32)__builtin_amdgcn_readfirstlane(e.x)); }
template <int C> DI void sig(u32 a, u32 one) { asm volatile("s_mov_b64 exec, 1\n\tds_add_u32 %0, %1 offset:%2\n\ts_mov_b64 exec, -1" :: "v"(a), "v"(one), "i"(4 * C) : "memory"); }
template <int OFF> DI void clr(u32 a, u32 zero) { asm volatile("s_mov_b64 exec, 1\n\tds_write_b32 %0, %1 offset:%2\n\ts_mov_b64 exec, -1" :: "v"(a), "v"(zero), "i"(OFF) : "memory"); }
DI void spin(u32 a, u32 target) {
    int n = 0;
    while ((u32)__builtin_amdgcn_readfirstlane(lds_ldv<u32>(a)) < target && ++n < (SPIN_CAP << 4)) {}
    asm volatile("" ::: "memory");
}
DI void poll_flags(const GAS u64* p, u64 tag) {
    int n = 0;
    while (true) {
        u64 f = __hip_atomic_load(p, RLX_AGENT);
        if (all_(f == tag) || ++n > SPIN_CAP) break;
        __builtin_amdgcn_s_sleep(PSLEEP);
    }
    asm volatile("" ::: "memory");
}
DI float bf_lo(u32 v) { return u2f(v << 16); }
DI float bf_hi(u32 v) { return u2f(v & 0xffff0000u); }
DI u32 pk_bf(float a, float b) { f32x2 v = {a, b}; return __builtin_bit_cast(u32, __builtin_convertvector(v, bf16x2)); }
typedef short s16x2 __attribute__((ext_vector_type(2)));
DI u32 relu_pk(u32 v) { return __builtin_bit_cast(u32, __builtin_elementwise_max(__builtin_bit_cast(s16x2, v), (s16x2){0, 0})); }
DI u32 umax(u32 a, u32 b) { return a > b ? a : b; }
DI float rbf(float a) { return bf_lo(pk_bf(a, a)); }
DI u32 pk_h(f32x2 v) { return __builtin_bit_cast(u32, __builtin_convertvector(v, f16x2)); }
DI f32x16 mfma32(v4u a, v4u b, f32x16 c) { return __builtin_amdgcn_mfma_f32_32x32x16_bf16(__builtin_bit_cast(bf16x8, a), __builtin_bit_cast(bf16x8, b), c, 0, 0, 0); }
DI f32x4 mfma16(v4u a, v4u b, f32x4 c) { return __builtin_amdgcn_mfma_f32_16x16x32_bf16(__builtin_bit_cast(bf16x8, a), __builtin_bit_cast(bf16x8, b), c, 0, 0, 0); }
DI f32x4 mfma16h(v4u a, v4u b, f32x4 c) { return __builtin_amdgcn_mfma_f32_16x16x32_f16(__builtin_bit_cast(f16x8, a), __builtin_bit_cast(f16x8, b), c, 0, 0, 0); }
template <int CTRL> DI float dpp_add(float v) { return v + __builtin_bit_cast(float, __builtin_amdgcn_update_dpp(0, __builtin_bit_cast(int, v), CTRL, 0xf, 0xf, true)); }
template <int CTRL> DI u32 dpp_max(u32 v) { const u32 o = (u32)__builtin_amdgcn_update_dpp(0, (int)v, CTRL, 0xf, 0xf, true); return v > o ? v : o; }
template <int T> DI float bcast(float v) { return __builtin_bit_cast(float, __builtin_amdgcn_update_dpp(0, __builtin_bit_cast(int, v), 0x150 + T, 0xf, 0xf, true)); }
DI float rl(float v, int l) { return __builtin_bit_cast(float, __builtin_amdgcn_readlane(__builtin_bit_cast(int, v), l)); }
DI f32x2 wave_sum2(f32x2 v) {
    float a = v.x, c = v.y;
    a = dpp_add<0xB1>(a); c = dpp_add<0xB1>(c); a = dpp_add<0x4E>(a); c = dpp_add<0x4E>(c);
    a = dpp_add<0x141>(a); c = dpp_add<0x141>(c); a = dpp_add<0x140>(a); c = dpp_add<0x140>(c);
    return ((f32x2){rl(a, 0), rl(c, 0)} + (f32x2){rl(a, 16), rl(c, 16)}) + ((f32x2){rl(a, 32), rl(c, 32)} + (f32x2){rl(a, 48), rl(c, 48)});
}
DI u32 wave_max(u32 v) {
    v = dpp_max<0xB1>(v); v = dpp_max<0x4E>(v); v = dpp_max<0x141>(v); v = dpp_max<0x140>(v);
    const u32 a = __builtin_amdgcn_readlane(v, 0), c = __builtin_amdgcn_readlane(v, 16), d = __builtin_amdgcn_readlane(v, 32), e = __builtin_amdgcn_readlane(v, 48);
    const u32 p = a > c ? a : c, q = d > e ? d : e;
    return p > q ? p : q;
}
struct cmult { f32x2 b, n; };
DI cmult mkc(f32x2 b) { cmult c; c.b = b; c.n = (f32x2){-b.y, b.x}; return c; }
DI f32x2 cmul(f32x2 a, const cmult& c) { return __builtin_elementwise_fma((f32x2){a.x, a.x}, c.b, (f32x2){a.y, a.y} * c.n); }
DI f32x16 mfma32h(v4u a, v4u b, f32x16 c) { return __builtin_amdgcn_mfma_f32_32x32x16_f16(__builtin_bit_cast(f16x8, a), __builtin_bit_cast(f16x8, b), c, 0, 0, 0); }
DI u32 pk_h2(float a, float b) { return pk_h((f32x2){a, b}); }
DI f32x2 cexp_pow(float lr, float th, float tl, float e) {
    float a = __builtin_amdgcn_fractf(th * e) + tl * e;
    float m = __builtin_amdgcn_exp2f(lr * e);
    f32x2 r; r.x = m * __builtin_amdgcn_cosf(a); r.y = m * __builtin_amdgcn_sinf(a); return r;
}
DI f32x4 sel4(bool c, f32x4 a, f32x4 b) { return c ? a : b; }
constexpr int w2loc(int n) { return n < 16 ? n : n < 28 ? n + 4 : n - 28; }
template <class F, int... I> DI void sfor_(F&& f, std::integer_sequence<int, I...>) { (f(std::integral_constant<int, I>{}), ...); }
template <int N, class F> DI void sfor(F&& f) { sfor_(f, std::make_integer_sequence<int, N>{}); }
template <int I> using IC = std::integral_constant<int, I>;
template <typename T> DI void pin(T& v) { asm volatile("" : "+v"(v)); }

__global__ void __launch_bounds__(768, 3)
fused_kernel(const u16* __restrict__ x, const u16* __restrict__ logA, const u16* __restrict__ Aim, const u16* __restrict__ Cre,
             const u16* __restrict__ Cim, const u16* __restrict__ logdt, const u16* __restrict__ Dskip, const u16* __restrict__ w1,
             const u16* __restrict__ b1, const u16* __restrict__ w2, const u16* __restrict__ b2, u16* __restrict__ out, char* ws) {
    __shared__ __attribute__((aligned(16))) char smem[163840];
    asm volatile("" :: "v"((u32)(size_t)(LAS char*)smem));
    const int tid = threadIdx.x, lane = tid & 63, wave = __builtin_amdgcn_readfirstlane(tid >> 6), w = wave & 3, role = wave >> 2;
    const int b = blockIdx.x, xcd = b & 7, slot = b >> 3;
    const int P = 4 * (xcd >> 1) + (slot >> 3), j = 8 * (xcd & 1) + (slot & 7);
    const int tr = 4 * xcd + (slot >> 3), tc = slot & 7;
    const int u0 = (j * 43) >> 4;
    const int prole = role; u32 probe_t = 0;
    const u32 t_start = (u32)__builtin_amdgcn_s_memrealtime();
    const int h = lane >> 5, l31 = lane & 31, l15 = lane & 15, g = lane >> 4;
    const u32 l16 = lane * 16, lw = w * 32768, w4 = w * 4;
    const int rho = HROT ? ((slot >> 3) + slot) & 3 : (slot >> 3);
    const int tb = 64 * (P >> 2) + 32 * (j & 1) + 16 * ((P >> 1) & 1) + 2 * (j >> 1) + (P & 1);
    const int cp = 32 * xcd + slot, chl = w >> 1, hf = w & 1, ch = 2 * cp + chl;
    asm volatile("" :: "s"(x), "s"(logA), "s"(Aim), "s"(Cre), "s"(Cim), "s"(logdt), "s"(Dskip), "s"(w1), "s"(b1), "s"(w2), "s"(b2), "s"(out), "s"(ws));
    const v4u rx = mkrsrc(x, 2 << 20);
    const u32 aF = opq(L_FLG + (lane & 3) * 4);
    auto flg = [&](auto R_) { flag_imm<L_FLG + decltype(R_)::value * 16>(rx, w4); };
    u32 aV0, aV1, aFw, aCn, one, f2o;
    auto pre2 = [&]() {
        aV0 = opq(lw + (4 * h + (l15 >> 2)) * 128 + ((l15 >> 3) & 1) * 64 + (g & 1) * 32 + (lane & 3) * 8); aV1 = opq(aV0 ^ 64);
        aFw = opq(L_FLG + w4); aCn = opq(L_CNT + w4); one = opq(1u);
        const int jj = 4 * w + (lane < 4 ? lane : 4);
        f2o = opq(((2 * tr + role) * 16 + (jj < 15 ? jj : 15)) * FS2 * 8);
    };

    auto rdV = [&](int s, int dt) { v2u lo = lds_tr((dt ? aV1 : aV0) + w2loc(2 * s) * 1024), hi = lds_tr((dt ? aV1 : aV0) + w2loc(2 * s + 1) * 1024); return (v4u){lo.x, lo.y, hi.x, hi.y}; };
    auto gemm2 = [&](auto ROLE_, v4u (&stage)[16], u64 f2, u64 tag) __attribute__((always_inline)) {
        constexpr int role = decltype(ROLE_)::value;
        const GAS u64* f2p = (const GAS u64*)(ws + OFF_F2 + f2o);
        __builtin_amdgcn_s_setprio(role ? GP2 : 0);
        v4u park[8][2];
        if (role) {
            SB0();
            spin_zero4<1>(aFw + R_V1 * 16);
            STAMP(9);
            f2 = __hip_atomic_load(f2p, RLX_AGENT);
            SB0();
#pragma unroll
            for (int s = 0; s < 8; s++) { park[s][0] = rdV(s, 0); park[s][1] = rdV(s, 1); }
            clr<R_PK * 16>(aFw, opq(0u));
            SB0();
        }
        STAMP(10);
        if (!all_(f2 == tag)) poll_flags(f2p, tag);
        STAMP(5);
        const __amdgpu_buffer_rsrc_t rh = __builtin_amdgcn_make_buffer_rsrc(ws + OFF_H, 0, H_BYTES, 0x00020000);
        u32 sh2[4], sh2t[4];
#pragma unroll
        for (int k = 0; k < 4; k++) { sh2[k] = ((2 * tr + role) * 43 + 11 * w) * 2048 + (6 * rho - (k + rho >= 4 ? 22 : 0)) * 1024; sh2t[k] = (w == 3 && k + rho == 3) ? (u32)H_BYTES : sh2[k]; }
        constexpr int WIN = WING;
        v4u hq[WIN], vb[3][2];
        auto ldH = [&](int s) { hq[s % WIN] = __builtin_amdgcn_raw_buffer_load_b128(rh, l16, ((s + 2) % 6 >= 4 ? sh2t[(s + 2) / 6] : sh2[(s + 2) / 6]) + s * 1024, 16); };
#pragma unroll
        for (int s = 0; s < WIN; s++) ldH(s);
        const int fr = tid >> 3;
        const u32 fin_off = (512 * (tr >> 3) + 256 * ((fr >> 2) & 1) + 128 * ((tr >> 2) & 1) + 16 * (fr >> 3) + 4 * (tr & 3) + (fr & 3)) * 512 + tc * 64 + (tid & 7) * 8;
        v4u fin_x, fin_b; u32 fin_s, fv;
        f32x16 ac2[2];
#pragma unroll
        for (int i = 0; i < 16; i++) { ac2[0][i] = 0.f; ac2[1][i] = 0.f; }
        if (!role) {
            const u32 aSt = opq(lw + l16), zero = opq(0u);
            SB0();
            spin_zero4(opq(aFw + ((lane & 1) ? R_PK : R_V2) * 16));
            STAMP(9);
#pragma unroll
            for (int s = 0; s < 8; s++) {
                park[s][0] = rdV(s, 0); park[s][1] = rdV(s, 1);
                asm volatile("" ::: "memory");
                lds_st<v4u>(aSt + 2 * s * 1024, stage[2 * s]); lds_st<v4u>(aSt + (2 * s + 1) * 1024, stage[2 * s + 1]);
                SB0();
            }
            clr<R_V3 * 16>(aFw, zero);
            STAMP(13);
        }
#pragma unroll
        for (int s = 0; s < 22; s++) {
            SB0();
            if (role && (s == 4 || s == 10)) fv = lds_ldv<u32>(aFw + (s == 4 ? R_V2 : R_V3) * 16);
            if (role && (s == 6 || s == 12)) { if (__builtin_expect(any_(fv != 0), 0)) spin_zero4(aFw + (s == 6 ? R_V2 : R_V3) * 16); asm volatile("" ::: "memory"); }
            if (s + 2 >= 8 && s + 2 < 22) { vb[(s + 2) % 3][0] = rdV(s + 2, 0); vb[(s + 2) % 3][1] = rdV(s + 2, 1); }
            if (s == 19) sig<C_RD>(aCn, one);
            if (s == 21) fv = lds_ldv<u32>(aCn + 4 * C_RD);
            ac2[0] = mfma32(s < 8 ? park[s & 7][0] : vb[s % 3][0], hq[s % WIN], ac2[0]);
            ac2[1] = mfma32(s < 8 ? park[s & 7][1] : vb[s % 3][1], hq[s % WIN], ac2[1]);
            SB0();
            if (s + WIN < 22) ldH(s + WIN);
            if (s == 14) {
                fin_x = *(const GAS v4u*)(x + fin_off); fin_b = *(const GAS v4u*)(b2 + tc * 64 + (tid & 7) * 8);
                fin_s = __hip_atomic_load((GAS u32*)(ws + OFF_ST + (tr >> 1) * 1024 + (fr >> 2) * 64 + (tr & 1) * 32 + (fr & 3) * 8), RLX_AGENT);
            }
        }
        SB0();
        STAMP(7); CST(3);
        if (__builtin_expect(any_(fv < 2), 0)) spin(aCn + 4 * C_RD, 2);
        asm volatile("" ::: "memory");
        const u32 da = opq(lw + role * 20480 + l31 * 272 + h * 16);
#pragma unroll
        for (int dt = 0; dt < 2; dt++)
#pragma unroll
            for (int q = 0; q < 4; q++)
                lds_st<f32x4>(da + (32 * dt + 8 * q) * 4, (f32x4){ac2[dt][4 * q], ac2[dt][4 * q + 1], ac2[dt][4 * q + 2], ac2[dt][4 * q + 3]});
        f32x4 s0 = {bf_lo(fin_x.x) + bf_lo(fin_b.x), bf_hi(fin_x.x) + bf_hi(fin_b.x), bf_lo(fin_x.y) + bf_lo(fin_b.y), bf_hi(fin_x.y) + bf_hi(fin_b.y)};
        f32x4 s1 = {bf_lo(fin_x.z) + bf_lo(fin_b.z), bf_hi(fin_x.z) + bf_hi(fin_b.z), bf_lo(fin_x.w) + bf_lo(fin_b.w), bf_hi(fin_x.w) + bf_hi(fin_b.w)};
        const u32 ra = opq(role * 20480 + (fr & 31) * 272 + (tid & 7) * 32), rb = opq(ra + 65536), rc = opq(L_KB + fr * 144 + (tid & 7) * 16);
        float rs2 = __builtin_amdgcn_rcpf(bf_hi(fin_s)); rs2 *= rs2;
        asm volatile("" : "+v"(s0), "+v"(s1), "+v"(rs2));
        wait_lgkm0();
        __builtin_amdgcn_s_barrier();
        asm volatile("" ::: "memory");
        STAMP(11);
        const v4u av = lds_ld<v4u>(rc);
        s0 += ((lds_ld<f32x4>(ra) + lds_ld<f32x4>(ra + 32768)) + (lds_ld<f32x4>(rb) + lds_ld<f32x4>(rb + 32768))) * rs2;
        s1 += ((lds_ld<f32x4>(ra + 16) + lds_ld<f32x4>(ra + 32768 + 16)) + (lds_ld<f32x4>(rb + 16) + lds_ld<f32x4>(rb + 32768 + 16))) * rs2;
        s0 += (f32x4){bf_lo(av.x), bf_hi(av.x), bf_lo(av.y), bf_hi(av.y)};
        s1 += (f32x4){bf_lo(av.z), bf_hi(av.z), bf_lo(av.w), bf_hi(av.w)};
        *(GAS v4u*)(out + fin_off) = (v4u){pk_bf(s0[0], s0[1]), pk_bf(s0[2], s0[3]), pk_bf(s1[0], s1[1]), pk_bf(s1[2], s1[3])};
    };

    if (role == 0) {
        v4u stage[16]; u64 f2, tag;
        const int rot = ROT == 0 ? 0 : ROT == 1 ? (((slot >> 3) + slot) & 1) * 4 : ROT == 2 ? (((slot >> 3) + slot) & 3) * 2 : (2 * (slot >> 3) + (slot & 7)) & 7;
        const v4u rw1 = mkrsrc(w1, 512 * 1376 * 2);
        const u32 sx = (512 * (P >> 2) + 128 * ((P >> 1) & 1) + 64 * (w & 1) + 8 * (P & 1) + 4 * (w >> 1)) * 1024;
        const u32 sw1 = 16 * w * 2752 + u0 * 64, sw2 = 352 * w * 1024 + tc * 128;
        u32 vx[4], vw[3];
#pragma unroll
        for (int i = 0; i < 3; i++) { const int p = 64 * i + lane, r = (p * 171) >> 11; vw[i] = r * (2752 - 192) + l16; }
#pragma unroll
        for (int i = 0; i < 4; i++) vx[i] = (((h * 256 + ((lane >> 3) & 3)) * 1024 + ((lane & 7) ^ g) * 16) ^ ((i & 1) * 64)) + i * (16384 - 1024);
        auto ldX = [&](auto C_) { constexpr int c = decltype(C_)::value;
            const u32 so = glds_first<(16 + (c % 4) * 4) * 1024>(rx, vx[0], sx, ((c + rot) & 7) * 128, lw);
            glds_next<1024>(rx, vx[1], so); glds_next<2048>(rx, vx[2], so); glds_next<3072>(rx, vx[3], so); };
        auto ldW = [&](auto C_) { constexpr int c = decltype(C_)::value;
            const u32 so = glds_first<3 * (c % 5) * 1024>(rw1, vw[0], sw1, ((c + rot) & 7) * (64 * 2752), lw);
            glds_next<1024>(rw1, vw[1], so); glds_next<2048>(rw1, vw[2], so); };
        ldW(IC<0>{}); ldX(IC<0>{});
        __builtin_amdgcn_s_barrier();
        asm volatile("" ::: "memory");
        flg(IC<R_F>{});
        const u32 vv = (lane >> 3) * 1024 + ((lane & 7) ^ ((g & 1) << 2)) * 16;
        u32 sv[4];
#pragma unroll
        for (int k = 0; k < 4; k++) sv[k] = sw2 + (12 * rho - (k + rho >= 4 ? 44 : 0)) * 8192;
        const __amdgpu_buffer_rsrc_t rv = __builtin_amdgcn_make_buffer_rsrc((void*)w2, 0, 512 * 1376 * 2, 0x00020000);
        auto ldV = [&](int r) { stage[r] = __builtin_amdgcn_raw_buffer_load_b128(rv, vv, sv[(r + 32) / 12] + (r + 28) * 8192, 0); };
        sfor<7>([&](auto I) {
            constexpr int c = decltype(I)::value + 1;
            if constexpr (THR && c >= 2) wait_vm<THR>();
            if (!(AB & 4)) ldW(IC<c>{});
            if constexpr (c >= 4) {
                SB0();
#pragma unroll
                for (int k = 0; k < NE; k++) ldV(NE * (c - 4) + k);
                SB0();
            }
            if constexpr (c >= 4) TIMED(spin_zero4<1>(aF + (R_P + c - 4) * 16));
            if constexpr (THR && c >= 2) wait_vm<THR>();
            if (!(AB & 4)) ldX(IC<c>{});
            flg(IC<R_F + c>{});
        });
        STAMP(1);
        {
            const u32 vS = ((lane >> 1) * 4 + (lane & 1)) * 16;
            const v4u rst = mkrsrc(ws + OFF_ST, 16384);
            asm volatile("s_mov_b32 m0, %3\n\ts_mov_b64 exec, 0xffff\n\tbuffer_load_dwordx4 %0, %1, %2 offen sc1 lds\n\ts_mov_b64 exec, -1" :: "v"(vS), "s"(rst), "s"(P * 1024 + (w & 1) * 512 + (w >> 1) * 32), "s"(L_STAT + w * 256) : "memory");
        }
        spin_zero4<1>(aF + R_X * 16);
        tag = get_tag();
        const GAS u64* f1p = (const GAS u64*)(ws + OFF_F1) + ((xcd >> 1) * 256 + 64 * w + lane) * FS1;
        const u64 f1 = __hip_atomic_load(f1p, RLX_AGENT);
#pragma unroll
        for (int n = 4 * NE; n < 16; n++) ldV(n);
        SB0();
        if (!all_(f1 == tag)) poll_flags(f1p, tag);
        STAMP(16);
        {
            const v4u rus = mkrsrc(ws + OFF_U, 2 << 20);
            const u32 vU = (lane >> 1) * 8192 + ((lane ^ g) & 1) * 16;
#pragma unroll
            for (int i = 0; i < 2; i++) glds_sc1(rus, vU, (64 * w + 32 * i) * 8192 + cp * 32, L_U + 1024 + w * 2048 + i * 1024);
            flag_dma(rus, L_UF + w * 4);
        }
        STAMP(2);
        pre2();
        spin_zero4<1>(aF + R_P7 * 16);
        STAMP(3);
#pragma unroll
        for (int n = 0; n < 16; n++) pin(stage[n]);
        {
            const u32 aK = L_CNT + 4 * C_K + (lane & 1) * 4;
            int k = 0;
            while (any_(lds_ldv<u32>(aK) < 2) && ++k < (SPIN_CAP << 4)) { __builtin_amdgcn_s_sleep(1); }
        }
        STAMP(14);
        const v4u rw2 = mkrsrc(w2, 512 * 1376 * 2);
        sfor<28>([&](auto I) {
            constexpr int n = decltype(I)::value;
            if constexpr (n == 28 - NV2) asm volatile("global_load_dwordx2 %0, %1, %2 sc1" : "=v"(f2) : "v"(f2o), "s"(ws + OFF_F2) : "memory");
            glds_imm<w2loc(n) * 1024, n * 8192>(rw2, vv, sv[(n + 4) / 12], lw);
            if constexpr (n == 15) flg(IC<R_V1>{});
        });
        flg(IC<R_V2>{});
        asm volatile("s_waitcnt vmcnt(%1)" : "+v"(f2) : "i"(NV2 + 1) : "memory");
        STAMP(8);
        gemm2(IC<0>{}, stage, f2, tag);
    } else if (role == 1) {
        u64 tag;
        __builtin_amdgcn_s_setprio(PRIO / 10);
        {
            const int t = tid - 256;
            const u32 v1 = t < 192 ? ~0u : 0u, v2 = (t >= 26 && t < 30) ? 0u : ~0u;
            lds_st<v4u>((t < 192 ? L_CSP : L_U - 192 * 16) + t * 16, (v4u){v1, v1, v1, v1});
            if (t < 32) lds_st<v4u>(L_C + t * 16, (v4u){v2, v2, v2, v2});
            wait_lgkm0();
            __builtin_amdgcn_s_barrier();
            asm volatile("" ::: "memory");
        }
        u32 aW[4], aX[4];
#pragma unroll
        for (int s = 0; s < 4; s++) {
            const int ks = (s + w) & 3;
            aW[s] = opq(ks * 32768 + (8 * h + (l15 >> 2)) * 192 + (g & 1) * 32 + (lane & 3) * 8);
            aX[s] = opq(lw + 16384 + l31 * 128 + (((2 * ks + h) ^ ((l31 >> 1) & 7)) * 16));
        }
        const u32 aC = opq(L_CSP + l31 * 32 + h * 16), aCw = opq(L_CSP + lane * 32 + w * 8), aS = opq(L_STAT + w * 256 + l31 * 8), aB = opq(L_B1 + lane * 2);
        pre2();
        const u32 zero = opq(0u);
        f32x16 acc[3]; f32x4 cs = {0.f, 0.f, 0.f, 0.f};
        v4u sel[3];
#pragma unroll
        for (int u = 0; u < 3; u++) {
            for (int i = 0; i < 16; i++) acc[u][i] = 0.f;
            const u32 sv = (l15 == (u < 2 ? 8 * u : 1) + 4 * (g & 1)) ? 0xBF80BF80u : 0u; sel[u] = (v4u){sv, sv, sv, sv}; pin(sel[u]);
        }
        v4u fa[3][3], fb[2][4];
        auto rdW = [&](int n, int u) { const u32 a = aW[n & 3] + ((n >> 2) % 5) * 3072 + u * 64; v2u lo = lds_tr(a), hi = lds_tr(a + 768); fa[n % 3][u] = (v4u){lo.x, lo.y, hi.x, hi.y}; };
        auto rdX = [&](int c, int s) { fb[c & 1][s] = lds_ld<v4u>(aX[s] + (c % 4) * 4096); };
        const v4u rhs = mkrsrc(ws + OFF_H, H_BYTES);
        const int nown = (((j + 1) * 43) >> 4) - u0;
        const u32 sh = ((4 * P + w) * 43 + u0) * 2048, sh2 = nown == 3 ? sh + 4096 : (u32)H_BYTES;
        spin_zero4(aF);
        STAMP(1);
        rdX(0, 0);
#pragma unroll
        for (int u = 0; u < 3; u++) rdW(0, u);
        rdX(0, 1);
#pragma unroll
        for (int u = 0; u < 3; u++) rdW(1, u);
        rdX(0, 2); rdX(0, 3);
        u32 fl, fx, bb[2]; v2u ee; v2u stg; v4u fas[3], fbs;
        sfor<32>([&](auto N_) __attribute__((always_inline)) {
            constexpr int n = decltype(N_)::value, c = n >> 2, s = n & 3;
            SB0();
            if constexpr (s == 2 && c < 5) {
                pin(fa[(n + 1) % 3][2]);
                flg(IC<R_P + c>{});
            }
            if (s == 2 && c < 7 && !(AB & 2)) {
                if (__builtin_expect(any_(fl != 0), 0)) TIMED(spin_zero4(aF + (R_F + c + 1) * 16));
                asm volatile("" ::: "memory");
            }
            sfor<3>([&](auto U_) __attribute__((always_inline)) {
                constexpr int u = decltype(U_)::value;
                acc[u] = mfma32(fa[n % 3][u], fb[c & 1][s], acc[u]);
                if (s == 0 && c < 7) cs = mfma16(sel[u], fa[n % 3][u], cs);
                if (n == 27) cs = mfma16(sel[u], fa[28 % 3][u], cs);
                SB0();
                if (s >= 2 && c < 7 && u < 2) rdX(c + 1, 2 * (s - 2) + u);
                if (n + 2 < 32) rdW(n + 2, u);
                if (s == 1 && u == 0 && c < 7) fl = lds_ldv<u32>(aF + (R_F + c + 1) * 16);
                if constexpr (n == 29 && u == 2) clr<R_P7 * 16>(aFw, zero);
                if (n == 25 && u == 2) { bb[0] = lds_ld<u16>(aB); bb[1] = lds_ld<u16>(aB + 128); }
                if (n == 21 && u == 1) fx = lds_ldv<u32>(aF + R_X * 16);
                if (n == 23 && u == 1) { if (__builtin_expect(any_(fx != 0), 0)) spin_zero4(aF + R_X * 16); asm volatile("" ::: "memory"); ee = lds_ldv<v2u>(L_E); }
                if (n == 28 && u == 1) {
                    v2u t[2];
#pragma unroll
                    for (int k = 0; k < 2; k++) {
                        const float d = cs[k] - bf_hi(f2u(cs[k]));
                        t[k] = (v2u){__builtin_amdgcn_perm(f2u(d), f2u(cs[k]), 0x07060302u), pk_bf(d - bf_hi(f2u(d)), bf_lo(bb[k]))};
                    }
                    asm volatile("ds_write2st64_b64 %0, %1, %2 offset1:4" :: "v"(aCw), "v"(t[0]), "v"(t[1]) : "memory");
                }
                if (n == 31 && u == 0) { fas[0] = lds_ldv<v4u>(aC); fas[1] = lds_ldv<v4u>(aC + 1024); fas[2] = lds_ldv<v4u>(aC + 2048); }
                if (n == 30 && u == 0) stg = lds_ldv<v2u>(aS);
                if (n == 30 && u == 1) { tag = mix64(((u64)(u32)__builtin_amdgcn_readfirstlane(ee.y) << 32) | (u32)__builtin_amdgcn_readfirstlane(ee.x)); asm volatile("" : "+s"(tag)); }
                SB0();
            });
        });
        {
            auto csbad = [&]() { return umax(umax(umax(fas[0].x, fas[0].z), umax(fas[1].x, fas[1].z)), umax(fas[2].x, fas[2].z)) == ~0u; };
            const u32 want = (lane & 1) ? (u32)(tag >> 32) : (u32)tag;
            if (__builtin_expect(any_(csbad() || stg.y != want), 0)) {
                int k = 0;
                while (any_(csbad()) && ++k < (SPIN_CAP << 2)) { fas[0] = lds_ldv<v4u>(aC); fas[1] = lds_ldv<v4u>(aC + 1024); fas[2] = lds_ldv<v4u>(aC + 2048); }
                k = 0;
                const GAS u64* sp = (const GAS u64*)(ws + OFF_ST + P * 1024 + (w & 1) * 512 + (w >> 1) * 32 + (l31 >> 2) * 64 + (lane & 3) * 8);
                while (any_(stg.y != want) && ++k < SPIN_CAP) { const u64 gr = __hip_atomic_load(sp, RLX_AGENT); stg = (v2u){(u32)gr, (u32)(gr >> 32)}; }
            }
            const u32 m1 = stg.x & 0xffffu, m2 = m1 * 0x10001u;
            fbs = (v4u){m2, h ? m1 : stg.x, m2, m1};
        }
#pragma unroll
        for (int u = 0; u < 3; u++) acc[u] = mfma32(fas[u], fbs, acc[u]);
        STAMP(2); CST(1);
        sfor<6>([&](auto K_) __attribute__((always_inline)) {
            constexpr int u = decltype(K_)::value >> 1, s = decltype(K_)::value & 1;
            v4u o;
#pragma unroll
            for (int e = 0; e < 4; e++) {
                const float a0 = acc[u][8 * s + 2 * e], a1 = acc[u][8 * s + 2 * e + 1];
                o[e] = relu_pk(pk_bf(a0 * __builtin_fabsf(a0), a1 * __builtin_fabsf(a1)));
            }
            st_sc1(o, rhs, l16, (u < 2 ? sh + u * 2048 : sh2) + s * 1024);
            SB0();
        });
        STAMP(8);
        wait_vm<0>();
        STAMP(4);
        if (lane == 0) __hip_atomic_store((GAS u64*)(ws + OFF_F2) + ((4 * P + w) * 16 + j) * FS2, tag, RLX_AGENT);
        v4u none[16];
        gemm2(IC<1>{}, none, ~tag, tag);
    } else {
        u64 tag;
        __builtin_amdgcn_s_setprio(3);
        glds(rx, l16, (8 * tb + 2 * w) * 1024, L_KB + (2 * w) * 1088);
        glds(rx, l16, (8 * tb + 2 * w + 1) * 1024, L_KB + (2 * w + 1) * 1088);
        if (w == 0) {
            const v4u re = mkrsrc(ws, 16);
            asm volatile("s_mov_b32 m0, %1\n\ts_mov_b64 exec, 1\n\tbuffer_load_dwordx4 off, %0, 0 sc1 lds\n\ts_mov_b64 exec, -1" :: "s"(re), "s"(L_E) : "memory");
        } else if (w == 1) {
            const v4u rb1 = mkrsrc(b1, 1376 * 2);
            asm volatile("s_mov_b32 m0, %3\n\ts_nop 0\n\tbuffer_load_dword %0, %1, %2 offen lds" :: "v"(lane * 4), "s"(rb1), "s"(u0 * 64), "s"(L_B1) : "memory");
        }
        const u32 pi = opq((hf * 64 + lane) * 2);
        u32 pA = *(const GAS u16*)((const GAS char*)(logA + ch * 128) + pi), pI = *(const GAS u16*)((const GAS char*)(Aim + ch * 128) + pi);
        u32 pR = *(const GAS u16*)((const GAS char*)(Cre + ch * 128) + pi), pC = *(const GAS u16*)((const GAS char*)(Cim + ch * 128) + pi);
        u32 pT = logdt[ch], pD = Dskip[ch];
        __builtin_amdgcn_s_barrier();
        asm volatile("" ::: "memory");
        flg(IC<R_X>{});
        one = opq(1u);
        const u32 aCnt = opq(L_CNT + chl * 4), aCtl = opq(L_C);
        const v4u rus = mkrsrc(ws + OFF_U, 2 << 20);
        v4u selLo, selHi;
#pragma unroll
        for (int e = 0; e < 4; e++) {
            const u32 o1 = (h == (g & 1) && ((l15 & 7) >> 1) == e) ? ((l15 & 1) ? 0x3C000000u : 0x00003C00u) : 0u;
            selLo[e] = l15 < 8 ? o1 : 0u; selHi[e] = l15 < 8 ? 0u : o1;
        }
        pin(selLo); pin(selHi);
        const int m = 64 * hf + lane;
        const u32 aT = opq(w * 32768 + 16384 + l15 * 64 + g * 16), t0 = opq((2 * chl) * 32768 + 16384 + m * 4), kr = opq(L_KB + chl * 8320 + (1016 - 8 * m) * 2);
        u32 tg[6];
        {
            const bool r1 = m < 32, r2 = m < 64;
            tg[0] = opq(t0 + (r1 ? 528 : 0) * 4); tg[1] = opq(t0 + (r2 ? 400 : 0) * 4); tg[2] = opq(t0 + (r1 ? 560 : 272) * 4);
            tg[3] = opq(t0 + (r1 ? 592 : 272) * 4); tg[4] = opq(t0 + (r2 ? 464 : 1) * 4); tg[5] = opq(t0 + (r1 ? 624 : 1) * 4);
        }
        u32 kb[4], ua[4];
#pragma unroll
        for (int i = 0; i < 4; i++) {
            kb[i] = opq(L_KB + chl * 8320 + ((l15 & 1) ? (239 - l15 + 8 * g) * 2 : 4160 + (240 - l15 + 8 * g) * 2) - hf * 256 - 192 + i * 1024);
            const int os = 30 + 2 * l15 + g - 16 * hf - 4 * i;
            ua[i] = opq(L_U + os * 32 + ((chl ^ (os >> 3)) & 1) * 16);
        }
        const u32 aZ = opq(L_C + (lane & 3) * 4), aZ2 = opq(L_C + (lane & 3) * 4 + ((lane & 4) ? R_P + 4 : R_F + 7) * 16);
        const u32 vA = opq((lane >> 3) * 4096 + (lane & 7) * 16);
        pin(pA); pin(pI); pin(pR); pin(pC); pin(pT); pin(pD);
        {
            const u32 aRow = opq(L_KB + (l15 & 7) * 1088 + (l15 >> 3) * 512 + g * 16), aTr = opq(L_KB + (l15 >> 2) * 1088 + (128 * w + 16 * g + 4 * (lane & 3)) * 2);
            v4u ones = {0x3F803F80u, 0x3F803F80u, 0x3F803F80u, 0x3F803F80u}; pin(ones);
            spin_zero4(aF + R_X * 16);
            STAMP(1);
            v4u xb[8]; v2u xt[2][2];
#pragma unroll
            for (int s = 0; s < 8; s++) xb[s] = lds_ld<v4u>(aRow + s * 64);
#pragma unroll
            for (int i = 0; i < 2; i++) { xt[i][0] = lds_tr(aTr + i * 128); xt[i][1] = lds_tr(aTr + i * 128 + 4 * 1088); }
            tag = get_tag();
            f32x4 sm = {0.f, 0.f, 0.f, 0.f}, sq = {0.f, 0.f, 0.f, 0.f};
#pragma unroll
            for (int s = 0; s < 8; s++) { sq = mfma16(xb[s], xb[s], sq); sm = mfma16(ones, xb[s], sm); }
            float q = (l15 & 2) ? ((l15 & 1) ? sq[3] : sq[2]) : ((l15 & 1) ? sq[1] : sq[0]);
            q = g == (l15 >> 2) ? q : 0.f;
            { const auto r = __builtin_amdgcn_permlane32_swap(f2u(q), f2u(q), false, false); q = u2f(r[0]) + u2f(r[1]); }
            { const auto r = __builtin_amdgcn_permlane16_swap(f2u(q), f2u(q), false, false); q = u2f(r[0]) + u2f(r[1]); }
            const float mean = dpp_add<0x128>(sm[0]) * (1.f / 512.f);
            const float var = fmaxf(__builtin_fmaf(-mean, mean, dpp_add<0x128>(q) * (1.f / 512.f)), 0.f);
            const u32 p = pk_bf(mean, __builtin_amdgcn_sqrtf(rbf(rbf(var) + 1e-5f)));
            const float mb = bf_lo(p), rr = __builtin_amdgcn_rcpf(bf_hi(p));
            if (w == 0 && lane < 8) __hip_atomic_store((GAS u64*)(ws + OFF_ST + (P * 16 + j) * 64) + lane, ((u64)((lane & 1) ? (u32)(tag >> 32) : (u32)tag) << 32) | p, RLX_AGENT);
            sfor<2>([&](auto I_) {
                constexpr int i = decltype(I_)::value;
                v4u o;
                sfor<4>([&](auto E_) {
                    constexpr int e = decltype(E_)::value;
                    const u32 xv = xt[i][e >> 1][e & 1];
                    const u32 d = pk_bf(bf_lo(xv) - bcast<2 * e>(mb), bf_hi(xv) - bcast<2 * e + 1>(mb));
                    o[e] = pk_bf(bf_lo(d) * bcast<2 * e>(rr), bf_hi(d) * bcast<2 * e + 1>(rr));
                });
                st_sc1(o, rus, l16, tb * 8192 + w * 2048 + i * 1024);
            });
        }
        STAMP(2);
        __builtin_amdgcn_s_setprio(PRIO % 10);
        f32x4 kt[3];
        {
            const float dt = rbf(expf(bf_lo(pT)));
            const float zr = -rbf(expf(bf_lo(pA))) * dt, zi = bf_lo(pI) * dt;
            const float lr = zr * 1.44269504088896341f;
            const float th = zi * 0.159154943091895336f, tl = __builtin_fmaf(zi, 0.159154943091895336f, -th) + zi * 6.4206383e-09f;
            const cmult w1c = mkc(cexp_pow(lr, -th, -tl, 1.f)), qc = mkc(cexp_pow(lr, -th, -tl, 128.f)), q2 = mkc(cexp_pow(lr, -th, -tl, 256.f));
            const cmult w8 = mkc(cexp_pow(lr, th, tl, 8.f)), w32 = mkc(cexp_pow(lr, th, tl, 32.f));
            const u32 aR = pR & 0x7fffu, aI = pC & 0x7fffu;
            int ex = (int)(wave_max(aR > aI ? aR : aI) >> 7); ex = ex < 12 ? 12 : (ex > 240 ? 240 : ex);
            const float sdn = u2f((u32)(262 - ex) << 23), sup = u2f((u32)(ex - 8) << 23);
            auto transpose = [&](const f32x2 (&v)[16], v4u (&f)[4]) {
                f32x16 z; for (int i = 0; i < 16; i++) z[i] = 0.f;
                f32x16 dr = mfma32h((v4u){pk_h2(v[0].x, v[1].x), pk_h2(v[2].x, v[3].x), pk_h2(v[4].x, v[5].x), pk_h2(v[6].x, v[7].x)}, selLo, z);
                f32x16 di = mfma32h((v4u){pk_h2(v[0].y, v[1].y), pk_h2(v[2].y, v[3].y), pk_h2(v[4].y, v[5].y), pk_h2(v[6].y, v[7].y)}, selLo, z);
                dr = mfma32h((v4u){pk_h2(v[8].x, v[9].x), pk_h2(v[10].x, v[11].x), pk_h2(v[12].x, v[13].x), pk_h2(v[14].x, v[15].x)}, selHi, dr);
                di = mfma32h((v4u){pk_h2(v[8].y, v[9].y), pk_h2(v[10].y, v[11].y), pk_h2(v[12].y, v[13].y), pk_h2(v[14].y, v[15].y)}, selHi, di);
#pragma unroll
                for (int e = 0; e < 4; e++) {
                    f[0][e] = pk_h2(dr[2 * e], dr[2 * e + 1]); f[1][e] = pk_h2(dr[8 + 2 * e], dr[9 + 2 * e]);
                    f[2][e] = pk_h2(di[2 * e], di[2 * e + 1]); f[3][e] = pk_h2(di[8 + 2 * e], di[9 + 2 * e]);
                }
            };
            { f32x2 t0_ = w1c.b, t1_ = qc.b, t2_ = q2.b, t3_ = w8.b, t4_ = w32.b; pin(t0_); pin(t1_); pin(t2_); pin(t3_); pin(t4_); }
            STAMP(13);
            wait_vm<0>();
            if (lane == 0) __hip_atomic_store((GAS u64*)(ws + OFF_F1) + (w * 256 + tb) * FS1, tag, RLX_AGENT);
            STAMP(14);
            v4u fy[4], fx[4];
            f32x2 v[16], xo[8];
            v[0] = (f32x2){1.f, 0.f}; v[1] = w8.b; v[2] = cmul(w8.b, w8); v[3] = cmul(v[2], w8); v[4] = w32.b;
            xo[0] = (f32x2){bf_lo(pR) * sdn, -bf_lo(pC) * sdn};
#pragma unroll
            for (int i = 5; i < 16; i++) { v[i] = cmul(v[i - 4], w32); if (i < 12) xo[i - 4] = cmul(xo[i - 5], w1c); }
            STAMP(3); CST(2);
            transpose(v, fy);
            const float fin = 2.f * dt * sup;
            auto tile = [&](int t) {
                transpose(v, fx);
                f32x4 d = {0.f, 0.f, 0.f, 0.f};
#pragma unroll
                for (int s = 0; s < 4; s++) d = mfma16h(fy[s], fx[s], d);
                kt[t] = d * fin;
            };
            v[0] = xo[0]; v[1] = cmul(xo[0], qc);
#pragma unroll
            for (int s = 2; s < 16; s++) v[s] = cmul(v[s - 2], q2);
            const f32x2 last = cmul(v[14], q2);
            tile(0);
            v[0] = last; v[1] = xo[4]; v[9] = xo[2]; v[13] = xo[6];
            v[2] = cmul(v[1], qc); v[10] = cmul(v[9], qc); v[14] = cmul(v[13], qc);
#pragma unroll
            for (int s = 3; s < 9; s++) v[s] = cmul(v[s - 2], q2);
            v[11] = cmul(v[9], q2); v[12] = cmul(v[10], q2); v[15] = cmul(v[13], q2);
            const f32x2 last2 = cmul(v[14], q2);
            tile(1);
            v[0] = last2;
#pragma unroll
            for (int i = 0; i < 4; i++) { v[1 + 2 * i] = xo[2 * i + 1]; v[2 + 2 * i] = cmul(xo[2 * i + 1], qc); }
#pragma unroll
            for (int i = 9; i < 16; i++) v[i] = (f32x2){0.f, 0.f};
            tile(2);
        }
        CST(3); STAMP(17);
        pin(kt[0]); pin(kt[1]); pin(kt[2]);
        spin_zero4<1>(aZ2);
        STAMP(11);
#pragma unroll
        for (int t = 0; t < 3; t++) lds_st<f32x4>(aT + t * 1024, kt[t]);
        sig<C_A>(aCtl + (L_CNT - L_C), one);
        STAMP(4);
        spin(aCtl + (L_CNT - L_C) + 4 * C_A, 4);
        {
            auto tv = [&](u32 a) { return lds_ld<float>(a) + lds_ld<float>(a + 32768); };
            float k[9];
            k[0] = tv(t0); k[8] = tv(t0 + 4); k[4] = tv(t0 + 272 * 4);
            k[1] = tv(tg[0]); k[2] = tv(tg[1]); k[3] = tv(tg[2]); k[5] = tv(tg[3]); k[6] = tv(tg[4]); k[7] = tv(tg[5]);
            const float va = tv(t0 + 512), vb = tv(t0 + 516);
            if (m == 0) k[0] += bf_lo(pD);
            const u32 k10 = pk_bf(k[1], k[0]);
            lds_st<v4u>(kr + 2048, (v4u){pk_bf(k[7], k[6]), pk_bf(k[5], k[4]), pk_bf(k[3], k[2]), k10});
            lds_st<v4u>(kr + 2048 + 4160, (v4u){pk_bf(k[8], k[7]), pk_bf(k[6], k[5]), pk_bf(k[4], k[3]), pk_bf(k[2], k[1])});
            const u32 pa = pk_bf(va, va), pb = pk_bf(vb, vb), pba = pk_bf(vb, va);
            lds_st<v4u>(kr, (v4u){pb, (m & 1) ? pb : pba, pa, pa});
            lds_st<v4u>(kr + 4160, (v4u){pb, pb, (m & 1) ? pba : pa, pa});
            if (m < 4) lds_st<v4u>(L_KB + chl * 8320 + 4096 + (m & 1) * 16 + (m < 2 ? 4160 : 0), (v4u){m == 0 ? k10 >> 16 : 0u, 0u, 0u, 0u});
        }
        sig<C_K>(aCnt, one);
        STAMP(18);
        f32x4 cv[8];
        {
#pragma unroll
            for (int t = 0; t < 8; t++) cv[t] = (f32x4){0.f, 0.f, 0.f, 0.f};
            v4u A[8], B[8];
            auto rdA = [&](int r, int a) { const u32 p = kb[(7 - a) >> 1] + ((7 - a) & 1) * 512 + (3 - r) * 64; return (v4u){lds_ld<u32>(p), lds_ld<u32>(p + 4), lds_ld<u32>(p + 8), lds_ld<u32>(p + 12)}; };
            auto rdB = [&](int r, int d) { return lds_ld<v4u>(ua[r] + d * 1024); };
            spin(aCnt + 4 * C_K, 2);
#pragma unroll
            for (int i = 0; i < 8; i++) A[7 - i] = rdA(0, 7 - i);
            STAMP(19);
            spin_zero4(aZ + (L_UF - L_C));
            STAMP(6);
#pragma unroll
            for (int i = 0; i < 8; i++) B[i] = rdB(0, i);
            if (b == 0 && w == 0 && lane == 0) __hip_atomic_store((GAS u64*)ws, tag, RLX_AGENT);
            CST(0);
#pragma unroll
            for (int r = 0; r < 4; r++) {
#pragma unroll
                for (int a = 7; a >= 0; a--) {
#pragma unroll
                    for (int d = 0; d + a < 8; d++) {
                        cv[a + d] = mfma16(A[a], B[d], cv[a + d]);
                        if (a == 0 && r < 3) { SB0(); B[d] = rdB(r + 1, d); SB0(); }
                    }
                    if (r < 3) { SB0(); A[a] = rdA(r + 1, a); SB0(); }
                }
            }
        }
        STAMP(7); CST(1);
        {
            const u32 mine = opq(w * 32768 + 16384 + l16), other = opq((w ^ 1) * 32768 + 16384 + l16);
#pragma unroll
            for (int t = 0; t < 4; t++) lds_st<f32x4>(mine + t * 1024, sel4(hf, cv[t], cv[4 + t]));
            sig<C_E>(aCnt, one);
            sig<C_D>(aCtl + (L_CNT - L_C), one);
            spin(aCnt + 4 * C_E, 2);
            u32 o[8];
#pragma unroll
            for (int t = 0; t < 4; t++) {
                const f32x4 y = sel4(hf, cv[4 + t], cv[t]) + lds_ld<f32x4>(other + t * 1024);
                o[2 * t] = pk_bf(y[0], y[1]); o[2 * t + 1] = pk_bf(y[2], y[3]);
            }
            const v4u ras = mkrsrc(ws + OFF_A, 2 << 20);
            st_sc1((v4u){o[0], o[1], o[2], o[3]}, ras, l16, ch * 4096 + hf * 2048);
            st_sc1((v4u){o[4], o[5], o[6], o[7]}, ras, l16, ch * 4096 + hf * 2048 + 1024);
            STAMP(15);
            wait_vm<0>();
            STAMP(8);
            if (lane == 0) __hip_atomic_store((GAS u64*)(ws + OFF_F3) + (hf * 512 + ch) * FS3, tag, RLX_AGENT);
            spin(aCtl + (L_CNT - L_C) + 4 * C_D, 4);
            poll_flags((const GAS u64*)(ws + OFF_F3) + ((tr >> 4) * 512 + tc * 64 + 16 * w + l15) * FS3, tag);
            STAMP(9);
#pragma unroll
            for (int i = 0; i < 2; i++)
                glds_sc1(ras, vA, (tc * 64 + 16 * w + 8 * i) * 4096 + (tr >> 3) * 1024 + (tr & 3) * 256 + ((tr >> 2) & 1) * 128, L_U + w * 2048 + i * 1024);
            const u32 ta = opq(L_U + w * 2048 + (l15 >> 2) * 128 + g * 32 + (lane & 3) * 8), aw = opq(L_KB + lane * 144 + w * 32);
            wait_vm<0>();
            const v2u a0 = lds_tr(ta), a1 = lds_tr(ta + 512), a2 = lds_tr(ta + 1024), a3 = lds_tr(ta + 1536);
            lds_st<v4u>(aw, (v4u){a0.x, a0.y, a1.x, a1.y});
            lds_st<v4u>(aw + 16, (v4u){a2.x, a2.y, a3.x, a3.y});
            STAMP(10);
        }
        wait_lgkm0();
        __builtin_amdgcn_s_barrier();
    }
    if (lane == 0 && w == 0 && role == PROLE) { GAS u32* q = (GAS u32*)(ws + (40 << 20)) + b * 4; q[0] = t_start; q[1] = probe_t; q[2] = (u32)__builtin_amdgcn_s_memrealtime(); }
}

#if defined(__HIP_DEVICE_COMPILE__) && __HIP_DEVICE_COMPILE__
#pragma clang attribute pop
#endif

__global__ void tail_kernel(char* ws) {
    const u32 t0 = (u32)__builtin_amdgcn_s_memrealtime();
    const int lane = threadIdx.x;
    int smax = -(1 << 30), pmax = -(1 << 30), emax = -(1 << 30), psum = 0;
    for (int i = 0; i < 4; i++) { const u32* q = (const u32*)(ws + (40 << 20)) + (i * 64 + lane) * 4; int a = (int)(q[0] - t0), p = (int)(q[1] - t0), e = (int)(q[2] - t0); smax = a > smax ? a : smax; pmax = p > pmax ? p : pmax; emax = e > emax ? e : emax; psum += p; }
    for (int m = 32; m >= 1; m >>= 1) { int a = __shfl_xor(smax, m), p = __shfl_xor(pmax, m), e = __shfl_xor(emax, m); smax = a > smax ? a : smax; pmax = p > pmax ? p : pmax; emax = e > emax ? e : emax; psum += __shfl_xor(psum, m); }
    int extra = QTY == 0 ? 0 : QTY == 1 ? -emax : QTY == 2 ? emax - smax : QTY == 3 ? psum / 256 - smax : pmax - smax;
    extra = __builtin_amdgcn_readfirstlane(extra) * 2; if (extra > 20000) extra = 20000;
    while ((int)((u32)__builtin_amdgcn_s_memrealtime() - t0) < extra + 300) {}
}
extern "C" void kernel_launch(void* const* d_in, const int* in_sizes, int n_in, void* d_out, int out_size, void* d_ws, size_t ws_size,
                              hipStream_t stream) {
    fused_kernel<<<256, 768, 0, stream>>>((const u16*)d_in[0], (const u16*)d_in[1], (const u16*)d_in[2], (const u16*)d_in[3], (const u16*)d_in[4],
                                          (const u16*)d_in[5], (const u16*)d_in[6], (const u16*)d_in[8], (const u16*)d_in[9], (const u16*)d_in[10],
                                          (const u16*)d_in[11], (u16*)d_out, (char*)d_ws);
    tail_kernel<<<1, 64, 0, stream>>>((char*)d_ws);
}
```

```cpp
#define PF 2
#include <hip/hip_runtime.h>
#include <stdint.h>
#include <utility>

typedef unsigned short u16;
typedef unsigned int u32;
typedef unsigned long long u64;
typedef u32 v4u __attribute__((ext_vector_type(4)));
typedef u32 v2u __attribute__((ext_vector_type(2)));
typedef float f32x2 __attribute__((ext_vector_type(2)));
typedef float f32x4 __attribute__((ext_vector_type(4)));
typedef float f32x16 __attribute__((ext_vector_type(16)));
typedef __bf16 bf16x8 __attribute__((ext_vector_type(8)));
typedef __bf16 bf16x2 __attribute__((ext_vector_type(2)));
typedef _Float16 f16x8 __attribute__((ext_vector_type(8)));
typedef _Float16 f16x2 __attribute__((ext_vector_type(2)));
typedef short s16x4 __attribute__((ext_vector_type(4)));
#define GAS __attribute__((address_space(1)))
#define LAS __attribute__((address_space(3)))
#define DI __device__ __forceinline__
#define RLX_AGENT __ATOMIC_RELAXED, __HIP_MEMORY_SCOPE_AGENT
#define RLX_WG __ATOMIC_RELAXED, __HIP_MEMORY_SCOPE_WORKGROUP
#define SB0() __builtin_amdgcn_sched_barrier(0)
#define CHST 0
#define PSLEEP 2
#define ROT 2
#define POL 1
#define PDS 0
#define THR 0
#define HS 0
#define AB 0
#ifndef NV2
#define NV2 8
#endif
#define WING 10
#define HROT 0
#define GP2 0
#define NE 3
#define PRIO 21

#define NOH_OK
#define NWV 12
constexpr int OFF_ST = 16384, OFF_F1 = 128 << 10, OFF_F2 = 256 << 10, OFF_F3 = 512 << 10;
constexpr int FS1 = 8, FS2 = 16, FS3 = 16;
constexpr int OFF_U = 1 << 20, OFF_A = 4 << 20, OFF_H = 8 << 20;
constexpr int H_BYTES = 64 * 43 * 2048;
constexpr int L_C = 131072;
constexpr int L_FLG = L_C, L_UF = L_C + 320, L_E = L_C + 768, L_LNS = L_C + 352, L_CNT = L_C + 416, L_B1 = L_C + 512, L_STAT = L_C + 1024, L_CSP = L_C + 29696;
enum { R_F = 0, R_P = 8, R_P7 = 13, R_PK = 15, R_X = 14, R_V1 = 16, R_V2 = 17, R_V3 = 18 };
constexpr int L_U = L_C + 3072, L_KB = L_C + 12288;
constexpr int SPIN_CAP = 1 << 16;
enum { C_A = 0, C_D = 1, C_K = 2, C_E = 4, C_PK = 8, C_RD = 12 };

#define STAMP(i)
#define CST(i)
#define TIMED(x) x

#if defined(__HIP_DEVICE_COMPILE__) && __HIP_DEVICE_COMPILE__
#pragma clang attribute push(__attribute__((target("no-packed-fp32-ops"))), apply_to = function)
#endif
DI float u2f(u32 v) { return __builtin_bit_cast(float, v); }
DI u32 f2u(float v) { return __builtin_bit_cast(u32, v); }
DI bool any_(bool c) { return __builtin_amdgcn_ballot_w64(c) != 0; }
DI bool all_(bool c) { return __builtin_amdgcn_ballot_w64(!c) == 0; }
template <typename T> DI T lds_ld(u32 a) { return *(LAS T*)(size_t)a; }
template <typename T> DI void lds_st(u32 a, T v) { *(LAS T*)(size_t)a = v; }
DI v2u lds_tr(u32 a) { return __builtin_bit_cast(v2u, __builtin_amdgcn_ds_read_tr16_b64_v4i16((LAS s16x4*)(size_t)a)); }
DI v4u mkrsrc(const void* p, u32 bytes) { u64 a = (u64)p; v4u r; r.x = (u32)a; r.y = (u32)(a >> 32) & 0xffffu; r.z = bytes; r.w = 0x00020000u; return r; }
DI void glds(v4u rsrc, u32 voff, u32 soff, u32 lds) {
    asm volatile("s_mov_b32 m0, %3\n\ts_nop 0\n\tbuffer_load_dwordx4 %0, %1, %2 offen lds" :: "v"(voff), "s"(rsrc), "s"(soff), "s"(lds) : "memory");
}
template <int LOFF> DI u32 glds_first(v4u rsrc, u32 voff, u32 sa, u32 sb, u32 lbase) {
    u32 so;
    asm volatile("s_add_u32 m0, %5, %6\n\ts_add_u32 %0, %3, %4\n\tbuffer_load_dwordx4 %1, %2, %0 offen lds" : "=&s"(so) : "v"(voff), "s"(rsrc), "s"(sa), "s"(sb), "s"(lbase), "i"(LOFF) : "memory", "scc");
    return so;
}
template <int LOFF, int SOFF> DI void glds_imm(v4u rsrc, u32 voff, u32 sa, u32 lbase) {
    u32 so;
    asm volatile("s_add_u32 m0, %4, %5\n\ts_add_u32 %0, %3, %6\n\tbuffer_load_dwordx4 %1, %2, %0 offen lds" : "=&s"(so) : "v"(voff), "s"(rsrc), "s"(sa), "s"(lbase), "i"(LOFF), "i"(SOFF) : "memory", "scc");
}
template <int OFF> DI void glds_next(v4u rsrc, u32 voff, u32 so) {
    asm volatile("buffer_load_dwordx4 %0, %1, %2 offen offset:%3 lds" :: "v"(voff), "s"(rsrc), "s"(so), "i"(OFF) : "memory");
}
template <int LOFF> DI void flag_imm(v4u rsrc, u32 lbase) {
    asm volatile("s_add_u32 m0, %2, %3\n\ts_mov_b64 exec, 1\n\tbuffer_load_dword off, %0, %1 lds\n\ts_mov_b64 exec, -1" :: "s"(rsrc), "s"(rsrc.z), "s"(lbase), "i"(LOFF) : "memory", "scc");
}
DI void glds_sc1(v4u rsrc, u32 voff, u32 soff, u32 lds) {
    asm volatile("s_mov_b32 m0, %3\n\ts_nop 0\n\tbuffer_load_dwordx4 %0, %1, %2 offen sc1 lds" :: "v"(voff), "s"(rsrc), "s"(soff), "s"(lds) : "memory");
}
DI void flag_dma(v4u rsrc, u32 lds) {
    asm volatile("s_mov_b32 m0, %2\n\ts_mov_b64 exec, 1\n\tbuffer_load_dword off, %0, %1 lds\n\ts_mov_b64 exec, -1" :: "s"(rsrc), "s"(rsrc.z), "s"(lds) : "memory");
}
DI void st_sc1(v4u v, v4u rsrc, u32 voff, u32 soff) {
    asm volatile("buffer_store_dwordx4 %0, %1, %2, %3 offen sc1\n\ts_nop 1" :: "v"(v), "v"(voff), "s"(rsrc), "s"(soff) : "memory");
}
DI u32 opq(u32 a) { asm volatile("" : "+v"(a)); return a; }
template <typename T> DI T lds_ldv(u32 a) { return *(volatile LAS T*)(size_t)a; }
template <int SLEEP = 0> DI void spin_zero4(u32 a) {
    int n = 0;
    while (any_(lds_ldv<u32>(a) != 0) && ++n < (SPIN_CAP << 4)) { if (SLEEP) __builtin_amdgcn_s_sleep(1); }
    asm volatile("" ::: "memory");
}
template <int N> DI void wait_vm() { asm volatile("s_waitcnt vmcnt(%0)" :: "i"(N) : "memory"); }
DI void wait_lgkm0() { asm volatile("s_waitcnt lgkmcnt(0)" ::: "memory"); }
DI u64 mix64(u64 e) { e += 0x9E3779B97F4A7C15ull; e ^= e >> 31; e *= 0xBF58476D1CE4E5B9ull; e ^= e >> 29; return e; }
DI u64 get_tag() { const v2u e = lds_ldv<v2u>(L_E); return mix64(((u64)(u32)__builtin_amdgcn_readfirstlane(e.y) << 32) | (u32)__builtin_amdgcn_readfirstlane(e.x)); }
template <int C> DI void sig(u32 a, u32 one) { asm volatile("s_mov_b64 exec, 1\n\tds_add_u32 %0, %1 offset:%2\n\ts_mov_b64 exec, -1" :: "v"(a), "v"(one), "i"(4 * C) : "memory"); }
template <int OFF> DI void clr(u32 a, u32 zero) { asm volatile("s_mov_b64 exec, 1\n\tds_write_b32 %0, %1 offset:%2\n\ts_mov_b64 exec, -1" :: "v"(a), "v"(zero), "i"(OFF) : "memory"); }
DI void spin(u32 a, u32 target) {
    int n = 0;
    while ((u32)__builtin_amdgcn_readfirstlane(lds_ldv<u32>(a)) < target && ++n < (SPIN_CAP << 4)) {}
    asm volatile("" ::: "memory");
}
DI void poll_flags(const GAS u64* p, u64 tag) {
    int n = 0;
    while (true) {
        u64 f = __hip_atomic_load(p, RLX_AGENT);
        if (all_(f == tag) || ++n > SPIN_CAP) break;
        __builtin_amdgcn_s_sleep(PSLEEP);
    }
    asm volatile("" ::: "memory");
}
DI float bf_lo(u32 v) { return u2f(v << 16); }
DI float bf_hi(u32 v) { return u2f(v & 0xffff0000u); }
DI u32 pk_bf(float a, float b) { f32x2 v = {a, b}; return __builtin_bit_cast(u32, __builtin_convertvector(v, bf16x2)); }
typedef short s16x2 __attribute__((ext_vector_type(2)));
DI u32 relu_pk(u32 v) { return __builtin_bit_cast(u32, __builtin_elementwise_max(__builtin_bit_cast(s16x2, v), (s16x2){0, 0})); }
DI u32 umax(u32 a, u32 b) { return a > b ? a : b; }
DI float rbf(float a) { return bf_lo(pk_bf(a, a)); }
DI u32 pk_h(f32x2 v) { return __builtin_bit_cast(u32, __builtin_convertvector(v, f16x2)); }
DI f32x16 mfma32(v4u a, v4u b, f32x16 c) { return __builtin_amdgcn_mfma_f32_32x32x16_bf16(__builtin_bit_cast(bf16x8, a), __builtin_bit_cast(bf16x8, b), c, 0, 0, 0); }
DI f32x4 mfma16(v4u a, v4u b, f32x4 c) { return __builtin_amdgcn_mfma_f32_16x16x32_bf16(__builtin_bit_cast(bf16x8, a), __builtin_bit_cast(bf16x8, b), c, 0, 0, 0); }
DI f32x4 mfma16h(v4u a, v4u b, f32x4 c) { return __builtin_amdgcn_mfma_f32_16x16x32_f16(__builtin_bit_cast(f16x8, a), __builtin_bit_cast(f16x8, b), c, 0, 0, 0); }
template <int CTRL> DI float dpp_add(float v) { return v + __builtin_bit_cast(float, __builtin_amdgcn_update_dpp(0, __builtin_bit_cast(int, v), CTRL, 0xf, 0xf, true)); }
template <int CTRL> DI u32 dpp_max(u32 v) { const u32 o = (u32)__builtin_amdgcn_update_dpp(0, (int)v, CTRL, 0xf, 0xf, true); return v > o ? v : o; }
template <int T> DI float bcast(float v) { return __builtin_bit_cast(float, __builtin_amdgcn_update_dpp(0, __builtin_bit_cast(int, v), 0x150 + T, 0xf, 0xf, true)); }
DI float rl(float v, int l) { return __builtin_bit_cast(float, __builtin_amdgcn_readlane(__builtin_bit_cast(int, v), l)); }
DI f32x2 wave_sum2(f32x2 v) {
    float a = v.x, c = v.y;
    a = dpp_add<0xB1>(a); c = dpp_add<0xB1>(c); a = dpp_add<0x4E>(a); c = dpp_add<0x4E>(c);
    a = dpp_add<0x141>(a); c = dpp_add<0x141>(c); a = dpp_add<0x140>(a); c = dpp_add<0x140>(c);
    return ((f32x2){rl(a, 0), rl(c, 0)} + (f32x2){rl(a, 16), rl(c, 16)}) + ((f32x2){rl(a, 32), rl(c, 32)} + (f32x2){rl(a, 48), rl(c, 48)});
}
DI u32 wave_max(u32 v) {
    v = dpp_max<0xB1>(v); v = dpp_max<0x4E>(v); v = dpp_max<0x141>(v); v = dpp_max<0x140>(v);
    const u32 a = __builtin_amdgcn_readlane(v, 0), c = __builtin_amdgcn_readlane(v, 16), d = __builtin_amdgcn_readlane(v, 32), e = __builtin_amdgcn_readlane(v, 48);
    const u32 p = a > c ? a : c, q = d > e ? d : e;
    return p > q ? p : q;
}
struct cmult { f32x2 b, n; };
DI cmult mkc(f32x2 b) { cmult c; c.b = b; c.n = (f32x2){-b.y, b.x}; return c; }
DI f32x2 cmul(f32x2 a, const cmult& c) { return __builtin_elementwise_fma((f32x2){a.x, a.x}, c.b, (f32x2){a.y, a.y} * c.n); }
DI f32x16 mfma32h(v4u a, v4u b, f32x16 c) { return __builtin_amdgcn_mfma_f32_32x32x16_f16(__builtin_bit_cast(f16x8, a), __builtin_bit_cast(f16x8, b), c, 0, 0, 0); }
DI u32 pk_h2(float a, float b) { return pk_h((f32x2){a, b}); }
DI f32x2 cexp_pow(float lr, float th, float tl, float e) {
    float a = __builtin_amdgcn_fractf(th * e) + tl * e;
    float m = __builtin_amdgcn_exp2f(lr * e);
    f32x2 r; r.x = m * __builtin_amdgcn_cosf(a); r.y = m * __builtin_amdgcn_sinf(a); return r;
}
DI f32x4 sel4(bool c, f32x4 a, f32x4 b) { return c ? a : b; }
constexpr int w2loc(int n) { return n < 16 ? n : n < 28 ? n + 4 : n - 28; }
template <class F, int... I> DI void sfor_(F&& f, std::integer_sequence<int, I...>) { (f(std::integral_constant<int, I>{}), ...); }
template <int N, class F> DI void sfor(F&& f) { sfor_(f, std::make_integer_sequence<int, N>{}); }
template <int I> using IC = std::integral_constant<int, I>;
template <typename T> DI void pin(T& v) { asm volatile("" : "+v"(v)); }

__global__ void __launch_bounds__(768, 3)
fused_kernel(const u16* __restrict__ x, const u16* __restrict__ logA, const u16* __restrict__ Aim, const u16* __restrict__ Cre,
             const u16* __restrict__ Cim, const u16* __restrict__ logdt, const u16* __restrict__ Dskip, const u16* __restrict__ w1,
             const u16* __restrict__ b1, const u16* __restrict__ w2, const u16* __restrict__ b2, u16* __restrict__ out, char* ws) {
    __shared__ __attribute__((aligned(16))) char smem[163840];
    asm volatile("" :: "v"((u32)(size_t)(LAS char*)smem));
    const int tid = threadIdx.x, lane = tid & 63, wave = __builtin_amdgcn_readfirstlane(tid >> 6), w = wave & 3, role = wave >> 2;
    const int b = blockIdx.x, xcd = b & 7, slot = b >> 3;
    const int P = 4 * (xcd >> 1) + (slot >> 3), j = 8 * (xcd & 1) + (slot & 7);
    const int tr = 4 * xcd + (slot >> 3), tc = slot & 7;
    const int u0 = (j * 43) >> 4;
    STAMP(0);
    if ((PF & 1) && wave == 4) {
        u64 pc; asm volatile("s_getpc_b64 %0" : "=s"(pc));
        const v4u rc = mkrsrc((const void*)(pc & ~255ull), 29440);
        glds(rc, lane * 16, slot * 1024, L_C + 2048);
    }
    const int h = lane >> 5, l31 = lane & 31, l15 = lane & 15, g = lane >> 4;
    const u32 l16 = lane * 16, lw = w * 32768, w4 = w * 4;
    const int rho = HROT ? ((slot >> 3) + slot) & 3 : (slot >> 3);
    const int tb = 64 * (P >> 2) + 32 * (j & 1) + 16 * ((P >> 1) & 1) + 2 * (j >> 1) + (P & 1);
    const int cp = 32 * xcd + slot, chl = w >> 1, hf = w & 1, ch = 2 * cp + chl;
    asm volatile("" :: "s"(x), "s"(logA), "s"(Aim), "s"(Cre), "s"(Cim), "s"(logdt), "s"(Dskip), "s"(w1), "s"(b1), "s"(w2), "s"(b2), "s"(out), "s"(ws));
    const v4u rx = mkrsrc(x, 2 << 20);
    const u32 aF = opq(L_FLG + (lane & 3) * 4);
    auto flg = [&](auto R_) { flag_imm<L_FLG + decltype(R_)::value * 16>(rx, w4); };
    u32 aV0, aV1, aFw, aCn, one, f2o;
    auto pre2 = [&]() {
        aV0 = opq(lw + (4 * h + (l15 >> 2)) * 128 + ((l15 >> 3) & 1) * 64 + (g & 1) * 32 + (lane & 3) * 8); aV1 = opq(aV0 ^ 64);
        aFw = opq(L_FLG + w4); aCn = opq(L_CNT + w4); one = opq(1u);
        const int jj = 4 * w + (lane < 4 ? lane : 4);
        f2o = opq(((2 * tr + role) * 16 + (jj < 15 ? jj : 15)) * FS2 * 8);
    };

    auto rdV = [&](int s, int dt) { v2u lo = lds_tr((dt ? aV1 : aV0) + w2loc(2 * s) * 1024), hi = lds_tr((dt ? aV1 : aV0) + w2loc(2 * s + 1) * 1024); return (v4u){lo.x, lo.y, hi.x, hi.y}; };
    auto gemm2 = [&](auto ROLE_, v4u (&stage)[16], u64 f2, u64 tag) __attribute__((always_inline)) {
        constexpr int role = decltype(ROLE_)::value;
        const GAS u64* f2p = (const GAS u64*)(ws + OFF_F2 + f2o);
        __builtin_amdgcn_s_setprio(role ? GP2 : 0);
        v4u park[8][2];
        if (role) {
            SB0();
            spin_zero4<1>(aFw + R_V1 * 16);
            STAMP(9);
            f2 = __hip_atomic_load(f2p, RLX_AGENT);
            SB0();
#pragma unroll
            for (int s = 0; s < 8; s++) { park[s][0] = rdV(s, 0); park[s][1] = rdV(s, 1); }
            clr<R_PK * 16>(aFw, opq(0u));
            SB0();
        }
        STAMP(10);
        if (!all_(f2 == tag)) poll_flags(f2p, tag);
        STAMP(5);
        const __amdgpu_buffer_rsrc_t rh = __builtin_amdgcn_make_buffer_rsrc(ws + OFF_H, 0, H_BYTES, 0x00020000);
        u32 sh2[4], sh2t[4];
#pragma unroll
        for (int k = 0; k < 4; k++) { sh2[k] = ((2 * tr + role) * 43 + 11 * w) * 2048 + (6 * rho - (k + rho >= 4 ? 22 : 0)) * 1024; sh2t[k] = (w == 3 && k + rho == 3) ? (u32)H_BYTES : sh2[k]; }
        constexpr int WIN = WING;
        v4u hq[WIN], vb[3][2];
        auto ldH = [&](int s) { hq[s % WIN] = __builtin_amdgcn_raw_buffer_load_b128(rh, l16, ((s + 2) % 6 >= 4 ? sh2t[(s + 2) / 6] : sh2[(s + 2) / 6]) + s * 1024, 16); };
#pragma unroll
        for (int s = 0; s < WIN; s++) ldH(s);
        const int fr = tid >> 3;
        const u32 fin_off = (512 * (tr >> 3) + 256 * ((fr >> 2) & 1) + 128 * ((tr >> 2) & 1) + 16 * (fr >> 3) + 4 * (tr & 3) + (fr & 3)) * 512 + tc * 64 + (tid & 7) * 8;
        v4u fin_x, fin_b; u32 fin_s, fv;
        f32x16 ac2[2];
#pragma unroll
        for (int i = 0; i < 16; i++) { ac2[0][i] = 0.f; ac2[1][i] = 0.f; }
        if (!role) {
            const u32 aSt = opq(lw + l16), zero = opq(0u);
            SB0();
            spin_zero4(opq(aFw + ((lane & 1) ? R_PK : R_V2) * 16));
            STAMP(9);
#pragma unroll
            for (int s = 0; s < 8; s++) {
                park[s][0] = rdV(s, 0); park[s][1] = rdV(s, 1);
                asm volatile("" ::: "memory");
                lds_st<v4u>(aSt + 2 * s * 1024, stage[2 * s]); lds_st<v4u>(aSt + (2 * s + 1) * 1024, stage[2 * s + 1]);
                SB0();
            }
            clr<R_V3 * 16>(aFw, zero);
            STAMP(13);
        }
#pragma unroll
        for (int s = 0; s < 22; s++) {
            SB0();
            if (role && (s == 4 || s == 10)) fv = lds_ldv<u32>(aFw + (s == 4 ? R_V2 : R_V3) * 16);
            if (role && (s == 6 || s == 12)) { if (__builtin_expect(any_(fv != 0), 0)) spin_zero4(aFw + (s == 6 ? R_V2 : R_V3) * 16); asm volatile("" ::: "memory"); }
            if (s + 2 >= 8 && s + 2 < 22) { vb[(s + 2) % 3][0] = rdV(s + 2, 0); vb[(s + 2) % 3][1] = rdV(s + 2, 1); }
            if (s == 19) sig<C_RD>(aCn, one);
            if (s == 21) fv = lds_ldv<u32>(aCn + 4 * C_RD);
            ac2[0] = mfma32(s < 8 ? park[s & 7][0] : vb[s % 3][0], hq[s % WIN], ac2[0]);
            ac2[1] = mfma32(s < 8 ? park[s & 7][1] : vb[s % 3][1], hq[s % WIN], ac2[1]);
            SB0();
            if (s + WIN < 22) ldH(s + WIN);
            if (s == 14) {
                fin_x = *(const GAS v4u*)(x + fin_off); fin_b = *(const GAS v4u*)(b2 + tc * 64 + (tid & 7) * 8);
                fin_s = __hip_atomic_load((GAS u32*)(ws + OFF_ST + (tr >> 1) * 1024 + (fr >> 2) * 64 + (tr & 1) * 32 + (fr & 3) * 8), RLX_AGENT);
            }
        }
        SB0();
        STAMP(7); CST(3);
        if (__builtin_expect(any_(fv < 2), 0)) spin(aCn + 4 * C_RD, 2);
        asm volatile("" ::: "memory");
        const u32 da = opq(lw + role * 20480 + l31 * 272 + h * 16);
#pragma unroll
        for (int dt = 0; dt < 2; dt++)
#pragma unroll
            for (int q = 0; q < 4; q++)
                lds_st<f32x4>(da + (32 * dt + 8 * q) * 4, (f32x4){ac2[dt][4 * q], ac2[dt][4 * q + 1], ac2[dt][4 * q + 2], ac2[dt][4 * q + 3]});
        f32x4 s0 = {bf_lo(fin_x.x) + bf_lo(fin_b.x), bf_hi(fin_x.x) + bf_hi(fin_b.x), bf_lo(fin_x.y) + bf_lo(fin_b.y), bf_hi(fin_x.y) + bf_hi(fin_b.y)};
        f32x4 s1 = {bf_lo(fin_x.z) + bf_lo(fin_b.z), bf_hi(fin_x.z) + bf_hi(fin_b.z), bf_lo(fin_x.w) + bf_lo(fin_b.w), bf_hi(fin_x.w) + bf_hi(fin_b.w)};
        const u32 ra = opq(role * 20480 + (fr & 31) * 272 + (tid & 7) * 32), rb = opq(ra + 65536), rc = opq(L_KB + fr * 144 + (tid & 7) * 16);
        float rs2 = __builtin_amdgcn_rcpf(bf_hi(fin_s)); rs2 *= rs2;
        asm volatile("" : "+v"(s0), "+v"(s1), "+v"(rs2));
        wait_lgkm0();
        __builtin_amdgcn_s_barrier();
        asm volatile("" ::: "memory");
        STAMP(11);
        const v4u av = lds_ld<v4u>(rc);
        s0 += ((lds_ld<f32x4>(ra) + lds_ld<f32x4>(ra + 32768)) + (lds_ld<f32x4>(rb) + lds_ld<f32x4>(rb + 32768))) * rs2;
        s1 += ((lds_ld<f32x4>(ra + 16) + lds_ld<f32x4>(ra + 32768 + 16)) + (lds_ld<f32x4>(rb + 16) + lds_ld<f32x4>(rb + 32768 + 16))) * rs2;
        s0 += (f32x4){bf_lo(av.x), bf_hi(av.x), bf_lo(av.y), bf_hi(av.y)};
        s1 += (f32x4){bf_lo(av.z), bf_hi(av.z), bf_lo(av.w), bf_hi(av.w)};
        *(GAS v4u*)(out + fin_off) = (v4u){pk_bf(s0[0], s0[1]), pk_bf(s0[2], s0[3]), pk_bf(s1[0], s1[1]), pk_bf(s1[2], s1[3])};
    };

    if (role == 0) {
        v4u stage[16]; u64 f2, tag;
        const int rot = ROT == 0 ? 0 : ROT == 1 ? (((slot >> 3) + slot) & 1) * 4 : ROT == 2 ? (((slot >> 3) + slot) & 3) * 2 : (2 * (slot >> 3) + (slot & 7)) & 7;
        const v4u rw1 = mkrsrc(w1, 512 * 1376 * 2);
        const u32 sx = (512 * (P >> 2) + 128 * ((P >> 1) & 1) + 64 * (w & 1) + 8 * (P & 1) + 4 * (w >> 1)) * 1024;
        const u32 sw1 = 16 * w * 2752 + u0 * 64, sw2 = 352 * w * 1024 + tc * 128;
        u32 vx[4], vw[3];
#pragma unroll
        for (int i = 0; i < 3; i++) { const int p = 64 * i + lane, r = (p * 171) >> 11; vw[i] = r * (2752 - 192) + l16; }
#pragma unroll
        for (int i = 0; i < 4; i++) vx[i] = (((h * 256 + ((lane >> 3) & 3)) * 1024 + ((lane & 7) ^ g) * 16) ^ ((i & 1) * 64)) + i * (16384 - 1024);
        auto ldX = [&](auto C_) { constexpr int c = decltype(C_)::value;
            const u32 so = glds_first<(16 + (c % 4) * 4) * 1024>(rx, vx[0], sx, ((c + rot) & 7) * 128, lw);
            glds_next<1024>(rx, vx[1], so); glds_next<2048>(rx, vx[2], so); glds_next<3072>(rx, vx[3], so); };
        auto ldW = [&](auto C_) { constexpr int c = decltype(C_)::value;
            const u32 so = glds_first<3 * (c % 5) * 1024>(rw1, vw[0], sw1, ((c + rot) & 7) * (64 * 2752), lw);
            glds_next<1024>(rw1, vw[1], so); glds_next<2048>(rw1, vw[2], so); };
        ldW(IC<0>{}); ldX(IC<0>{});
        __builtin_amdgcn_s_barrier();
        asm volatile("" ::: "memory");
        flg(IC<R_F>{});
        const u32 vv = (lane >> 3) * 1024 + ((lane & 7) ^ ((g & 1) << 2)) * 16;
        u32 sv[4];
#pragma unroll
        for (int k = 0; k < 4; k++) sv[k] = sw2 + (12 * rho - (k + rho >= 4 ? 44 : 0)) * 8192;
        const __amdgpu_buffer_rsrc_t rv = __builtin_amdgcn_make_buffer_rsrc((void*)w2, 0, 512 * 1376 * 2, 0x00020000);
        auto ldV = [&](int r) { stage[r] = __builtin_amdgcn_raw_buffer_load_b128(rv, vv, sv[(r + 32) / 12] + (r + 28) * 8192, 0); };
        sfor<7>([&](auto I) {
            constexpr int c = decltype(I)::value + 1;
            if constexpr (THR && c >= 2) wait_vm<THR>();
            if (!(AB & 4)) ldW(IC<c>{});
            if constexpr (c >= 4) {
                SB0();
#pragma unroll
                for (int k = 0; k < NE; k++) ldV(NE * (c - 4) + k);
                SB0();
            }
            if constexpr (c >= 4) TIMED(spin_zero4<1>(aF + (R_P + c - 4) * 16));
            if constexpr (THR && c >= 2) wait_vm<THR>();
            if (!(AB & 4)) ldX(IC<c>{});
            flg(IC<R_F + c>{});
        });
        STAMP(1);
        {
            const u32 vS = ((lane >> 1) * 4 + (lane & 1)) * 16;
            const v4u rst = mkrsrc(ws + OFF_ST, 16384);
            asm volatile("s_mov_b32 m0, %3\n\ts_mov_b64 exec, 0xffff\n\tbuffer_load_dwordx4 %0, %1, %2 offen sc1 lds\n\ts_mov_b64 exec, -1" :: "v"(vS), "s"(rst), "s"(P * 1024 + (w & 1) * 512 + (w >> 1) * 32), "s"(L_STAT + w * 256) : "memory");
        }
        spin_zero4<1>(aF + R_X * 16);
        tag = get_tag();
        const GAS u64* f1p = (const GAS u64*)(ws + OFF_F1) + ((xcd >> 1) * 256 + 64 * w + lane) * FS1;
        const u64 f1 = __hip_atomic_load(f1p, RLX_AGENT);
#pragma unroll
        for (int n = 4 * NE; n < 16; n++) ldV(n);
        SB0();
        if (!all_(f1 == tag)) poll_flags(f1p, tag);
        STAMP(16);
        {
            const v4u rus = mkrsrc(ws + OFF_U, 2 << 20);
            const u32 vU = (lane >> 1) * 8192 + ((lane ^ g) & 1) * 16;
#pragma unroll
            for (int i = 0; i < 2; i++) glds_sc1(rus, vU, (64 * w + 32 * i) * 8192 + cp * 32, L_U + 1024 + w * 2048 + i * 1024);
            flag_dma(rus, L_UF + w * 4);
        }
        STAMP(2);
        pre2();
        spin_zero4<1>(aF + R_P7 * 16);
        STAMP(3);
#pragma unroll
        for (int n = 0; n < 16; n++) pin(stage[n]);
        {
            const u32 aK = L_CNT + 4 * C_K + (lane & 1) * 4;
            int k = 0;
            while (any_(lds_ldv<u32>(aK) < 2) && ++k < (SPIN_CAP << 4)) { __builtin_amdgcn_s_sleep(1); }
        }
        STAMP(14);
        const v4u rw2 = mkrsrc(w2, 512 * 1376 * 2);
        sfor<28>([&](auto I) {
            constexpr int n = decltype(I)::value;
            if constexpr (n == 28 - NV2) asm volatile("global_load_dwordx2 %0, %1, %2 sc1" : "=v"(f2) : "v"(f2o), "s"(ws + OFF_F2) : "memory");
            glds_imm<w2loc(n) * 1024, n * 8192>(rw2, vv, sv[(n + 4) / 12], lw);
            if constexpr (n == 15) flg(IC<R_V1>{});
        });
        flg(IC<R_V2>{});
        asm volatile("s_waitcnt vmcnt(%1)" : "+v"(f2) : "i"(NV2 + 1) : "memory");
        STAMP(8);
        gemm2(IC<0>{}, stage, f2, tag);
    } else if (role == 1) {
        u64 tag;
        __builtin_amdgcn_s_setprio(PRIO / 10);
        {
            const int t = tid - 256;
            const u32 v1 = t < 192 ? ~0u : 0u, v2 = (t >= 26 && t < 30) ? 0u : ~0u;
            lds_st<v4u>((t < 192 ? L_CSP : L_U - 192 * 16) + t * 16, (v4u){v1, v1, v1, v1});
            if (t < 32) lds_st<v4u>(L_C + t * 16, (v4u){v2, v2, v2, v2});
            wait_lgkm0();
            __builtin_amdgcn_s_barrier();
            asm volatile("" ::: "memory");
        }
        if ((PF & 2) && w == 1) {
            const u32 off = lane == 0 ? OFF_H + ((4 * P) * 43 + u0) * 2048 : lane == 1 ? OFF_H + (2 * tr * 43) * 2048 : lane == 2 ? OFF_A + ch * 4096 : lane == 3 ? OFF_U + tb * 8192 : lane == 4 ? OFF_H + ((4 * P + 3) * 43 + u0) * 2048 : lane == 5 ? OFF_H + ((2 * tr + 2) * 43 - 1) * 2048 : 0x80000000u;
            const v4u rw = mkrsrc(ws, 16 << 20), ro = mkrsrc(out, 2 << 20);
            asm volatile("s_mov_b32 m0, %2\n\ts_nop 0\n\tbuffer_load_dword %0, %1, 0 offen lds" :: "v"(off), "s"(rw), "s"(L_C + 2048) : "memory");
            asm volatile("s_mov_b32 m0, %2\n\ts_nop 0\n\tbuffer_load_dword %0, %1, 0 offen lds" :: "v"(lane ? 0x80000000u : (u32)(tr >> 3) * 524288u), "s"(ro), "s"(L_C + 2048) : "memory");
        }
        u32 aW[4], aX[4];
#pragma unroll
        for (int s = 0; s < 4; s++) {
            const int ks = (s + w) & 3;
            aW[s] = opq(ks * 32768 + (8 * h + (l15 >> 2)) * 192 + (g & 1) * 32 + (lane & 3) * 8);
            aX[s] = opq(lw + 16384 + l31 * 128 + (((2 * ks + h) ^ ((l31 >> 1) & 7)) * 16));
        }
        const u32 aC = opq(L_CSP + l31 * 32 + h * 16), aCw = opq(L_CSP + lane * 32 + w * 8), aS = opq(L_STAT + w * 256 + l31 * 8), aB = opq(L_B1 + lane * 2);
        pre2();
        const u32 zero = opq(0u);
        f32x16 acc[3]; f32x4 cs = {0.f, 0.f, 0.f, 0.f};
        v4u sel[3];
#pragma unroll
        for (int u = 0; u < 3; u++) {
            for (int i = 0; i < 16; i++) acc[u][i] = 0.f;
            const u32 sv = (l15 == (u < 2 ? 8 * u : 1) + 4 * (g & 1)) ? 0xBF80BF80u : 0u; sel[u] = (v4u){sv, sv, sv, sv}; pin(sel[u]);
        }
        v4u fa[3][3], fb[2][4];
        auto rdW = [&](int n, int u) { const u32 a = aW[n & 3] + ((n >> 2) % 5) * 3072 + u * 64; v2u lo = lds_tr(a), hi = lds_tr(a + 768); fa[n % 3][u] = (v4u){lo.x, lo.y, hi.x, hi.y}; };
        auto rdX = [&](int c, int s) { fb[c & 1][s] = lds_ld<v4u>(aX[s] + (c % 4) * 4096); };
        const v4u rhs = mkrsrc(ws + OFF_H, H_BYTES);
        const int nown = (((j + 1) * 43) >> 4) - u0;
        const u32 sh = ((4 * P + w) * 43 + u0) * 2048, sh2 = nown == 3 ? sh + 4096 : (u32)H_BYTES;
        spin_zero4(aF);
        STAMP(1);
        rdX(0, 0);
#pragma unroll
        for (int u = 0; u < 3; u++) rdW(0, u);
        rdX(0, 1);
#pragma unroll
        for (int u = 0; u < 3; u++) rdW(1, u);
        rdX(0, 2); rdX(0, 3);
        u32 fl, fx, bb[2]; v2u ee; v2u stg; v4u fas[3], fbs;
        sfor<32>([&](auto N_) __attribute__((always_inline)) {
            constexpr int n = decltype(N_)::value, c = n >> 2, s = n & 3;
            SB0();
            if constexpr (s == 2 && c < 5) {
                pin(fa[(n + 1) % 3][2]);
                flg(IC<R_P + c>{});
            }
            if (s == 2 && c < 7 && !(AB & 2)) {
                if (__builtin_expect(any_(fl != 0), 0)) TIMED(spin_zero4(aF + (R_F + c + 1) * 16));
                asm volatile("" ::: "memory");
            }
            sfor<3>([&](auto U_) __attribute__((always_inline)) {
                constexpr int u = decltype(U_)::value;
                acc[u] = mfma32(fa[n % 3][u], fb[c & 1][s], acc[u]);
                if (s == 0 && c < 7) cs = mfma16(sel[u], fa[n % 3][u], cs);
                if (n == 27) cs = mfma16(sel[u], fa[28 % 3][u], cs);
                SB0();
                if (s >= 2 && c < 7 && u < 2) rdX(c + 1, 2 * (s - 2) + u);
                if (n + 2 < 32) rdW(n + 2, u);
                if (s == 1 && u == 0 && c < 7) fl = lds_ldv<u32>(aF + (R_F + c + 1) * 16);
                if constexpr (n == 29 && u == 2) clr<R_P7 * 16>(aFw, zero);
                if (n == 25 && u == 2) { bb[0] = lds_ld<u16>(aB); bb[1] = lds_ld<u16>(aB + 128); }
                if (n == 21 && u == 1) fx = lds_ldv<u32>(aF + R_X * 16);
                if (n == 23 && u == 1) { if (__builtin_expect(any_(fx != 0), 0)) spin_zero4(aF + R_X * 16); asm volatile("" ::: "memory"); ee = lds_ldv<v2u>(L_E); }
                if (n == 28 && u == 1) {
                    v2u t[2];
#pragma unroll
                    for (int k = 0; k < 2; k++) {
                        const float d = cs[k] - bf_hi(f2u(cs[k]));
                        t[k] = (v2u){__builtin_amdgcn_perm(f2u(d), f2u(cs[k]), 0x07060302u), pk_bf(d - bf_hi(f2u(d)), bf_lo(bb[k]))};
                    }
                    asm volatile("ds_write2st64_b64 %0, %1, %2 offset1:4" :: "v"(aCw), "v"(t[0]), "v"(t[1]) : "memory");
                }
                if (n == 31 && u == 0) { fas[0] = lds_ldv<v4u>(aC); fas[1] = lds_ldv<v4u>(aC + 1024); fas[2] = lds_ldv<v4u>(aC + 2048); }
                if (n == 30 && u == 0) stg = lds_ldv<v2u>(aS);
                if (n == 30 && u == 1) { tag = mix64(((u64)(u32)__builtin_amdgcn_readfirstlane(ee.y) << 32) | (u32)__builtin_amdgcn_readfirstlane(ee.x)); asm volatile("" : "+s"(tag)); }
                SB0();
            });
        });
        {
            auto csbad = [&]() { return umax(umax(umax(fas[0].x, fas[0].z), umax(fas[1].x, fas[1].z)), umax(fas[2].x, fas[2].z)) == ~0u; };
            const u32 want = (lane & 1) ? (u32)(tag >> 32) : (u32)tag;
            if (__builtin_expect(any_(csbad() || stg.y != want), 0)) {
                int k = 0;
                while (any_(csbad()) && ++k < (SPIN_CAP << 2)) { fas[0] = lds_ldv<v4u>(aC); fas[1] = lds_ldv<v4u>(aC + 1024); fas[2] = lds_ldv<v4u>(aC + 2048); }
                k = 0;
                const GAS u64* sp = (const GAS u64*)(ws + OFF_ST + P * 1024 + (w & 1) * 512 + (w >> 1) * 32 + (l31 >> 2) * 64 + (lane & 3) * 8);
                while (any_(stg.y != want) && ++k < SPIN_CAP) { const u64 gr = __hip_atomic_load(sp, RLX_AGENT); stg = (v2u){(u32)gr, (u32)(gr >> 32)}; }
            }
            const u32 m1 = stg.x & 0xffffu, m2 = m1 * 0x10001u;
            fbs = (v4u){m2, h ? m1 : stg.x, m2, m1};
        }
#pragma unroll
        for (int u = 0; u < 3; u++) acc[u] = mfma32(fas[u], fbs, acc[u]);
        STAMP(2); CST(1);
        sfor<6>([&](auto K_) __attribute__((always_inline)) {
            constexpr int u = decltype(K_)::value >> 1, s = decltype(K_)::value & 1;
            v4u o;
#pragma unroll
            for (int e = 0; e < 4; e++) {
                const float a0 = acc[u][8 * s + 2 * e], a1 = acc[u][8 * s + 2 * e + 1];
                o[e] = relu_pk(pk_bf(a0 * __builtin_fabsf(a0), a1 * __builtin_fabsf(a1)));
            }
            st_sc1(o, rhs, l16, (u < 2 ? sh + u * 2048 : sh2) + s * 1024);
            SB0();
        });
        STAMP(8);
        wait_vm<0>();
        STAMP(4);
        if (lane == 0) __hip_atomic_store((GAS u64*)(ws + OFF_F2) + ((4 * P + w) * 16 + j) * FS2, tag, RLX_AGENT);
        v4u none[16];
        gemm2(IC<1>{}, none, ~tag, tag);
    } else {
        u64 tag;
        __builtin_amdgcn_s_setprio(3);
        glds(rx, l16, (8 * tb + 2 * w) * 1024, L_KB + (2 * w) * 1088);
        glds(rx, l16, (8 * tb + 2 * w + 1) * 1024, L_KB + (2 * w + 1) * 1088);
        if (w == 0) {
            const v4u re = mkrsrc(ws, 16);
            asm volatile("s_mov_b32 m0, %1\n\ts_mov_b64 exec, 1\n\tbuffer_load_dwordx4 off, %0, 0 sc1 lds\n\ts_mov_b64 exec, -1" :: "s"(re), "s"(L_E) : "memory");
        } else if (w == 1) {
            const v4u rb1 = mkrsrc(b1, 1376 * 2);
            asm volatile("s_mov_b32 m0, %3\n\ts_nop 0\n\tbuffer_load_dword %0, %1, %2 offen lds" :: "v"(lane * 4), "s"(rb1), "s"(u0 * 64), "s"(L_B1) : "memory");
        }
        const u32 pi = opq((hf * 64 + lane) * 2);
        u32 pA = *(const GAS u16*)((const GAS char*)(logA + ch * 128) + pi), pI = *(const GAS u16*)((const GAS char*)(Aim + ch * 128) + pi);
        u32 pR = *(const GAS u16*)((const GAS char*)(Cre + ch * 128) + pi), pC = *(const GAS u16*)((const GAS char*)(Cim + ch * 128) + pi);
        u32 pT = logdt[ch], pD = Dskip[ch];
        __builtin_amdgcn_s_barrier();
        asm volatile("" ::: "memory");
        flg(IC<R_X>{});
        one = opq(1u);
        const u32 aCnt = opq(L_CNT + chl * 4), aCtl = opq(L_C);
        const v4u rus = mkrsrc(ws + OFF_U, 2 << 20);
        v4u selLo, selHi;
#pragma unroll
        for (int e = 0; e < 4; e++) {
            const u32 o1 = (h == (g & 1) && ((l15 & 7) >> 1) == e) ? ((l15 & 1) ? 0x3C000000u : 0x00003C00u) : 0u;
            selLo[e] = l15 < 8 ? o1 : 0u; selHi[e] = l15 < 8 ? 0u : o1;
        }
        pin(selLo); pin(selHi);
        const int m = 64 * hf + lane;
        const u32 aT = opq(w * 32768 + 16384 + l15 * 64 + g * 16), t0 = opq((2 * chl) * 32768 + 16384 + m * 4), kr = opq(L_KB + chl * 8320 + (1016 - 8 * m) * 2);
        u32 tg[6];
        {
            const bool r1 = m < 32, r2 = m < 64;
            tg[0] = opq(t0 + (r1 ? 528 : 0) * 4); tg[1] = opq(t0 + (r2 ? 400 : 0) * 4); tg[2] = opq(t0 + (r1 ? 560 : 272) * 4);
            tg[3] = opq(t0 + (r1 ? 592 : 272) * 4); tg[4] = opq(t0 + (r2 ? 464 : 1) * 4); tg[5] = opq(t0 + (r1 ? 624 : 1) * 4);
        }
        u32 kb[4], ua[4];
#pragma unroll
        for (int i = 0; i < 4; i++) {
            kb[i] = opq(L_KB + chl * 8320 + ((l15 & 1) ? (239 - l15 + 8 * g) * 2 : 4160 + (240 - l15 + 8 * g) * 2) - hf * 256 - 192 + i * 1024);
            const int os = 30 + 2 * l15 + g - 16 * hf - 4 * i;
            ua[i] = opq(L_U + os * 32 + ((chl ^ (os >> 3)) & 1) * 16);
        }
        const u32 aZ = opq(L_C + (lane & 3) * 4), aZ2 = opq(L_C + (lane & 3) * 4 + ((lane & 4) ? R_P + 4 : R_F + 7) * 16);
        const u32 vA = opq((lane >> 3) * 4096 + (lane & 7) * 16);
        pin(pA); pin(pI); pin(pR); pin(pC); pin(pT); pin(pD);
        {
            const u32 aRow = opq(L_KB + (l15 & 7) * 1088 + (l15 >> 3) * 512 + g * 16), aTr = opq(L_KB + (l15 >> 2) * 1088 + (128 * w + 16 * g + 4 * (lane & 3)) * 2);
            v4u ones = {0x3F803F80u, 0x3F803F80u, 0x3F803F80u, 0x3F803F80u}; pin(ones);
            spin_zero4(aF + R_X * 16);
            STAMP(1);
            v4u xb[8]; v2u xt[2][2];
#pragma unroll
            for (int s = 0; s < 8; s++) xb[s] = lds_ld<v4u>(aRow + s * 64);
#pragma unroll
            for (int i = 0; i < 2; i++) { xt[i][0] = lds_tr(aTr + i * 128); xt[i][1] = lds_tr(aTr + i * 128 + 4 * 1088); }
            tag = get_tag();
            f32x4 sm = {0.f, 0.f, 0.f, 0.f}, sq = {0.f, 0.f, 0.f, 0.f};
#pragma unroll
            for (int s = 0; s < 8; s++) { sq = mfma16(xb[s], xb[s], sq); sm = mfma16(ones, xb[s], sm); }
            float q = (l15 & 2) ? ((l15 & 1) ? sq[3] : sq[2]) : ((l15 & 1) ? sq[1] : sq[0]);
            q = g == (l15 >> 2) ? q : 0.f;
            { const auto r = __builtin_amdgcn_permlane32_swap(f2u(q), f2u(q), false, false); q = u2f(r[0]) + u2f(r[1]); }
            { const auto r = __builtin_amdgcn_permlane16_swap(f2u(q), f2u(q), false, false); q = u2f(r[0]) + u2f(r[1]); }
            const float mean = dpp_add<0x128>(sm[0]) * (1.f / 512.f);
            const float var = fmaxf(__builtin_fmaf(-mean, mean, dpp_add<0x128>(q) * (1.f / 512.f)), 0.f);
            const u32 p = pk_bf(mean, __builtin_amdgcn_sqrtf(rbf(rbf(var) + 1e-5f)));
            const float mb = bf_lo(p), rr = __builtin_amdgcn_rcpf(bf_hi(p));
            if (w == 0 && lane < 8) __hip_atomic_store((GAS u64*)(ws + OFF_ST + (P * 16 + j) * 64) + lane, ((u64)((lane & 1) ? (u32)(tag >> 32) : (u32)tag) << 32) | p, RLX_AGENT);
            sfor<2>([&](auto I_) {
                constexpr int i = decltype(I_)::value;
                v4u o;
                sfor<4>([&](auto E_) {
                    constexpr int e = decltype(E_)::value;
                    const u32 xv = xt[i][e >> 1][e & 1];
                    const u32 d = pk_bf(bf_lo(xv) - bcast<2 * e>(mb), bf_hi(xv) - bcast<2 * e + 1>(mb));
                    o[e] = pk_bf(bf_lo(d) * bcast<2 * e>(rr), bf_hi(d) * bcast<2 * e + 1>(rr));
                });
                st_sc1(o, rus, l16, tb * 8192 + w * 2048 + i * 1024);
            });
        }
        STAMP(2);
        __builtin_amdgcn_s_setprio(PRIO % 10);
        f32x4 kt[3];
        {
            const float dt = rbf(expf(bf_lo(pT)));
            const float zr = -rbf(expf(bf_lo(pA))) * dt, zi = bf_lo(pI) * dt;
            const float lr = zr * 1.44269504088896341f;
            const float th = zi * 0.159154943091895336f, tl = __builtin_fmaf(zi, 0.159154943091895336f, -th) + zi * 6.4206383e-09f;
            const cmult w1c = mkc(cexp_pow(lr, -th, -tl, 1.f)), qc = mkc(cexp_pow(lr, -th, -tl, 128.f)), q2 = mkc(cexp_pow(lr, -th, -tl, 256.f));
            const cmult w8 = mkc(cexp_pow(lr, th, tl, 8.f)), w32 = mkc(cexp_pow(lr, th, tl, 32.f));
            const u32 aR = pR & 0x7fffu, aI = pC & 0x7fffu;
            int ex = (int)(wave_max(aR > aI ? aR : aI) >> 7); ex = ex < 12 ? 12 : (ex > 240 ? 240 : ex);
            const float sdn = u2f((u32)(262 - ex) << 23), sup = u2f((u32)(ex - 8) << 23);
            auto transpose = [&](const f32x2 (&v)[16], v4u (&f)[4]) {
                f32x16 z; for (int i = 0; i < 16; i++) z[i] = 0.f;
                f32x16 dr = mfma32h((v4u){pk_h2(v[0].x, v[1].x), pk_h2(v[2].x, v[3].x), pk_h2(v[4].x, v[5].x), pk_h2(v[6].x, v[7].x)}, selLo, z);
                f32x16 di = mfma32h((v4u){pk_h2(v[0].y, v[1].y), pk_h2(v[2].y, v[3].y), pk_h2(v[4].y, v[5].y), pk_h2(v[6].y, v[7].y)}, selLo, z);
                dr = mfma32h((v4u){pk_h2(v[8].x, v[9].x), pk_h2(v[10].x, v[11].x), pk_h2(v[12].x, v[13].x), pk_h2(v[14].x, v[15].x)}, selHi, dr);
                di = mfma32h((v4u){pk_h2(v[8].y, v[9].y), pk_h2(v[10].y, v[11].y), pk_h2(v[12].y, v[13].y), pk_h2(v[14].y, v[15].y)}, selHi, di);
#pragma unroll
                for (int e = 0; e < 4; e++) {
                    f[0][e] = pk_h2(dr[2 * e], dr[2 * e + 1]); f[1][e] = pk_h2(dr[8 + 2 * e], dr[9 + 2 * e]);
                    f[2][e] = pk_h2(di[2 * e], di[2 * e + 1]); f[3][e] = pk_h2(di[8 + 2 * e], di[9 + 2 * e]);
                }
            };
            { f32x2 t0_ = w1c.b, t1_ = qc.b, t2_ = q2.b, t3_ = w8.b, t4_ = w32.b; pin(t0_); pin(t1_); pin(t2_); pin(t3_); pin(t4_); }
            STAMP(13);
            wait_vm<0>();
            if (lane == 0) __hip_atomic_store((GAS u64*)(ws + OFF_F1) + (w * 256 + tb) * FS1, tag, RLX_AGENT);
            STAMP(14);
            v4u fy[4], fx[4];
            f32x2 v[16], xo[8];
            v[0] = (f32x2){1.f, 0.f}; v[1] = w8.b; v[2] = cmul(w8.b, w8); v[3] = cmul(v[2], w8); v[4] = w32.b;
            xo[0] = (f32x2){bf_lo(pR) * sdn, -bf_lo(pC) * sdn};
#pragma unroll
            for (int i = 5; i < 16; i++) { v[i] = cmul(v[i - 4], w32); if (i < 12) xo[i - 4] = cmul(xo[i - 5], w1c); }
            STAMP(3); CST(2);
            transpose(v, fy);
            const float fin = 2.f * dt * sup;
            auto tile = [&](int t) {
                transpose(v, fx);
                f32x4 d = {0.f, 0.f, 0.f, 0.f};
#pragma unroll
                for (int s = 0; s < 4; s++) d = mfma16h(fy[s], fx[s], d);
                kt[t] = d * fin;
            };
            v[0] = xo[0]; v[1] = cmul(xo[0], qc);
#pragma unroll
            for (int s = 2; s < 16; s++) v[s] = cmul(v[s - 2], q2);
            const f32x2 last = cmul(v[14], q2);
            tile(0);
            v[0] = last; v[1] = xo[4]; v[9] = xo[2]; v[13] = xo[6];
            v[2] = cmul(v[1], qc); v[10] = cmul(v[9], qc); v[14] = cmul(v[13], qc);
#pragma unroll
            for (int s = 3; s < 9; s++) v[s] = cmul(v[s - 2], q2);
            v[11] = cmul(v[9], q2); v[12] = cmul(v[10], q2); v[15] = cmul(v[13], q2);
            const f32x2 last2 = cmul(v[14], q2);
            tile(1);
            v[0] = last2;
#pragma unroll
            for (int i = 0; i < 4; i++) { v[1 + 2 * i] = xo[2 * i + 1]; v[2 + 2 * i] = cmul(xo[2 * i + 1], qc); }
#pragma unroll
            for (int i = 9; i < 16; i++) v[i] = (f32x2){0.f, 0.f};
            tile(2);
        }
        CST(3); STAMP(17);
        pin(kt[0]); pin(kt[1]); pin(kt[2]);
        spin_zero4<1>(aZ2);
        STAMP(11);
#pragma unroll
        for (int t = 0; t < 3; t++) lds_st<f32x4>(aT + t * 1024, kt[t]);
        sig<C_A>(aCtl + (L_CNT - L_C), one);
        STAMP(4);
        spin(aCtl + (L_CNT - L_C) + 4 * C_A, 4);
        {
            auto tv = [&](u32 a) { return lds_ld<float>(a) + lds_ld<float>(a + 32768); };
            float k[9];
            k[0] = tv(t0); k[8] = tv(t0 + 4); k[4] = tv(t0 + 272 * 4);
            k[1] = tv(tg[0]); k[2] = tv(tg[1]); k[3] = tv(tg[2]); k[5] = tv(tg[3]); k[6] = tv(tg[4]); k[7] = tv(tg[5]);
            const float va = tv(t0 + 512), vb = tv(t0 + 516);
            if (m == 0) k[0] += bf_lo(pD);
            const u32 k10 = pk_bf(k[1], k[0]);
            lds_st<v4u>(kr + 2048, (v4u){pk_bf(k[7], k[6]), pk_bf(k[5], k[4]), pk_bf(k[3], k[2]), k10});
            lds_st<v4u>(kr + 2048 + 4160, (v4u){pk_bf(k[8], k[7]), pk_bf(k[6], k[5]), pk_bf(k[4], k[3]), pk_bf(k[2], k[1])});
            const u32 pa = pk_bf(va, va), pb = pk_bf(vb, vb), pba = pk_bf(vb, va);
            lds_st<v4u>(kr, (v4u){pb, (m & 1) ? pb : pba, pa, pa});
            lds_st<v4u>(kr + 4160, (v4u){pb, pb, (m & 1) ? pba : pa, pa});
            if (m < 4) lds_st<v4u>(L_KB + chl * 8320 + 4096 + (m & 1) * 16 + (m < 2 ? 4160 : 0), (v4u){m == 0 ? k10 >> 16 : 0u, 0u, 0u, 0u});
        }
        sig<C_K>(aCnt, one);
        STAMP(18);
        f32x4 cv[8];
        {
#pragma unroll
            for (int t = 0; t < 8; t++) cv[t] = (f32x4){0.f, 0.f, 0.f, 0.f};
            v4u A[8], B[8];
            auto rdA = [&](int r, int a) { const u32 p = kb[(7 - a) >> 1] + ((7 - a) & 1) * 512 + (3 - r) * 64; return (v4u){lds_ld<u32>(p), lds_ld<u32>(p + 4), lds_ld<u32>(p + 8), lds_ld<u32>(p + 12)}; };
            auto rdB = [&](int r, int d) { return lds_ld<v4u>(ua[r] + d * 1024); };
            spin(aCnt + 4 * C_K, 2);
#pragma unroll
            for (int i = 0; i < 8; i++) A[7 - i] = rdA(0, 7 - i);
            STAMP(19);
            spin_zero4(aZ + (L_UF - L_C));
            STAMP(6);
#pragma unroll
            for (int i = 0; i < 8; i++) B[i] = rdB(0, i);
            if (b == 0 && w == 0 && lane == 0) __hip_atomic_store((GAS u64*)ws, tag, RLX_AGENT);
            CST(0);
#pragma unroll
            for (int r = 0; r < 4; r++) {
#pragma unroll
                for (int a = 7; a >= 0; a--) {
#pragma unroll
                    for (int d = 0; d + a < 8; d++) {
                        cv[a + d] = mfma16(A[a], B[d], cv[a + d]);
                        if (a == 0 && r < 3) { SB0(); B[d] = rdB(r + 1, d); SB0(); }
                    }
                    if (r < 3) { SB0(); A[a] = rdA(r + 1, a); SB0(); }
                }
            }
        }
        STAMP(7); CST(1);
        {
            const u32 mine = opq(w * 32768 + 16384 + l16), other = opq((w ^ 1) * 32768 + 16384 + l16);
#pragma unroll
            for (int t = 0; t < 4; t++) lds_st<f32x4>(mine + t * 1024, sel4(hf, cv[t], cv[4 + t]));
            sig<C_E>(aCnt, one);
            sig<C_D>(aCtl + (L_CNT - L_C), one);
            spin(aCnt + 4 * C_E, 2);
            u32 o[8];
#pragma unroll
            for (int t = 0; t < 4; t++) {
                const f32x4 y = sel4(hf, cv[4 + t], cv[t]) + lds_ld<f32x4>(other + t * 1024);
                o[2 * t] = pk_bf(y[0], y[1]); o[2 * t + 1] = pk_bf(y[2], y[3]);
            }
            const v4u ras = mkrsrc(ws + OFF_A, 2 << 20);
            st_sc1((v4u){o[0], o[1], o[2], o[3]}, ras, l16, ch * 4096 + hf * 2048);
            st_sc1((v4u){o[4], o[5], o[6], o[7]}, ras, l16, ch * 4096 + hf * 2048 + 1024);
            STAMP(15);
            wait_vm<0>();
            STAMP(8);
            if (lane == 0) __hip_atomic_store((GAS u64*)(ws + OFF_F3) + (hf * 512 + ch) * FS3, tag, RLX_AGENT);
            spin(aCtl + (L_CNT - L_C) + 4 * C_D, 4);
            poll_flags((const GAS u64*)(ws + OFF_F3) + ((tr >> 4) * 512 + tc * 64 + 16 * w + l15) * FS3, tag);
            STAMP(9);
#pragma unroll
            for (int i = 0; i < 2; i++)
                glds_sc1(ras, vA, (tc * 64 + 16 * w + 8 * i) * 4096 + (tr >> 3) * 1024 + (tr & 3) * 256 + ((tr >> 2) & 1) * 128, L_U + w * 2048 + i * 1024);
            const u32 ta = opq(L_U + w * 2048 + (l15 >> 2) * 128 + g * 32 + (lane & 3) * 8), aw = opq(L_KB + lane * 144 + w * 32);
            wait_vm<0>();
            const v2u a0 = lds_tr(ta), a1 = lds_tr(ta + 512), a2 = lds_tr(ta + 1024), a3 = lds_tr(ta + 1536);
            lds_st<v4u>(aw, (v4u){a0.x, a0.y, a1.x, a1.y});
            lds_st<v4u>(aw + 16, (v4u){a2.x, a2.y, a3.x, a3.y});
            STAMP(10);
        }
        wait_lgkm0();
        __builtin_amdgcn_s_barrier();
    }
}

#if defined(__HIP_DEVICE_COMPILE__) && __HIP_DEVICE_COMPILE__
#pragma clang attribute pop
#endif

extern "C" void kernel_launch(void* const* d_in, const int* in_sizes, int n_in, void* d_out, int out_size, void* d_ws, size_t ws_size,
                              hipStream_t stream) {
    fused_kernel<<<256, 768, 0, stream>>>((const u16*)d_in[0], (const u16*)d_in[1], (const u16*)d_in[2], (const u16*)d_in[3], (const u16*)d_in[4],
                                          (const u16*)d_in[5], (const u16*)d_in[6], (const u16*)d_in[8], (const u16*)d_in[9], (const u16*)d_in[10],
                                          (const u16*)d_in[11], (u16*)d_out, (char*)d_ws);
}
```

```cpp
#include <hip/hip_runtime.h>
#include <stdint.h>
#include <utility>

typedef unsigned short u16;
typedef unsigned int u32;
typedef unsigned long long u64;
typedef u32 v4u __attribute__((ext_vector_type(4)));
typedef u32 v2u __attribute__((ext_vector_type(2)));
typedef float f32x2 __attribute__((ext_vector_type(2)));
typedef float f32x4 __attribute__((ext_vector_type(4)));
typedef float f32x16 __attribute__((ext_vector_type(16)));
typedef __bf16 bf16x8 __attribute__((ext_vector_type(8)));
typedef __bf16 bf16x2 __attribute__((ext_vector_type(2)));
typedef _Float16 f16x8 __attribute__((ext_vector_type(8)));
typedef _Float16 f16x2 __attribute__((ext_vector_type(2)));
typedef short s16x4 __attribute__((ext_vector_type(4)));
#define GAS __attribute__((address_space(1)))
#define LAS __attribute__((address_space(3)))
#define DI __device__ __forceinline__
#define RLX_AGENT __ATOMIC_RELAXED, __HIP_MEMORY_SCOPE_AGENT
#define RLX_WG __ATOMIC_RELAXED, __HIP_MEMORY_SCOPE_WORKGROUP
#define SB0() __builtin_amdgcn_sched_barrier(0)
#define CHST 0
#define PSLEEP 2
#define ROT 2
#define POL 1
#define PDS 0
#define THR 0
#define HS 0
#define AB 0
#ifndef NV2
#define NV2 2
#endif
#define WING 10
#define HROT 0
#define GP2 0
#define PFB 29440
#define PF 1
#define FULLF 0
#define WSOFF 0
#define PACE 0
#define PACEV 9
#define F3D 30
#define WARM 1
#define NE 3
#define PRIO 21

#define NOH_OK
#define NWV 12
constexpr int OFF_ST = 16384, OFF_F1 = 128 << 10, OFF_F2 = 256 << 10, OFF_F3 = 512 << 10;
constexpr int FS1 = 8, FS2 = 16, FS3 = 16;
constexpr int OFF_U = 1 << 20, OFF_A = 4 << 20, OFF_H = 8 << 20;
constexpr int H_BYTES = 64 * 43 * 2048;
constexpr int L_C = 131072;
constexpr int L_FLG = L_C, L_UF = L_C + 320, L_E = L_C + 768, L_B2 = L_C + 896, L_LNS = L_C + 352, L_CNT = L_C + 416, L_B1 = L_C + 512, L_STAT = L_C + 1024, L_CSP = L_C + 29696;
enum { R_F = 0, R_P = 8, R_P7 = 13, R_PK = 15, R_X = 14, R_V1 = 16, R_V2 = 17, R_V3 = 18 };
constexpr int L_U = L_C + 3072, L_KB = L_C + 12288;
constexpr int SPIN_CAP = 1 << 16;
enum { C_A = 0, C_D = 1, C_K = 2, C_E = 4, C_PK = 8, C_RD = 12 };

#define STAMP(i)
#define CST(i)
#define TIMED(x) x

#if defined(__HIP_DEVICE_COMPILE__) && __HIP_DEVICE_COMPILE__
#pragma clang attribute push(__attribute__((target("no-packed-fp32-ops"))), apply_to = function)
#endif
DI float u2f(u32 v) { return __builtin_bit_cast(float, v); }
DI u32 f2u(float v) { return __builtin_bit_cast(u32, v); }
DI bool any_(bool c) { return __builtin_amdgcn_ballot_w64(c) != 0; }
DI bool all_(bool c) { return __builtin_amdgcn_ballot_w64(!c) == 0; }
template <typename T> DI T lds_ld(u32 a) { return *(LAS T*)(size_t)a; }
template <typename T> DI void lds_st(u32 a, T v) { *(LAS T*)(size_t)a = v; }
DI v2u lds_tr(u32 a) { return __builtin_bit_cast(v2u, __builtin_amdgcn_ds_read_tr16_b64_v4i16((LAS s16x4*)(size_t)a)); }
DI v4u mkrsrc(const void* p, u32 bytes) { u64 a = (u64)p; v4u r; r.x = (u32)a; r.y = (u32)(a >> 32) & 0xffffu; r.z = bytes; r.w = 0x00020000u; return r; }
DI void glds(v4u rsrc, u32 voff, u32 soff, u32 lds) {
    asm volatile("s_mov_b32 m0, %3\n\ts_nop 0\n\tbuffer_load_dwordx4 %0, %1, %2 offen lds" :: "v"(voff), "s"(rsrc), "s"(soff), "s"(lds) : "memory");
}
template <int LOFF> DI u32 glds_first(v4u rsrc, u32 voff, u32 sa, u32 sb, u32 lbase) {
    u32 so;
    asm volatile("s_add_u32 m0, %5, %6\n\ts_add_u32 %0, %3, %4\n\tbuffer_load_dwordx4 %1, %2, %0 offen lds" : "=&s"(so) : "v"(voff), "s"(rsrc), "s"(sa), "s"(sb), "s"(lbase), "i"(LOFF) : "memory", "scc");
    return so;
}
template <int LOFF, int SOFF> DI void glds_imm(v4u rsrc, u32 voff, u32 sa, u32 lbase) {
    u32 so;
    asm volatile("s_add_u32 m0, %4, %5\n\ts_add_u32 %0, %3, %6\n\tbuffer_load_dwordx4 %1, %2, %0 offen lds" : "=&s"(so) : "v"(voff), "s"(rsrc), "s"(sa), "s"(lbase), "i"(LOFF), "i"(SOFF) : "memory", "scc");
}
template <int OFF> DI void glds_next(v4u rsrc, u32 voff, u32 so) {
    asm volatile("buffer_load_dwordx4 %0, %1, %2 offen offset:%3 lds" :: "v"(voff), "s"(rsrc), "s"(so), "i"(OFF) : "memory");
}
template <int LOFF> DI void flag_imm(v4u rsrc, u32 lbase) {
    asm volatile("s_add_u32 m0, %2, %3\n\ts_mov_b64 exec, 1\n\tbuffer_load_dword off, %0, %1 lds\n\ts_mov_b64 exec, -1" :: "s"(rsrc), "s"(rsrc.z), "s"(lbase), "i"(LOFF) : "memory", "scc");
}
DI void glds_sc1(v4u rsrc, u32 voff, u32 soff, u32 lds) {
    asm volatile("s_mov_b32 m0, %3\n\ts_nop 0\n\tbuffer_load_dwordx4 %0, %1, %2 offen sc1 lds" :: "v"(voff), "s"(rsrc), "s"(soff), "s"(lds) : "memory");
}
DI void flag_dma(v4u rsrc, u32 lds) {
    asm volatile("s_mov_b32 m0, %2\n\ts_mov_b64 exec, 1\n\tbuffer_load_dword off, %0, %1 lds\n\ts_mov_b64 exec, -1" :: "s"(rsrc), "s"(rsrc.z), "s"(lds) : "memory");
}
DI void st_sc1(v4u v, v4u rsrc, u32 voff, u32 soff) {
    asm volatile("buffer_store_dwordx4 %0, %1, %2, %3 offen sc1\n\ts_nop 1" :: "v"(v), "v"(voff), "s"(rsrc), "s"(soff) : "memory");
}
template <int BYTES> DI void put_flag(const char* base, u32 off, u32 l16, u64 tag) {
    asm volatile("s_mov_b64 exec, 1\n\tglobal_store_dwordx2 %0, %1, %2 sc1\n\ts_mov_b64 exec, -1" :: "v"(l16), "v"(tag), "s"(base + off) : "memory");
}
template <int BYTES> DI void warm_flag(const char* base, u32 off, u32 l16) {
    const v4u v = {0u, 0u, 0u, 0u};
    asm volatile("s_mov_b64 exec, %3\n\tglobal_store_dwordx4 %0, %1, %2 sc1\n\ts_mov_b64 exec, -1\n\ts_nop 0" :: "v"(l16), "v"(v), "s"(base + off), "i"((1 << (BYTES / 16)) - 1) : "memory");
}
DI u32 opq(u32 a) { asm volatile("" : "+v"(a)); return a; }
template <typename T> DI T lds_ldv(u32 a) { return *(volatile LAS T*)(size_t)a; }
template <int SLEEP = 0> DI void spin_zero4(u32 a) {
    int n = 0;
    while (any_(lds_ldv<u32>(a) != 0) && ++n < (SPIN_CAP << 4)) { if (SLEEP) __builtin_amdgcn_s_sleep(1); }
    asm volatile("" ::: "memory");
}
template <int N> DI void wait_vm() { asm volatile("s_waitcnt vmcnt(%0)" :: "i"(N) : "memory"); }
DI void wait_lgkm0() { asm volatile("s_waitcnt lgkmcnt(0)" ::: "memory"); }
DI u64 mix64(u64 e) { e += 0x9E3779B97F4A7C15ull; e ^= e >> 31; e *= 0xBF58476D1CE4E5B9ull; e ^= e >> 29; return e; }
DI u64 get_tag() { const v2u e = lds_ldv<v2u>(L_E); return mix64(((u64)(u32)__builtin_amdgcn_readfirstlane(e.y) << 32) | (u32)__builtin_amdgcn_readfirstlane(e.x)); }
template <int C> DI void sig(u32 a, u32 one) { asm volatile("s_mov_b64 exec, 1\n\tds_add_u32 %0, %1 offset:%2\n\ts_mov_b64 exec, -1" :: "v"(a), "v"(one), "i"(4 * C) : "memory"); }
template <int OFF> DI void clr(u32 a, u32 zero) { asm volatile("s_mov_b64 exec, 1\n\tds_write_b32 %0, %1 offset:%2\n\ts_mov_b64 exec, -1" :: "v"(a), "v"(zero), "i"(OFF) : "memory"); }
DI void spin(u32 a, u32 target) {
    int n = 0;
    while ((u32)__builtin_amdgcn_readfirstlane(lds_ldv<u32>(a)) < target && ++n < (SPIN_CAP << 4)) {}
    asm volatile("" ::: "memory");
}
DI void poll_flags(const GAS u64* p, u64 tag) {
    int n = 0;
    while (true) {
        u64 f = __hip_atomic_load(p, RLX_AGENT);
        if (all_(f == tag) || ++n > SPIN_CAP) break;
        __builtin_amdgcn_s_sleep(PSLEEP);
    }
    asm volatile("" ::: "memory");
}
DI float bf_lo(u32 v) { return u2f(v << 16); }
DI float bf_hi(u32 v) { return u2f(v & 0xffff0000u); }
DI u32 pk_bf(float a, float b) { f32x2 v = {a, b}; return __builtin_bit_cast(u32, __builtin_convertvector(v, bf16x2)); }
typedef short s16x2 __attribute__((ext_vector_type(2)));
DI u32 relu_pk(u32 v) { return __builtin_bit_cast(u32, __builtin_elementwise_max(__builtin_bit_cast(s16x2, v), (s16x2){0, 0})); }
DI u32 umax(u32 a, u32 b) { return a > b ? a : b; }
DI float rbf(float a) { return bf_lo(pk_bf(a, a)); }
DI u32 pk_h(f32x2 v) { return __builtin_bit_cast(u32, __builtin_convertvector(v, f16x2)); }
DI f32x16 mfma32(v4u a, v4u b, f32x16 c) { return __builtin_amdgcn_mfma_f32_32x32x16_bf16(__builtin_bit_cast(bf16x8, a), __builtin_bit_cast(bf16x8, b), c, 0, 0, 0); }
DI f32x4 mfma16(v4u a, v4u b, f32x4 c) { return __builtin_amdgcn_mfma_f32_16x16x32_bf16(__builtin_bit_cast(bf16x8, a), __builtin_bit_cast(bf16x8, b), c, 0, 0, 0); }
DI f32x4 mfma16h(v4u a, v4u b, f32x4 c) { return __builtin_amdgcn_mfma_f32_16x16x32_f16(__builtin_bit_cast(f16x8, a), __builtin_bit_cast(f16x8, b), c, 0, 0, 0); }
template <int CTRL> DI float dpp_add(float v) { return v + __builtin_bit_cast(float, __builtin_amdgcn_update_dpp(0, __builtin_bit_cast(int, v), CTRL, 0xf, 0xf, true)); }
template <int CTRL> DI u32 dpp_max(u32 v) { const u32 o = (u32)__builtin_amdgcn_update_dpp(0, (int)v, CTRL, 0xf, 0xf, true); return v > o ? v : o; }
template <int T> DI float bcast(float v) { return __builtin_bit_cast(float, __builtin_amdgcn_update_dpp(0, __builtin_bit_cast(int, v), 0x150 + T, 0xf, 0xf, true)); }
DI float rl(float v, int l) { return __builtin_bit_cast(float, __builtin_amdgcn_readlane(__builtin_bit_cast(int, v), l)); }
DI f32x2 wave_sum2(f32x2 v) {
    float a = v.x, c = v.y;
    a = dpp_add<0xB1>(a); c = dpp_add<0xB1>(c); a = dpp_add<0x4E>(a); c = dpp_add<0x4E>(c);
    a = dpp_add<0x141>(a); c = dpp_add<0x141>(c); a = dpp_add<0x140>(a); c = dpp_add<0x140>(c);
    return ((f32x2){rl(a, 0), rl(c, 0)} + (f32x2){rl(a, 16), rl(c, 16)}) + ((f32x2){rl(a, 32), rl(c, 32)} + (f32x2){rl(a, 48), rl(c, 48)});
}
DI u32 wave_max(u32 v) {
    v = dpp_max<0xB1>(v); v = dpp_max<0x4E>(v); v = dpp_max<0x141>(v); v = dpp_max<0x140>(v);
    const u32 a = __builtin_amdgcn_readlane(v, 0), c = __builtin_amdgcn_readlane(v, 16), d = __builtin_amdgcn_readlane(v, 32), e = __builtin_amdgcn_readlane(v, 48);
    const u32 p = a > c ? a : c, q = d > e ? d : e;
    return p > q ? p : q;
}
struct cmult { f32x2 b, n; };
DI cmult mkc(f32x2 b) { cmult c; c.b = b; c.n = (f32x2){-b.y, b.x}; return c; }
DI f32x2 cmul(f32x2 a, const cmult& c) { return __builtin_elementwise_fma((f32x2){a.x, a.x}, c.b, (f32x2){a.y, a.y} * c.n); }
DI f32x16 mfma32h(v4u a, v4u b, f32x16 c) { return __builtin_amdgcn_mfma_f32_32x32x16_f16(__builtin_bit_cast(f16x8, a), __builtin_bit_cast(f16x8, b), c, 0, 0, 0); }
DI u32 pk_h2(float a, float b) { return pk_h((f32x2){a, b}); }
DI f32x2 cexp_pow(float lr, float th, float tl, float e) {
    float a = __builtin_amdgcn_fractf(th * e) + tl * e;
    float m = __builtin_amdgcn_exp2f(lr * e);
    f32x2 r; r.x = m * __builtin_amdgcn_cosf(a); r.y = m * __builtin_amdgcn_sinf(a); return r;
}
DI f32x4 sel4(bool c, f32x4 a, f32x4 b) { return c ? a : b; }
constexpr int w2loc(int n) { return n < 16 ? n : n < 28 ? n + 4 : n - 28; }
template <class F, int... I> DI void sfor_(F&& f, std::integer_sequence<int, I...>) { (f(std::integral_constant<int, I>{}), ...); }
template <int N, class F> DI void sfor(F&& f) { sfor_(f, std::make_integer_sequence<int, N>{}); }
template <int I> using IC = std::integral_constant<int, I>;
template <typename T> DI void pin(T& v) { asm volatile("" : "+v"(v)); }

__global__ void __launch_bounds__(768, 3)
fused_kernel(const u16* __restrict__ x, const u16* __restrict__ logA, const u16* __restrict__ Aim, const u16* __restrict__ Cre,
             const u16* __restrict__ Cim, const u16* __restrict__ logdt, const u16* __restrict__ Dskip, const u16* __restrict__ w1,
             const u16* __restrict__ b1, const u16* __restrict__ w2, const u16* __restrict__ b2, u16* __restrict__ out, char* ws) {
    __shared__ __attribute__((aligned(16))) char smem[163840];
    asm volatile("" :: "v"((u32)(size_t)(LAS char*)smem));
    const int tid = threadIdx.x, lane = tid & 63, wave = __builtin_amdgcn_readfirstlane(tid >> 6), w = wave & 3, role = wave >> 2;
    const int b = blockIdx.x, xcd = b & 7, slot = b >> 3;
    const int P = 4 * (xcd >> 1) + (slot >> 3), j = 8 * (xcd & 1) + (slot & 7);
    const int tr = 4 * xcd + (slot >> 3), tc = slot & 7;
    const int u0 = (j * 43) >> 4;
    STAMP(0);
    if ((PF & 1) && wave == 4) {
        u64 pc; asm volatile("s_getpc_b64 %0" : "=s"(pc));
        const v4u rc = mkrsrc((const void*)(pc & ~255ull), PFB);
        glds(rc, lane * 16, slot * 1024, L_C + 2048);
    }
    const int h = lane >> 5, l31 = lane & 31, l15 = lane & 15, g = lane >> 4;
    const u32 l16 = lane * 16, lw = w * 32768, w4 = w * 4;
    const int rho = HROT ? ((slot >> 3) + slot) & 3 : (slot >> 3);
    const int tb = 64 * (P >> 2) + 32 * (j & 1) + 16 * ((P >> 1) & 1) + 2 * (j >> 1) + (P & 1);
    const int cp = 32 * xcd + slot, chl = w >> 1, hf = w & 1, ch = 2 * cp + chl;
    asm volatile("" :: "s"(x), "s"(logA), "s"(Aim), "s"(Cre), "s"(Cim), "s"(logdt), "s"(Dskip), "s"(w1), "s"(b1), "s"(w2), "s"(b2), "s"(out), "s"(ws));
    const v4u rx = mkrsrc(x, 2 << 20);
    const u32 aF = opq(L_FLG + (lane & 3) * 4);
    auto flg = [&](auto R_) { flag_imm<L_FLG + decltype(R_)::value * 16>(rx, w4); };
    u32 aV0, aV1, aFw, aCn, one, f2o;
    auto pre2 = [&]() {
        aV0 = opq(lw + (4 * h + (l15 >> 2)) * 128 + ((l15 >> 3) & 1) * 64 + (g & 1) * 32 + (lane & 3) * 8); aV1 = opq(aV0 ^ 64);
        aFw = opq(L_FLG + w4); aCn = opq(L_CNT + w4); one = opq(1u);
        const int jj = 4 * w + (lane < 4 ? lane : 4);
        f2o = opq(((2 * tr + role) * 16 + (jj < 15 ? jj : 15)) * FS2 * 8);
    };

    auto rdV = [&](int s, int dt) { v2u lo = lds_tr((dt ? aV1 : aV0) + w2loc(2 * s) * 1024), hi = lds_tr((dt ? aV1 : aV0) + w2loc(2 * s + 1) * 1024); return (v4u){lo.x, lo.y, hi.x, hi.y}; };
    auto gemm2 = [&](auto ROLE_, v4u (&stage)[16], u64 f2, u64 tag) __attribute__((always_inline)) {
        constexpr int role = decltype(ROLE_)::value;
        const GAS u64* f2p = (const GAS u64*)(ws + OFF_F2 + f2o);
        __builtin_amdgcn_s_setprio(role ? GP2 : 0);
        v4u park[8][2];
        if (role) {
            SB0();
            spin_zero4<1>(aFw + R_V1 * 16);
            STAMP(9);
            f2 = __hip_atomic_load(f2p, RLX_AGENT);
            SB0();
#pragma unroll
            for (int s = 0; s < 8; s++) { park[s][0] = rdV(s, 0); park[s][1] = rdV(s, 1); }
            clr<R_PK * 16>(aFw, opq(0u));
            SB0();
        }
        STAMP(10);
        if (!all_(f2 == tag)) poll_flags(f2p, tag);
        STAMP(5);
        const __amdgpu_buffer_rsrc_t rh = __builtin_amdgcn_make_buffer_rsrc(ws + OFF_H, 0, H_BYTES, 0x00020000);
        u32 sh2[4], sh2t[4];
#pragma unroll
        for (int k = 0; k < 4; k++) { sh2[k] = ((2 * tr + role) * 43 + 11 * w) * 2048 + (6 * rho - (k + rho >= 4 ? 22 : 0)) * 1024; sh2t[k] = (w == 3 && k + rho == 3) ? (u32)H_BYTES : sh2[k]; }
        constexpr int WIN = WING;
        v4u hq[WIN], vb[3][2];
        auto ldH = [&](int s) { hq[s % WIN] = __builtin_amdgcn_raw_buffer_load_b128(rh, l16, ((s + 2) % 6 >= 4 ? sh2t[(s + 2) / 6] : sh2[(s + 2) / 6]) + s * 1024, 16); };
#pragma unroll
        for (int s = 0; s < WIN; s++) ldH(s);
        const int fr = tid >> 3;
        const u32 fin_off = (512 * (tr >> 3) + 256 * ((fr >> 2) & 1) + 128 * ((tr >> 2) & 1) + 16 * (fr >> 3) + 4 * (tr & 3) + (fr & 3)) * 512 + tc * 64 + (tid & 7) * 8;
        v4u fin_x, fin_b; u32 fin_s, fv;
        f32x16 ac2[2];
#pragma unroll
        for (int i = 0; i < 16; i++) { ac2[0][i] = 0.f; ac2[1][i] = 0.f; }
        if (!role) {
            const u32 aSt = opq(lw + l16), zero = opq(0u);
            SB0();
            spin_zero4(opq(aFw + ((lane & 1) ? R_PK : R_V2) * 16));
            STAMP(9);
#pragma unroll
            for (int s = 0; s < 8; s++) {
                park[s][0] = rdV(s, 0); park[s][1] = rdV(s, 1);
                asm volatile("" ::: "memory");
                lds_st<v4u>(aSt + 2 * s * 1024, stage[2 * s]); lds_st<v4u>(aSt + (2 * s + 1) * 1024, stage[2 * s + 1]);
                SB0();
            }
            clr<R_V3 * 16>(aFw, zero);
            STAMP(13);
        }
#pragma unroll
        for (int s = 0; s < 22; s++) {
            SB0();
            if (role && (s == 4 || s == 10)) fv = lds_ldv<u32>(aFw + (s == 4 ? R_V2 : R_V3) * 16);
            if (role && (s == 6 || s == 12)) { if (__builtin_expect(any_(fv != 0), 0)) spin_zero4(aFw + (s == 6 ? R_V2 : R_V3) * 16); asm volatile("" ::: "memory"); }
            if (s + 2 >= 8 && s + 2 < 22) { vb[(s + 2) % 3][0] = rdV(s + 2, 0); vb[(s + 2) % 3][1] = rdV(s + 2, 1); }
            if (s == 19) sig<C_RD>(aCn, one);
            if (s == 21) fv = lds_ldv<u32>(aCn + 4 * C_RD);
            ac2[0] = mfma32(s < 8 ? park[s & 7][0] : vb[s % 3][0], hq[s % WIN], ac2[0]);
            ac2[1] = mfma32(s < 8 ? park[s & 7][1] : vb[s % 3][1], hq[s % WIN], ac2[1]);
            SB0();
            if (s + WIN < 22) ldH(s + WIN);
            if (s == 14) {
                fin_x = *(const GAS v4u*)(x + fin_off); fin_b = lds_ld<v4u>(L_B2 + (lane & 7) * 16);
                fin_s = __hip_atomic_load((GAS u32*)(ws + OFF_ST + (tr >> 1) * 1024 + (fr >> 2) * 64 + (tr & 1) * 32 + (fr & 3) * 8), RLX_AGENT);
            }
        }
        SB0();
        STAMP(7); CST(3);
        if (__builtin_expect(any_(fv < 2), 0)) spin(aCn + 4 * C_RD, 2);
        asm volatile("" ::: "memory");
        const u32 da = opq(lw + role * 20480 + l31 * 272 + h * 16);
#pragma unroll
        for (int dt = 0; dt < 2; dt++)
#pragma unroll
            for (int q = 0; q < 4; q++)
                lds_st<f32x4>(da + (32 * dt + 8 * q) * 4, (f32x4){ac2[dt][4 * q], ac2[dt][4 * q + 1], ac2[dt][4 * q + 2], ac2[dt][4 * q + 3]});
        f32x4 s0 = {bf_lo(fin_x.x) + bf_lo(fin_b.x), bf_hi(fin_x.x) + bf_hi(fin_b.x), bf_lo(fin_x.y) + bf_lo(fin_b.y), bf_hi(fin_x.y) + bf_hi(fin_b.y)};
        f32x4 s1 = {bf_lo(fin_x.z) + bf_lo(fin_b.z), bf_hi(fin_x.z) + bf_hi(fin_b.z), bf_lo(fin_x.w) + bf_lo(fin_b.w), bf_hi(fin_x.w) + bf_hi(fin_b.w)};
        const u32 ra = opq(role * 20480 + (fr & 31) * 272 + (tid & 7) * 32), rb = opq(ra + 65536), rc = opq(L_KB + fr * 144 + (tid & 7) * 16);
        float rs2 = __builtin_amdgcn_rcpf(bf_hi(fin_s)); rs2 *= rs2;
        asm volatile("" : "+v"(s0), "+v"(s1), "+v"(rs2));
        wait_lgkm0();
        __builtin_amdgcn_s_barrier();
        asm volatile("" ::: "memory");
        STAMP(11);
        const v4u av = lds_ld<v4u>(rc);
        s0 += ((lds_ld<f32x4>(ra) + lds_ld<f32x4>(ra + 32768)) + (lds_ld<f32x4>(rb) + lds_ld<f32x4>(rb + 32768))) * rs2;
        s1 += ((lds_ld<f32x4>(ra + 16) + lds_ld<f32x4>(ra + 32768 + 16)) + (lds_ld<f32x4>(rb + 16) + lds_ld<f32x4>(rb + 32768 + 16))) * rs2;
        s0 += (f32x4){bf_lo(av.x), bf_hi(av.x), bf_lo(av.y), bf_hi(av.y)};
        s1 += (f32x4){bf_lo(av.z), bf_hi(av.z), bf_lo(av.w), bf_hi(av.w)};
        *(GAS v4u*)(out + fin_off) = (v4u){pk_bf(s0[0], s0[1]), pk_bf(s0[2], s0[3]), pk_bf(s1[0], s1[1]), pk_bf(s1[2], s1[3])};
    };

    if (role == 0) {
        v4u stage[16]; u64 f2, tag;
        const int rot = ROT == 0 ? 0 : ROT == 1 ? (((slot >> 3) + slot) & 1) * 4 : ROT == 2 ? (((slot >> 3) + slot) & 3) * 2 : (2 * (slot >> 3) + (slot & 7)) & 7;
        const v4u rw1 = mkrsrc(w1, 512 * 1376 * 2);
        const u32 sx = (512 * (P >> 2) + 128 * ((P >> 1) & 1) + 64 * (w & 1) + 8 * (P & 1) + 4 * (w >> 1)) * 1024;
        const u32 sw1 = 16 * w * 2752 + u0 * 64, sw2 = 352 * w * 1024 + tc * 128;
        u32 vx[4], vw[3];
#pragma unroll
        for (int i = 0; i < 3; i++) { const int p = 64 * i + lane, r = (p * 171) >> 11; vw[i] = r * (2752 - 192) + l16; }
#pragma unroll
        for (int i = 0; i < 4; i++) vx[i] = (((h * 256 + ((lane >> 3) & 3)) * 1024 + ((lane & 7) ^ g) * 16) ^ ((i & 1) * 64)) + i * (16384 - 1024);
        auto ldX = [&](auto C_) { constexpr int c = decltype(C_)::value;
            const u32 so = glds_first<(16 + (c % 4) * 4) * 1024>(rx, vx[0], sx, ((c + rot) & 7) * 128, lw);
            glds_next<1024>(rx, vx[1], so); glds_next<2048>(rx, vx[2], so); glds_next<3072>(rx, vx[3], so); };
        auto ldW = [&](auto C_) { constexpr int c = decltype(C_)::value;
            const u32 so = glds_first<3 * (c % 5) * 1024>(rw1, vw[0], sw1, ((c + rot) & 7) * (64 * 2752), lw);
            glds_next<1024>(rw1, vw[1], so); glds_next<2048>(rw1, vw[2], so); };
        ldW(IC<0>{}); ldX(IC<0>{});
        __builtin_amdgcn_s_barrier();
        asm volatile("" ::: "memory");
        flg(IC<R_F>{});
        const u32 vv = (lane >> 3) * 1024 + ((lane & 7) ^ ((g & 1) << 2)) * 16;
        u32 sv[4];
#pragma unroll
        for (int k = 0; k < 4; k++) sv[k] = sw2 + (12 * rho - (k + rho >= 4 ? 44 : 0)) * 8192;
        const __amdgpu_buffer_rsrc_t rv = __builtin_amdgcn_make_buffer_rsrc((void*)w2, 0, 512 * 1376 * 2, 0x00020000);
        auto ldV = [&](int r) { stage[r] = __builtin_amdgcn_raw_buffer_load_b128(rv, vv, sv[(r + 32) / 12] + (r + 28) * 8192, 0); };
        sfor<7>([&](auto I) {
            constexpr int c = decltype(I)::value + 1;
            if constexpr (THR && c >= 2) wait_vm<THR>();
            if (!(AB & 4)) ldW(IC<c>{});
            if constexpr (c >= 4) {
                SB0();
#pragma unroll
                for (int k = 0; k < NE; k++) ldV(NE * (c - 4) + k);
                SB0();
            }
            if constexpr (c >= 4) TIMED(spin_zero4<1>(aF + (R_P + c - 4) * 16));
            if constexpr (THR && c >= 2) wait_vm<THR>();
            if (!(AB & 4)) ldX(IC<c>{});
            flg(IC<R_F + c>{});
        });
        STAMP(1);
        {
            const u32 vS = ((lane >> 1) * 4 + (lane & 1)) * 16;
            const v4u rst = mkrsrc(ws + OFF_ST, 16384);
            asm volatile("s_mov_b32 m0, %3\n\ts_mov_b64 exec, 0xffff\n\tbuffer_load_dwordx4 %0, %1, %2 offen sc1 lds\n\ts_mov_b64 exec, -1" :: "v"(vS), "s"(rst), "s"(P * 1024 + (w & 1) * 512 + (w >> 1) * 32), "s"(L_STAT + w * 256) : "memory");
        }
        spin_zero4<1>(aF + R_X * 16);
        tag = get_tag();
        const GAS u64* f1p = (const GAS u64*)(ws + OFF_F1) + ((xcd >> 1) * 256 + 64 * w + lane) * FS1;
        const u64 f1 = __hip_atomic_load(f1p, RLX_AGENT);
#pragma unroll
        for (int n = 4 * NE; n < 16; n++) ldV(n);
        SB0();
        if (!all_(f1 == tag)) poll_flags(f1p, tag);
        STAMP(16);
        {
            const v4u rus = mkrsrc(ws + OFF_U, 2 << 20);
            const u32 vU = (lane >> 1) * 8192 + ((lane ^ g) & 1) * 16;
#pragma unroll
            for (int i = 0; i < 2; i++) glds_sc1(rus, vU, (64 * w + 32 * i) * 8192 + cp * 32, L_U + 1024 + w * 2048 + i * 1024);
            flag_dma(rus, L_UF + w * 4);
        }
        STAMP(2);
        pre2();
        spin_zero4<1>(aF + R_P7 * 16);
        STAMP(3);
#pragma unroll
        for (int n = 0; n < 16; n++) pin(stage[n]);
        {
            const u32 aK = L_CNT + 4 * C_K + (lane & 1) * 4;
            int k = 0;
            while (any_(lds_ldv<u32>(aK) < 2) && ++k < (SPIN_CAP << 4)) { __builtin_amdgcn_s_sleep(1); }
        }
        STAMP(14);
        const v4u rw2 = mkrsrc(w2, 512 * 1376 * 2);
        sfor<28>([&](auto I) {
            constexpr int n = decltype(I)::value;
            if constexpr (n == 28 - NV2) asm volatile("global_load_dwordx2 %0, %1, %2 sc1" : "=v"(f2) : "v"(f2o), "s"(ws + OFF_F2) : "memory");
            glds_imm<w2loc(n) * 1024, n * 8192>(rw2, vv, sv[(n + 4) / 12], lw);
            if constexpr (n == 15) flg(IC<R_V1>{});
        });
        flg(IC<R_V2>{});
        asm volatile("s_waitcnt vmcnt(%1)" : "+v"(f2) : "i"(NV2 + 1) : "memory");
        STAMP(8);
        gemm2(IC<0>{}, stage, f2, tag);
    } else if (role == 1) {
        u64 tag;
        __builtin_amdgcn_s_setprio(PRIO / 10);
        {
            const int t = tid - 256;
            const u32 v1 = t < 192 ? ~0u : 0u, v2 = (t >= 26 && t < 30) ? 0u : ~0u;
            lds_st<v4u>((t < 192 ? L_CSP : L_U - 192 * 16) + t * 16, (v4u){v1, v1, v1, v1});
            if (t < 32) lds_st<v4u>(L_C + t * 16, (v4u){v2, v2, v2, v2});
            wait_lgkm0();
            __builtin_amdgcn_s_barrier();
            asm volatile("" ::: "memory");
        }
        if (WARM) warm_flag<FS2 * 8>(ws + OFF_F2, ((4 * P + w) * 16 + j) * FS2 * 8, l16);
        u32 aW[4], aX[4];
#pragma unroll
        for (int s = 0; s < 4; s++) {
            const int ks = (s + w) & 3;
            aW[s] = opq(ks * 32768 + (8 * h + (l15 >> 2)) * 192 + (g & 1) * 32 + (lane & 3) * 8);
            aX[s] = opq(lw + 16384 + l31 * 128 + (((2 * ks + h) ^ ((l31 >> 1) & 7)) * 16));
        }
        const u32 aC = opq(L_CSP + l31 * 32 + h * 16), aCw = opq(L_CSP + lane * 32 + w * 8), aS = opq(L_STAT + w * 256 + l31 * 8), aB = opq(L_B1 + lane * 2);
        pre2();
        const u32 zero = opq(0u);
        f32x16 acc[3]; f32x4 cs = {0.f, 0.f, 0.f, 0.f};
        v4u sel[3];
#pragma unroll
        for (int u = 0; u < 3; u++) {
            for (int i = 0; i < 16; i++) acc[u][i] = 0.f;
            const u32 sv = (l15 == (u < 2 ? 8 * u : 1) + 4 * (g & 1)) ? 0xBF80BF80u : 0u; sel[u] = (v4u){sv, sv, sv, sv}; pin(sel[u]);
        }
        v4u fa[3][3], fb[2][4];
        auto rdW = [&](int n, int u) { const u32 a = aW[n & 3] + ((n >> 2) % 5) * 3072 + u * 64; v2u lo = lds_tr(a), hi = lds_tr(a + 768); fa[n % 3][u] = (v4u){lo.x, lo.y, hi.x, hi.y}; };
        auto rdX = [&](int c, int s) { fb[c & 1][s] = lds_ld<v4u>(aX[s] + (c % 4) * 4096); };
        const v4u rhs = mkrsrc(ws + OFF_H, H_BYTES);
        const int nown = (((j + 1) * 43) >> 4) - u0;
        const u32 sh = ((4 * P + w) * 43 + u0) * 2048, sh2 = nown == 3 ? sh + 4096 : (u32)H_BYTES;
        spin_zero4(aF);
        STAMP(1);
        rdX(0, 0);
#pragma unroll
        for (int u = 0; u < 3; u++) rdW(0, u);
        rdX(0, 1);
#pragma unroll
        for (int u = 0; u < 3; u++) rdW(1, u);
        rdX(0, 2); rdX(0, 3);
        u32 fl, fx, bb[2]; v2u ee; v2u stg; v4u fas[3], fbs;
        sfor<32>([&](auto N_) __attribute__((always_inline)) {
            constexpr int n = decltype(N_)::value, c = n >> 2, s = n & 3;
            SB0();
            if constexpr (s == 2 && c < 5) {
                pin(fa[(n + 1) % 3][2]);
                flg(IC<R_P + c>{});
            }
            if (s == 2 && c < 7 && !(AB & 2)) {
                if (__builtin_expect(any_(fl != 0), 0)) TIMED(spin_zero4(aF + (R_F + c + 1) * 16));
                asm volatile("" ::: "memory");
            }
            sfor<3>([&](auto U_) __attribute__((always_inline)) {
                constexpr int u = decltype(U_)::value;
                acc[u] = mfma32(fa[n % 3][u], fb[c & 1][s], acc[u]);
                if (s == 0 && c < 7) cs = mfma16(sel[u], fa[n % 3][u], cs);
                if (n == 27) cs = mfma16(sel[u], fa[28 % 3][u], cs);
                SB0();
                if (s >= 2 && c < 7 && u < 2) rdX(c + 1, 2 * (s - 2) + u);
                if (n + 2 < 32) rdW(n + 2, u);
                if (s == 1 && u == 0 && c < 7) fl = lds_ldv<u32>(aF + (R_F + c + 1) * 16);
                if constexpr (n == 29 && u == 2) clr<R_P7 * 16>(aFw, zero);
                if (n == 25 && u == 2) { bb[0] = lds_ld<u16>(aB); bb[1] = lds_ld<u16>(aB + 128); }
                if (n == 21 && u == 1) fx = lds_ldv<u32>(aF + R_X * 16);
                if (n == 23 && u == 1) { if (__builtin_expect(any_(fx != 0), 0)) spin_zero4(aF + R_X * 16); asm volatile("" ::: "memory"); ee = lds_ldv<v2u>(L_E); }
                if (n == 28 && u == 1) {
                    v2u t[2];
#pragma unroll
                    for (int k = 0; k < 2; k++) {
                        const float d = cs[k] - bf_hi(f2u(cs[k]));
                        t[k] = (v2u){__builtin_amdgcn_perm(f2u(d), f2u(cs[k]), 0x07060302u), pk_bf(d - bf_hi(f2u(d)), bf_lo(bb[k]))};
                    }
                    asm volatile("ds_write2st64_b64 %0, %1, %2 offset1:4" :: "v"(aCw), "v"(t[0]), "v"(t[1]) : "memory");
                }
                if (n == 31 && u == 0) { fas[0] = lds_ldv<v4u>(aC); fas[1] = lds_ldv<v4u>(aC + 1024); fas[2] = lds_ldv<v4u>(aC + 2048); }
                if (n == 30 && u == 0) stg = lds_ldv<v2u>(aS);
                if (n == 30 && u == 1) { tag = mix64(((u64)(u32)__builtin_amdgcn_readfirstlane(ee.y) << 32) | (u32)__builtin_amdgcn_readfirstlane(ee.x)); asm volatile("" : "+s"(tag)); }
                SB0();
            });
        });
        {
            auto csbad = [&]() { return umax(umax(umax(fas[0].x, fas[0].z), umax(fas[1].x, fas[1].z)), umax(fas[2].x, fas[2].z)) == ~0u; };
            const u32 want = (lane & 1) ? (u32)(tag >> 32) : (u32)tag;
            if (__builtin_expect(any_(csbad() || stg.y != want), 0)) {
                int k = 0;
                while (any_(csbad()) && ++k < (SPIN_CAP << 2)) { fas[0] = lds_ldv<v4u>(aC); fas[1] = lds_ldv<v4u>(aC + 1024); fas[2] = lds_ldv<v4u>(aC + 2048); }
                k = 0;
                const GAS u64* sp = (const GAS u64*)(ws + OFF_ST + P * 1024 + (w & 1) * 512 + (w >> 1) * 32 + (l31 >> 2) * 64 + (lane & 3) * 8);
                while (any_(stg.y != want) && ++k < SPIN_CAP) { const u64 gr = __hip_atomic_load(sp, RLX_AGENT); stg = (v2u){(u32)gr, (u32)(gr >> 32)}; }
            }
            const u32 m1 = stg.x & 0xffffu, m2 = m1 * 0x10001u;
            fbs = (v4u){m2, h ? m1 : stg.x, m2, m1};
        }
#pragma unroll
        for (int u = 0; u < 3; u++) acc[u] = mfma32(fas[u], fbs, acc[u]);
        STAMP(2); CST(1);
        sfor<6>([&](auto K_) __attribute__((always_inline)) {
            constexpr int u = decltype(K_)::value >> 1, s = decltype(K_)::value & 1;
            v4u o;
#pragma unroll
            for (int e = 0; e < 4; e++) {
                const float a0 = acc[u][8 * s + 2 * e], a1 = acc[u][8 * s + 2 * e + 1];
                o[e] = relu_pk(pk_bf(a0 * __builtin_fabsf(a0), a1 * __builtin_fabsf(a1)));
            }
            st_sc1(o, rhs, l16, (u < 2 ? sh + u * 2048 : sh2) + s * 1024);
            if (PACE && decltype(K_)::value < 5) __builtin_amdgcn_s_sleep(PACE);
            if (PACEV < 9) wait_vm<PACEV>();
            SB0();
        });
        STAMP(8);
        wait_vm<0>();
        STAMP(4);
        put_flag<FS2 * 8>(ws + OFF_F2, ((4 * P + w) * 16 + j) * FS2 * 8, l16, tag);
        v4u none[16];
        gemm2(IC<1>{}, none, ~tag, tag);
    } else {
        u64 tag;
        __builtin_amdgcn_s_setprio(3);
        glds(rx, l16, (8 * tb + 2 * w) * 1024, L_KB + (2 * w) * 1088);
        glds(rx, l16, (8 * tb + 2 * w + 1) * 1024, L_KB + (2 * w + 1) * 1088);
        if (w == 0) {
            const v4u re = mkrsrc(ws, 16);
            asm volatile("s_mov_b32 m0, %1\n\ts_mov_b64 exec, 1\n\tbuffer_load_dwordx4 off, %0, 0 sc1 lds\n\ts_mov_b64 exec, -1" :: "s"(re), "s"(L_E) : "memory");
        } else if (w == 2) {
            const v4u rb2 = mkrsrc(b2, 1024);
            asm volatile("s_mov_b32 m0, %3\n\ts_mov_b64 exec, 0xff\n\tbuffer_load_dwordx4 %0, %1, %2 offen lds\n\ts_mov_b64 exec, -1" :: "v"(l16), "s"(rb2), "s"(tc * 128), "s"(L_B2) : "memory");
        } else if (w == 1) {
            const v4u rb1 = mkrsrc(b1, 1376 * 2);
            asm volatile("s_mov_b32 m0, %3\n\ts_nop 0\n\tbuffer_load_dword %0, %1, %2 offen lds" :: "v"(lane * 4), "s"(rb1), "s"(u0 * 64), "s"(L_B1) : "memory");
        }
        const u32 pi = opq((hf * 64 + lane) * 2);
        u32 pA = *(const GAS u16*)((const GAS char*)(logA + ch * 128) + pi), pI = *(const GAS u16*)((const GAS char*)(Aim + ch * 128) + pi);
        u32 pR = *(const GAS u16*)((const GAS char*)(Cre + ch * 128) + pi), pC = *(const GAS u16*)((const GAS char*)(Cim + ch * 128) + pi);
        u32 pT = logdt[ch], pD = Dskip[ch];
        __builtin_amdgcn_s_barrier();
        asm volatile("" ::: "memory");
        flg(IC<R_X>{});
        one = opq(1u);
        const u32 aCnt = opq(L_CNT + chl * 4), aCtl = opq(L_C);
        const v4u rus = mkrsrc(ws + OFF_U, 2 << 20);
        v4u selLo, selHi;
#pragma unroll
        for (int e = 0; e < 4; e++) {
            const u32 o1 = (h == (g & 1) && ((l15 & 7) >> 1) == e) ? ((l15 & 1) ? 0x3C000000u : 0x00003C00u) : 0u;
            selLo[e] = l15 < 8 ? o1 : 0u; selHi[e] = l15 < 8 ? 0u : o1;
        }
        pin(selLo); pin(selHi);
        const int m = 64 * hf + lane;
        const u32 aT = opq(w * 32768 + 16384 + l15 * 64 + g * 16), t0 = opq((2 * chl) * 32768 + 16384 + m * 4), kr = opq(L_KB + chl * 8320 + (1016 - 8 * m) * 2);
        u32 tg[6];
        {
            const bool r1 = m < 32, r2 = m < 64;
            tg[0] = opq(t0 + (r1 ? 528 : 0) * 4); tg[1] = opq(t0 + (r2 ? 400 : 0) * 4); tg[2] = opq(t0 + (r1 ? 560 : 272) * 4);
            tg[3] = opq(t0 + (r1 ? 592 : 272) * 4); tg[4] = opq(t0 + (r2 ? 464 : 1) * 4); tg[5] = opq(t0 + (r1 ? 624 : 1) * 4);
        }
        u32 kb[4], ua[4];
#pragma unroll
        for (int i = 0; i < 4; i++) {
            kb[i] = opq(L_KB + chl * 8320 + ((l15 & 1) ? (239 - l15 + 8 * g) * 2 : 4160 + (240 - l15 + 8 * g) * 2) - hf * 256 - 192 + i * 1024);
            const int os = 30 + 2 * l15 + g - 16 * hf - 4 * i;
            ua[i] = opq(L_U + os * 32 + ((chl ^ (os >> 3)) & 1) * 16);
        }
        const u32 aZ = opq(L_C + (lane & 3) * 4), aZ2 = opq(L_C + (lane & 3) * 4 + ((lane & 4) ? R_P + 4 : R_F + 7) * 16);
        const u32 vA = opq((lane >> 3) * 4096 + (lane & 7) * 16);
        pin(pA); pin(pI); pin(pR); pin(pC); pin(pT); pin(pD);
        if (WARM & 2) warm_flag<FS1 * 8>(ws + OFF_F1, (w * 256 + tb) * FS1 * 8, l16);
        {
            const u32 aRow = opq(L_KB + (l15 & 7) * 1088 + (l15 >> 3) * 512 + g * 16), aTr = opq(L_KB + (l15 >> 2) * 1088 + (128 * w + 16 * g + 4 * (lane & 3)) * 2);
            v4u ones = {0x3F803F80u, 0x3F803F80u, 0x3F803F80u, 0x3F803F80u}; pin(ones);
            spin_zero4(aF + R_X * 16);
            STAMP(1);
            v4u xb[8]; v2u xt[2][2];
#pragma unroll
            for (int s = 0; s < 8; s++) xb[s] = lds_ld<v4u>(aRow + s * 64);
#pragma unroll
            for (int i = 0; i < 2; i++) { xt[i][0] = lds_tr(aTr + i * 128); xt[i][1] = lds_tr(aTr + i * 128 + 4 * 1088); }
            tag = get_tag();
            f32x4 sm = {0.f, 0.f, 0.f, 0.f}, sq = {0.f, 0.f, 0.f, 0.f};
#pragma unroll
            for (int s = 0; s < 8; s++) { sq = mfma16(xb[s], xb[s], sq); sm = mfma16(ones, xb[s], sm); }
            float q = (l15 & 2) ? ((l15 & 1) ? sq[3] : sq[2]) : ((l15 & 1) ? sq[1] : sq[0]);
            q = g == (l15 >> 2) ? q : 0.f;
            { const auto r = __builtin_amdgcn_permlane32_swap(f2u(q), f2u(q), false, false); q = u2f(r[0]) + u2f(r[1]); }
            { const auto r = __builtin_amdgcn_permlane16_swap(f2u(q), f2u(q), false, false); q = u2f(r[0]) + u2f(r[1]); }
            const float mean = dpp_add<0x128>(sm[0]) * (1.f / 512.f);
            const float var = fmaxf(__builtin_fmaf(-mean, mean, dpp_add<0x128>(q) * (1.f / 512.f)), 0.f);
            const u32 p = pk_bf(mean, __builtin_amdgcn_sqrtf(rbf(rbf(var) + 1e-5f)));
            const float mb = bf_lo(p), rr = __builtin_amdgcn_rcpf(bf_hi(p));
            if (w == 0 && lane < 8) __hip_atomic_store((GAS u64*)(ws + OFF_ST + (P * 16 + j) * 64) + lane, ((u64)((lane & 1) ? (u32)(tag >> 32) : (u32)tag) << 32) | p, RLX_AGENT);
            sfor<2>([&](auto I_) {
                constexpr int i = decltype(I_)::value;
                v4u o;
                sfor<4>([&](auto E_) {
                    constexpr int e = decltype(E_)::value;
                    const u32 xv = xt[i][e >> 1][e & 1];
                    const u32 d = pk_bf(bf_lo(xv) - bcast<2 * e>(mb), bf_hi(xv) - bcast<2 * e + 1>(mb));
                    o[e] = pk_bf(bf_lo(d) * bcast<2 * e>(rr), bf_hi(d) * bcast<2 * e + 1>(rr));
                });
                st_sc1(o, rus, l16, tb * 8192 + w * 2048 + i * 1024);
            });
        }
        STAMP(2);
        __builtin_amdgcn_s_setprio(PRIO % 10);
        f32x4 kt[3];
        {
            const float dt = rbf(expf(bf_lo(pT)));
            const float zr = -rbf(expf(bf_lo(pA))) * dt, zi = bf_lo(pI) * dt;
            const float lr = zr * 1.44269504088896341f;
            const float th = zi * 0.159154943091895336f, tl = __builtin_fmaf(zi, 0.159154943091895336f, -th) + zi * 6.4206383e-09f;
            const cmult w1c = mkc(cexp_pow(lr, -th, -tl, 1.f)), qc = mkc(cexp_pow(lr, -th, -tl, 128.f)), q2 = mkc(cexp_pow(lr, -th, -tl, 256.f));
            const cmult w8 = mkc(cexp_pow(lr, th, tl, 8.f)), w32 = mkc(cexp_pow(lr, th, tl, 32.f));
            const u32 aR = pR & 0x7fffu, aI = pC & 0x7fffu;
            int ex = (int)(wave_max(aR > aI ? aR : aI) >> 7); ex = ex < 12 ? 12 : (ex > 240 ? 240 : ex);
            const float sdn = u2f((u32)(262 - ex) << 23), sup = u2f((u32)(ex - 8) << 23);
            auto transpose = [&](const f32x2 (&v)[16], v4u (&f)[4]) {
                f32x16 z; for (int i = 0; i < 16; i++) z[i] = 0.f;
                f32x16 dr = mfma32h((v4u){pk_h2(v[0].x, v[1].x), pk_h2(v[2].x, v[3].x), pk_h2(v[4].x, v[5].x), pk_h2(v[6].x, v[7].x)}, selLo, z);
                f32x16 di = mfma32h((v4u){pk_h2(v[0].y, v[1].y), pk_h2(v[2].y, v[3].y), pk_h2(v[4].y, v[5].y), pk_h2(v[6].y, v[7].y)}, selLo, z);
                dr = mfma32h((v4u){pk_h2(v[8].x, v[9].x), pk_h2(v[10].x, v[11].x), pk_h2(v[12].x, v[13].x), pk_h2(v[14].x, v[15].x)}, selHi, dr);
                di = mfma32h((v4u){pk_h2(v[8].y, v[9].y), pk_h2(v[10].y, v[11].y), pk_h2(v[12].y, v[13].y), pk_h2(v[14].y, v[15].y)}, selHi, di);
#pragma unroll
                for (int e = 0; e < 4; e++) {
                    f[0][e] = pk_h2(dr[2 * e], dr[2 * e + 1]); f[1][e] = pk_h2(dr[8 + 2 * e], dr[9 + 2 * e]);
                    f[2][e] = pk_h2(di[2 * e], di[2 * e + 1]); f[3][e] = pk_h2(di[8 + 2 * e], di[9 + 2 * e]);
                }
            };
            { f32x2 t0_ = w1c.b, t1_ = qc.b, t2_ = q2.b, t3_ = w8.b, t4_ = w32.b; pin(t0_); pin(t1_); pin(t2_); pin(t3_); pin(t4_); }
            STAMP(13);
            wait_vm<0>();
            put_flag<FS1 * 8>(ws + OFF_F1, (w * 256 + tb) * FS1 * 8, l16, tag);
            warm_flag<FS3 * 8>(ws + OFF_F3, (hf * 512 + ch) * FS3 * 8, l16);
            STAMP(14);
            v4u fy[4], fx[4];
            f32x2 v[16], xo[8];
            v[0] = (f32x2){1.f, 0.f}; v[1] = w8.b; v[2] = cmul(w8.b, w8); v[3] = cmul(v[2], w8); v[4] = w32.b;
            xo[0] = (f32x2){bf_lo(pR) * sdn, -bf_lo(pC) * sdn};
#pragma unroll
            for (int i = 5; i < 16; i++) { v[i] = cmul(v[i - 4], w32); if (i < 12) xo[i - 4] = cmul(xo[i - 5], w1c); }
            STAMP(3); CST(2);
            transpose(v, fy);
            const float fin = 2.f * dt * sup;
            auto tile = [&](int t) {
                transpose(v, fx);
                f32x4 d = {0.f, 0.f, 0.f, 0.f};
#pragma unroll
                for (int s = 0; s < 4; s++) d = mfma16h(fy[s], fx[s], d);
                kt[t] = d * fin;
            };
            v[0] = xo[0]; v[1] = cmul(xo[0], qc);
#pragma unroll
            for (int s = 2; s < 16; s++) v[s] = cmul(v[s - 2], q2);
            const f32x2 last = cmul(v[14], q2);
            tile(0);
            v[0] = last; v[1] = xo[4]; v[9] = xo[2]; v[13] = xo[6];
            v[2] = cmul(v[1], qc); v[10] = cmul(v[9], qc); v[14] = cmul(v[13], qc);
#pragma unroll
            for (int s = 3; s < 9; s++) v[s] = cmul(v[s - 2], q2);
            v[11] = cmul(v[9], q2); v[12] = cmul(v[10], q2); v[15] = cmul(v[13], q2);
            const f32x2 last2 = cmul(v[14], q2);
            tile(1);
            v[0] = last2;
#pragma unroll
            for (int i = 0; i < 4; i++) { v[1 + 2 * i] = xo[2 * i + 1]; v[2 + 2 * i] = cmul(xo[2 * i + 1], qc); }
#pragma unroll
            for (int i = 9; i < 16; i++) v[i] = (f32x2){0.f, 0.f};
            tile(2);
        }
        CST(3); STAMP(17);
        pin(kt[0]); pin(kt[1]); pin(kt[2]);
        spin_zero4<1>(aZ2);
        STAMP(11);
#pragma unroll
        for (int t = 0; t < 3; t++) lds_st<f32x4>(aT + t * 1024, kt[t]);
        sig<C_A>(aCtl + (L_CNT - L_C), one);
        STAMP(4);
        spin(aCtl + (L_CNT - L_C) + 4 * C_A, 4);
        {
            auto tv = [&](u32 a) { return lds_ld<float>(a) + lds_ld<float>(a + 32768); };
            float k[9];
            k[0] = tv(t0); k[8] = tv(t0 + 4); k[4] = tv(t0 + 272 * 4);
            k[1] = tv(tg[0]); k[2] = tv(tg[1]); k[3] = tv(tg[2]); k[5] = tv(tg[3]); k[6] = tv(tg[4]); k[7] = tv(tg[5]);
            const float va = tv(t0 + 512), vb = tv(t0 + 516);
            if (m == 0) k[0] += bf_lo(pD);
            const u32 k10 = pk_bf(k[1], k[0]);
            lds_st<v4u>(kr + 2048, (v4u){pk_bf(k[7], k[6]), pk_bf(k[5], k[4]), pk_bf(k[3], k[2]), k10});
            lds_st<v4u>(kr + 2048 + 4160, (v4u){pk_bf(k[8], k[7]), pk_bf(k[6], k[5]), pk_bf(k[4], k[3]), pk_bf(k[2], k[1])});
            const u32 pa = pk_bf(va, va), pb = pk_bf(vb, vb), pba = pk_bf(vb, va);
            lds_st<v4u>(kr, (v4u){pb, (m & 1) ? pb : pba, pa, pa});
            lds_st<v4u>(kr + 4160, (v4u){pb, pb, (m & 1) ? pba : pa, pa});
            if (m < 4) lds_st<v4u>(L_KB + chl * 8320 + 4096 + (m & 1) * 16 + (m < 2 ? 4160 : 0), (v4u){m == 0 ? k10 >> 16 : 0u, 0u, 0u, 0u});
        }
        sig<C_K>(aCnt, one);
        STAMP(18);
        f32x4 cv[8];
        {
#pragma unroll
            for (int t = 0; t < 8; t++) cv[t] = (f32x4){0.f, 0.f, 0.f, 0.f};
            v4u A[8], B[8];
            auto rdA = [&](int r, int a) { const u32 p = kb[(7 - a) >> 1] + ((7 - a) & 1) * 512 + (3 - r) * 64; return (v4u){lds_ld<u32>(p), lds_ld<u32>(p + 4), lds_ld<u32>(p + 8), lds_ld<u32>(p + 12)}; };
            auto rdB = [&](int r, int d) { return lds_ld<v4u>(ua[r] + d * 1024); };
            spin(aCnt + 4 * C_K, 2);
#pragma unroll
            for (int i = 0; i < 8; i++) A[7 - i] = rdA(0, 7 - i);
            STAMP(19);
            spin_zero4(aZ + (L_UF - L_C));
            STAMP(6);
#pragma unroll
            for (int i = 0; i < 8; i++) B[i] = rdB(0, i);
            if (b == 0 && w == 0 && lane == 0) __hip_atomic_store((GAS u64*)ws, tag, RLX_AGENT);
            CST(0);
#pragma unroll
            for (int r = 0; r < 4; r++) {
#pragma unroll
                for (int a = 7; a >= 0; a--) {
#pragma unroll
                    for (int d = 0; d + a < 8; d++) {
                        cv[a + d] = mfma16(A[a], B[d], cv[a + d]);
                        if (a == 0 && r < 3) { SB0(); B[d] = rdB(r + 1, d); SB0(); }
                    }
                    if (r < 3) { SB0(); A[a] = rdA(r + 1, a); SB0(); }
                }
            }
        }
        STAMP(7); CST(1);
        {
            const u32 mine = opq(w * 32768 + 16384 + l16), other = opq((w ^ 1) * 32768 + 16384 + l16);
#pragma unroll
            for (int t = 0; t < 4; t++) lds_st<f32x4>(mine + t * 1024, sel4(hf, cv[t], cv[4 + t]));
            sig<C_E>(aCnt, one);
            sig<C_D>(aCtl + (L_CNT - L_C), one);
            spin(aCnt + 4 * C_E, 2);
            u32 o[8];
#pragma unroll
            for (int t = 0; t < 4; t++) {
                const f32x4 y = sel4(hf, cv[4 + t], cv[t]) + lds_ld<f32x4>(other + t * 1024);
                o[2 * t] = pk_bf(y[0], y[1]); o[2 * t + 1] = pk_bf(y[2], y[3]);
            }
            const v4u ras = mkrsrc(ws + OFF_A, 2 << 20);
            st_sc1((v4u){o[0], o[1], o[2], o[3]}, ras, l16, ch * 4096 + hf * 2048);
            st_sc1((v4u){o[4], o[5], o[6], o[7]}, ras, l16, ch * 4096 + hf * 2048 + 1024);
            STAMP(15);
            wait_vm<0>();
            STAMP(8);
            put_flag<FS3 * 8>(ws + OFF_F3, (hf * 512 + ch) * FS3 * 8, l16, tag);
            spin(aCtl + (L_CNT - L_C) + 4 * C_D, 4);
            if (F3D) __builtin_amdgcn_s_sleep(F3D);
            poll_flags((const GAS u64*)(ws + OFF_F3) + ((tr >> 4) * 512 + tc * 64 + 16 * w + l15) * FS3, tag);
            STAMP(9);
#pragma unroll
            for (int i = 0; i < 2; i++)
                glds_sc1(ras, vA, (tc * 64 + 16 * w + 8 * i) * 4096 + (tr >> 3) * 1024 + (tr & 3) * 256 + ((tr >> 2) & 1) * 128, L_U + w * 2048 + i * 1024);
            const u32 ta = opq(L_U + w * 2048 + (l15 >> 2) * 128 + g * 32 + (lane & 3) * 8), aw = opq(L_KB + lane * 144 + w * 32);
            wait_vm<0>();
            const v2u a0 = lds_tr(ta), a1 = lds_tr(ta + 512), a2 = lds_tr(ta + 1024), a3 = lds_tr(ta + 1536);
            lds_st<v4u>(aw, (v4u){a0.x, a0.y, a1.x, a1.y});
            lds_st<v4u>(aw + 16, (v4u){a2.x, a2.y, a3.x, a3.y});
            STAMP(10);
        }
        wait_lgkm0();
        __builtin_amdgcn_s_barrier();
    }
}

#if defined(__HIP_DEVICE_COMPILE__) && __HIP_DEVICE_COMPILE__
#pragma clang attribute pop
#endif

extern "C" void kernel_launch(void* const* d_in, const int* in_sizes, int n_in, void* d_out, int out_size, void* d_ws, size_t ws_size,
                              hipStream_t stream) {
    fused_kernel<<<256, 768, 0, stream>>>((const u16*)d_in[0], (const u16*)d_in[1], (const u16*)d_in[2], (const u16*)d_in[3], (const u16*)d_in[4],
                                          (const u16*)d_in[5], (const u16*)d_in[6], (const u16*)d_in[8], (const u16*)d_in[9], (const u16*)d_in[10],
                                          (const u16*)d_in[11], (u16*)d_out, (char*)d_ws + (WSOFF == 1 ? ((ws_size - (16u << 20)) & ~(size_t)0x1fffff) : WSOFF == 2 ? ((ws_size / 2) & ~(size_t)0x1fffff) : 0));
}
```

```cpp
#include <hip/hip_runtime.h>
#include <stdint.h>
#include <utility>

typedef unsigned short u16;
typedef unsigned int u32;
typedef unsigned long long u64;
typedef u32 v4u __attribute__((ext_vector_type(4)));
typedef u32 v2u __attribute__((ext_vector_type(2)));
typedef float f32x2 __attribute__((ext_vector_type(2)));
typedef float f32x4 __attribute__((ext_vector_type(4)));
typedef float f32x16 __attribute__((ext_vector_type(16)));
typedef __bf16 bf16x8 __attribute__((ext_vector_type(8)));
typedef __bf16 bf16x2 __attribute__((ext_vector_type(2)));
typedef _Float16 f16x8 __attribute__((ext_vector_type(8)));
typedef _Float16 f16x2 __attribute__((ext_vector_type(2)));
typedef short s16x4 __attribute__((ext_vector_type(4)));
typedef u64 u64x8 __attribute__((ext_vector_type(8)));
typedef u64 u64x4 __attribute__((ext_vector_type(4)));
#define GAS __attribute__((address_space(1)))
#define LAS __attribute__((address_space(3)))
#define DI __device__ __forceinline__
#define RLX_AGENT __ATOMIC_RELAXED, __HIP_MEMORY_SCOPE_AGENT
#define RLX_WG __ATOMIC_RELAXED, __HIP_MEMORY_SCOPE_WORKGROUP
#define SB0() __builtin_amdgcn_sched_barrier(0)
#define CHST 0
#define PSLEEP 2
#define ROT 1
#define POL 1
#define PDS 0
#define THR 0
#define HS 0
#define AB 0
#ifndef NV2
#define NV2 2
#endif
#define WING 10
#define HROT 0
#define GP2 0
#define PFB 29440
#define PF 1
#define FULLF 0
#define WSOFF 0
#define PACE 0
#define PACEV 9
#define F3D 30
#define WARM 1
#define W0G 5
#define W0L 5
#define EX 0
#define MPF 1
#define P7AT 5
#define AX 0
#define SKIP3 1
#define NE 3
#define PRIO 21

#define NOH_OK
#define NWV 12
constexpr int OFF_ST = 16384, OFF_F1 = 128 << 10, OFF_F2 = 256 << 10, OFF_F3 = 512 << 10;
constexpr int FS1 = 8, FS2 = 16, FS3 = 16;
constexpr int OFF_U = 1 << 20, OFF_A = 4 << 20, OFF_H = 8 << 20;
constexpr int H_BYTES = 64 * 43 * 2048;
constexpr int L_C = 131072;
constexpr int L_FLG = L_C, L_UF = L_C + 320, L_E = L_C + 768, L_B2 = L_C + 896, L_LNS = L_C + 352, L_CNT = L_C + 416, L_B1 = L_C + 512, L_STAT = L_C + 1024, L_CSP = L_C + 29696;
enum { R_F = 0, R_P = 8, R_P7 = 13, R_PK = 15, R_X = 14, R_V1 = 16, R_V2 = 17, R_V3 = 18 };
constexpr int L_U = L_C + 3072, L_KB = L_C + 12288;
constexpr int SPIN_CAP = 1 << 16;
enum { C_A = 0, C_D = 1, C_K = 2, C_E = 4, C_PK = 8, C_RD = 12 };

#define STAMP(i)
#define CST(i)
#define TIMED(x) x

#if defined(__HIP_DEVICE_COMPILE__) && __HIP_DEVICE_COMPILE__
#pragma clang attribute push(__attribute__((target("no-packed-fp32-ops"))), apply_to = function)
#endif
DI float u2f(u32 v) { return __builtin_bit_cast(float, v); }
DI u32 f2u(float v) { return __builtin_bit_cast(u32, v); }
DI bool any_(bool c) { return __builtin_amdgcn_ballot_w64(c) != 0; }
DI bool all_(bool c) { return __builtin_amdgcn_ballot_w64(!c) == 0; }
template <typename T> DI T lds_ld(u32 a) { return *(LAS T*)(size_t)a; }
template <typename T> DI void lds_st(u32 a, T v) { *(LAS T*)(size_t)a = v; }
DI v2u lds_tr(u32 a) { return __builtin_bit_cast(v2u, __builtin_amdgcn_ds_read_tr16_b64_v4i16((LAS s16x4*)(size_t)a)); }
DI v4u mkrsrc(const void* p, u32 bytes) { u64 a = (u64)p; v4u r; r.x = (u32)a; r.y = (u32)(a >> 32) & 0xffffu; r.z = bytes; r.w = 0x00020000u; return r; }
DI void glds(v4u rsrc, u32 voff, u32 soff, u32 lds) {
    asm volatile("s_mov_b32 m0, %3\n\ts_nop 0\n\tbuffer_load_dwordx4 %0, %1, %2 offen lds" :: "v"(voff), "s"(rsrc), "s"(soff), "s"(lds) : "memory");
}
template <int LOFF> DI u32 glds_first(v4u rsrc, u32 voff, u32 sa, u32 sb, u32 lbase) {
    u32 so;
    asm volatile("s_add_u32 m0, %5, %6\n\ts_add_u32 %0, %3, %4\n\tbuffer_load_dwordx4 %1, %2, %0 offen lds" : "=&s"(so) : "v"(voff), "s"(rsrc), "s"(sa), "s"(sb), "s"(lbase), "i"(LOFF) : "memory", "scc");
    return so;
}
template <int LOFF, int SOFF> DI void glds_imm(v4u rsrc, u32 voff, u32 sa, u32 lbase) {
    u32 so;
    asm volatile("s_add_u32 m0, %4, %5\n\ts_add_u32 %0, %3, %6\n\tbuffer_load_dwordx4 %1, %2, %0 offen lds" : "=&s"(so) : "v"(voff), "s"(rsrc), "s"(sa), "s"(lbase), "i"(LOFF), "i"(SOFF) : "memory", "scc");
}
template <int OFF> DI void glds_next(v4u rsrc, u32 voff, u32 so) {
    asm volatile("buffer_load_dwordx4 %0, %1, %2 offen offset:%3 lds" :: "v"(voff), "s"(rsrc), "s"(so), "i"(OFF) : "memory");
}
template <int LOFF> DI void flag_imm(v4u rsrc, u32 lbase) {
    asm volatile("s_add_u32 m0, %2, %3\n\ts_mov_b64 exec, 1\n\tbuffer_load_dword off, %0, %1 lds\n\ts_mov_b64 exec, -1" :: "s"(rsrc), "s"(rsrc.z), "s"(lbase), "i"(LOFF) : "memory", "scc");
}
DI void glds_sc1(v4u rsrc, u32 voff, u32 soff, u32 lds) {
    asm volatile("s_mov_b32 m0, %3\n\ts_nop 0\n\tbuffer_load_dwordx4 %0, %1, %2 offen sc1 lds" :: "v"(voff), "s"(rsrc), "s"(soff), "s"(lds) : "memory");
}
DI void flag_dma(v4u rsrc, u32 lds) {
    asm volatile("s_mov_b32 m0, %2\n\ts_mov_b64 exec, 1\n\tbuffer_load_dword off, %0, %1 lds\n\ts_mov_b64 exec, -1" :: "s"(rsrc), "s"(rsrc.z), "s"(lds) : "memory");
}
DI void st_sc1(v4u v, v4u rsrc, u32 voff, u32 soff) {
    asm volatile("buffer_store_dwordx4 %0, %1, %2, %3 offen sc1\n\ts_nop 1" :: "v"(v), "v"(voff), "s"(rsrc), "s"(soff) : "memory");
}
template <int BYTES> DI void put_flag(const char* base, u32 off, u32 l16, u64 tag) {
    asm volatile("s_mov_b64 exec, 1\n\tglobal_store_dwordx2 %0, %1, %2 sc1\n\ts_mov_b64 exec, -1" :: "v"(l16), "v"(tag), "s"(base + off) : "memory");
}
template <int BYTES> DI void warm_flag(const char* base, u32 off, u32 l16) {
    const v4u v = {0u, 0u, 0u, 0u};
    asm volatile("s_mov_b64 exec, %3\n\tglobal_store_dwordx4 %0, %1, %2 sc1\n\ts_mov_b64 exec, -1\n\ts_nop 0" :: "v"(l16), "v"(v), "s"(base + off), "i"((1 << (BYTES / 16)) - 1) : "memory");
}
DI u32 opq(u32 a) { asm volatile("" : "+v"(a)); return a; }
template <typename T> DI T lds_ldv(u32 a) { return *(volatile LAS T*)(size_t)a; }
template <int SLEEP = 0> DI void spin_zero4(u32 a) {
    int n = 0;
    while (any_(lds_ldv<u32>(a) != 0) && ++n < (SPIN_CAP << 4)) { if (SLEEP) __builtin_amdgcn_s_sleep(1); }
    asm volatile("" ::: "memory");
}
template <int N> DI void wait_vm() { asm volatile("s_waitcnt vmcnt(%0)" :: "i"(N) : "memory"); }
DI void wait_lgkm0() { asm volatile("s_waitcnt lgkmcnt(0)" ::: "memory"); }
DI u64 mix64(u64 e) { e += 0x9E3779B97F4A7C15ull; e ^= e >> 31; e *= 0xBF58476D1CE4E5B9ull; e ^= e >> 29; return e; }
DI u64 get_tag() { const v2u e = lds_ldv<v2u>(L_E); return mix64(((u64)(u32)__builtin_amdgcn_readfirstlane(e.y) << 32) | (u32)__builtin_amdgcn_readfirstlane(e.x)); }
template <int C> DI void sig(u32 a, u32 one) { asm volatile("s_mov_b64 exec, 1\n\tds_add_u32 %0, %1 offset:%2\n\ts_mov_b64 exec, -1" :: "v"(a), "v"(one), "i"(4 * C) : "memory"); }
template <int OFF> DI void clr(u32 a, u32 zero) { asm volatile("s_mov_b64 exec, 1\n\tds_write_b32 %0, %1 offset:%2\n\ts_mov_b64 exec, -1" :: "v"(a), "v"(zero), "i"(OFF) : "memory"); }
DI void spin(u32 a, u32 target) {
    int n = 0;
    while ((u32)__builtin_amdgcn_readfirstlane(lds_ldv<u32>(a)) < target && ++n < (SPIN_CAP << 4)) {}
    asm volatile("" ::: "memory");
}
DI void poll_flags(const GAS u64* p, u64 tag) {
    int n = 0;
    while (true) {
        u64 f = __hip_atomic_load(p, RLX_AGENT);
        if (all_(f == tag) || ++n > SPIN_CAP) break;
        __builtin_amdgcn_s_sleep(PSLEEP);
    }
    asm volatile("" ::: "memory");
}
DI float bf_lo(u32 v) { return u2f(v << 16); }
DI float bf_hi(u32 v) { return u2f(v & 0xffff0000u); }
DI u32 pk_bf(float a, float b) { f32x2 v = {a, b}; return __builtin_bit_cast(u32, __builtin_convertvector(v, bf16x2)); }
typedef short s16x2 __attribute__((ext_vector_type(2)));
DI u32 relu_pk(u32 v) { return __builtin_bit_cast(u32, __builtin_elementwise_max(__builtin_bit_cast(s16x2, v), (s16x2){0, 0})); }
DI u32 umax(u32 a, u32 b) { return a > b ? a : b; }
DI float rbf(float a) { return bf_lo(pk_bf(a, a)); }
DI u32 pk_h(f32x2 v) { return __builtin_bit_cast(u32, __builtin_convertvector(v, f16x2)); }
DI f32x16 mfma32(v4u a, v4u b, f32x16 c) { return __builtin_amdgcn_mfma_f32_32x32x16_bf16(__builtin_bit_cast(bf16x8, a), __builtin_bit_cast(bf16x8, b), c, 0, 0, 0); }
DI f32x4 mfma16(v4u a, v4u b, f32x4 c) { return __builtin_amdgcn_mfma_f32_16x16x32_bf16(__builtin_bit_cast(bf16x8, a), __builtin_bit_cast(bf16x8, b), c, 0, 0, 0); }
DI f32x4 mfma16h(v4u a, v4u b, f32x4 c) { return __builtin_amdgcn_mfma_f32_16x16x32_f16(__builtin_bit_cast(f16x8, a), __builtin_bit_cast(f16x8, b), c, 0, 0, 0); }
template <int CTRL> DI float dpp_add(float v) { return v + __builtin_bit_cast(float, __builtin_amdgcn_update_dpp(0, __builtin_bit_cast(int, v), CTRL, 0xf, 0xf, true)); }
template <int CTRL> DI u32 dpp_max(u32 v) { const u32 o = (u32)__builtin_amdgcn_update_dpp(0, (int)v, CTRL, 0xf, 0xf, true); return v > o ? v : o; }
template <int T> DI float bcast(float v) { return __builtin_bit_cast(float, __builtin_amdgcn_update_dpp(0, __builtin_bit_cast(int, v), 0x150 + T, 0xf, 0xf, true)); }
DI float rl(float v, int l) { return __builtin_bit_cast(float, __builtin_amdgcn_readlane(__builtin_bit_cast(int, v), l)); }
DI f32x2 wave_sum2(f32x2 v) {
    float a = v.x, c = v.y;
    a = dpp_add<0xB1>(a); c = dpp_add<0xB1>(c); a = dpp_add<0x4E>(a); c = dpp_add<0x4E>(c);
    a = dpp_add<0x141>(a); c = dpp_add<0x141>(c); a = dpp_add<0x140>(a); c = dpp_add<0x140>(c);
    return ((f32x2){rl(a, 0), rl(c, 0)} + (f32x2){rl(a, 16), rl(c, 16)}) + ((f32x2){rl(a, 32), rl(c, 32)} + (f32x2){rl(a, 48), rl(c, 48)});
}
DI u32 wave_max(u32 v) {
    v = dpp_max<0xB1>(v); v = dpp_max<0x4E>(v); v = dpp_max<0x141>(v); v = dpp_max<0x140>(v);
    const u32 a = __builtin_amdgcn_readlane(v, 0), c = __builtin_amdgcn_readlane(v, 16), d = __builtin_amdgcn_readlane(v, 32), e = __builtin_amdgcn_readlane(v, 48);
    const u32 p = a > c ? a : c, q = d > e ? d : e;
    return p > q ? p : q;
}
struct cmult { f32x2 b, n; };
DI cmult mkc(f32x2 b) { cmult c; c.b = b; c.n = (f32x2){-b.y, b.x}; return c; }
DI f32x2 cmul(f32x2 a, const cmult& c) { return __builtin_elementwise_fma((f32x2){a.x, a.x}, c.b, (f32x2){a.y, a.y} * c.n); }
DI f32x16 mfma32h(v4u a, v4u b, f32x16 c) { return __builtin_amdgcn_mfma_f32_32x32x16_f16(__builtin_bit_cast(f16x8, a), __builtin_bit_cast(f16x8, b), c, 0, 0, 0); }
DI u32 pk_h2(float a, float b) { return pk_h((f32x2){a, b}); }
DI f32x2 cexp_pow(float lr, float th, float tl, float e) {
    float a = __builtin_amdgcn_fractf(th * e) + tl * e;
    float m = __builtin_amdgcn_exp2f(lr * e);
    f32x2 r; r.x = m * __builtin_amdgcn_cosf(a); r.y = m * __builtin_amdgcn_sinf(a); return r;
}
DI f32x4 sel4(bool c, f32x4 a, f32x4 b) { return c ? a : b; }
constexpr int w2loc(int n) { return n < 16 ? n : n < 28 ? n + 4 : n - 28; }
template <class F, int... I> DI void sfor_(F&& f, std::integer_sequence<int, I...>) { (f(std::integral_constant<int, I>{}), ...); }
template <int N, class F> DI void sfor(F&& f) { sfor_(f, std::make_integer_sequence<int, N>{}); }
template <int I> using IC = std::integral_constant<int, I>;
template <typename T> DI void pin(T& v) { asm volatile("" : "+v"(v)); }

__global__ void __launch_bounds__(768, 3)
fused_kernel(const u16* x_, const u16* logA_, const u16* Aim_, const u16* Cre_, const u16* Cim_, const u16* logdt_, const u16* Dskip_, const u16* w1_,
             const u16* b1_, const u16* w2_, const u16* b2_, u16* out_, char* ws_) {
    u64x8 ka; u64x4 kb4; u64 kc;
    asm volatile("s_load_dwordx16 %0, %3, 0x0\n\ts_load_dwordx8 %1, %3, 0x40\n\ts_load_dwordx2 %2, %3, 0x60" : "=&s"(ka), "=&s"(kb4), "=&s"(kc) : "s"(__builtin_amdgcn_kernarg_segment_ptr()) : "memory");
    __shared__ __attribute__((aligned(16))) char smem[163840];
    asm volatile("" :: "v"((u32)(size_t)(LAS char*)smem));
    const int tid = threadIdx.x, lane = tid & 63, wave = __builtin_amdgcn_readfirstlane(tid >> 6) & 15, w = wave & 3, role = wave >> 2;
    const int b = blockIdx.x & 255, xcd = b & 7, slot = b >> 3;
    const int P = 4 * (xcd >> 1) + (slot >> 3), j = 8 * (xcd & 1) + (slot & 7);
    const int tr = 4 * xcd + (slot >> 3), tc = slot & 7;
    const int u0 = (j * 43) >> 4;
    if ((PF & 1) && wave == 4) {
        u64 pc; asm volatile("s_getpc_b64 %0" : "=s"(pc));
        const v4u rc = mkrsrc((const void*)(pc & ~255ull), PFB);
        glds(rc, lane * 16, slot * 1024, L_C + 2048);
    }
    const int h = lane >> 5, l31 = lane & 31, l15 = lane & 15, g = lane >> 4;
    const u32 l16 = lane * 16, lw = w * 32768, w4 = w * 4;
    const int rho = HROT ? ((slot >> 3) + slot) & 3 : (slot >> 3);
    const int tb = 64 * (P >> 2) + 32 * (j & 1) + 16 * ((P >> 1) & 1) + 2 * (j >> 1) + (P & 1);
    const int cp = 32 * xcd + slot, chl = w >> 1, hf = w & 1, ch = 2 * cp + chl;
    {
        const u32 v = (tid >= 192 && tid < 256) || (tid >= 282 && tid < 286) ? 0u : ~0u;
        if (tid < 288) lds_st<v4u>((tid < 192 ? L_CSP : tid < 256 ? L_U - 192 * 16 : L_C - 256 * 16) + tid * 16, (v4u){v, v, v, v});
    }
    const int rot = ROT == 0 ? 0 : ROT == 1 ? (((slot >> 3) + slot) & 1) * 4 : ROT == 2 ? (((slot >> 3) + slot) & 3) * 2 : (2 * (slot >> 3) + (slot & 7)) & 7;
    u32 sx = (512 * (P >> 2) + 128 * ((P >> 1) & 1) + 64 * (w & 1) + 8 * (P & 1) + 4 * (w >> 1)) * 1024, sw1 = 16 * w * 2752 + u0 * 64;
    u32 sx0 = rot * 128, sw0 = rot * (64 * 2752);
    u32 vx[4], vw[3];
    const int lim3 = 4 * ((((j + 1) * 43) >> 4) - u0);
#pragma unroll
    for (int i = 0; i < 3; i++) { const int p = 64 * i + lane, r = (p * 171) >> 11; vw[i] = opq((SKIP3 && p - 12 * r >= lim3) ? 0x40000000u : r * (2752 - 192) + l16); }
#pragma unroll
    for (int i = 0; i < 4; i++) vx[i] = opq((((h * 256 + ((lane >> 3) & 3)) * 1024 + ((lane & 7) ^ g) * 16) ^ ((i & 1) * 64)) + i * (16384 - 1024));
    asm volatile("s_waitcnt lgkmcnt(0)\n\ts_barrier" : "+s"(ka), "+s"(kb4), "+s"(kc), "+s"(sx), "+s"(sw1), "+s"(sx0), "+s"(sw0) :: "memory");
    const u16 *x = (const u16*)ka[0], *logA = (const u16*)ka[1], *Aim = (const u16*)ka[2], *Cre = (const u16*)ka[3], *Cim = (const u16*)ka[4], *logdt = (const u16*)ka[5], *Dskip = (const u16*)ka[6], *w1 = (const u16*)ka[7];
    const u16 *b1 = (const u16*)kb4[0], *w2 = (const u16*)kb4[1], *b2 = (const u16*)kb4[2]; u16* out = (u16*)kb4[3]; char* ws = (char*)kc;
    if (CHST == 3) STAMP(17);
    const v4u rx = mkrsrc(x, 2 << 20);
    const u32 aF = opq(L_FLG + (lane & 3) * 4);
    auto flg = [&](auto R_) { flag_imm<L_FLG + decltype(R_)::value * 16>(rx, w4); };
    const v4u rw1 = mkrsrc(w1, 512 * 1376 * 2);
    auto ldX = [&](auto C_) { constexpr int c = decltype(C_)::value;
        const u32 so = glds_first<(16 + (c % 4) * 4) * 1024>(rx, vx[0], sx, c ? ((c + rot) & 7) * 128 : sx0, lw);
        glds_next<1024>(rx, vx[1], so); glds_next<2048>(rx, vx[2], so); glds_next<3072>(rx, vx[3], so); };
    auto ldW = [&](auto C_) { constexpr int c = decltype(C_)::value;
        const u32 so = glds_first<3 * (c % 5) * 1024>(rw1, vw[0], sw1, c ? ((c + rot) & 7) * (64 * 2752) : sw0, lw);
        glds_next<1024>(rw1, vw[1], so); if (!(AX & 4)) glds_next<2048>(rw1, vw[2], so); };
    if (role == 0) {
        if (CHST == 3) STAMP(18);
        ldW(IC<0>{}); ldX(IC<0>{}); flg(IC<R_F>{});
        ldW(IC<1>{}); ldX(IC<1>{}); flg(IC<R_F + 1>{});
        if (CHST == 3) { STAMP(19); wait_vm<15>(); STAMP(20); }
    }
    asm volatile("" ::: "memory");
    u32 aV0, aV1, aFw, aCn, one, f2o;
    auto pre2 = [&]() {
        aV0 = opq(lw + (4 * h + (l15 >> 2)) * 128 + ((l15 >> 3) & 1) * 64 + (g & 1) * 32 + (lane & 3) * 8); aV1 = opq(aV0 ^ 64);
        aFw = opq(L_FLG + w4); aCn = opq(L_CNT + w4); one = opq(1u);
        const int jj = 4 * w + (lane < 4 ? lane : 4);
        f2o = opq(((2 * tr + role) * 16 + (jj < 15 ? jj : 15)) * FS2 * 8);
    };

    auto rdV = [&](int s, int dt) { v2u lo = lds_tr((dt ? aV1 : aV0) + w2loc(2 * s) * 1024), hi = lds_tr((dt ? aV1 : aV0) + w2loc(2 * s + 1) * 1024); return (v4u){lo.x, lo.y, hi.x, hi.y}; };
    auto gemm2 = [&](auto ROLE_, v4u (&stage)[16], u64 f2, u64 tag) __attribute__((always_inline)) {
        constexpr int role = decltype(ROLE_)::value;
        const GAS u64* f2p = (const GAS u64*)(ws + OFF_F2 + f2o);
        __builtin_amdgcn_s_setprio(role ? GP2 : 0);
        v4u park[8][2];
        if (role) {
            SB0();
            spin_zero4<1>(aFw + R_V1 * 16);
            STAMP(9);
            f2 = __hip_atomic_load(f2p, RLX_AGENT);
            SB0();
#pragma unroll
            for (int s = 0; s < 8; s++) { park[s][0] = rdV(s, 0); park[s][1] = rdV(s, 1); }
            clr<R_PK * 16>(aFw, opq(0u));
            SB0();
        }
        STAMP(10);
        if (!all_(f2 == tag)) poll_flags(f2p, tag);
        STAMP(5);
        const __amdgpu_buffer_rsrc_t rh = __builtin_amdgcn_make_buffer_rsrc(ws + OFF_H, 0, H_BYTES, 0x00020000);
        u32 sh2[4], sh2t[4];
#pragma unroll
        for (int k = 0; k < 4; k++) { sh2[k] = ((2 * tr + role) * 43 + 11 * w) * 2048 + (6 * rho - (k + rho >= 4 ? 22 : 0)) * 1024; sh2t[k] = (w == 3 && k + rho == 3) ? (u32)H_BYTES : sh2[k]; }
        constexpr int WIN = WING;
        v4u hq[WIN], vb[3][2];
        auto ldH = [&](int s) { hq[s % WIN] = __builtin_amdgcn_raw_buffer_load_b128(rh, l16, ((s + 2) % 6 >= 4 ? sh2t[(s + 2) / 6] : sh2[(s + 2) / 6]) + s * 1024, 16); };
        constexpr int W0 = role ? W0G : W0L;
#pragma unroll
        for (int s = 0; s < W0; s++) ldH(s);
        const int fr = tid >> 3;
        const u32 fin_off = (512 * (tr >> 3) + 256 * ((fr >> 2) & 1) + 128 * ((tr >> 2) & 1) + 16 * (fr >> 3) + 4 * (tr & 3) + (fr & 3)) * 512 + tc * 64 + (tid & 7) * 8;
        v4u fin_x, fin_b; u32 fin_s, fv;
        f32x16 ac2[2];
#pragma unroll
        for (int i = 0; i < 16; i++) { ac2[0][i] = 0.f; ac2[1][i] = 0.f; }
        if (!role) {
            const u32 aSt = opq(lw + l16), zero = opq(0u);
            SB0();
            spin_zero4(opq(aFw + ((lane & 1) ? R_PK : R_V2) * 16));
            STAMP(9);
#pragma unroll
            for (int s = 0; s < 8; s++) {
                park[s][0] = rdV(s, 0); park[s][1] = rdV(s, 1);
                asm volatile("" ::: "memory");
                lds_st<v4u>(aSt + 2 * s * 1024, stage[2 * s]); lds_st<v4u>(aSt + (2 * s + 1) * 1024, stage[2 * s + 1]);
                SB0();
            }
            clr<R_V3 * 16>(aFw, zero);
            STAMP(13);
        }
#pragma unroll
        for (int s = 0; s < 22; s++) {
            SB0();
            if (role && (s == 4 || s == 10)) fv = lds_ldv<u32>(aFw + (s == 4 ? R_V2 : R_V3) * 16);
            if (role && (s == 6 || s == 12)) { if (__builtin_expect(any_(fv != 0), 0)) spin_zero4(aFw + (s == 6 ? R_V2 : R_V3) * 16); asm volatile("" ::: "memory"); }
            if (s + 2 >= 8 && s + 2 < 22) { vb[(s + 2) % 3][0] = rdV(s + 2, 0); vb[(s + 2) % 3][1] = rdV(s + 2, 1); }
            if (s == 19) sig<C_RD>(aCn, one);
            if (s == 21) fv = lds_ldv<u32>(aCn + 4 * C_RD);
            ac2[0] = mfma32(s < 8 ? park[s & 7][0] : vb[s % 3][0], hq[s % WIN], ac2[0]);
            ac2[1] = mfma32(s < 8 ? park[s & 7][1] : vb[s % 3][1], hq[s % WIN], ac2[1]);
            SB0();
            if (s < WIN - W0) { ldH(W0 + 2 * s); ldH(W0 + 2 * s + 1); }
            else if (s + WIN < 22) ldH(s + WIN);
            if (s == 14) {
                fin_x = *(const GAS v4u*)(x + fin_off); fin_b = lds_ld<v4u>(L_B2 + (lane & 7) * 16);
                fin_s = __hip_atomic_load((GAS u32*)(ws + OFF_ST + (tr >> 1) * 1024 + (fr >> 2) * 64 + (tr & 1) * 32 + (fr & 3) * 8), RLX_AGENT);
            }
        }
        SB0();
        STAMP(7); CST(3);
        if (__builtin_expect(any_(fv < 2), 0)) spin(aCn + 4 * C_RD, 2);
        asm volatile("" ::: "memory");
        const u32 da = opq(lw + role * 20480 + l31 * 272 + h * 16);
#pragma unroll
        for (int dt = 0; dt < 2; dt++)
#pragma unroll
            for (int q = 0; q < 4; q++)
                lds_st<f32x4>(da + (32 * dt + 8 * q) * 4, (f32x4){ac2[dt][4 * q], ac2[dt][4 * q + 1], ac2[dt][4 * q + 2], ac2[dt][4 * q + 3]});
        f32x4 s0 = {bf_lo(fin_x.x) + bf_lo(fin_b.x), bf_hi(fin_x.x) + bf_hi(fin_b.x), bf_lo(fin_x.y) + bf_lo(fin_b.y), bf_hi(fin_x.y) + bf_hi(fin_b.y)};
        f32x4 s1 = {bf_lo(fin_x.z) + bf_lo(fin_b.z), bf_hi(fin_x.z) + bf_hi(fin_b.z), bf_lo(fin_x.w) + bf_lo(fin_b.w), bf_hi(fin_x.w) + bf_hi(fin_b.w)};
        const u32 ra = opq(role * 20480 + (fr & 31) * 272 + (tid & 7) * 32), rb = opq(ra + 65536), rc = opq(L_KB + fr * 144 + (tid & 7) * 16);
        float rs2 = __builtin_amdgcn_rcpf(bf_hi(fin_s)); rs2 *= rs2;
        asm volatile("" : "+v"(s0), "+v"(s1), "+v"(rs2));
        wait_lgkm0();
        __builtin_amdgcn_s_barrier();
        asm volatile("" ::: "memory");
        STAMP(11);
        const v4u av = lds_ld<v4u>(rc);
        s0 += ((lds_ld<f32x4>(ra) + lds_ld<f32x4>(ra + 32768)) + (lds_ld<f32x4>(rb) + lds_ld<f32x4>(rb + 32768))) * rs2;
        s1 += ((lds_ld<f32x4>(ra + 16) + lds_ld<f32x4>(ra + 32768 + 16)) + (lds_ld<f32x4>(rb + 16) + lds_ld<f32x4>(rb + 32768 + 16))) * rs2;
        s0 += (f32x4){bf_lo(av.x), bf_hi(av.x), bf_lo(av.y), bf_hi(av.y)};
        s1 += (f32x4){bf_lo(av.z), bf_hi(av.z), bf_lo(av.w), bf_hi(av.w)};
        *(GAS v4u*)(out + fin_off) = (v4u){pk_bf(s0[0], s0[1]), pk_bf(s0[2], s0[3]), pk_bf(s1[0], s1[1]), pk_bf(s1[2], s1[3])};
    };

    if (__builtin_expect(role == 0, 1)) {
        v4u stage[16]; u64 f2, tag;
        const u32 sw2 = 352 * w * 1024 + tc * 128;
        const u32 vv = (lane >> 3) * 1024 + ((lane & 7) ^ ((g & 1) << 2)) * 16;
        u32 sv[4];
#pragma unroll
        for (int k = 0; k < 4; k++) sv[k] = sw2 + (12 * rho - (k + rho >= 4 ? 44 : 0)) * 8192;
        const __amdgpu_buffer_rsrc_t rv = __builtin_amdgcn_make_buffer_rsrc((void*)w2, 0, (AX & 64) ? 0 : 512 * 1376 * 2, 0x00020000);
        auto ldV = [&](int r) { stage[r] = __builtin_amdgcn_raw_buffer_load_b128(rv, vv, sv[(r + 32) / 12] + (r + 28) * 8192, 0); };
        sfor<6>([&](auto I) {
            constexpr int c = decltype(I)::value + 2;
            if constexpr (THR && c >= 2) wait_vm<THR>();
            if (!(AB & 4)) ldW(IC<c>{});
            if constexpr (c >= 4) {
                SB0();
#pragma unroll
                for (int k = 0; k < NE; k++) ldV(NE * (c - 4) + k);
                SB0();
            }
            if constexpr (c >= 4) TIMED(spin_zero4<1>(aF + (R_P + c - 4) * 16));
            if constexpr (THR && c >= 2) wait_vm<THR>();
            if (!(AB & 4)) ldX(IC<c>{});
            flg(IC<R_F + c>{});
        });
        STAMP(1);
        {
            const u32 vS = ((lane >> 1) * 4 + (lane & 1)) * 16;
            const v4u rst = mkrsrc(ws + OFF_ST, 16384);
            asm volatile("s_mov_b32 m0, %3\n\ts_mov_b64 exec, 0xffff\n\tbuffer_load_dwordx4 %0, %1, %2 offen sc1 lds\n\ts_mov_b64 exec, -1" :: "v"(vS), "s"(rst), "s"(P * 1024 + (w & 1) * 512 + (w >> 1) * 32), "s"(L_STAT + w * 256) : "memory");
        }
        spin_zero4<1>(aF + R_X * 16);
        tag = get_tag();
        const GAS u64* f1p = (const GAS u64*)(ws + OFF_F1) + ((xcd >> 1) * 256 + 64 * w + ((AX & 1) ? (lane & 3) : lane)) * FS1;
        const u64 f1 = __hip_atomic_load(f1p, RLX_AGENT);
#pragma unroll
        for (int n = 4 * NE; n < 16; n++) ldV(n);
        SB0();
        if (!all_(f1 == tag)) poll_flags(f1p, tag);
        STAMP(16);
        {
            const v4u rus = mkrsrc(ws + OFF_U, 2 << 20);
            const u32 vU = (lane >> 1) * 8192 + ((lane ^ g) & 1) * 16;
#pragma unroll
            for (int i = 0; i < ((AX & 1) ? 0 : 2); i++) glds_sc1(rus, vU, (64 * w + 32 * i) * 8192 + cp * 32, L_U + 1024 + w * 2048 + i * 1024);
            flag_dma(rus, L_UF + w * 4);
        }
        STAMP(2);
        pre2();
        spin_zero4<1>(aF + R_P7 * 16);
        STAMP(3);
#pragma unroll
        for (int n = 0; n < 16; n++) pin(stage[n]);
        {
            const u32 aK = L_CNT + 4 * C_K + (lane & 1) * 4;
            int k = 0;
            while (any_(lds_ldv<u32>(aK) < 2) && ++k < (SPIN_CAP << 4)) { __builtin_amdgcn_s_sleep(1); }
        }
        STAMP(14);
        const v4u rw2 = mkrsrc(w2, 512 * 1376 * 2);
        sfor<28>([&](auto I) {
            constexpr int n = decltype(I)::value;
            if constexpr (n == 28 - NV2) asm volatile("global_load_dwordx2 %0, %1, %2 sc1" : "=v"(f2) : "v"(f2o), "s"(ws + OFF_F2) : "memory");
            if constexpr (!(AX & 32) || n < 4 || n >= 16) glds_imm<w2loc(n) * 1024, n * 8192>(rw2, vv, sv[(n + 4) / 12], lw);
            if constexpr (n == 15) flg(IC<R_V1>{});
        });
        flg(IC<R_V2>{});
        asm volatile("s_waitcnt vmcnt(%1)" : "+v"(f2) : "i"(NV2 + 1) : "memory");
        STAMP(8);
        gemm2(IC<0>{}, stage, f2, tag);
    } else if (role == 1) {
        u64 tag;
        __builtin_amdgcn_s_setprio(PRIO / 10);
        if (WARM) warm_flag<FS2 * 8>(ws + OFF_F2, ((4 * P + w) * 16 + j) * FS2 * 8, l16);
        if (MPF) {
            const v4u rp = mkrsrc(w2, 512 * 1376 * 2);
            if (MPF & 2) {
                glds(rw1, l16, (b * 11 + 2 * w) * 512, L_C + 2048);
                if (w >= 2) glds(rw1, l16, (b * 11 + 4 + 2 * w) * 512, L_C + 2048);
            }
            glds(rp, l16, (b * 11 + 2 * w) * 512, L_C + 2048);
            if (w < 2) glds(rp, l16, (b * 11 + 8 + 2 * w) * 512, L_C + 2048);
        }
        u32 aW[4], aX[4];
#pragma unroll
        for (int s = 0; s < 4; s++) {
            const int ks = (s + w) & 3;
            aW[s] = opq(ks * 32768 + (8 * h + (l15 >> 2)) * 192 + (g & 1) * 32 + (lane & 3) * 8);
            aX[s] = opq(lw + 16384 + l31 * 128 + (((2 * ks + h) ^ ((l31 >> 1) & 7)) * 16));
        }
        const u32 aC = opq(L_CSP + l31 * 32 + h * 16), aCw = opq(L_CSP + lane * 32 + w * 8), aS = opq(L_STAT + w * 256 + l31 * 8), aB = opq(L_B1 + lane * 2);
        pre2();
        const u32 zero = opq(0u);
        f32x16 acc[3]; f32x4 cs = {0.f, 0.f, 0.f, 0.f};
        v4u sel[3];
#pragma unroll
        for (int u = 0; u < 3; u++) {
            for (int i = 0; i < 16; i++) acc[u][i] = 0.f;
            const u32 sv = (l15 == (u < 2 ? 8 * u : 1) + 4 * (g & 1)) ? 0xBF80BF80u : 0u; sel[u] = (v4u){sv, sv, sv, sv}; pin(sel[u]);
        }
        v4u fa[3][3], fb[2][4];
        auto rdW = [&](int n, int u) { const u32 a = aW[n & 3] + ((n >> 2) % 5) * 3072 + u * 64; v2u lo = lds_tr(a), hi = lds_tr(a + 768); fa[n % 3][u] = (v4u){lo.x, lo.y, hi.x, hi.y}; };
        auto rdX = [&](int c, int s) { fb[c & 1][s] = lds_ld<v4u>(aX[s] + (c % 4) * 4096); };
        const v4u rhs = mkrsrc(ws + OFF_H, (AX & 8) ? 0 : H_BYTES);
        const int nown = (((j + 1) * 43) >> 4) - u0;
        const u32 sh = ((4 * P + w) * 43 + u0) * 2048, sh2 = nown == 3 ? sh + 4096 : (u32)H_BYTES;
        spin_zero4<(EX >> 2) & 1>(aF);
        STAMP(1);
        rdX(0, 0);
#pragma unroll
        for (int u = 0; u < 3; u++) rdW(0, u);
        rdX(0, 1);
#pragma unroll
        for (int u = 0; u < 3; u++) rdW(1, u);
        rdX(0, 2); rdX(0, 3);
        u32 fl, fx, bb[2]; v2u ee; v2u stg; v4u fas[3], fbs;
        sfor<32>([&](auto N_) __attribute__((always_inline)) {
            constexpr int n = decltype(N_)::value, c = n >> 2, s = n & 3;
            SB0();
            if constexpr (s == 2 && c < 5) {
                pin(fa[(n + 1) % 3][2]);
                flg(IC<R_P + c>{});
            }
            if (s == 2 && c < 7 && !(AB & 2)) {
                if (__builtin_expect(any_(fl != 0), 0)) TIMED(spin_zero4<(EX >> 2) & 1>(aF + (R_F + c + 1) * 16));
                asm volatile("" ::: "memory");
            }
            if (CHST == 2 && n == 26) STAMP(3);
            sfor<3>([&](auto U_) __attribute__((always_inline)) {
                constexpr int u = decltype(U_)::value;
                if (!(EX & 2) || u < 2) acc[u] = mfma32(fa[n % 3][u], fb[c & 1][s], acc[u]);
                if (EX & 2) pin(fa[n % 3][u]);
                if (s == 0 && c < 7 && !(EX & 1)) cs = mfma16(sel[u], fa[n % 3][u], cs);
                if (n == 27) cs = mfma16(sel[u], fa[28 % 3][u], cs);
                SB0();
                if (s >= 2 && c < 7 && u < 2) rdX(c + 1, 2 * (s - 2) + u);
                if (n + 2 < 32) rdW(n + 2, u);
                if (s == 1 && u == 0 && c < 7) fl = lds_ldv<u32>(aF + (R_F + c + 1) * 16);
                if constexpr (P7AT < 0 && n == 29 && u == 2) clr<R_P7 * 16>(aFw, zero);
                if (n == 25 && u == 2) { bb[0] = lds_ld<u16>(aB); bb[1] = lds_ld<u16>(aB + 128); }
                if (n == 21 && u == 1) fx = lds_ldv<u32>(aF + R_X * 16);
                if (n == 23 && u == 1) { if (__builtin_expect(any_(fx != 0), 0)) spin_zero4(aF + R_X * 16); asm volatile("" ::: "memory"); ee = lds_ldv<v2u>(L_E); }
                if (n == 28 && u == 1) {
                    v2u t[2];
#pragma unroll
                    for (int k = 0; k < 2; k++) {
                        const float d = cs[k] - bf_hi(f2u(cs[k]));
                        t[k] = (v2u){__builtin_amdgcn_perm(f2u(d), f2u(cs[k]), 0x07060302u), pk_bf(d - bf_hi(f2u(d)), bf_lo(bb[k]))};
                    }
                    asm volatile("ds_write2st64_b64 %0, %1, %2 offset1:4" :: "v"(aCw), "v"(t[0]), "v"(t[1]) : "memory");
                }
                if (n == 31 && u == 0) { fas[0] = lds_ldv<v4u>(aC); fas[1] = lds_ldv<v4u>(aC + 1024); fas[2] = lds_ldv<v4u>(aC + 2048); }
                if (n == 30 && u == 0) stg = lds_ldv<v2u>(aS);
                if (n == 30 && u == 1) { tag = mix64(((u64)(u32)__builtin_amdgcn_readfirstlane(ee.y) << 32) | (u32)__builtin_amdgcn_readfirstlane(ee.x)); asm volatile("" : "+s"(tag)); }
                SB0();
            });
        });
        if (CHST == 2) STAMP(19);
        {
            auto csbad = [&]() { return umax(umax(umax(fas[0].x, fas[0].z), umax(fas[1].x, fas[1].z)), umax(fas[2].x, fas[2].z)) == ~0u; };
            const u32 want = (lane & 1) ? (u32)(tag >> 32) : (u32)tag;
            if (__builtin_expect(any_(csbad() || stg.y != want), 0)) {
                int k = 0;
                while (any_(csbad()) && ++k < (SPIN_CAP << 2)) { fas[0] = lds_ldv<v4u>(aC); fas[1] = lds_ldv<v4u>(aC + 1024); fas[2] = lds_ldv<v4u>(aC + 2048); }
                k = 0;
                const GAS u64* sp = (const GAS u64*)(ws + OFF_ST + P * 1024 + (w & 1) * 512 + (w >> 1) * 32 + (l31 >> 2) * 64 + (lane & 3) * 8);
                while (any_(stg.y != want) && ++k < SPIN_CAP) { const u64 gr = __hip_atomic_load(sp, RLX_AGENT); stg = (v2u){(u32)gr, (u32)(gr >> 32)}; }
            }
            const u32 m1 = stg.x & 0xffffu, m2 = m1 * 0x10001u;
            fbs = (v4u){m2, h ? m1 : stg.x, m2, m1};
        }
#pragma unroll
        for (int u = 0; u < 3; u++) acc[u] = mfma32(fas[u], fbs, acc[u]);
        STAMP(2); CST(1);
        sfor<6>([&](auto K_) __attribute__((always_inline)) {
            constexpr int u = decltype(K_)::value >> 1, s = decltype(K_)::value & 1;
            v4u o;
#pragma unroll
            for (int e = 0; e < 4; e++) {
                const float a0 = acc[u][8 * s + 2 * e], a1 = acc[u][8 * s + 2 * e + 1];
                o[e] = relu_pk(pk_bf(a0 * __builtin_fabsf(a0), a1 * __builtin_fabsf(a1)));
            }
            st_sc1(o, rhs, l16, (u < 2 ? sh + u * 2048 : sh2) + s * 1024);
            if constexpr (decltype(K_)::value == P7AT) clr<R_P7 * 16>(aFw, zero);
            if (PACE && decltype(K_)::value < 5) __builtin_amdgcn_s_sleep(PACE);
            if (PACEV < 9) wait_vm<PACEV>();
            SB0();
        });
        STAMP(8);
        if (P7AT >= 6) { wait_vm<(P7AT >= 6 ? 11 - P7AT : 0)>(); clr<R_P7 * 16>(aFw, zero); }
        wait_vm<0>();
        STAMP(4);
        put_flag<FS2 * 8>(ws + OFF_F2, ((4 * P + w) * 16 + j) * FS2 * 8, l16, tag);
        v4u none[16];
        gemm2(IC<1>{}, none, ~tag, tag);
    } else {
        u64 tag;
        __builtin_amdgcn_s_setprio(3);
        glds(rx, l16, (8 * tb + 2 * w) * 1024, L_KB + (2 * w) * 1088);
        glds(rx, l16, (8 * tb + 2 * w + 1) * 1024, L_KB + (2 * w + 1) * 1088);
        if (w == 0) {
            const v4u re = mkrsrc(ws, 16);
            asm volatile("s_mov_b32 m0, %1\n\ts_mov_b64 exec, 1\n\tbuffer_load_dwordx4 off, %0, 0 sc1 lds\n\ts_mov_b64 exec, -1" :: "s"(re), "s"(L_E) : "memory");
        } else if (w == 2) {
            const v4u rb2 = mkrsrc(b2, 1024);
            asm volatile("s_mov_b32 m0, %3\n\ts_mov_b64 exec, 0xff\n\tbuffer_load_dwordx4 %0, %1, %2 offen lds\n\ts_mov_b64 exec, -1" :: "v"(l16), "s"(rb2), "s"(tc * 128), "s"(L_B2) : "memory");
        } else if (w == 1) {
            const v4u rb1 = mkrsrc(b1, 1376 * 2);
            asm volatile("s_mov_b32 m0, %3\n\ts_nop 0\n\tbuffer_load_dword %0, %1, %2 offen lds" :: "v"(lane * 4), "s"(rb1), "s"(u0 * 64), "s"(L_B1) : "memory");
        }
        const u32 pi = opq((hf * 64 + lane) * 2);
        u32 pA = *(const GAS u16*)((const GAS char*)(logA + ch * 128) + pi), pI = *(const GAS u16*)((const GAS char*)(Aim + ch * 128) + pi);
        u32 pR = *(const GAS u16*)((const GAS char*)(Cre + ch * 128) + pi), pC = *(const GAS u16*)((const GAS char*)(Cim + ch * 128) + pi);
        u32 pT = *(const GAS u16*)(logdt + ch), pD = *(const GAS u16*)(Dskip + ch);
        flg(IC<R_X>{});
        one = opq(1u);
        const u32 aCnt = opq(L_CNT + chl * 4), aCtl = opq(L_C);
        const v4u rus = mkrsrc(ws + OFF_U, 2 << 20);
        v4u selLo, selHi;
#pragma unroll
        for (int e = 0; e < 4; e++) {
            const u32 o1 = (h == (g & 1) && ((l15 & 7) >> 1) == e) ? ((l15 & 1) ? 0x3C000000u : 0x00003C00u) : 0u;
            selLo[e] = l15 < 8 ? o1 : 0u; selHi[e] = l15 < 8 ? 0u : o1;
        }
        pin(selLo); pin(selHi);
        const int m = 64 * hf + lane;
        const u32 aT = opq(w * 32768 + 16384 + l15 * 64 + g * 16), t0 = opq((2 * chl) * 32768 + 16384 + m * 4), kr = opq(L_KB + chl * 8320 + (1016 - 8 * m) * 2);
        u32 tg[6];
        {
            const bool r1 = m < 32, r2 = m < 64;
            tg[0] = opq(t0 + (r1 ? 528 : 0) * 4); tg[1] = opq(t0 + (r2 ? 400 : 0) * 4); tg[2] = opq(t0 + (r1 ? 560 : 272) * 4);
            tg[3] = opq(t0 + (r1 ? 592 : 272) * 4); tg[4] = opq(t0 + (r2 ? 464 : 1) * 4); tg[5] = opq(t0 + (r1 ? 624 : 1) * 4);
        }
        u32 kb[4], ua[4];
#pragma unroll
        for (int i = 0; i < 4; i++) {
            kb[i] = opq(L_KB + chl * 8320 + ((l15 & 1) ? (239 - l15 + 8 * g) * 2 : 4160 + (240 - l15 + 8 * g) * 2) - hf * 256 - 192 + i * 1024);
            const int os = 30 + 2 * l15 + g - 16 * hf - 4 * i;
            ua[i] = opq(L_U + os * 32 + ((chl ^ (os >> 3)) & 1) * 16);
        }
        const u32 aZ = opq(L_C + (lane & 3) * 4), aZ2 = opq(L_C + (lane & 3) * 4 + ((lane & 4) ? R_P + 4 : R_F + 7) * 16);
        const u32 vA = opq((lane >> 3) * 4096 + (lane & 7) * 16);
        pin(pA); pin(pI); pin(pR); pin(pC); pin(pT); pin(pD);
        if (WARM & 2) warm_flag<FS1 * 8>(ws + OFF_F1, (w * 256 + tb) * FS1 * 8, l16);
        {
            const u32 aRow = opq(L_KB + (l15 & 7) * 1088 + (l15 >> 3) * 512 + g * 16), aTr = opq(L_KB + (l15 >> 2) * 1088 + (128 * w + 16 * g + 4 * (lane & 3)) * 2);
            v4u ones = {0x3F803F80u, 0x3F803F80u, 0x3F803F80u, 0x3F803F80u}; pin(ones);
            spin_zero4(aF + R_X * 16);
            STAMP(1);
            v4u xb[8]; v2u xt[2][2];
#pragma unroll
            for (int s = 0; s < 8; s++) xb[s] = lds_ld<v4u>(aRow + s * 64);
#pragma unroll
            for (int i = 0; i < 2; i++) { xt[i][0] = lds_tr(aTr + i * 128); xt[i][1] = lds_tr(aTr + i * 128 + 4 * 1088); }
            tag = get_tag();
            f32x4 sm = {0.f, 0.f, 0.f, 0.f}, sq = {0.f, 0.f, 0.f, 0.f};
#pragma unroll
            for (int s = 0; s < 8; s++) { sq = mfma16(xb[s], xb[s], sq); sm = mfma16(ones, xb[s], sm); }
            float q = (l15 & 2) ? ((l15 & 1) ? sq[3] : sq[2]) : ((l15 & 1) ? sq[1] : sq[0]);
            q = g == (l15 >> 2) ? q : 0.f;
            { const auto r = __builtin_amdgcn_permlane32_swap(f2u(q), f2u(q), false, false); q = u2f(r[0]) + u2f(r[1]); }
            { const auto r = __builtin_amdgcn_permlane16_swap(f2u(q), f2u(q), false, false); q = u2f(r[0]) + u2f(r[1]); }
            const float mean = dpp_add<0x128>(sm[0]) * (1.f / 512.f);
            const float var = fmaxf(__builtin_fmaf(-mean, mean, dpp_add<0x128>(q) * (1.f / 512.f)), 0.f);
            const u32 p = pk_bf(mean, __builtin_amdgcn_sqrtf(rbf(rbf(var) + 1e-5f)));
            const float mb = bf_lo(p), rr = __builtin_amdgcn_rcpf(bf_hi(p));
            if (w == 0 && lane < 8) __hip_atomic_store((GAS u64*)(ws + OFF_ST + (P * 16 + j) * 64) + lane, ((u64)((lane & 1) ? (u32)(tag >> 32) : (u32)tag) << 32) | p, RLX_AGENT);
            sfor<2>([&](auto I_) {
                constexpr int i = decltype(I_)::value;
                v4u o;
                sfor<4>([&](auto E_) {
                    constexpr int e = decltype(E_)::value;
                    const u32 xv = xt[i][e >> 1][e & 1];
                    const u32 d = pk_bf(bf_lo(xv) - bcast<2 * e>(mb), bf_hi(xv) - bcast<2 * e + 1>(mb));
                    o[e] = pk_bf(bf_lo(d) * bcast<2 * e>(rr), bf_hi(d) * bcast<2 * e + 1>(rr));
                });
                st_sc1(o, rus, l16, (AX & 2) ? 2u << 20 : tb * 8192 + w * 2048 + i * 1024);
            });
        }
        STAMP(2);
        __builtin_amdgcn_s_setprio(PRIO % 10);
        f32x4 kt[3];
        {
            const float dt = rbf(expf(bf_lo(pT)));
            const float zr = -rbf(expf(bf_lo(pA))) * dt, zi = bf_lo(pI) * dt;
            const float lr = zr * 1.44269504088896341f;
            const float th = zi * 0.159154943091895336f, tl = __builtin_fmaf(zi, 0.159154943091895336f, -th) + zi * 6.4206383e-09f;
            const cmult w1c = mkc(cexp_pow(lr, -th, -tl, 1.f)), qc = mkc(cexp_pow(lr, -th, -tl, 128.f)), q2 = mkc(cexp_pow(lr, -th, -tl, 256.f));
            const cmult w8 = mkc(cexp_pow(lr, th, tl, 8.f)), w32 = mkc(cexp_pow(lr, th, tl, 32.f));
            const u32 aR = pR & 0x7fffu, aI = pC & 0x7fffu;
            int ex = (int)(wave_max(aR > aI ? aR : aI) >> 7); ex = ex < 12 ? 12 : (ex > 240 ? 240 : ex);
            const float sdn = u2f((u32)(262 - ex) << 23), sup = u2f((u32)(ex - 8) << 23);
            auto transpose = [&](const f32x2 (&v)[16], v4u (&f)[4]) {
                f32x16 z; for (int i = 0; i < 16; i++) z[i] = 0.f;
                f32x16 dr = mfma32h((v4u){pk_h2(v[0].x, v[1].x), pk_h2(v[2].x, v[3].x), pk_h2(v[4].x, v[5].x), pk_h2(v[6].x, v[7].x)}, selLo, z);
                f32x16 di = mfma32h((v4u){pk_h2(v[0].y, v[1].y), pk_h2(v[2].y, v[3].y), pk_h2(v[4].y, v[5].y), pk_h2(v[6].y, v[7].y)}, selLo, z);
                dr = mfma32h((v4u){pk_h2(v[8].x, v[9].x), pk_h2(v[10].x, v[11].x), pk_h2(v[12].x, v[13].x), pk_h2(v[14].x, v[15].x)}, selHi, dr);
                di = mfma32h((v4u){pk_h2(v[8].y, v[9].y), pk_h2(v[10].y, v[11].y), pk_h2(v[12].y, v[13].y), pk_h2(v[14].y, v[15].y)}, selHi, di);
#pragma unroll
                for (int e = 0; e < 4; e++) {
                    f[0][e] = pk_h2(dr[2 * e], dr[2 * e + 1]); f[1][e] = pk_h2(dr[8 + 2 * e], dr[9 + 2 * e]);
                    f[2][e] = pk_h2(di[2 * e], di[2 * e + 1]); f[3][e] = pk_h2(di[8 + 2 * e], di[9 + 2 * e]);
                }
            };
            { f32x2 t0_ = w1c.b, t1_ = qc.b, t2_ = q2.b, t3_ = w8.b, t4_ = w32.b; pin(t0_); pin(t1_); pin(t2_); pin(t3_); pin(t4_); }
            STAMP(13);
            wait_vm<0>();
            put_flag<FS1 * 8>(ws + OFF_F1, (w * 256 + tb) * FS1 * 8, l16, tag);
            warm_flag<FS3 * 8>(ws + OFF_F3, (hf * 512 + ch) * FS3 * 8, l16);
            STAMP(14);
            v4u fy[4], fx[4];
            f32x2 v[16], xo[8];
            v[0] = (f32x2){1.f, 0.f}; v[1] = w8.b; v[2] = cmul(w8.b, w8); v[3] = cmul(v[2], w8); v[4] = w32.b;
            xo[0] = (f32x2){bf_lo(pR) * sdn, -bf_lo(pC) * sdn};
#pragma unroll
            for (int i = 5; i < 16; i++) { v[i] = cmul(v[i - 4], w32); if (i < 12) xo[i - 4] = cmul(xo[i - 5], w1c); }
            STAMP(3); CST(2);
            transpose(v, fy);
            const float fin = 2.f * dt * sup;
            auto tile = [&](int t) {
                transpose(v, fx);
                f32x4 d = {0.f, 0.f, 0.f, 0.f};
#pragma unroll
                for (int s = 0; s < 4; s++) d = mfma16h(fy[s], fx[s], d);
                kt[t] = d * fin;
            };
            v[0] = xo[0]; v[1] = cmul(xo[0], qc);
#pragma unroll
            for (int s = 2; s < 16; s++) v[s] = cmul(v[s - 2], q2);
            const f32x2 last = cmul(v[14], q2);
            tile(0);
            v[0] = last; v[1] = xo[4]; v[9] = xo[2]; v[13] = xo[6];
            v[2] = cmul(v[1], qc); v[10] = cmul(v[9], qc); v[14] = cmul(v[13], qc);
#pragma unroll
            for (int s = 3; s < 9; s++) v[s] = cmul(v[s - 2], q2);
            v[11] = cmul(v[9], q2); v[12] = cmul(v[10], q2); v[15] = cmul(v[13], q2);
            const f32x2 last2 = cmul(v[14], q2);
            tile(1);
            v[0] = last2;
#pragma unroll
            for (int i = 0; i < 4; i++) { v[1 + 2 * i] = xo[2 * i + 1]; v[2 + 2 * i] = cmul(xo[2 * i + 1], qc); }
#pragma unroll
            for (int i = 9; i < 16; i++) v[i] = (f32x2){0.f, 0.f};
            tile(2);
        }
        CST(3); STAMP(17);
        pin(kt[0]); pin(kt[1]); pin(kt[2]);
        spin_zero4<1>(aZ2);
        STAMP(11);
#pragma unroll
        for (int t = 0; t < 3; t++) lds_st<f32x4>(aT + t * 1024, kt[t]);
        sig<C_A>(aCtl + (L_CNT - L_C), one);
        STAMP(4);
        spin(aCtl + (L_CNT - L_C) + 4 * C_A, 4);
        {
            auto tv = [&](u32 a) { return lds_ld<float>(a) + lds_ld<float>(a + 32768); };
            float k[9];
            k[0] = tv(t0); k[8] = tv(t0 + 4); k[4] = tv(t0 + 272 * 4);
            k[1] = tv(tg[0]); k[2] = tv(tg[1]); k[3] = tv(tg[2]); k[5] = tv(tg[3]); k[6] = tv(tg[4]); k[7] = tv(tg[5]);
            const float va = tv(t0 + 512), vb = tv(t0 + 516);
            if (m == 0) k[0] += bf_lo(pD);
            const u32 k10 = pk_bf(k[1], k[0]);
            lds_st<v4u>(kr + 2048, (v4u){pk_bf(k[7], k[6]), pk_bf(k[5], k[4]), pk_bf(k[3], k[2]), k10});
            lds_st<v4u>(kr + 2048 + 4160, (v4u){pk_bf(k[8], k[7]), pk_bf(k[6], k[5]), pk_bf(k[4], k[3]), pk_bf(k[2], k[1])});
            const u32 pa = pk_bf(va, va), pb = pk_bf(vb, vb), pba = pk_bf(vb, va);
            lds_st<v4u>(kr, (v4u){pb, (m & 1) ? pb : pba, pa, pa});
            lds_st<v4u>(kr + 4160, (v4u){pb, pb, (m & 1) ? pba : pa, pa});
            if (m < 4) lds_st<v4u>(L_KB + chl * 8320 + 4096 + (m & 1) * 16 + (m < 2 ? 4160 : 0), (v4u){m == 0 ? k10 >> 16 : 0u, 0u, 0u, 0u});
        }
        sig<C_K>(aCnt, one);
        STAMP(18);
        f32x4 cv[8];
        {
#pragma unroll
            for (int t = 0; t < 8; t++) cv[t] = (f32x4){0.f, 0.f, 0.f, 0.f};
            v4u A[8], B[8];
            auto rdA = [&](int r, int a) { const u32 p = kb[(7 - a) >> 1] + ((7 - a) & 1) * 512 + (3 - r) * 64; return (v4u){lds_ld<u32>(p), lds_ld<u32>(p + 4), lds_ld<u32>(p + 8), lds_ld<u32>(p + 12)}; };
            auto rdB = [&](int r, int d) { return lds_ld<v4u>(ua[r] + d * 1024); };
            spin(aCnt + 4 * C_K, 2);
#pragma unroll
            for (int i = 0; i < 8; i++) A[7 - i] = rdA(0, 7 - i);
            STAMP(19);
            spin_zero4(aZ + (L_UF - L_C));
            STAMP(6);
#pragma unroll
            for (int i = 0; i < 8; i++) B[i] = rdB(0, i);
            if (b == 0 && w == 0 && lane == 0) __hip_atomic_store((GAS u64*)ws, tag, RLX_AGENT);
            CST(0);
#pragma unroll
            for (int r = 0; r < 4; r++) {
#pragma unroll
                for (int a = 7; a >= 0; a--) {
#pragma unroll
                    for (int d = 0; d + a < 8; d++) {
                        cv[a + d] = mfma16(A[a], B[d], cv[a + d]);
                        if (a == 0 && r < 3) { SB0(); B[d] = rdB(r + 1, d); SB0(); }
                    }
                    if (r < 3) { SB0(); A[a] = rdA(r + 1, a); SB0(); }
                }
            }
        }
        STAMP(7); CST(1);
        {
            const u32 mine = opq(w * 32768 + 16384 + l16), other = opq((w ^ 1) * 32768 + 16384 + l16);
#pragma unroll
            for (int t = 0; t < 4; t++) lds_st<f32x4>(mine + t * 1024, sel4(hf, cv[t], cv[4 + t]));
            sig<C_E>(aCnt, one);
            sig<C_D>(aCtl + (L_CNT - L_C), one);
            spin(aCnt + 4 * C_E, 2);
            u32 o[8];
#pragma unroll
            for (int t = 0; t < 4; t++) {
                const f32x4 y = sel4(hf, cv[4 + t], cv[t]) + lds_ld<f32x4>(other + t * 1024);
                o[2 * t] = pk_bf(y[0], y[1]); o[2 * t + 1] = pk_bf(y[2], y[3]);
            }
            const v4u ras = mkrsrc(ws + OFF_A, 2 << 20);
            st_sc1((v4u){o[0], o[1], o[2], o[3]}, ras, l16, (AX & 16) ? 2u << 20 : ch * 4096 + hf * 2048);
            st_sc1((v4u){o[4], o[5], o[6], o[7]}, ras, l16, (AX & 16) ? 2u << 20 : ch * 4096 + hf * 2048 + 1024);
            STAMP(15);
            wait_vm<0>();
            STAMP(8);
            put_flag<FS3 * 8>(ws + OFF_F3, (hf * 512 + ch) * FS3 * 8, l16, tag);
            spin(aCtl + (L_CNT - L_C) + 4 * C_D, 4);
            if (F3D) __builtin_amdgcn_s_sleep(F3D);
            poll_flags((const GAS u64*)(ws + OFF_F3) + ((tr >> 4) * 512 + tc * 64 + 16 * w + l15) * FS3, tag);
            STAMP(9);
#pragma unroll
            for (int i = 0; i < 2; i++)
                glds_sc1(ras, vA, (tc * 64 + 16 * w + 8 * i) * 4096 + (tr >> 3) * 1024 + (tr & 3) * 256 + ((tr >> 2) & 1) * 128, L_U + w * 2048 + i * 1024);
            const u32 ta = opq(L_U + w * 2048 + (l15 >> 2) * 128 + g * 32 + (lane & 3) * 8), aw = opq(L_KB + lane * 144 + w * 32);
            wait_vm<0>();
            const v2u a0 = lds_tr(ta), a1 = lds_tr(ta + 512), a2 = lds_tr(ta + 1024), a3 = lds_tr(ta + 1536);
            lds_st<v4u>(aw, (v4u){a0.x, a0.y, a1.x, a1.y});
            lds_st<v4u>(aw + 16, (v4u){a2.x, a2.y, a3.x, a3.y});
            STAMP(10);
        }
        wait_lgkm0();
        __builtin_amdgcn_s_barrier();
    }
}

#if defined(__HIP_DEVICE_COMPILE__) && __HIP_DEVICE_COMPILE__
#pragma clang attribute pop
#endif

extern "C" void kernel_launch(void* const* d_in, const int* in_sizes, int n_in, void* d_out, int out_size, void* d_ws, size_t ws_size,
                              hipStream_t stream) {
    fused_kernel<<<256, 768, 0, stream>>>((const u16*)d_in[0], (const u16*)d_in[1], (const u16*)d_in[2], (const u16*)d_in[3], (const u16*)d_in[4],
                                          (const u16*)d_in[5], (const u16*)d_in[6], (const u16*)d_in[8], (const u16*)d_in[9], (const u16*)d_in[10],
                                          (const u16*)d_in[11], (u16*)d_out, (char*)d_ws + (WSOFF == 1 ? ((ws_size - (16u << 20)) & ~(size_t)0x1fffff) : WSOFF == 2 ? ((ws_size / 2) & ~(size_t)0x1fffff) : 0));
}
```

```cpp
#define QTY 0
#define PROLE 1
#define PROBE 1
#include <hip/hip_runtime.h>
#include <stdint.h>
#include <utility>

typedef unsigned short u16;
typedef unsigned int u32;
typedef unsigned long long u64;
typedef u32 v4u __attribute__((ext_vector_type(4)));
typedef u32 v2u __attribute__((ext_vector_type(2)));
typedef float f32x2 __attribute__((ext_vector_type(2)));
typedef float f32x4 __attribute__((ext_vector_type(4)));
typedef float f32x16 __attribute__((ext_vector_type(16)));
typedef __bf16 bf16x8 __attribute__((ext_vector_type(8)));
typedef __bf16 bf16x2 __attribute__((ext_vector_type(2)));
typedef _Float16 f16x8 __attribute__((ext_vector_type(8)));
typedef _Float16 f16x2 __attribute__((ext_vector_type(2)));
typedef short s16x4 __attribute__((ext_vector_type(4)));
typedef u64 u64x8 __attribute__((ext_vector_type(8)));
typedef u64 u64x4 __attribute__((ext_vector_type(4)));
#define GAS __attribute__((address_space(1)))
#define LAS __attribute__((address_space(3)))
#define DI __device__ __forceinline__
#define RLX_AGENT __ATOMIC_RELAXED, __HIP_MEMORY_SCOPE_AGENT
#define RLX_WG __ATOMIC_RELAXED, __HIP_MEMORY_SCOPE_WORKGROUP
#define SB0() __builtin_amdgcn_sched_barrier(0)
#define CHST 0
#define PSLEEP 2
#define ROT 1
#define POL 1
#define PDS 0
#define THR 0
#define HS 0
#define AB 0
#ifndef NV2
#define NV2 2
#endif
#define WING 10
#define HROT 0
#define GP2 0
#define PFB 29440
#define PF 1
#define FULLF 0
#define WSOFF 0
#define PACE 0
#define PACEV 9
#define F3D 30
#define WARM 1
#define W0G 5
#define W0L 5
#define EX 0
#define MPF 1
#define P7AT 5
#define AX 0
#define SKIP3 1
#define NE 3
#define PRIO 21

#define NOH_OK
#define NWV 12
constexpr int OFF_ST = 16384, OFF_F1 = 128 << 10, OFF_F2 = 256 << 10, OFF_F3 = 512 << 10;
constexpr int FS1 = 8, FS2 = 16, FS3 = 16;
constexpr int OFF_U = 1 << 20, OFF_A = 4 << 20, OFF_H = 8 << 20;
constexpr int H_BYTES = 64 * 43 * 2048;
constexpr int L_C = 131072;
constexpr int L_FLG = L_C, L_UF = L_C + 320, L_E = L_C + 768, L_B2 = L_C + 896, L_LNS = L_C + 352, L_CNT = L_C + 416, L_B1 = L_C + 512, L_STAT = L_C + 1024, L_CSP = L_C + 29696;
enum { R_F = 0, R_P = 8, R_P7 = 13, R_PK = 15, R_X = 14, R_V1 = 16, R_V2 = 17, R_V3 = 18 };
constexpr int L_U = L_C + 3072, L_KB = L_C + 12288;
constexpr int SPIN_CAP = 1 << 16;
enum { C_A = 0, C_D = 1, C_K = 2, C_E = 4, C_PK = 8, C_RD = 12 };

#define STAMP(i) do { if ((i) == PROBE && prole == PROLE) { probe_t = (u32)__builtin_amdgcn_s_memrealtime(); } } while (0)
#define CST(i)
#define TIMED(x) x

#if defined(__HIP_DEVICE_COMPILE__) && __HIP_DEVICE_COMPILE__
#pragma clang attribute push(__attribute__((target("no-packed-fp32-ops"))), apply_to = function)
#endif
DI float u2f(u32 v) { return __builtin_bit_cast(float, v); }
DI u32 f2u(float v) { return __builtin_bit_cast(u32, v); }
DI bool any_(bool c) { return __builtin_amdgcn_ballot_w64(c) != 0; }
DI bool all_(bool c) { return __builtin_amdgcn_ballot_w64(!c) == 0; }
template <typename T> DI T lds_ld(u32 a) { return *(LAS T*)(size_t)a; }
template <typename T> DI void lds_st(u32 a, T v) { *(LAS T*)(size_t)a = v; }
DI v2u lds_tr(u32 a) { return __builtin_bit_cast(v2u, __builtin_amdgcn_ds_read_tr16_b64_v4i16((LAS s16x4*)(size_t)a)); }
DI v4u mkrsrc(const void* p, u32 bytes) { u64 a = (u64)p; v4u r; r.x = (u32)a; r.y = (u32)(a >> 32) & 0xffffu; r.z = bytes; r.w = 0x00020000u; return r; }
DI void glds(v4u rsrc, u32 voff, u32 soff, u32 lds) {
    asm volatile("s_mov_b32 m0, %3\n\ts_nop 0\n\tbuffer_load_dwordx4 %0, %1, %2 offen lds" :: "v"(voff), "s"(rsrc), "s"(soff), "s"(lds) : "memory");
}
template <int LOFF> DI u32 glds_first(v4u rsrc, u32 voff, u32 sa, u32 sb, u32 lbase) {
    u32 so;
    asm volatile("s_add_u32 m0, %5, %6\n\ts_add_u32 %0, %3, %4\n\tbuffer_load_dwordx4 %1, %2, %0 offen lds" : "=&s"(so) : "v"(voff), "s"(rsrc), "s"(sa), "s"(sb), "s"(lbase), "i"(LOFF) : "memory", "scc");
    return so;
}
template <int LOFF, int SOFF> DI void glds_imm(v4u rsrc, u32 voff, u32 sa, u32 lbase) {
    u32 so;
    asm volatile("s_add_u32 m0, %4, %5\n\ts_add_u32 %0, %3, %6\n\tbuffer_load_dwordx4 %1, %2, %0 offen lds" : "=&s"(so) : "v"(voff), "s"(rsrc), "s"(sa), "s"(lbase), "i"(LOFF), "i"(SOFF) : "memory", "scc");
}
template <int OFF> DI void glds_next(v4u rsrc, u32 voff, u32 so) {
    asm volatile("buffer_load_dwordx4 %0, %1, %2 offen offset:%3 lds" :: "v"(voff), "s"(rsrc), "s"(so), "i"(OFF) : "memory");
}
template <int LOFF> DI void flag_imm(v4u rsrc, u32 lbase) {
    asm volatile("s_add_u32 m0, %2, %3\n\ts_mov_b64 exec, 1\n\tbuffer_load_dword off, %0, %1 lds\n\ts_mov_b64 exec, -1" :: "s"(rsrc), "s"(rsrc.z), "s"(lbase), "i"(LOFF) : "memory", "scc");
}
DI void glds_sc1(v4u rsrc, u32 voff, u32 soff, u32 lds) {
    asm volatile("s_mov_b32 m0, %3\n\ts_nop 0\n\tbuffer_load_dwordx4 %0, %1, %2 offen sc1 lds" :: "v"(voff), "s"(rsrc), "s"(soff), "s"(lds) : "memory");
}
DI void flag_dma(v4u rsrc, u32 lds) {
    asm volatile("s_mov_b32 m0, %2\n\ts_mov_b64 exec, 1\n\tbuffer_load_dword off, %0, %1 lds\n\ts_mov_b64 exec, -1" :: "s"(rsrc), "s"(rsrc.z), "s"(lds) : "memory");
}
DI void st_sc1(v4u v, v4u rsrc, u32 voff, u32 soff) {
    asm volatile("buffer_store_dwordx4 %0, %1, %2, %3 offen sc1\n\ts_nop 1" :: "v"(v), "v"(voff), "s"(rsrc), "s"(soff) : "memory");
}
template <int BYTES> DI void put_flag(const char* base, u32 off, u32 l16, u64 tag) {
    asm volatile("s_mov_b64 exec, 1\n\tglobal_store_dwordx2 %0, %1, %2 sc1\n\ts_mov_b64 exec, -1" :: "v"(l16), "v"(tag), "s"(base + off) : "memory");
}
template <int BYTES> DI void warm_flag(const char* base, u32 off, u32 l16) {
    const v4u v = {0u, 0u, 0u, 0u};
    asm volatile("s_mov_b64 exec, %3\n\tglobal_store_dwordx4 %0, %1, %2 sc1\n\ts_mov_b64 exec, -1\n\ts_nop 0" :: "v"(l16), "v"(v), "s"(base + off), "i"((1 << (BYTES / 16)) - 1) : "memory");
}
DI u32 opq(u32 a) { asm volatile("" : "+v"(a)); return a; }
template <typename T> DI T lds_ldv(u32 a) { return *(volatile LAS T*)(size_t)a; }
template <int SLEEP = 0> DI void spin_zero4(u32 a) {
    int n = 0;
    while (any_(lds_ldv<u32>(a) != 0) && ++n < (SPIN_CAP << 4)) { if (SLEEP) __builtin_amdgcn_s_sleep(1); }
    asm volatile("" ::: "memory");
}
template <int N> DI void wait_vm() { asm volatile("s_waitcnt vmcnt(%0)" :: "i"(N) : "memory"); }
DI void wait_lgkm0() { asm volatile("s_waitcnt lgkmcnt(0)" ::: "memory"); }
DI u64 mix64(u64 e) { e += 0x9E3779B97F4A7C15ull; e ^= e >> 31; e *= 0xBF58476D1CE4E5B9ull; e ^= e >> 29; return e; }
DI u64 get_tag() { const v2u e = lds_ldv<v2u>(L_E); return mix64(((u64)(u32)__builtin_amdgcn_readfirstlane(e.y) << 32) | (u32)__builtin_amdgcn_readfirstlane(e.x)); }
template <int C> DI void sig(u32 a, u32 one) { asm volatile("s_mov_b64 exec, 1\n\tds_add_u32 %0, %1 offset:%2\n\ts_mov_b64 exec, -1" :: "v"(a), "v"(one), "i"(4 * C) : "memory"); }
template <int OFF> DI void clr(u32 a, u32 zero) { asm volatile("s_mov_b64 exec, 1\n\tds_write_b32 %0, %1 offset:%2\n\ts_mov_b64 exec, -1" :: "v"(a), "v"(zero), "i"(OFF) : "memory"); }
DI void spin(u32 a, u32 target) {
    int n = 0;
    while ((u32)__builtin_amdgcn_readfirstlane(lds_ldv<u32>(a)) < target && ++n < (SPIN_CAP << 4)) {}
    asm volatile("" ::: "memory");
}
DI void poll_flags(const GAS u64* p, u64 tag) {
    int n = 0;
    while (true) {
        u64 f = __hip_atomic_load(p, RLX_AGENT);
        if (all_(f == tag) || ++n > SPIN_CAP) break;
        __builtin_amdgcn_s_sleep(PSLEEP);
    }
    asm volatile("" ::: "memory");
}
DI float bf_lo(u32 v) { return u2f(v << 16); }
DI float bf_hi(u32 v) { return u2f(v & 0xffff0000u); }
DI u32 pk_bf(float a, float b) { f32x2 v = {a, b}; return __builtin_bit_cast(u32, __builtin_convertvector(v, bf16x2)); }
typedef short s16x2 __attribute__((ext_vector_type(2)));
DI u32 relu_pk(u32 v) { return __builtin_bit_cast(u32, __builtin_elementwise_max(__builtin_bit_cast(s16x2, v), (s16x2){0, 0})); }
DI u32 umax(u32 a, u32 b) { return a > b ? a : b; }
DI float rbf(float a) { return bf_lo(pk_bf(a, a)); }
DI u32 pk_h(f32x2 v) { return __builtin_bit_cast(u32, __builtin_convertvector(v, f16x2)); }
DI f32x16 mfma32(v4u a, v4u b, f32x16 c) { return __builtin_amdgcn_mfma_f32_32x32x16_bf16(__builtin_bit_cast(bf16x8, a), __builtin_bit_cast(bf16x8, b), c, 0, 0, 0); }
DI f32x4 mfma16(v4u a, v4u b, f32x4 c) { return __builtin_amdgcn_mfma_f32_16x16x32_bf16(__builtin_bit_cast(bf16x8, a), __builtin_bit_cast(bf16x8, b), c, 0, 0, 0); }
DI f32x4 mfma16h(v4u a, v4u b, f32x4 c) { return __builtin_amdgcn_mfma_f32_16x16x32_f16(__builtin_bit_cast(f16x8, a), __builtin_bit_cast(f16x8, b), c, 0, 0, 0); }
template <int CTRL> DI float dpp_add(float v) { return v + __builtin_bit_cast(float, __builtin_amdgcn_update_dpp(0, __builtin_bit_cast(int, v), CTRL, 0xf, 0xf, true)); }
template <int CTRL> DI u32 dpp_max(u32 v) { const u32 o = (u32)__builtin_amdgcn_update_dpp(0, (int)v, CTRL, 0xf, 0xf, true); return v > o ? v : o; }
template <int T> DI float bcast(float v) { return __builtin_bit_cast(float, __builtin_amdgcn_update_dpp(0, __builtin_bit_cast(int, v), 0x150 + T, 0xf, 0xf, true)); }
DI float rl(float v, int l) { return __builtin_bit_cast(float, __builtin_amdgcn_readlane(__builtin_bit_cast(int, v), l)); }
DI f32x2 wave_sum2(f32x2 v) {
    float a = v.x, c = v.y;
    a = dpp_add<0xB1>(a); c = dpp_add<0xB1>(c); a = dpp_add<0x4E>(a); c = dpp_add<0x4E>(c);
    a = dpp_add<0x141>(a); c = dpp_add<0x141>(c); a = dpp_add<0x140>(a); c = dpp_add<0x140>(c);
    return ((f32x2){rl(a, 0), rl(c, 0)} + (f32x2){rl(a, 16), rl(c, 16)}) + ((f32x2){rl(a, 32), rl(c, 32)} + (f32x2){rl(a, 48), rl(c, 48)});
}
DI u32 wave_max(u32 v) {
    v = dpp_max<0xB1>(v); v = dpp_max<0x4E>(v); v = dpp_max<0x141>(v); v = dpp_max<0x140>(v);
    const u32 a = __builtin_amdgcn_readlane(v, 0), c = __builtin_amdgcn_readlane(v, 16), d = __builtin_amdgcn_readlane(v, 32), e = __builtin_amdgcn_readlane(v, 48);
    const u32 p = a > c ? a : c, q = d > e ? d : e;
    return p > q ? p : q;
}
struct cmult { f32x2 b, n; };
DI cmult mkc(f32x2 b) { cmult c; c.b = b; c.n = (f32x2){-b.y, b.x}; return c; }
DI f32x2 cmul(f32x2 a, const cmult& c) { return __builtin_elementwise_fma((f32x2){a.x, a.x}, c.b, (f32x2){a.y, a.y} * c.n); }
DI f32x16 mfma32h(v4u a, v4u b, f32x16 c) { return __builtin_amdgcn_mfma_f32_32x32x16_f16(__builtin_bit_cast(f16x8, a), __builtin_bit_cast(f16x8, b), c, 0, 0, 0); }
DI u32 pk_h2(float a, float b) { return pk_h((f32x2){a, b}); }
DI f32x2 cexp_pow(float lr, float th, float tl, float e) {
    float a = __builtin_amdgcn_fractf(th * e) + tl * e;
    float m = __builtin_amdgcn_exp2f(lr * e);
    f32x2 r; r.x = m * __builtin_amdgcn_cosf(a); r.y = m * __builtin_amdgcn_sinf(a); return r;
}
DI f32x4 sel4(bool c, f32x4 a, f32x4 b) { return c ? a : b; }
constexpr int w2loc(int n) { return n < 16 ? n : n < 28 ? n + 4 : n - 28; }
template <class F, int... I> DI void sfor_(F&& f, std::integer_sequence<int, I...>) { (f(std::integral_constant<int, I>{}), ...); }
template <int N, class F> DI void sfor(F&& f) { sfor_(f, std::make_integer_sequence<int, N>{}); }
template <int I> using IC = std::integral_constant<int, I>;
template <typename T> DI void pin(T& v) { asm volatile("" : "+v"(v)); }

__global__ void __launch_bounds__(768, 3)
fused_kernel(const u16* x_, const u16* logA_, const u16* Aim_, const u16* Cre_, const u16* Cim_, const u16* logdt_, const u16* Dskip_, const u16* w1_,
             const u16* b1_, const u16* w2_, const u16* b2_, u16* out_, char* ws_) {
    u64x8 ka; u64x4 kb4; u64 kc;
    asm volatile("s_load_dwordx16 %0, %3, 0x0\n\ts_load_dwordx8 %1, %3, 0x40\n\ts_load_dwordx2 %2, %3, 0x60" : "=&s"(ka), "=&s"(kb4), "=&s"(kc) : "s"(__builtin_amdgcn_kernarg_segment_ptr()) : "memory");
    __shared__ __attribute__((aligned(16))) char smem[163840];
    asm volatile("" :: "v"((u32)(size_t)(LAS char*)smem));
    const int tid = threadIdx.x, lane = tid & 63, wave = __builtin_amdgcn_readfirstlane(tid >> 6) & 15, w = wave & 3, role = wave >> 2;
    const int b = blockIdx.x & 255, xcd = b & 7, slot = b >> 3;
    const int P = 4 * (xcd >> 1) + (slot >> 3), j = 8 * (xcd & 1) + (slot & 7);
    const int tr = 4 * xcd + (slot >> 3), tc = slot & 7;
    const int u0 = (j * 43) >> 4;
    const int prole = role; u32 probe_t = 0;
    u64 t00; asm volatile("s_memrealtime %0" : "=s"(t00) :: "memory");
    if ((PF & 1) && wave == 4) {
        u64 pc; asm volatile("s_getpc_b64 %0" : "=s"(pc));
        const v4u rc = mkrsrc((const void*)(pc & ~255ull), PFB);
        glds(rc, lane * 16, slot * 1024, L_C + 2048);
    }
    const int h = lane >> 5, l31 = lane & 31, l15 = lane & 15, g = lane >> 4;
    const u32 l16 = lane * 16, lw = w * 32768, w4 = w * 4;
    const int rho = HROT ? ((slot >> 3) + slot) & 3 : (slot >> 3);
    const int tb = 64 * (P >> 2) + 32 * (j & 1) + 16 * ((P >> 1) & 1) + 2 * (j >> 1) + (P & 1);
    const int cp = 32 * xcd + slot, chl = w >> 1, hf = w & 1, ch = 2 * cp + chl;
    {
        const u32 v = (tid >= 192 && tid < 256) || (tid >= 282 && tid < 286) ? 0u : ~0u;
        if (tid < 288) lds_st<v4u>((tid < 192 ? L_CSP : tid < 256 ? L_U - 192 * 16 : L_C - 256 * 16) + tid * 16, (v4u){v, v, v, v});
    }
    const int rot = ROT == 0 ? 0 : ROT == 1 ? (((slot >> 3) + slot) & 1) * 4 : ROT == 2 ? (((slot >> 3) + slot) & 3) * 2 : (2 * (slot >> 3) + (slot & 7)) & 7;
    u32 sx = (512 * (P >> 2) + 128 * ((P >> 1) & 1) + 64 * (w & 1) + 8 * (P & 1) + 4 * (w >> 1)) * 1024, sw1 = 16 * w * 2752 + u0 * 64;
    u32 sx0 = rot * 128, sw0 = rot * (64 * 2752);
    u32 vx[4], vw[3];
    const int lim3 = 4 * ((((j + 1) * 43) >> 4) - u0);
#pragma unroll
    for (int i = 0; i < 3; i++) { const int p = 64 * i + lane, r = (p * 171) >> 11; vw[i] = opq((SKIP3 && p - 12 * r >= lim3) ? 0x40000000u : r * (2752 - 192) + l16); }
#pragma unroll
    for (int i = 0; i < 4; i++) vx[i] = opq((((h * 256 + ((lane >> 3) & 3)) * 1024 + ((lane & 7) ^ g) * 16) ^ ((i & 1) * 64)) + i * (16384 - 1024));
    asm volatile("s_waitcnt lgkmcnt(0)\n\ts_barrier" : "+s"(ka), "+s"(kb4), "+s"(kc), "+s"(sx), "+s"(sw1), "+s"(sx0), "+s"(sw0) :: "memory");
    asm volatile("" : "+s"(t00)); const u32 t_start = (u32)t00;
    STAMP(30);
    const u16 *x = (const u16*)ka[0], *logA = (const u16*)ka[1], *Aim = (const u16*)ka[2], *Cre = (const u16*)ka[3], *Cim = (const u16*)ka[4], *logdt = (const u16*)ka[5], *Dskip = (const u16*)ka[6], *w1 = (const u16*)ka[7];
    const u16 *b1 = (const u16*)kb4[0], *w2 = (const u16*)kb4[1], *b2 = (const u16*)kb4[2]; u16* out = (u16*)kb4[3]; char* ws = (char*)kc;
    if (CHST == 3) STAMP(17);
    const v4u rx = mkrsrc(x, 2 << 20);
    const u32 aF = opq(L_FLG + (lane & 3) * 4);
    auto flg = [&](auto R_) { flag_imm<L_FLG + decltype(R_)::value * 16>(rx, w4); };
    const v4u rw1 = mkrsrc(w1, 512 * 1376 * 2);
    auto ldX = [&](auto C_) { constexpr int c = decltype(C_)::value;
        const u32 so = glds_first<(16 + (c % 4) * 4) * 1024>(rx, vx[0], sx, c ? ((c + rot) & 7) * 128 : sx0, lw);
        glds_next<1024>(rx, vx[1], so); glds_next<2048>(rx, vx[2], so); glds_next<3072>(rx, vx[3], so); };
    auto ldW = [&](auto C_) { constexpr int c = decltype(C_)::value;
        const u32 so = glds_first<3 * (c % 5) * 1024>(rw1, vw[0], sw1, c ? ((c + rot) & 7) * (64 * 2752) : sw0, lw);
        glds_next<1024>(rw1, vw[1], so); if (!(AX & 4)) glds_next<2048>(rw1, vw[2], so); };
    if (role == 0) {
        if (CHST == 3) STAMP(18);
        ldW(IC<0>{}); ldX(IC<0>{}); flg(IC<R_F>{});
        ldW(IC<1>{}); ldX(IC<1>{}); flg(IC<R_F + 1>{});
        if (CHST == 3) { STAMP(19); wait_vm<15>(); STAMP(20); }
    }
    asm volatile("" ::: "memory");
    u32 aV0, aV1, aFw, aCn, one, f2o;
    auto pre2 = [&]() {
        aV0 = opq(lw + (4 * h + (l15 >> 2)) * 128 + ((l15 >> 3) & 1) * 64 + (g & 1) * 32 + (lane & 3) * 8); aV1 = opq(aV0 ^ 64);
        aFw = opq(L_FLG + w4); aCn = opq(L_CNT + w4); one = opq(1u);
        const int jj = 4 * w + (lane < 4 ? lane : 4);
        f2o = opq(((2 * tr + role) * 16 + (jj < 15 ? jj : 15)) * FS2 * 8);
    };

    auto rdV = [&](int s, int dt) { v2u lo = lds_tr((dt ? aV1 : aV0) + w2loc(2 * s) * 1024), hi = lds_tr((dt ? aV1 : aV0) + w2loc(2 * s + 1) * 1024); return (v4u){lo.x, lo.y, hi.x, hi.y}; };
    auto gemm2 = [&](auto ROLE_, v4u (&stage)[16], u64 f2, u64 tag) __attribute__((always_inline)) {
        constexpr int role = decltype(ROLE_)::value;
        const GAS u64* f2p = (const GAS u64*)(ws + OFF_F2 + f2o);
        __builtin_amdgcn_s_setprio(role ? GP2 : 0);
        v4u park[8][2];
        if (role) {
            SB0();
            spin_zero4<1>(aFw + R_V1 * 16);
            STAMP(9);
            f2 = __hip_atomic_load(f2p, RLX_AGENT);
            SB0();
#pragma unroll
            for (int s = 0; s < 8; s++) { park[s][0] = rdV(s, 0); park[s][1] = rdV(s, 1); }
            clr<R_PK * 16>(aFw, opq(0u));
            SB0();
        }
        STAMP(10);
        if (!all_(f2 == tag)) poll_flags(f2p, tag);
        STAMP(5);
        const __amdgpu_buffer_rsrc_t rh = __builtin_amdgcn_make_buffer_rsrc(ws + OFF_H, 0, H_BYTES, 0x00020000);
        u32 sh2[4], sh2t[4];
#pragma unroll
        for (int k = 0; k < 4; k++) { sh2[k] = ((2 * tr + role) * 43 + 11 * w) * 2048 + (6 * rho - (k + rho >= 4 ? 22 : 0)) * 1024; sh2t[k] = (w == 3 && k + rho == 3) ? (u32)H_BYTES : sh2[k]; }
        constexpr int WIN = WING;
        v4u hq[WIN], vb[3][2];
        auto ldH = [&](int s) { hq[s % WIN] = __builtin_amdgcn_raw_buffer_load_b128(rh, l16, ((s + 2) % 6 >= 4 ? sh2t[(s + 2) / 6] : sh2[(s + 2) / 6]) + s * 1024, 16); };
        constexpr int W0 = role ? W0G : W0L;
#pragma unroll
        for (int s = 0; s < W0; s++) ldH(s);
        const int fr = tid >> 3;
        const u32 fin_off = (512 * (tr >> 3) + 256 * ((fr >> 2) & 1) + 128 * ((tr >> 2) & 1) + 16 * (fr >> 3) + 4 * (tr & 3) + (fr & 3)) * 512 + tc * 64 + (tid & 7) * 8;
        v4u fin_x, fin_b; u32 fin_s, fv;
        f32x16 ac2[2];
#pragma unroll
        for (int i = 0; i < 16; i++) { ac2[0][i] = 0.f; ac2[1][i] = 0.f; }
        if (!role) {
            const u32 aSt = opq(lw + l16), zero = opq(0u);
            SB0();
            spin_zero4(opq(aFw + ((lane & 1) ? R_PK : R_V2) * 16));
            STAMP(9);
#pragma unroll
            for (int s = 0; s < 8; s++) {
                park[s][0] = rdV(s, 0); park[s][1] = rdV(s, 1);
                asm volatile("" ::: "memory");
                lds_st<v4u>(aSt + 2 * s * 1024, stage[2 * s]); lds_st<v4u>(aSt + (2 * s + 1) * 1024, stage[2 * s + 1]);
                SB0();
            }
            clr<R_V3 * 16>(aFw, zero);
            STAMP(13);
        }
#pragma unroll
        for (int s = 0; s < 22; s++) {
            SB0();
            if (role && (s == 4 || s == 10)) fv = lds_ldv<u32>(aFw + (s == 4 ? R_V2 : R_V3) * 16);
            if (role && (s == 6 || s == 12)) { if (__builtin_expect(any_(fv != 0), 0)) spin_zero4(aFw + (s == 6 ? R_V2 : R_V3) * 16); asm volatile("" ::: "memory"); }
            if (s + 2 >= 8 && s + 2 < 22) { vb[(s + 2) % 3][0] = rdV(s + 2, 0); vb[(s + 2) % 3][1] = rdV(s + 2, 1); }
            if (s == 19) sig<C_RD>(aCn, one);
            if (s == 21) fv = lds_ldv<u32>(aCn + 4 * C_RD);
            ac2[0] = mfma32(s < 8 ? park[s & 7][0] : vb[s % 3][0], hq[s % WIN], ac2[0]);
            ac2[1] = mfma32(s < 8 ? park[s & 7][1] : vb[s % 3][1], hq[s % WIN], ac2[1]);
            SB0();
            if (s < WIN - W0) { ldH(W0 + 2 * s); ldH(W0 + 2 * s + 1); }
            else if (s + WIN < 22) ldH(s + WIN);
            if (s == 14) {
                fin_x = *(const GAS v4u*)(x + fin_off); fin_b = lds_ld<v4u>(L_B2 + (lane & 7) * 16);
                fin_s = __hip_atomic_load((GAS u32*)(ws + OFF_ST + (tr >> 1) * 1024 + (fr >> 2) * 64 + (tr & 1) * 32 + (fr & 3) * 8), RLX_AGENT);
            }
        }
        SB0();
        STAMP(7); CST(3);
        if (__builtin_expect(any_(fv < 2), 0)) spin(aCn + 4 * C_RD, 2);
        asm volatile("" ::: "memory");
        const u32 da = opq(lw + role * 20480 + l31 * 272 + h * 16);
#pragma unroll
        for (int dt = 0; dt < 2; dt++)
#pragma unroll
            for (int q = 0; q < 4; q++)
                lds_st<f32x4>(da + (32 * dt + 8 * q) * 4, (f32x4){ac2[dt][4 * q], ac2[dt][4 * q + 1], ac2[dt][4 * q + 2], ac2[dt][4 * q + 3]});
        f32x4 s0 = {bf_lo(fin_x.x) + bf_lo(fin_b.x), bf_hi(fin_x.x) + bf_hi(fin_b.x), bf_lo(fin_x.y) + bf_lo(fin_b.y), bf_hi(fin_x.y) + bf_hi(fin_b.y)};
        f32x4 s1 = {bf_lo(fin_x.z) + bf_lo(fin_b.z), bf_hi(fin_x.z) + bf_hi(fin_b.z), bf_lo(fin_x.w) + bf_lo(fin_b.w), bf_hi(fin_x.w) + bf_hi(fin_b.w)};
        const u32 ra = opq(role * 20480 + (fr & 31) * 272 + (tid & 7) * 32), rb = opq(ra + 65536), rc = opq(L_KB + fr * 144 + (tid & 7) * 16);
        float rs2 = __builtin_amdgcn_rcpf(bf_hi(fin_s)); rs2 *= rs2;
        asm volatile("" : "+v"(s0), "+v"(s1), "+v"(rs2));
        wait_lgkm0();
        __builtin_amdgcn_s_barrier();
        asm volatile("" ::: "memory");
        STAMP(11);
        const v4u av = lds_ld<v4u>(rc);
        s0 += ((lds_ld<f32x4>(ra) + lds_ld<f32x4>(ra + 32768)) + (lds_ld<f32x4>(rb) + lds_ld<f32x4>(rb + 32768))) * rs2;
        s1 += ((lds_ld<f32x4>(ra + 16) + lds_ld<f32x4>(ra + 32768 + 16)) + (lds_ld<f32x4>(rb + 16) + lds_ld<f32x4>(rb + 32768 + 16))) * rs2;
        s0 += (f32x4){bf_lo(av.x), bf_hi(av.x), bf_lo(av.y), bf_hi(av.y)};
        s1 += (f32x4){bf_lo(av.z), bf_hi(av.z), bf_lo(av.w), bf_hi(av.w)};
        *(GAS v4u*)(out + fin_off) = (v4u){pk_bf(s0[0], s0[1]), pk_bf(s0[2], s0[3]), pk_bf(s1[0], s1[1]), pk_bf(s1[2], s1[3])};
    };

    if (__builtin_expect(role == 0, 1)) {
        v4u stage[16]; u64 f2, tag;
        const u32 sw2 = 352 * w * 1024 + tc * 128;
        const u32 vv = (lane >> 3) * 1024 + ((lane & 7) ^ ((g & 1) << 2)) * 16;
        u32 sv[4];
#pragma unroll
        for (int k = 0; k < 4; k++) sv[k] = sw2 + (12 * rho - (k + rho >= 4 ? 44 : 0)) * 8192;
        const __amdgpu_buffer_rsrc_t rv = __builtin_amdgcn_make_buffer_rsrc((void*)w2, 0, (AX & 64) ? 0 : 512 * 1376 * 2, 0x00020000);
        auto ldV = [&](int r) { stage[r] = __builtin_amdgcn_raw_buffer_load_b128(rv, vv, sv[(r + 32) / 12] + (r + 28) * 8192, 0); };
        sfor<6>([&](auto I) {
            constexpr int c = decltype(I)::value + 2;
            if constexpr (THR && c >= 2) wait_vm<THR>();
            if (!(AB & 4)) ldW(IC<c>{});
            if constexpr (c >= 4) {
                SB0();
#pragma unroll
                for (int k = 0; k < NE; k++) ldV(NE * (c - 4) + k);
                SB0();
            }
            if constexpr (c >= 4) TIMED(spin_zero4<1>(aF + (R_P + c - 4) * 16));
            if constexpr (THR && c >= 2) wait_vm<THR>();
            if (!(AB & 4)) ldX(IC<c>{});
            flg(IC<R_F + c>{});
        });
        STAMP(1);
        {
            const u32 vS = ((lane >> 1) * 4 + (lane & 1)) * 16;
            const v4u rst = mkrsrc(ws + OFF_ST, 16384);
            asm volatile("s_mov_b32 m0, %3\n\ts_mov_b64 exec, 0xffff\n\tbuffer_load_dwordx4 %0, %1, %2 offen sc1 lds\n\ts_mov_b64 exec, -1" :: "v"(vS), "s"(rst), "s"(P * 1024 + (w & 1) * 512 + (w >> 1) * 32), "s"(L_STAT + w * 256) : "memory");
        }
        spin_zero4<1>(aF + R_X * 16);
        tag = get_tag();
        const GAS u64* f1p = (const GAS u64*)(ws + OFF_F1) + ((xcd >> 1) * 256 + 64 * w + ((AX & 1) ? (lane & 3) : lane)) * FS1;
        const u64 f1 = __hip_atomic_load(f1p, RLX_AGENT);
#pragma unroll
        for (int n = 4 * NE; n < 16; n++) ldV(n);
        SB0();
        if (!all_(f1 == tag)) poll_flags(f1p, tag);
        STAMP(16);
        {
            const v4u rus = mkrsrc(ws + OFF_U, 2 << 20);
            const u32 vU = (lane >> 1) * 8192 + ((lane ^ g) & 1) * 16;
#pragma unroll
            for (int i = 0; i < ((AX & 1) ? 0 : 2); i++) glds_sc1(rus, vU, (64 * w + 32 * i) * 8192 + cp * 32, L_U + 1024 + w * 2048 + i * 1024);
            flag_dma(rus, L_UF + w * 4);
        }
        STAMP(2);
        pre2();
        spin_zero4<1>(aF + R_P7 * 16);
        STAMP(3);
#pragma unroll
        for (int n = 0; n < 16; n++) pin(stage[n]);
        {
            const u32 aK = L_CNT + 4 * C_K + (lane & 1) * 4;
            int k = 0;
            while (any_(lds_ldv<u32>(aK) < 2) && ++k < (SPIN_CAP << 4)) { __builtin_amdgcn_s_sleep(1); }
        }
        STAMP(14);
        const v4u rw2 = mkrsrc(w2, 512 * 1376 * 2);
        sfor<28>([&](auto I) {
            constexpr int n = decltype(I)::value;
            if constexpr (n == 28 - NV2) asm volatile("global_load_dwordx2 %0, %1, %2 sc1" : "=v"(f2) : "v"(f2o), "s"(ws + OFF_F2) : "memory");
            if constexpr (!(AX & 32) || n < 4 || n >= 16) glds_imm<w2loc(n) * 1024, n * 8192>(rw2, vv, sv[(n + 4) / 12], lw);
            if constexpr (n == 15) flg(IC<R_V1>{});
        });
        flg(IC<R_V2>{});
        asm volatile("s_waitcnt vmcnt(%1)" : "+v"(f2) : "i"(NV2 + 1) : "memory");
        STAMP(8);
        gemm2(IC<0>{}, stage, f2, tag);
    } else if (role == 1) {
        u64 tag;
        __builtin_amdgcn_s_setprio(PRIO / 10);
        if (WARM) warm_flag<FS2 * 8>(ws + OFF_F2, ((4 * P + w) * 16 + j) * FS2 * 8, l16);
        if (MPF) {
            const v4u rp = mkrsrc(w2, 512 * 1376 * 2);
            if (MPF & 2) {
                glds(rw1, l16, (b * 11 + 2 * w) * 512, L_C + 2048);
                if (w >= 2) glds(rw1, l16, (b * 11 + 4 + 2 * w) * 512, L_C + 2048);
            }
            glds(rp, l16, (b * 11 + 2 * w) * 512, L_C + 2048);
            if (w < 2) glds(rp, l16, (b * 11 + 8 + 2 * w) * 512, L_C + 2048);
        }
        u32 aW[4], aX[4];
#pragma unroll
        for (int s = 0; s < 4; s++) {
            const int ks = (s + w) & 3;
            aW[s] = opq(ks * 32768 + (8 * h + (l15 >> 2)) * 192 + (g & 1) * 32 + (lane & 3) * 8);
            aX[s] = opq(lw + 16384 + l31 * 128 + (((2 * ks + h) ^ ((l31 >> 1) & 7)) * 16));
        }
        const u32 aC = opq(L_CSP + l31 * 32 + h * 16), aCw = opq(L_CSP + lane * 32 + w * 8), aS = opq(L_STAT + w * 256 + l31 * 8), aB = opq(L_B1 + lane * 2);
        pre2();
        const u32 zero = opq(0u);
        f32x16 acc[3]; f32x4 cs = {0.f, 0.f, 0.f, 0.f};
        v4u sel[3];
#pragma unroll
        for (int u = 0; u < 3; u++) {
            for (int i = 0; i < 16; i++) acc[u][i] = 0.f;
            const u32 sv = (l15 == (u < 2 ? 8 * u : 1) + 4 * (g & 1)) ? 0xBF80BF80u : 0u; sel[u] = (v4u){sv, sv, sv, sv}; pin(sel[u]);
        }
        v4u fa[3][3], fb[2][4];
        auto rdW = [&](int n, int u) { const u32 a = aW[n & 3] + ((n >> 2) % 5) * 3072 + u * 64; v2u lo = lds_tr(a), hi = lds_tr(a + 768); fa[n % 3][u] = (v4u){lo.x, lo.y, hi.x, hi.y}; };
        auto rdX = [&](int c, int s) { fb[c & 1][s] = lds_ld<v4u>(aX[s] + (c % 4) * 4096); };
        const v4u rhs = mkrsrc(ws + OFF_H, (AX & 8) ? 0 : H_BYTES);
        const int nown = (((j + 1) * 43) >> 4) - u0;
        const u32 sh = ((4 * P + w) * 43 + u0) * 2048, sh2 = nown == 3 ? sh + 4096 : (u32)H_BYTES;
        spin_zero4<(EX >> 2) & 1>(aF);
        STAMP(1);
        rdX(0, 0);
#pragma unroll
        for (int u = 0; u < 3; u++) rdW(0, u);
        rdX(0, 1);
#pragma unroll
        for (int u = 0; u < 3; u++) rdW(1, u);
        rdX(0, 2); rdX(0, 3);
        u32 fl, fx, bb[2]; v2u ee; v2u stg; v4u fas[3], fbs;
        sfor<32>([&](auto N_) __attribute__((always_inline)) {
            constexpr int n = decltype(N_)::value, c = n >> 2, s = n & 3;
            SB0();
            if constexpr (s == 2 && c < 5) {
                pin(fa[(n + 1) % 3][2]);
                flg(IC<R_P + c>{});
            }
            if (s == 2 && c < 7 && !(AB & 2)) {
                if (__builtin_expect(any_(fl != 0), 0)) TIMED(spin_zero4<(EX >> 2) & 1>(aF + (R_F + c + 1) * 16));
                asm volatile("" ::: "memory");
            }
            if (CHST == 2 && n == 26) STAMP(3);
            sfor<3>([&](auto U_) __attribute__((always_inline)) {
                constexpr int u = decltype(U_)::value;
                if (!(EX & 2) || u < 2) acc[u] = mfma32(fa[n % 3][u], fb[c & 1][s], acc[u]);
                if (EX & 2) pin(fa[n % 3][u]);
                if (s == 0 && c < 7 && !(EX & 1)) cs = mfma16(sel[u], fa[n % 3][u], cs);
                if (n == 27) cs = mfma16(sel[u], fa[28 % 3][u], cs);
                SB0();
                if (s >= 2 && c < 7 && u < 2) rdX(c + 1, 2 * (s - 2) + u);
                if (n + 2 < 32) rdW(n + 2, u);
                if (s == 1 && u == 0 && c < 7) fl = lds_ldv<u32>(aF + (R_F + c + 1) * 16);
                if constexpr (P7AT < 0 && n == 29 && u == 2) clr<R_P7 * 16>(aFw, zero);
                if (n == 25 && u == 2) { bb[0] = lds_ld<u16>(aB); bb[1] = lds_ld<u16>(aB + 128); }
                if (n == 21 && u == 1) fx = lds_ldv<u32>(aF + R_X * 16);
                if (n == 23 && u == 1) { if (__builtin_expect(any_(fx != 0), 0)) spin_zero4(aF + R_X * 16); asm volatile("" ::: "memory"); ee = lds_ldv<v2u>(L_E); }
                if (n == 28 && u == 1) {
                    v2u t[2];
#pragma unroll
                    for (int k = 0; k < 2; k++) {
                        const float d = cs[k] - bf_hi(f2u(cs[k]));
                        t[k] = (v2u){__builtin_amdgcn_perm(f2u(d), f2u(cs[k]), 0x07060302u), pk_bf(d - bf_hi(f2u(d)), bf_lo(bb[k]))};
                    }
                    asm volatile("ds_write2st64_b64 %0, %1, %2 offset1:4" :: "v"(aCw), "v"(t[0]), "v"(t[1]) : "memory");
                }
                if (n == 31 && u == 0) { fas[0] = lds_ldv<v4u>(aC); fas[1] = lds_ldv<v4u>(aC + 1024); fas[2] = lds_ldv<v4u>(aC + 2048); }
                if (n == 30 && u == 0) stg = lds_ldv<v2u>(aS);
                if (n == 30 && u == 1) { tag = mix64(((u64)(u32)__builtin_amdgcn_readfirstlane(ee.y) << 32) | (u32)__builtin_amdgcn_readfirstlane(ee.x)); asm volatile("" : "+s"(tag)); }
                SB0();
            });
        });
        if (CHST == 2) STAMP(19);
        {
            auto csbad = [&]() { return umax(umax(umax(fas[0].x, fas[0].z), umax(fas[1].x, fas[1].z)), umax(fas[2].x, fas[2].z)) == ~0u; };
            const u32 want = (lane & 1) ? (u32)(tag >> 32) : (u32)tag;
            if (__builtin_expect(any_(csbad() || stg.y != want), 0)) {
                int k = 0;
                while (any_(csbad()) && ++k < (SPIN_CAP << 2)) { fas[0] = lds_ldv<v4u>(aC); fas[1] = lds_ldv<v4u>(aC + 1024); fas[2] = lds_ldv<v4u>(aC + 2048); }
                k = 0;
                const GAS u64* sp = (const GAS u64*)(ws + OFF_ST + P * 1024 + (w & 1) * 512 + (w >> 1) * 32 + (l31 >> 2) * 64 + (lane & 3) * 8);
                while (any_(stg.y != want) && ++k < SPIN_CAP) { const u64 gr = __hip_atomic_load(sp, RLX_AGENT); stg = (v2u){(u32)gr, (u32)(gr >> 32)}; }
            }
            const u32 m1 = stg.x & 0xffffu, m2 = m1 * 0x10001u;
            fbs = (v4u){m2, h ? m1 : stg.x, m2, m1};
        }
#pragma unroll
        for (int u = 0; u < 3; u++) acc[u] = mfma32(fas[u], fbs, acc[u]);
        STAMP(2); CST(1);
        sfor<6>([&](auto K_) __attribute__((always_inline)) {
            constexpr int u = decltype(K_)::value >> 1, s = decltype(K_)::value & 1;
            v4u o;
#pragma unroll
            for (int e = 0; e < 4; e++) {
                const float a0 = acc[u][8 * s + 2 * e], a1 = acc[u][8 * s + 2 * e + 1];
                o[e] = relu_pk(pk_bf(a0 * __builtin_fabsf(a0), a1 * __builtin_fabsf(a1)));
            }
            st_sc1(o, rhs, l16, (u < 2 ? sh + u * 2048 : sh2) + s * 1024);
            if constexpr (decltype(K_)::value == P7AT) clr<R_P7 * 16>(aFw, zero);
            if (PACE && decltype(K_)::value < 5) __builtin_amdgcn_s_sleep(PACE);
            if (PACEV < 9) wait_vm<PACEV>();
            SB0();
        });
        STAMP(8);
        if (P7AT >= 6) { wait_vm<(P7AT >= 6 ? 11 - P7AT : 0)>(); clr<R_P7 * 16>(aFw, zero); }
        wait_vm<0>();
        STAMP(4);
        put_flag<FS2 * 8>(ws + OFF_F2, ((4 * P + w) * 16 + j) * FS2 * 8, l16, tag);
        v4u none[16];
        gemm2(IC<1>{}, none, ~tag, tag);
    } else {
        u64 tag;
        __builtin_amdgcn_s_setprio(3);
        glds(rx, l16, (8 * tb + 2 * w) * 1024, L_KB + (2 * w) * 1088);
        glds(rx, l16, (8 * tb + 2 * w + 1) * 1024, L_KB + (2 * w + 1) * 1088);
        if (w == 0) {
            const v4u re = mkrsrc(ws, 16);
            asm volatile("s_mov_b32 m0, %1\n\ts_mov_b64 exec, 1\n\tbuffer_load_dwordx4 off, %0, 0 sc1 lds\n\ts_mov_b64 exec, -1" :: "s"(re), "s"(L_E) : "memory");
        } else if (w == 2) {
            const v4u rb2 = mkrsrc(b2, 1024);
            asm volatile("s_mov_b32 m0, %3\n\ts_mov_b64 exec, 0xff\n\tbuffer_load_dwordx4 %0, %1, %2 offen lds\n\ts_mov_b64 exec, -1" :: "v"(l16), "s"(rb2), "s"(tc * 128), "s"(L_B2) : "memory");
        } else if (w == 1) {
            const v4u rb1 = mkrsrc(b1, 1376 * 2);
            asm volatile("s_mov_b32 m0, %3\n\ts_nop 0\n\tbuffer_load_dword %0, %1, %2 offen lds" :: "v"(lane * 4), "s"(rb1), "s"(u0 * 64), "s"(L_B1) : "memory");
        }
        const u32 pi = opq((hf * 64 + lane) * 2);
        u32 pA = *(const GAS u16*)((const GAS char*)(logA + ch * 128) + pi), pI = *(const GAS u16*)((const GAS char*)(Aim + ch * 128) + pi);
        u32 pR = *(const GAS u16*)((const GAS char*)(Cre + ch * 128) + pi), pC = *(const GAS u16*)((const GAS char*)(Cim + ch * 128) + pi);
        u32 pT = *(const GAS u16*)(logdt + ch), pD = *(const GAS u16*)(Dskip + ch);
        flg(IC<R_X>{});
        one = opq(1u);
        const u32 aCnt = opq(L_CNT + chl * 4), aCtl = opq(L_C);
        const v4u rus = mkrsrc(ws + OFF_U, 2 << 20);
        v4u selLo, selHi;
#pragma unroll
        for (int e = 0; e < 4; e++) {
            const u32 o1 = (h == (g & 1) && ((l15 & 7) >> 1) == e) ? ((l15 & 1) ? 0x3C000000u : 0x00003C00u) : 0u;
            selLo[e] = l15 < 8 ? o1 : 0u; selHi[e] = l15 < 8 ? 0u : o1;
        }
        pin(selLo); pin(selHi);
        const int m = 64 * hf + lane;
        const u32 aT = opq(w * 32768 + 16384 + l15 * 64 + g * 16), t0 = opq((2 * chl) * 32768 + 16384 + m * 4), kr = opq(L_KB + chl * 8320 + (1016 - 8 * m) * 2);
        u32 tg[6];
        {
            const bool r1 = m < 32, r2 = m < 64;
            tg[0] = opq(t0 + (r1 ? 528 : 0) * 4); tg[1] = opq(t0 + (r2 ? 400 : 0) * 4); tg[2] = opq(t0 + (r1 ? 560 : 272) * 4);
            tg[3] = opq(t0 + (r1 ? 592 : 272) * 4); tg[4] = opq(t0 + (r2 ? 464 : 1) * 4); tg[5] = opq(t0 + (r1 ? 624 : 1) * 4);
        }
        u32 kb[4], ua[4];
#pragma unroll
        for (int i = 0; i < 4; i++) {
            kb[i] = opq(L_KB + chl * 8320 + ((l15 & 1) ? (239 - l15 + 8 * g) * 2 : 4160 + (240 - l15 + 8 * g) * 2) - hf * 256 - 192 + i * 1024);
            const int os = 30 + 2 * l15 + g - 16 * hf - 4 * i;
            ua[i] = opq(L_U + os * 32 + ((chl ^ (os >> 3)) & 1) * 16);
        }
        const u32 aZ = opq(L_C + (lane & 3) * 4), aZ2 = opq(L_C + (lane & 3) * 4 + ((lane & 4) ? R_P + 4 : R_F + 7) * 16);
        const u32 vA = opq((lane >> 3) * 4096 + (lane & 7) * 16);
        pin(pA); pin(pI); pin(pR); pin(pC); pin(pT); pin(pD);
        if (WARM & 2) warm_flag<FS1 * 8>(ws + OFF_F1, (w * 256 + tb) * FS1 * 8, l16);
        {
            const u32 aRow = opq(L_KB + (l15 & 7) * 1088 + (l15 >> 3) * 512 + g * 16), aTr = opq(L_KB + (l15 >> 2) * 1088 + (128 * w + 16 * g + 4 * (lane & 3)) * 2);
            v4u ones = {0x3F803F80u, 0x3F803F80u, 0x3F803F80u, 0x3F803F80u}; pin(ones);
            spin_zero4(aF + R_X * 16);
            STAMP(1);
            v4u xb[8]; v2u xt[2][2];
#pragma unroll
            for (int s = 0; s < 8; s++) xb[s] = lds_ld<v4u>(aRow + s * 64);
#pragma unroll
            for (int i = 0; i < 2; i++) { xt[i][0] = lds_tr(aTr + i * 128); xt[i][1] = lds_tr(aTr + i * 128 + 4 * 1088); }
            tag = get_tag();
            f32x4 sm = {0.f, 0.f, 0.f, 0.f}, sq = {0.f, 0.f, 0.f, 0.f};
#pragma unroll
            for (int s = 0; s < 8; s++) { sq = mfma16(xb[s], xb[s], sq); sm = mfma16(ones, xb[s], sm); }
            float q = (l15 & 2) ? ((l15 & 1) ? sq[3] : sq[2]) : ((l15 & 1) ? sq[1] : sq[0]);
            q = g == (l15 >> 2) ? q : 0.f;
            { const auto r = __builtin_amdgcn_permlane32_swap(f2u(q), f2u(q), false, false); q = u2f(r[0]) + u2f(r[1]); }
            { const auto r = __builtin_amdgcn_permlane16_swap(f2u(q), f2u(q), false, false); q = u2f(r[0]) + u2f(r[1]); }
            const float mean = dpp_add<0x128>(sm[0]) * (1.f / 512.f);
            const float var = fmaxf(__builtin_fmaf(-mean, mean, dpp_add<0x128>(q) * (1.f / 512.f)), 0.f);
            const u32 p = pk_bf(mean, __builtin_amdgcn_sqrtf(rbf(rbf(var) + 1e-5f)));
            const float mb = bf_lo(p), rr = __builtin_amdgcn_rcpf(bf_hi(p));
            if (w == 0 && lane < 8) __hip_atomic_store((GAS u64*)(ws + OFF_ST + (P * 16 + j) * 64) + lane, ((u64)((lane & 1) ? (u32)(tag >> 32) : (u32)tag) << 32) | p, RLX_AGENT);
            sfor<2>([&](auto I_) {
                constexpr int i = decltype(I_)::value;
                v4u o;
                sfor<4>([&](auto E_) {
                    constexpr int e = decltype(E_)::value;
                    const u32 xv = xt[i][e >> 1][e & 1];
                    const u32 d = pk_bf(bf_lo(xv) - bcast<2 * e>(mb), bf_hi(xv) - bcast<2 * e + 1>(mb));
                    o[e] = pk_bf(bf_lo(d) * bcast<2 * e>(rr), bf_hi(d) * bcast<2 * e + 1>(rr));
                });
                st_sc1(o, rus, l16, (AX & 2) ? 2u << 20 : tb * 8192 + w * 2048 + i * 1024);
            });
        }
        STAMP(2);
        __builtin_amdgcn_s_setprio(PRIO % 10);
        f32x4 kt[3];
        {
            const float dt = rbf(expf(bf_lo(pT)));
            const float zr = -rbf(expf(bf_lo(pA))) * dt, zi = bf_lo(pI) * dt;
            const float lr = zr * 1.44269504088896341f;
            const float th = zi * 0.159154943091895336f, tl = __builtin_fmaf(zi, 0.159154943091895336f, -th) + zi * 6.4206383e-09f;
            const cmult w1c = mkc(cexp_pow(lr, -th, -tl, 1.f)), qc = mkc(cexp_pow(lr, -th, -tl, 128.f)), q2 = mkc(cexp_pow(lr, -th, -tl, 256.f));
            const cmult w8 = mkc(cexp_pow(lr, th, tl, 8.f)), w32 = mkc(cexp_pow(lr, th, tl, 32.f));
            const u32 aR = pR & 0x7fffu, aI = pC & 0x7fffu;
            int ex = (int)(wave_max(aR > aI ? aR : aI) >> 7); ex = ex < 12 ? 12 : (ex > 240 ? 240 : ex);
            const float sdn = u2f((u32)(262 - ex) << 23), sup = u2f((u32)(ex - 8) << 23);
            auto transpose = [&](const f32x2 (&v)[16], v4u (&f)[4]) {
                f32x16 z; for (int i = 0; i < 16; i++) z[i] = 0.f;
                f32x16 dr = mfma32h((v4u){pk_h2(v[0].x, v[1].x), pk_h2(v[2].x, v[3].x), pk_h2(v[4].x, v[5].x), pk_h2(v[6].x, v[7].x)}, selLo, z);
                f32x16 di = mfma32h((v4u){pk_h2(v[0].y, v[1].y), pk_h2(v[2].y, v[3].y), pk_h2(v[4].y, v[5].y), pk_h2(v[6].y, v[7].y)}, selLo, z);
                dr = mfma32h((v4u){pk_h2(v[8].x, v[9].x), pk_h2(v[10].x, v[11].x), pk_h2(v[12].x, v[13].x), pk_h2(v[14].x, v[15].x)}, selHi, dr);
                di = mfma32h((v4u){pk_h2(v[8].y, v[9].y), pk_h2(v[10].y, v[11].y), pk_h2(v[12].y, v[13].y), pk_h2(v[14].y, v[15].y)}, selHi, di);
#pragma unroll
                for (int e = 0; e < 4; e++) {
                    f[0][e] = pk_h2(dr[2 * e], dr[2 * e + 1]); f[1][e] = pk_h2(dr[8 + 2 * e], dr[9 + 2 * e]);
                    f[2][e] = pk_h2(di[2 * e], di[2 * e + 1]); f[3][e] = pk_h2(di[8 + 2 * e], di[9 + 2 * e]);
                }
            };
            { f32x2 t0_ = w1c.b, t1_ = qc.b, t2_ = q2.b, t3_ = w8.b, t4_ = w32.b; pin(t0_); pin(t1_); pin(t2_); pin(t3_); pin(t4_); }
            STAMP(13);
            wait_vm<0>();
            put_flag<FS1 * 8>(ws + OFF_F1, (w * 256 + tb) * FS1 * 8, l16, tag);
            warm_flag<FS3 * 8>(ws + OFF_F3, (hf * 512 + ch) * FS3 * 8, l16);
            STAMP(14);
            v4u fy[4], fx[4];
            f32x2 v[16], xo[8];
            v[0] = (f32x2){1.f, 0.f}; v[1] = w8.b; v[2] = cmul(w8.b, w8); v[3] = cmul(v[2], w8); v[4] = w32.b;
            xo[0] = (f32x2){bf_lo(pR) * sdn, -bf_lo(pC) * sdn};
#pragma unroll
            for (int i = 5; i < 16; i++) { v[i] = cmul(v[i - 4], w32); if (i < 12) xo[i - 4] = cmul(xo[i - 5], w1c); }
            STAMP(3); CST(2);
            transpose(v, fy);
            const float fin = 2.f * dt * sup;
            auto tile = [&](int t) {
                transpose(v, fx);
                f32x4 d = {0.f, 0.f, 0.f, 0.f};
#pragma unroll
                for (int s = 0; s < 4; s++) d = mfma16h(fy[s], fx[s], d);
                kt[t] = d * fin;
            };
            v[0] = xo[0]; v[1] = cmul(xo[0], qc);
#pragma unroll
            for (int s = 2; s < 16; s++) v[s] = cmul(v[s - 2], q2);
            const f32x2 last = cmul(v[14], q2);
            tile(0);
            v[0] = last; v[1] = xo[4]; v[9] = xo[2]; v[13] = xo[6];
            v[2] = cmul(v[1], qc); v[10] = cmul(v[9], qc); v[14] = cmul(v[13], qc);
#pragma unroll
            for (int s = 3; s < 9; s++) v[s] = cmul(v[s - 2], q2);
            v[11] = cmul(v[9], q2); v[12] = cmul(v[10], q2); v[15] = cmul(v[13], q2);
            const f32x2 last2 = cmul(v[14], q2);
            tile(1);
            v[0] = last2;
#pragma unroll
            for (int i = 0; i < 4; i++) { v[1 + 2 * i] = xo[2 * i + 1]; v[2 + 2 * i] = cmul(xo[2 * i + 1], qc); }
#pragma unroll
            for (int i = 9; i < 16; i++) v[i] = (f32x2){0.f, 0.f};
            tile(2);
        }
        CST(3); STAMP(17);
        pin(kt[0]); pin(kt[1]); pin(kt[2]);
        spin_zero4<1>(aZ2);
        STAMP(11);
#pragma unroll
        for (int t = 0; t < 3; t++) lds_st<f32x4>(aT + t * 1024, kt[t]);
        sig<C_A>(aCtl + (L_CNT - L_C), one);
        STAMP(4);
        spin(aCtl + (L_CNT - L_C) + 4 * C_A, 4);
        {
            auto tv = [&](u32 a) { return lds_ld<float>(a) + lds_ld<float>(a + 32768); };
            float k[9];
            k[0] = tv(t0); k[8] = tv(t0 + 4); k[4] = tv(t0 + 272 * 4);
            k[1] = tv(tg[0]); k[2] = tv(tg[1]); k[3] = tv(tg[2]); k[5] = tv(tg[3]); k[6] = tv(tg[4]); k[7] = tv(tg[5]);
            const float va = tv(t0 + 512), vb = tv(t0 + 516);
            if (m == 0) k[0] += bf_lo(pD);
            const u32 k10 = pk_bf(k[1], k[0]);
            lds_st<v4u>(kr + 2048, (v4u){pk_bf(k[7], k[6]), pk_bf(k[5], k[4]), pk_bf(k[3], k[2]), k10});
            lds_st<v4u>(kr + 2048 + 4160, (v4u){pk_bf(k[8], k[7]), pk_bf(k[6], k[5]), pk_bf(k[4], k[3]), pk_bf(k[2], k[1])});
            const u32 pa = pk_bf(va, va), pb = pk_bf(vb, vb), pba = pk_bf(vb, va);
            lds_st<v4u>(kr, (v4u){pb, (m & 1) ? pb : pba, pa, pa});
            lds_st<v4u>(kr + 4160, (v4u){pb, pb, (m & 1) ? pba : pa, pa});
            if (m < 4) lds_st<v4u>(L_KB + chl * 8320 + 4096 + (m & 1) * 16 + (m < 2 ? 4160 : 0), (v4u){m == 0 ? k10 >> 16 : 0u, 0u, 0u, 0u});
        }
        sig<C_K>(aCnt, one);
        STAMP(18);
        f32x4 cv[8];
        {
#pragma unroll
            for (int t = 0; t < 8; t++) cv[t] = (f32x4){0.f, 0.f, 0.f, 0.f};
            v4u A[8], B[8];
            auto rdA = [&](int r, int a) { const u32 p = kb[(7 - a) >> 1] + ((7 - a) & 1) * 512 + (3 - r) * 64; return (v4u){lds_ld<u32>(p), lds_ld<u32>(p + 4), lds_ld<u32>(p + 8), lds_ld<u32>(p + 12)}; };
            auto rdB = [&](int r, int d) { return lds_ld<v4u>(ua[r] + d * 1024); };
            spin(aCnt + 4 * C_K, 2);
#pragma unroll
            for (int i = 0; i < 8; i++) A[7 - i] = rdA(0, 7 - i);
            STAMP(19);
            spin_zero4(aZ + (L_UF - L_C));
            STAMP(6);
#pragma unroll
            for (int i = 0; i < 8; i++) B[i] = rdB(0, i);
            if (b == 0 && w == 0 && lane == 0) __hip_atomic_store((GAS u64*)ws, tag, RLX_AGENT);
            CST(0);
#pragma unroll
            for (int r = 0; r < 4; r++) {
#pragma unroll
                for (int a = 7; a >= 0; a--) {
#pragma unroll
                    for (int d = 0; d + a < 8; d++) {
                        cv[a + d] = mfma16(A[a], B[d], cv[a + d]);
                        if (a == 0 && r < 3) { SB0(); B[d] = rdB(r + 1, d); SB0(); }
                    }
                    if (r < 3) { SB0(); A[a] = rdA(r + 1, a); SB0(); }
                }
            }
        }
        STAMP(7); CST(1);
        {
            const u32 mine = opq(w * 32768 + 16384 + l16), other = opq((w ^ 1) * 32768 + 16384 + l16);
#pragma unroll
            for (int t = 0; t < 4; t++) lds_st<f32x4>(mine + t * 1024, sel4(hf, cv[t], cv[4 + t]));
            sig<C_E>(aCnt, one);
            sig<C_D>(aCtl + (L_CNT - L_C), one);
            spin(aCnt + 4 * C_E, 2);
            u32 o[8];
#pragma unroll
            for (int t = 0; t < 4; t++) {
                const f32x4 y = sel4(hf, cv[4 + t], cv[t]) + lds_ld<f32x4>(other + t * 1024);
                o[2 * t] = pk_bf(y[0], y[1]); o[2 * t + 1] = pk_bf(y[2], y[3]);
            }
            const v4u ras = mkrsrc(ws + OFF_A, 2 << 20);
            st_sc1((v4u){o[0], o[1], o[2], o[3]}, ras, l16, (AX & 16) ? 2u << 20 : ch * 4096 + hf * 2048);
            st_sc1((v4u){o[4], o[5], o[6], o[7]}, ras, l16, (AX & 16) ? 2u << 20 : ch * 4096 + hf * 2048 + 1024);
            STAMP(15);
            wait_vm<0>();
            STAMP(8);
            put_flag<FS3 * 8>(ws + OFF_F3, (hf * 512 + ch) * FS3 * 8, l16, tag);
            spin(aCtl + (L_CNT - L_C) + 4 * C_D, 4);
            if (F3D) __builtin_amdgcn_s_sleep(F3D);
            poll_flags((const GAS u64*)(ws + OFF_F3) + ((tr >> 4) * 512 + tc * 64 + 16 * w + l15) * FS3, tag);
            STAMP(9);
#pragma unroll
            for (int i = 0; i < 2; i++)
                glds_sc1(ras, vA, (tc * 64 + 16 * w + 8 * i) * 4096 + (tr >> 3) * 1024 + (tr & 3) * 256 + ((tr >> 2) & 1) * 128, L_U + w * 2048 + i * 1024);
            const u32 ta = opq(L_U + w * 2048 + (l15 >> 2) * 128 + g * 32 + (lane & 3) * 8), aw = opq(L_KB + lane * 144 + w * 32);
            wait_vm<0>();
            const v2u a0 = lds_tr(ta), a1 = lds_tr(ta + 512), a2 = lds_tr(ta + 1024), a3 = lds_tr(ta + 1536);
            lds_st<v4u>(aw, (v4u){a0.x, a0.y, a1.x, a1.y});
            lds_st<v4u>(aw + 16, (v4u){a2.x, a2.y, a3.x, a3.y});
            STAMP(10);
        }
        wait_lgkm0();
        __builtin_amdgcn_s_barrier();
    }
    if (lane == 0 && w == 0 && role == PROLE) { GAS u32* q = (GAS u32*)(ws + (40 << 20)) + b * 4; q[0] = t_start; q[1] = probe_t; q[2] = (u32)__builtin_amdgcn_s_memrealtime(); }
}

#if defined(__HIP_DEVICE_COMPILE__) && __HIP_DEVICE_COMPILE__
#pragma clang attribute pop
#endif

__global__ void tail_kernel(char* ws) {
    const u32 t0 = (u32)__builtin_amdgcn_s_memrealtime();
    const int lane = threadIdx.x;
    int smax = -(1 << 30), smin = 1 << 30, pmax = -(1 << 30), emax = -(1 << 30), psum = 0;
    for (int i = 0; i < 4; i++) { const u32* q = (const u32*)(ws + (40 << 20)) + (i * 64 + lane) * 4; int a = (int)(q[0] - t0), p = (int)(q[1] - t0), e = (int)(q[2] - t0); smax = a > smax ? a : smax; smin = a < smin ? a : smin; pmax = p > pmax ? p : pmax; emax = e > emax ? e : emax; psum += p; }
    for (int m = 32; m >= 1; m >>= 1) { int a = __shfl_xor(smax, m), a2 = __shfl_xor(smin, m), p = __shfl_xor(pmax, m), e = __shfl_xor(emax, m); smax = a > smax ? a : smax; smin = a2 < smin ? a2 : smin; pmax = p > pmax ? p : pmax; emax = e > emax ? e : emax; psum += __shfl_xor(psum, m); }
    int extra = QTY == 0 ? (smax > 0 ? 1 : 0) : QTY == 1 ? smax - smin : QTY == 2 ? emax - smax : QTY == 3 ? psum / 256 - smax : pmax - smax;
    extra = __builtin_amdgcn_readfirstlane(extra) * 2; if (extra > 20000) extra = 20000;
    while ((int)((u32)__builtin_amdgcn_s_memrealtime() - t0) < extra + 600) {}
}
extern "C" void kernel_launch(void* const* d_in, const int* in_sizes, int n_in, void* d_out, int out_size, void* d_ws, size_t ws_size,
                              hipStream_t stream) {
    fused_kernel<<<256, 768, 0, stream>>>((const u16*)d_in[0], (const u16*)d_in[1], (const u16*)d_in[2], (const u16*)d_in[3], (const u16*)d_in[4],
                                          (const u16*)d_in[5], (const u16*)d_in[6], (const u16*)d_in[8], (const u16*)d_in[9], (const u16*)d_in[10],
                                          (const u16*)d_in[11], (u16*)d_out, (char*)d_ws + (WSOFF == 1 ? ((ws_size - (16u << 20)) & ~(size_t)0x1fffff) : WSOFF == 2 ? ((ws_size / 2) & ~(size_t)0x1fffff) : 0));
    tail_kernel<<<1, 64, 0, stream>>>((char*)d_ws);
}
```

```cpp
#include <hip/hip_runtime.h>
#include <stdint.h>
#include <utility>

typedef unsigned short u16;
typedef unsigned int u32;
typedef unsigned long long u64;
typedef u32 v4u __attribute__((ext_vector_type(4)));
typedef u32 v2u __attribute__((ext_vector_type(2)));
typedef float f32x2 __attribute__((ext_vector_type(2)));
typedef float f32x4 __attribute__((ext_vector_type(4)));
typedef float f32x16 __attribute__((ext_vector_type(16)));
typedef __bf16 bf16x8 __attribute__((ext_vector_type(8)));
typedef __bf16 bf16x2 __attribute__((ext_vector_type(2)));
typedef _Float16 f16x8 __attribute__((ext_vector_type(8)));
typedef _Float16 f16x2 __attribute__((ext_vector_type(2)));
typedef short s16x4 __attribute__((ext_vector_type(4)));
typedef u64 u64x8 __attribute__((ext_vector_type(8)));
typedef u64 u64x4 __attribute__((ext_vector_type(4)));
#define GAS __attribute__((address_space(1)))
#define LAS __attribute__((address_space(3)))
#define DI __device__ __forceinline__
#define RLX_AGENT __ATOMIC_RELAXED, __HIP_MEMORY_SCOPE_AGENT
#define RLX_WG __ATOMIC_RELAXED, __HIP_MEMORY_SCOPE_WORKGROUP
#define SB0() __builtin_amdgcn_sched_barrier(0)
#define CHST 0
#define PSLEEP 2
#define ROT 1
#define POL 1
#define PDS 0
#define THR 0
#define HS 0
#define AB 0
#ifndef NV2
#define NV2 2
#endif
#define WING 10
#define HROT 0
#define GP2 0
#define PFB 30720
#define PF 1
#define FULLF 0
#define WSOFF 0
#define PACE 0
#define PACEV 9
#define F3D 30
#define WARM 1
#define W0G 8
#define W0L 8
#define EX 0
#define MPF 0
#define P7AT 5
#define AX 0
#define SKIP3 1
#define NTX 2
#define EARLY 0
#define THR2 6
#define WINL 8
#define FS1V 2
#define NBD 3333
#define UE 0
#define LP 0
#define LP2 0
#define SPLITF 0
#define OUTP 2
#define WP 1
#define NE 3
#define PRIO 21

#define NOH_OK
#define NWV 12
constexpr int OFF_ST = 16384, OFF_F1 = 128 << 10, OFF_F2 = 256 << 10, OFF_F3 = 512 << 10;
constexpr int FS1 = FS1V, FS2 = 16, FS3 = 16;
constexpr int OFF_U = 1 << 20, OFF_A = 4 << 20, OFF_H = 8 << 20;
constexpr int H_BYTES = 64 * 43 * 2048;
constexpr int L_C = 131072;
constexpr int L_FLG = L_C, L_UF = L_C + 320, L_E = L_C + 768, L_B2 = L_C + 896, L_LNS = L_C + 352, L_CNT = L_C + 416, L_B1 = L_C + 512, L_STAT = L_C + 1024, L_CSP = L_C + 29696;
enum { R_F = 0, R_P = 8, R_P7 = 13, R_PK = 15, R_LN = 19, R_X = 14, R_V1 = 16, R_V2 = 17, R_V3 = 18 };
constexpr int L_U = L_C + 3072, L_KB = L_C + 12288;
constexpr int SPIN_CAP = 1 << 16;
enum { C_A = 0, C_K = 2, C_E = 4, C_FN = 6, C_D = 8, C_RD = 12 };

#define STAMP(i)
#define CST(i)
#define TIMED(x) x

#if defined(__HIP_DEVICE_COMPILE__) && __HIP_DEVICE_COMPILE__
#pragma clang attribute push(__attribute__((target("no-packed-fp32-ops"))), apply_to = function)
#endif
DI float u2f(u32 v) { return __builtin_bit_cast(float, v); }
DI u32 f2u(float v) { return __builtin_bit_cast(u32, v); }
DI bool any_(bool c) { return __builtin_amdgcn_ballot_w64(c) != 0; }
DI bool all_(bool c) { return __builtin_amdgcn_ballot_w64(!c) == 0; }
template <typename T> DI T lds_ld(u32 a) { return *(LAS T*)(size_t)a; }
template <typename T> DI void lds_st(u32 a, T v) { *(LAS T*)(size_t)a = v; }
DI v2u lds_tr(u32 a) { return __builtin_bit_cast(v2u, __builtin_amdgcn_ds_read_tr16_b64_v4i16((LAS s16x4*)(size_t)a)); }
DI v4u mkrsrc(const void* p, u32 bytes) { u64 a = (u64)p; v4u r; r.x = (u32)a; r.y = (u32)(a >> 32) & 0xffffu; r.z = bytes; r.w = 0x00020000u; return r; }
DI void glds(v4u rsrc, u32 voff, u32 soff, u32 lds) {
    asm volatile("s_mov_b32 m0, %3\n\ts_nop 0\n\tbuffer_load_dwordx4 %0, %1, %2 offen lds" :: "v"(voff), "s"(rsrc), "s"(soff), "s"(lds) : "memory");
}
template <int LOFF, int NT = 0> DI u32 glds_first(v4u rsrc, u32 voff, u32 sa, u32 sb, u32 lbase) {
    u32 so;
    if constexpr (NT) asm volatile("s_add_u32 m0, %5, %6\n\ts_add_u32 %0, %3, %4\n\tbuffer_load_dwordx4 %1, %2, %0 offen nt lds" : "=&s"(so) : "v"(voff), "s"(rsrc), "s"(sa), "s"(sb), "s"(lbase), "i"(LOFF) : "memory", "scc"); else
    asm volatile("s_add_u32 m0, %5, %6\n\ts_add_u32 %0, %3, %4\n\tbuffer_load_dwordx4 %1, %2, %0 offen lds" : "=&s"(so) : "v"(voff), "s"(rsrc), "s"(sa), "s"(sb), "s"(lbase), "i"(LOFF) : "memory", "scc");
    return so;
}
template <int LOFF, int SOFF> DI void glds_imm(v4u rsrc, u32 voff, u32 sa, u32 lbase) {
    u32 so;
    asm volatile("s_add_u32 m0, %4, %5\n\ts_add_u32 %0, %3, %6\n\tbuffer_load_dwordx4 %1, %2, %0 offen lds" : "=&s"(so) : "v"(voff), "s"(rsrc), "s"(sa), "s"(lbase), "i"(LOFF), "i"(SOFF) : "memory", "scc");
}
template <int OFF, int NT = 0> DI void glds_next(v4u rsrc, u32 voff, u32 so) {
    if constexpr (NT) asm volatile("buffer_load_dwordx4 %0, %1, %2 offen offset:%3 nt lds" :: "v"(voff), "s"(rsrc), "s"(so), "i"(OFF) : "memory"); else
    asm volatile("buffer_load_dwordx4 %0, %1, %2 offen offset:%3 lds" :: "v"(voff), "s"(rsrc), "s"(so), "i"(OFF) : "memory");
}
template <int LOFF> DI void flag_imm(v4u rsrc, u32 lbase) {
    asm volatile("s_add_u32 m0, %2, %3\n\ts_mov_b64 exec, 1\n\tbuffer_load_dword off, %0, %1 lds\n\ts_mov_b64 exec, -1" :: "s"(rsrc), "s"(rsrc.z), "s"(lbase), "i"(LOFF) : "memory", "scc");
}
DI void glds_nt(v4u rsrc, u32 voff, u32 soff, u32 lds) {
    asm volatile("s_mov_b32 m0, %3\n\ts_nop 0\n\tbuffer_load_dwordx4 %0, %1, %2 offen nt lds" :: "v"(voff), "s"(rsrc), "s"(soff), "s"(lds) : "memory");
}
DI void glds_sc1(v4u rsrc, u32 voff, u32 soff, u32 lds) {
    asm volatile("s_mov_b32 m0, %3\n\ts_nop 0\n\tbuffer_load_dwordx4 %0, %1, %2 offen sc1 lds" :: "v"(voff), "s"(rsrc), "s"(soff), "s"(lds) : "memory");
}
DI void flag_dma(v4u rsrc, u32 lds) {
    asm volatile("s_mov_b32 m0, %2\n\ts_mov_b64 exec, 1\n\tbuffer_load_dword off, %0, %1 lds\n\ts_mov_b64 exec, -1" :: "s"(rsrc), "s"(rsrc.z), "s"(lds) : "memory");
}
DI void st_sc1(v4u v, v4u rsrc, u32 voff, u32 soff) {
    asm volatile("buffer_store_dwordx4 %0, %1, %2, %3 offen sc1\n\ts_nop 1" :: "v"(v), "v"(voff), "s"(rsrc), "s"(soff) : "memory");
}
template <int BYTES> DI void put_flag(const char* base, u32 off, u32 l16, u64 tag) {
    asm volatile("s_mov_b64 exec, 1\n\tglobal_store_dwordx2 %0, %1, %2 sc1\n\ts_mov_b64 exec, -1" :: "v"(l16), "v"(tag), "s"(base + off) : "memory");
}
template <int BYTES> DI void warm_flag(const char* base, u32 off, u32 l16) {
    const v4u v = {0u, 0u, 0u, 0u};
    asm volatile("s_mov_b64 exec, %3\n\tglobal_store_dwordx4 %0, %1, %2 sc1\n\ts_mov_b64 exec, -1\n\ts_nop 0" :: "v"(l16), "v"(v), "s"(base + off), "i"((1 << (BYTES / 16)) - 1) : "memory");
}
DI u32 ldp(const GAS u16* p) { return (NTX & 2) ? __builtin_nontemporal_load(p) : *p; }
DI u32 opq(u32 a) { asm volatile("" : "+v"(a)); return a; }
template <typename T> DI T lds_ldv(u32 a) { return *(volatile LAS T*)(size_t)a; }
template <int SLEEP = 0> DI void spin_zero4(u32 a) {
    int n = 0;
    while (any_(lds_ldv<u32>(a) != 0) && ++n < (SPIN_CAP << 4)) { if (SLEEP) __builtin_amdgcn_s_sleep(1); }
    asm volatile("" ::: "memory");
}
template <int N> DI void wait_vm() { asm volatile("s_waitcnt vmcnt(%0)" :: "i"(N) : "memory"); }
DI void wait_lgkm0() { asm volatile("s_waitcnt lgkmcnt(0)" ::: "memory"); }
DI u64 mix64(u64 e) { e += 0x9E3779B97F4A7C15ull; e ^= e >> 31; e *= 0xBF58476D1CE4E5B9ull; e ^= e >> 29; return e; }
DI u64 get_tag() { const v2u e = lds_ldv<v2u>(L_E); return mix64(((u64)(u32)__builtin_amdgcn_readfirstlane(e.y) << 32) | (u32)__builtin_amdgcn_readfirstlane(e.x)); }
template <int C> DI void sig(u32 a, u32 one) { asm volatile("s_mov_b64 exec, 1\n\tds_add_u32 %0, %1 offset:%2\n\ts_mov_b64 exec, -1" :: "v"(a), "v"(one), "i"(4 * C) : "memory"); }
template <int OFF> DI void clr(u32 a, u32 zero) { asm volatile("s_mov_b64 exec, 1\n\tds_write_b32 %0, %1 offset:%2\n\ts_mov_b64 exec, -1" :: "v"(a), "v"(zero), "i"(OFF) : "memory"); }
DI void spin(u32 a, u32 target) {
    int n = 0;
    while ((u32)__builtin_amdgcn_readfirstlane(lds_ldv<u32>(a)) < target && ++n < (SPIN_CAP << 4)) {}
    asm volatile("" ::: "memory");
}
DI u64 peek_flags(const GAS u64* p, u64 tag, bool on) { u64 f = tag; if (on) f = __hip_atomic_load(p, RLX_AGENT); return f; }
DI void poll_flags(const GAS u64* p, u64 tag, bool on) {
    int n = 0;
    while (true) {
        u64 f = peek_flags(p, tag, on);
        if (all_(f == tag) || ++n > SPIN_CAP) break;
        __builtin_amdgcn_s_sleep(PSLEEP);
    }
    asm volatile("" ::: "memory");
}
DI float bf_lo(u32 v) { return u2f(v << 16); }
DI float bf_hi(u32 v) { return u2f(v & 0xffff0000u); }
DI u32 pk_bf(float a, float b) { f32x2 v = {a, b}; return __builtin_bit_cast(u32, __builtin_convertvector(v, bf16x2)); }
typedef short s16x2 __attribute__((ext_vector_type(2)));
DI u32 relu_pk(u32 v) { return __builtin_bit_cast(u32, __builtin_elementwise_max(__builtin_bit_cast(s16x2, v), (s16x2){0, 0})); }
DI u32 umax(u32 a, u32 b) { return a > b ? a : b; }
DI float rbf(float a) { return bf_lo(pk_bf(a, a)); }
DI u32 pk_h(f32x2 v) { return __builtin_bit_cast(u32, __builtin_convertvector(v, f16x2)); }
DI f32x16 mfma32(v4u a, v4u b, f32x16 c) { return __builtin_amdgcn_mfma_f32_32x32x16_bf16(__builtin_bit_cast(bf16x8, a), __builtin_bit_cast(bf16x8, b), c, 0, 0, 0); }
DI f32x4 mfma16(v4u a, v4u b, f32x4 c) { return __builtin_amdgcn_mfma_f32_16x16x32_bf16(__builtin_bit_cast(bf16x8, a), __builtin_bit_cast(bf16x8, b), c, 0, 0, 0); }
DI f32x4 mfma16h(v4u a, v4u b, f32x4 c) { return __builtin_amdgcn_mfma_f32_16x16x32_f16(__builtin_bit_cast(f16x8, a), __builtin_bit_cast(f16x8, b), c, 0, 0, 0); }
template <int CTRL> DI float dpp_add(float v) { return v + __builtin_bit_cast(float, __builtin_amdgcn_update_dpp(0, __builtin_bit_cast(int, v), CTRL, 0xf, 0xf, true)); }
template <int CTRL> DI u32 dpp_max(u32 v) { const u32 o = (u32)__builtin_amdgcn_update_dpp(0, (int)v, CTRL, 0xf, 0xf, true); return v > o ? v : o; }
template <int T> DI float bcast(float v) { return __builtin_bit_cast(float, __builtin_amdgcn_update_dpp(0, __builtin_bit_cast(int, v), 0x150 + T, 0xf, 0xf, true)); }
DI float rl(float v, int l) { return __builtin_bit_cast(float, __builtin_amdgcn_readlane(__builtin_bit_cast(int, v), l)); }
DI f32x2 wave_sum2(f32x2 v) {
    float a = v.x, c = v.y;
    a = dpp_add<0xB1>(a); c = dpp_add<0xB1>(c); a = dpp_add<0x4E>(a); c = dpp_add<0x4E>(c);
    a = dpp_add<0x141>(a); c = dpp_add<0x141>(c); a = dpp_add<0x140>(a); c = dpp_add<0x140>(c);
    return ((f32x2){rl(a, 0), rl(c, 0)} + (f32x2){rl(a, 16), rl(c, 16)}) + ((f32x2){rl(a, 32), rl(c, 32)} + (f32x2){rl(a, 48), rl(c, 48)});
}
DI u32 wave_max(u32 v) {
    v = dpp_max<0xB1>(v); v = dpp_max<0x4E>(v); v = dpp_max<0x141>(v); v = dpp_max<0x140>(v);
    const u32 a = __builtin_amdgcn_readlane(v, 0), c = __builtin_amdgcn_readlane(v, 16), d = __builtin_amdgcn_readlane(v, 32), e = __builtin_amdgcn_readlane(v, 48);
    const u32 p = a > c ? a : c, q = d > e ? d : e;
    return p > q ? p : q;
}
struct cmult { f32x2 b, n; };
DI cmult mkc(f32x2 b) { cmult c; c.b = b; c.n = (f32x2){-b.y, b.x}; return c; }
DI f32x2 cmul(f32x2 a, const cmult& c) { return __builtin_elementwise_fma((f32x2){a.x, a.x}, c.b, (f32x2){a.y, a.y} * c.n); }
DI f32x16 mfma32h(v4u a, v4u b, f32x16 c) { return __builtin_amdgcn_mfma_f32_32x32x16_f16(__builtin_bit_cast(f16x8, a), __builtin_bit_cast(f16x8, b), c, 0, 0, 0); }
DI u32 pk_h2(float a, float b) { return pk_h((f32x2){a, b}); }
DI f32x2 cexp_pow(float lr, float th, float tl, float e) {
    float a = __builtin_amdgcn_fractf(th * e) + tl * e;
    float m = __builtin_amdgcn_exp2f(lr * e);
    f32x2 r; r.x = m * __builtin_amdgcn_cosf(a); r.y = m * __builtin_amdgcn_sinf(a); return r;
}
DI f32x4 sel4(bool c, f32x4 a, f32x4 b) { return c ? a : b; }
constexpr int w2loc(int n) { return n < 16 ? n : n < 28 ? n + 4 : n - 28; }
constexpr int nbs(int k) { return k <= 0 ? 0 : nbs(k - 1) + (k == 1 ? NBD / 1000 : k == 2 ? NBD / 100 % 10 : k == 3 ? NBD / 10 % 10 : NBD % 10); }
template <class F, int... I> DI void sfor_(F&& f, std::integer_sequence<int, I...>) { (f(std::integral_constant<int, I>{}), ...); }
template <int N, class F> DI void sfor(F&& f) { sfor_(f, std::make_integer_sequence<int, N>{}); }
template <int I> using IC = std::integral_constant<int, I>;
template <typename T> DI void pin(T& v) { asm volatile("" : "+v"(v)); }

__global__ void __launch_bounds__(768, 3)
fused_kernel(const u16* x_, const u16* logA_, const u16* Aim_, const u16* Cre_, const u16* Cim_, const u16* logdt_, const u16* Dskip_, const u16* w1_,
             const u16* b1_, const u16* w2_, const u16* b2_, u16* out_, char* ws_) {
    u64x8 ka; u64x4 kb4; u64 kc;
    asm volatile("s_load_dwordx16 %0, %3, 0x0\n\ts_load_dwordx8 %1, %3, 0x40\n\ts_load_dwordx2 %2, %3, 0x60" : "=&s"(ka), "=&s"(kb4), "=&s"(kc) : "s"(__builtin_amdgcn_kernarg_segment_ptr()) : "memory");
    __shared__ __attribute__((aligned(16))) char smem[163840];
    asm volatile("" :: "v"((u32)(size_t)(LAS char*)smem));
    const int tid = threadIdx.x, lane = tid & 63, wave = __builtin_amdgcn_readfirstlane(tid >> 6) & 15, w = wave & 3, role = wave >> 2;
    const int b = blockIdx.x & 255, xcd = b & 7, slot = b >> 3;
    const int P = 4 * (xcd >> 1) + (slot >> 3), j = 8 * (xcd & 1) + (slot & 7);
    const int tr = 4 * xcd + (slot >> 3), tc = slot & 7;
    const int u0 = (j * 43) >> 4;
    if ((PF & 1) && wave == 4) {
        u64 pc; asm volatile("s_getpc_b64 %0" : "=s"(pc));
        const v4u rc = mkrsrc((const void*)(pc & ~255ull), PFB);
        glds(rc, lane * 16, slot * 1024, L_C + 2048);
    }
    const int h = lane >> 5, l31 = lane & 31, l15 = lane & 15, g = lane >> 4;
    const u32 l16 = lane * 16, lw = w * 32768, w4 = w * 4;
    const int rho = HROT ? ((slot >> 3) + slot) & 3 : (slot >> 3);
    const int tb = 64 * (P >> 2) + 32 * (j & 1) + 16 * ((P >> 1) & 1) + 2 * (j >> 1) + (P & 1);
    const int cp = 32 * xcd + slot, chl = w >> 1, hf = w & 1, ch = 2 * cp + chl;
    {
        const u32 v = (tid >= 192 && tid < 256) || (tid >= 282 && tid < 286) ? 0u : ~0u;
        if (tid < 288) lds_st<v4u>((tid < 192 ? L_CSP : tid < 256 ? L_U - 192 * 16 : L_C - 256 * 16) + tid * 16, (v4u){v, v, v, v});
    }
    const int rot = ROT == 0 ? 0 : ROT == 1 ? (((slot >> 3) + slot) & 1) * 4 : ROT == 2 ? (((slot >> 3) + slot) & 3) * 2 : (2 * (slot >> 3) + (slot & 7)) & 7;
    u32 sx = (512 * (P >> 2) + 128 * ((P >> 1) & 1) + 64 * (w & 1) + 8 * (P & 1) + 4 * (w >> 1)) * 1024, sw1 = 16 * w * 2752 + u0 * 64;
    u32 sx0 = rot * 128, sw0 = rot * (64 * 2752);
    u32 vx[4], vw[3];
    const int lim3 = 4 * ((((j + 1) * 43) >> 4) - u0);
#pragma unroll
    for (int i = 0; i < 3; i++) { const int p = 64 * i + lane, r = (p * 171) >> 11; vw[i] = opq((SKIP3 && p - 12 * r >= lim3) ? 0x40000000u : r * (2752 - 192) + l16); }
#pragma unroll
    for (int i = 0; i < 4; i++) vx[i] = opq((((h * 256 + ((lane >> 3) & 3)) * 1024 + ((lane & 7) ^ g) * 16) ^ ((i & 1) * 64)) + i * (16384 - 1024));
    asm volatile("s_waitcnt lgkmcnt(0)\n\ts_barrier" : "+s"(ka), "+s"(kb4), "+s"(kc), "+s"(sx), "+s"(sw1), "+s"(sx0), "+s"(sw0) :: "memory");
    const u16 *x = (const u16*)ka[0], *logA = (const u16*)ka[1], *Aim = (const u16*)ka[2], *Cre = (const u16*)ka[3], *Cim = (const u16*)ka[4], *logdt = (const u16*)ka[5], *Dskip = (const u16*)ka[6], *w1 = (const u16*)ka[7];
    const u16 *b1 = (const u16*)kb4[0], *w2 = (const u16*)kb4[1], *b2 = (const u16*)kb4[2]; u16* out = (u16*)kb4[3]; char* ws = (char*)kc;
    if (CHST == 3) STAMP(17);
    const v4u rx = mkrsrc(x, 2 << 20);
    const u32 aF = opq(L_FLG + (lane & 3) * 4);
    auto flg = [&](auto R_) { flag_imm<L_FLG + decltype(R_)::value * 16>(rx, w4); };
    const v4u rw1 = mkrsrc(w1, 512 * 1376 * 2);
    auto ldX = [&](auto C_) { constexpr int c = decltype(C_)::value;
        const u32 so = glds_first<(16 + (c % 4) * 4) * 1024, NTX & 1>(rx, vx[0], sx, c ? ((c + rot) & 7) * 128 : sx0, lw);
        glds_next<1024, NTX & 1>(rx, vx[1], so); glds_next<2048, NTX & 1>(rx, vx[2], so); glds_next<3072, NTX & 1>(rx, vx[3], so); };
    auto ldW = [&](auto C_) { constexpr int c = decltype(C_)::value;
        const u32 so = glds_first<3 * (c % 5) * 1024, (NTX >> 2) & 1>(rw1, vw[0], sw1, c ? ((c + rot) & 7) * (64 * 2752) : sw0, lw);
        glds_next<1024, (NTX >> 2) & 1>(rw1, vw[1], so); if (!(AX & 4)) glds_next<2048, (NTX >> 2) & 1>(rw1, vw[2], so); };
    if (role == 0) {
        if (CHST == 3) STAMP(18);
        ldW(IC<0>{}); ldX(IC<0>{}); flg(IC<R_F>{});
        ldW(IC<1>{}); ldX(IC<1>{}); flg(IC<R_F + 1>{});
        if (CHST == 3) { STAMP(19); wait_vm<15>(); STAMP(20); }
    }
    asm volatile("" ::: "memory");
    u32 aV0, aV1, aFw, aCn, one, f2o;
    u32 sh2[4], sh2t[4];
    auto pre2 = [&]() {
        aV0 = opq(lw + (4 * h + (l15 >> 2)) * 128 + ((l15 >> 3) & 1) * 64 + (g & 1) * 32 + (lane & 3) * 8); aV1 = opq(aV0 ^ 64);
        aFw = opq(L_FLG + w4); aCn = opq(L_CNT + w4); one = opq(1u);
        const int jj = 4 * w + (lane < 4 ? lane : 4);
        f2o = opq(((2 * tr + role) * 16 + (jj < 15 ? jj : 15)) * FS2 * 8);
#pragma unroll
        for (int k = 0; k < 4; k++) {
            sh2[k] = ((2 * tr + role) * 43 + 11 * w) * 2048 + (6 * rho - (k + rho >= 4 ? 22 : 0)) * 1024; sh2t[k] = (w == 3 && k + rho == 3) ? (u32)H_BYTES : sh2[k];
            asm volatile("" : "+s"(sh2[k]), "+s"(sh2t[k]));
        }
    };

    auto rdV = [&](int s, int dt) { v2u lo = lds_tr((dt ? aV1 : aV0) + w2loc(2 * s) * 1024), hi = lds_tr((dt ? aV1 : aV0) + w2loc(2 * s + 1) * 1024); return (v4u){lo.x, lo.y, hi.x, hi.y}; };
    auto gemm2 = [&](auto ROLE_, v4u (&stage)[16], u64 f2, u64 tag) __attribute__((always_inline)) {
        constexpr int role = decltype(ROLE_)::value;
        const GAS u64* f2p = (const GAS u64*)(ws + OFF_F2 + f2o);
        __builtin_amdgcn_s_setprio(role ? GP2 : LP2);
        const __amdgpu_buffer_rsrc_t rh = __builtin_amdgcn_make_buffer_rsrc(ws + OFF_H, 0, (AX & 128) ? 0 : H_BYTES, 0x00020000);
        constexpr int WIN = role ? WING : WINL;
        v4u park[8][2], hq[WIN], vb[3][2];
        auto ldH = [&](int s) { hq[s % WIN] = __builtin_amdgcn_raw_buffer_load_b128(rh, l16, ((s + 2) % 6 >= 4 ? sh2t[(s + 2) / 6] : sh2[(s + 2) / 6]) + s * 1024, 16); };
        constexpr int W0 = role ? W0G : W0L;
        if (role) {
            SB0();
            spin_zero4<1>(aFw + R_V1 * 16);
            STAMP(9);
            f2 = tag;
            asm volatile("s_mov_b64 exec, 31\n\tglobal_load_dwordx2 %0, %1, %2 sc1\n\ts_mov_b64 exec, -1" : "+v"(f2) : "v"(f2o), "s"(ws + OFF_F2) : "memory");
            SB0();
#pragma unroll
            for (int s = 0; s < 8; s++) { park[s][0] = rdV(s, 0); park[s][1] = rdV(s, 1); }
            clr<R_PK * 16>(aFw, opq(0u));
            asm volatile("s_waitcnt vmcnt(0)" : "+v"(f2) :: "memory");
            SB0();
        }
        STAMP(10);
        if (!all_(f2 == tag)) poll_flags(f2p, tag, lane < 5);
        STAMP(5);
#pragma unroll
        for (int s = 0; s < W0; s++) ldH(s);
        const int q3 = (lane >> 2) & 7, fr = 8 * wave + 4 * h + 2 * ((q3 ^ (q3 >> 1) ^ (q3 >> 2)) & 1) + (q3 >> 2), fo = ((q3 >> 1) & 1) * 4 + (lane & 3);
        const u32 fin_off = (512 * (tr >> 3) + 256 * ((fr >> 2) & 1) + 128 * ((tr >> 2) & 1) + 16 * (fr >> 3) + 4 * (tr & 3) + (fr & 3)) * 512 + tc * 64 + fo * 8;
        f32x4 s0, s1; float rs2; u32 da, ra, rb, rc, aFn;
        v4u fin_x, fin_b; u32 fin_s, fv;
        f32x16 ac2[2];
#pragma unroll
        for (int i = 0; i < 16; i++) { ac2[0][i] = 0.f; ac2[1][i] = 0.f; }
        const u32 aSt = opq(lw + l16), zero = opq(0u);
        if (!role) {
            SB0();
            spin_zero4(opq(aFw + ((lane & 1) ? R_PK : R_V2) * 16));
            STAMP(9);
            vb[0][0] = rdV(0, 0); vb[0][1] = rdV(0, 1); vb[1][0] = rdV(1, 0); vb[1][1] = rdV(1, 1);
            asm volatile("" ::: "memory");
#pragma unroll
            for (int k = 0; k < 4; k++) lds_st<v4u>(aSt + k * 1024, stage[k]);
        }
#pragma unroll
        for (int s = 0; s < 22; s++) {
            SB0();
            if (role && (s == 4 || s == 10)) fv = lds_ldv<u32>(aFw + (s == 4 ? R_V2 : R_V3) * 16);
            if (role && (s == 6 || s == 12)) { if (__builtin_expect(any_(fv != 0), 0)) spin_zero4(aFw + (s == 6 ? R_V2 : R_V3) * 16); asm volatile("" ::: "memory"); }
            if ((!role || s + 2 >= 8) && s + 2 < 22) { vb[(s + 2) % 3][0] = rdV(s + 2, 0); vb[(s + 2) % 3][1] = rdV(s + 2, 1); }
            if (s == 19) sig<C_RD>(aCn, one);
            if (s == 21) fv = lds_ldv<u32>(aCn + 4 * C_RD);
            ac2[0] = mfma32(role && s < 8 ? park[s & 7][0] : vb[s % 3][0], hq[s % WIN], ac2[0]);
            if (AX & 256) { pin(vb[s % 3][1]); if (role && s < 8) pin(park[s & 7][1]); } else
            ac2[1] = mfma32(role && s < 8 ? park[s & 7][1] : vb[s % 3][1], hq[s % WIN], ac2[1]);
            SB0();
            if (!role && s < 6) {
                asm volatile("" ::: "memory");
                lds_st<v4u>(aSt + (2 * s + 4) * 1024, stage[(2 * s + 4) & 15]); lds_st<v4u>(aSt + (2 * s + 5) * 1024, stage[(2 * s + 5) & 15]);
                if (s == 5) { clr<R_V3 * 16>(aFw, zero); STAMP(13); }
            }
            if (s < WIN - W0) { ldH(W0 + 2 * s); ldH(W0 + 2 * s + 1); }
            else if (s + WIN < 22) ldH(s + WIN);
            if (s == 12) {
                fin_x = *(const GAS v4u*)(x + fin_off); fin_b = lds_ld<v4u>(L_B2 + fo * 16);
                fin_s = __hip_atomic_load((GAS u32*)(ws + OFF_ST + (tr >> 1) * 1024 + (fr >> 2) * 64 + (tr & 1) * 32 + (fr & 3) * 8), RLX_AGENT);
            }
            if (s == 16) { da = opq(lw + role * 20480 + l31 * 272 + h * 16); ra = opq(role * 20480 + (fr & 31) * 272 + fo * 32); rb = opq(ra + 65536); rc = opq(L_KB + fr * 144 + fo * 16); aFn = opq(L_CNT + 4 * (C_FN + role)); }
            if (s == 21) {
                s0 = (f32x4){bf_lo(fin_x.x) + bf_lo(fin_b.x), bf_hi(fin_x.x) + bf_hi(fin_b.x), bf_lo(fin_x.y) + bf_lo(fin_b.y), bf_hi(fin_x.y) + bf_hi(fin_b.y)};
                pin(s0);
            }
        }
        SB0();
        STAMP(7); CST(3);
        if (__builtin_expect(any_(fv < 2), 0)) spin(aCn + 4 * C_RD, 2);
        asm volatile("" ::: "memory");
#pragma unroll
        for (int dt = 0; dt < 2; dt++)
#pragma unroll
            for (int q = 0; q < 4; q++)
                lds_st<f32x4>(da + (32 * dt + 8 * q) * 4, (f32x4){ac2[dt][4 * q], ac2[dt][4 * q + 1], ac2[dt][4 * q + 2], ac2[dt][4 * q + 3]});
        if (SPLITF) sig<0>(aFn, one);
        SB0();
        s1 = (f32x4){bf_lo(fin_x.z) + bf_lo(fin_b.z), bf_hi(fin_x.z) + bf_hi(fin_b.z), bf_lo(fin_x.w) + bf_lo(fin_b.w), bf_hi(fin_x.w) + bf_hi(fin_b.w)};
        rs2 = __builtin_amdgcn_rcpf(bf_hi(fin_s)); rs2 *= rs2;
        asm volatile("" : "+v"(s0), "+v"(s1), "+v"(rs2));
        if (SPLITF) spin(aFn, 8);
        else { wait_lgkm0(); __builtin_amdgcn_s_barrier(); asm volatile("" ::: "memory"); }
        STAMP(11);
        const v4u av = lds_ld<v4u>(rc);
        s0 += ((lds_ld<f32x4>(ra) + lds_ld<f32x4>(ra + 32768)) + (lds_ld<f32x4>(rb) + lds_ld<f32x4>(rb + 32768))) * rs2;
        s1 += ((lds_ld<f32x4>(ra + 16) + lds_ld<f32x4>(ra + 32768 + 16)) + (lds_ld<f32x4>(rb + 16) + lds_ld<f32x4>(rb + 32768 + 16))) * rs2;
        s0 += (f32x4){bf_lo(av.x), bf_hi(av.x), bf_lo(av.y), bf_hi(av.y)};
        s1 += (f32x4){bf_lo(av.z), bf_hi(av.z), bf_lo(av.w), bf_hi(av.w)};
        const v4u ov = {pk_bf(s0[0], s0[1]), pk_bf(s0[2], s0[3]), pk_bf(s1[0], s1[1]), pk_bf(s1[2], s1[3])};
        if (OUTP == 0) *(GAS v4u*)(out + fin_off) = ov;
        else if (OUTP == 1) __builtin_nontemporal_store(ov, (GAS v4u*)(out + fin_off));
        else st_sc1(ov, mkrsrc(out, 2 << 20), fin_off * 2, 0);
    };

    if (__builtin_expect(role == 0, 1)) {
        v4u stage[16]; u64 f2, tag;
        const u32 sw2 = 352 * w * 1024 + tc * 128;
        const u32 vv = (lane >> 3) * 1024 + ((lane & 7) ^ ((g & 1) << 2)) * 16;
        u32 sv[4];
#pragma unroll
        for (int k = 0; k < 4; k++) sv[k] = sw2 + (12 * rho - (k + rho >= 4 ? 44 : 0)) * 8192;
        const __amdgpu_buffer_rsrc_t rv = __builtin_amdgcn_make_buffer_rsrc((void*)w2, 0, (AX & 64) ? 0 : 512 * 1376 * 2, 0x00020000);
        auto ldV = [&](int r) { stage[r] = __builtin_amdgcn_raw_buffer_load_b128(rv, vv, sv[(r + 32) / 12] + (r + 28) * 8192, 0); };
        const GAS u64* f1p = (const GAS u64*)(ws + OFF_F1) + ((xcd >> 1) * 256 + 64 * w + lane) * FS1;
        u64 f1; bool got = false;
        auto look = [&]() {
            spin_zero4<1>(aF + R_X * 16);
            tag = get_tag();
            asm volatile("global_load_dwordx2 %0, %1, off sc1" : "=&v"(f1) : "v"(f1p) : "memory");
        };
        auto gather = [&]() {
            const v4u rus = mkrsrc(ws + OFF_U, 2 << 20);
            const u32 vU = (lane >> 1) * 8192 + ((lane ^ g) & 1) * 16;
#pragma unroll
            for (int i = 0; i < 2; i++) glds_sc1(rus, vU, (64 * w + 32 * i) * 8192 + cp * 32, L_U + 1024 + w * 2048 + i * 1024);
            flag_dma(rus, L_UF + w * 4);
        };
        sfor<6>([&](auto I) {
            constexpr int c = decltype(I)::value + 2;
            if constexpr (THR && c >= 2) wait_vm<THR>();
            if (!(AB & 4)) ldW(IC<c>{});
            if constexpr (c >= 4) {
                SB0();
#pragma unroll
                for (int k = nbs(c - 4); k < nbs(c - 3); k++) ldV(k);
                SB0();
            }
            if constexpr (c >= 4) TIMED(spin_zero4<1>(aF + (R_P + c - 4) * 16));
            if constexpr (THR && c >= 2) wait_vm<THR>();
            if constexpr (UE && c == 6) look();
            if constexpr (UE && c == 7) {
                asm volatile("s_waitcnt vmcnt(%1)" : "+v"(f1) : "i"(8 + nbs(4) - nbs(3)) : "memory");
                got = all_(f1 == tag);
                if (got) { STAMP(16); gather(); }
            }
            ldX(IC<c>{});
            flg(IC<R_F + c>{});
        });
        STAMP(1);
        {
            const u32 vS = ((lane >> 1) * 4 + (lane & 1)) * 16;
            const v4u rst = mkrsrc(ws + OFF_ST, 16384);
            asm volatile("s_mov_b32 m0, %3\n\ts_mov_b64 exec, 0xffff\n\tbuffer_load_dwordx4 %0, %1, %2 offen sc1 lds\n\ts_mov_b64 exec, -1" :: "v"(vS), "s"(rst), "s"(P * 1024 + (w & 1) * 512 + (w >> 1) * 32), "s"(L_STAT + w * 256) : "memory");
        }
        if (!UE) look();
#pragma unroll
        for (int n = nbs(4); n < 16; n++) ldV(n);
        SB0();
        if (!UE) asm volatile("s_waitcnt vmcnt(%1)" : "+v"(f1) : "i"(16 - nbs(4)) : "memory");
        if (!got) {
            if (!all_(f1 == tag)) poll_flags(f1p, tag, true);
            STAMP(16);
            gather();
        }
        STAMP(2);
        pre2();
#pragma unroll
        for (int n = 0; n < 16; n++) pin(stage[n]);
        __builtin_amdgcn_s_setprio(LP);
        {
            const u32 aK = opq((lane & 4) ? L_CNT + 4 * C_K + (lane & 1) * 4 : L_FLG + R_P7 * 16 + (lane & 3) * 4);
            const u32 want = opq((lane & 4) ? 2u : 0u);
            int k = 0;
            while (any_(lds_ldv<u32>(aK) != want) && ++k < (SPIN_CAP << 4)) { __builtin_amdgcn_s_sleep(1); }
            asm volatile("" ::: "memory");
        }
        STAMP(14);
        const v4u rw2 = mkrsrc(w2, 512 * 1376 * 2);
        f2 = tag;
        sfor<28>([&](auto I) {
            constexpr int n = decltype(I)::value;
            if constexpr (THR2 && n >= THR2) wait_vm<THR2>();
            if constexpr (n == 28 - NV2) asm volatile("s_mov_b64 exec, 31\n\tglobal_load_dwordx2 %0, %1, %2 sc1\n\ts_mov_b64 exec, -1" : "+v"(f2) : "v"(f2o), "s"(ws + OFF_F2) : "memory");
            if constexpr (!(AX & 32) || n < 4 || n >= 16) glds_imm<w2loc(n) * 1024, n * 8192>(rw2, vv, sv[(n + 4) / 12], lw);
            if constexpr (n == 15) flg(IC<R_V1>{});
        });
        flg(IC<R_V2>{});
        asm volatile("s_waitcnt vmcnt(%1)" : "+v"(f2) : "i"(NV2 + 1) : "memory");
        STAMP(8);
        gemm2(IC<0>{}, stage, f2, tag);
    } else if (role == 1) {
        u64 tag;
        __builtin_amdgcn_s_setprio(PRIO / 10);
        if (WARM) warm_flag<FS2 * 8>(ws + OFF_F2, ((4 * P + w) * 16 + j) * FS2 * 8, l16);
        if (MPF) {
            const v4u rp = mkrsrc(w2, 512 * 1376 * 2);
            if (MPF & 2) {
                glds(rw1, l16, (b * 11 + 2 * w) * 512, L_C + 2048);
                if (w >= 2) glds(rw1, l16, (b * 11 + 4 + 2 * w) * 512, L_C + 2048);
            }
            glds(rp, l16, (b * 11 + 2 * w) * 512, L_C + 2048);
            if (w < 2) glds(rp, l16, (b * 11 + 8 + 2 * w) * 512, L_C + 2048);
        }
        u32 aW[4], aX[4];
#pragma unroll
        for (int s = 0; s < 4; s++) {
            const int ks = (s + w) & 3;
            aW[s] = opq(ks * 32768 + (8 * h + (l15 >> 2)) * 192 + (g & 1) * 32 + (lane & 3) * 8);
            aX[s] = opq(lw + 16384 + l31 * 128 + (((2 * ks + h) ^ ((l31 >> 1) & 7)) * 16));
        }
        const u32 aC = opq(L_CSP + l31 * 32 + h * 16), aCw = opq(L_CSP + lane * 32 + w * 8), aS = opq(L_STAT + w * 256 + l31 * 8), aB = opq(L_B1 + lane * 2);
        pre2();
        const u32 zero = opq(0u);
        f32x16 acc[3]; f32x4 cs = {0.f, 0.f, 0.f, 0.f};
        v4u sel[3];
#pragma unroll
        for (int u = 0; u < 3; u++) {
            for (int i = 0; i < 16; i++) acc[u][i] = 0.f;
            const u32 sv = (l15 == (u < 2 ? 8 * u : 1) + 4 * (g & 1)) ? 0xBF80BF80u : 0u; sel[u] = (v4u){sv, sv, sv, sv}; pin(sel[u]);
        }
        v4u fa[3][3], fb[2][4];
        auto rdW = [&](int n, int u) { const u32 a = aW[n & 3] + ((n >> 2) % 5) * 3072 + u * 64; v2u lo = lds_tr(a), hi = lds_tr(a + 768); fa[n % 3][u] = (v4u){lo.x, lo.y, hi.x, hi.y}; };
        auto rdX = [&](int c, int s) { fb[c & 1][s] = lds_ld<v4u>(aX[s] + (c % 4) * 4096); };
        const v4u rhs = mkrsrc(ws + OFF_H, (AX & 8) ? 0 : H_BYTES);
        const int nown = (((j + 1) * 43) >> 4) - u0;
        const u32 sh = ((4 * P + w) * 43 + u0) * 2048, shx = nown == 3 ? sh + 4096 : (u32)H_BYTES;
        spin_zero4<(EX >> 2) & 1>(aF);
        STAMP(1);
        rdX(0, 0);
#pragma unroll
        for (int u = 0; u < 3; u++) rdW(0, u);
        rdX(0, 1);
#pragma unroll
        for (int u = 0; u < 3; u++) rdW(1, u);
        rdX(0, 2); rdX(0, 3);
        u32 fl, fx, bb[2]; v2u ee; v2u stg; v4u fas[3], fbs;
        sfor<32>([&](auto N_) __attribute__((always_inline)) {
            constexpr int n = decltype(N_)::value, c = n >> 2, s = n & 3;
            SB0();
            if constexpr (s == 2 && c < 5) {
                pin(fa[(n + 1) % 3][2]);
                flg(IC<R_P + c>{});
            }
            if (s == 2 && c < 7 && !(AB & 2)) {
                if (__builtin_expect(any_(fl != 0), 0)) TIMED(spin_zero4<(EX >> 2) & 1>(aF + (R_F + c + 1) * 16));
                asm volatile("" ::: "memory");
            }
            if (CHST == 2 && n == 26) STAMP(3);
            sfor<3>([&](auto U_) __attribute__((always_inline)) {
                constexpr int u = decltype(U_)::value;
                if (!(EX & 2) || u < 2) acc[u] = mfma32(fa[n % 3][u], fb[c & 1][s], acc[u]);
                if (EX & 2) pin(fa[n % 3][u]);
                if (s == 0 && c < 7 && !(EX & 1)) cs = mfma16(sel[u], fa[n % 3][u], cs);
                if (n == 27) cs = mfma16(sel[u], fa[28 % 3][u], cs);
                SB0();
                if (s >= 2 && c < 7 && u < 2) rdX(c + 1, 2 * (s - 2) + u);
                if (n + 2 < 32) rdW(n + 2, u);
                if (s == 1 && u == 0 && c < 7) fl = lds_ldv<u32>(aF + (R_F + c + 1) * 16);
                if constexpr (P7AT < 0 && n == 29 && u == 2) clr<R_P7 * 16>(aFw, zero);
                if (n == 25 && u == 2) { bb[0] = lds_ld<u16>(aB); bb[1] = lds_ld<u16>(aB + 128); }
                if (n == 21 && u == 1) fx = lds_ldv<u32>(aF + R_X * 16);
                if (n == 23 && u == 1) { if (__builtin_expect(any_(fx != 0), 0)) spin_zero4(aF + R_X * 16); asm volatile("" ::: "memory"); ee = lds_ldv<v2u>(L_E); }
                if (n == 28 && u == 1) {
                    v2u t[2];
#pragma unroll
                    for (int k = 0; k < 2; k++) {
                        const float d = cs[k] - bf_hi(f2u(cs[k]));
                        t[k] = (v2u){__builtin_amdgcn_perm(f2u(d), f2u(cs[k]), 0x07060302u), pk_bf(d - bf_hi(f2u(d)), bf_lo(bb[k]))};
                    }
                    asm volatile("ds_write2st64_b64 %0, %1, %2 offset1:4" :: "v"(aCw), "v"(t[0]), "v"(t[1]) : "memory");
                }
                if (n == 31 && u == 0) { fas[0] = lds_ldv<v4u>(aC); fas[1] = lds_ldv<v4u>(aC + 1024); fas[2] = lds_ldv<v4u>(aC + 2048); }
                if (n == 30 && u == 0) stg = lds_ldv<v2u>(aS);
                if (n == 30 && u == 1) { tag = mix64(((u64)(u32)__builtin_amdgcn_readfirstlane(ee.y) << 32) | (u32)__builtin_amdgcn_readfirstlane(ee.x)); asm volatile("" : "+s"(tag)); }
                SB0();
            });
        });
        if (CHST == 2) STAMP(19);
        {
            auto csbad = [&]() { return umax(umax(umax(fas[0].x, fas[0].z), umax(fas[1].x, fas[1].z)), umax(fas[2].x, fas[2].z)) == ~0u; };
            const u32 want = (lane & 1) ? (u32)(tag >> 32) : (u32)tag;
            if (__builtin_expect(any_(csbad() || stg.y != want), 0)) {
                int k = 0;
                while (any_(csbad()) && ++k < (SPIN_CAP << 2)) { fas[0] = lds_ldv<v4u>(aC); fas[1] = lds_ldv<v4u>(aC + 1024); fas[2] = lds_ldv<v4u>(aC + 2048); }
                k = 0;
                const GAS u64* sp = (const GAS u64*)(ws + OFF_ST + P * 1024 + (w & 1) * 512 + (w >> 1) * 32 + (l31 >> 2) * 64 + (lane & 3) * 8);
                while (any_(stg.y != want) && ++k < SPIN_CAP) { const u64 gr = __hip_atomic_load(sp, RLX_AGENT); stg = (v2u){(u32)gr, (u32)(gr >> 32)}; }
            }
            const u32 m1 = stg.x & 0xffffu, m2 = m1 * 0x10001u;
            fbs = (v4u){m2, h ? m1 : stg.x, m2, m1};
        }
#pragma unroll
        for (int u = 0; u < 3; u++) acc[u] = mfma32(fas[u], fbs, acc[u]);
        STAMP(2); CST(1);
        sfor<6>([&](auto K_) __attribute__((always_inline)) {
            constexpr int u = decltype(K_)::value >> 1, s = decltype(K_)::value & 1;
            v4u o;
#pragma unroll
            for (int e = 0; e < 4; e++) {
                const float a0 = acc[u][8 * s + 2 * e], a1 = acc[u][8 * s + 2 * e + 1];
                o[e] = relu_pk(pk_bf(a0 * __builtin_fabsf(a0), a1 * __builtin_fabsf(a1)));
            }
            st_sc1(o, rhs, l16, (u < 2 ? sh + u * 2048 : shx) + s * 1024);
            if constexpr (decltype(K_)::value == P7AT) clr<R_P7 * 16>(aFw, zero);
            if (PACE && decltype(K_)::value < 5) __builtin_amdgcn_s_sleep(PACE);
            if (PACEV < 9) wait_vm<PACEV>();
            SB0();
        });
        STAMP(8);
        if (P7AT >= 6) { wait_vm<(P7AT >= 6 ? 11 - P7AT : 0)>(); clr<R_P7 * 16>(aFw, zero); }
        wait_vm<0>();
        STAMP(4);
        put_flag<FS2 * 8>(ws + OFF_F2, ((4 * P + w) * 16 + j) * FS2 * 8, l16, tag);
        v4u none[16];
        gemm2(IC<1>{}, none, ~tag, tag);
    } else {
        u64 tag;
        __builtin_amdgcn_s_setprio(3);
        if (NTX & 8) { glds_nt(rx, l16, (8 * tb + 2 * w) * 1024, L_KB + (2 * w) * 1088); glds_nt(rx, l16, (8 * tb + 2 * w + 1) * 1024, L_KB + (2 * w + 1) * 1088); }
        else { glds(rx, l16, (8 * tb + 2 * w) * 1024, L_KB + (2 * w) * 1088); glds(rx, l16, (8 * tb + 2 * w + 1) * 1024, L_KB + (2 * w + 1) * 1088); }
        if (w == 0) {
            const v4u re = mkrsrc(ws, 16);
            asm volatile("s_mov_b32 m0, %1\n\ts_mov_b64 exec, 1\n\tbuffer_load_dwordx4 off, %0, 0 sc1 lds\n\ts_mov_b64 exec, -1" :: "s"(re), "s"(L_E) : "memory");
        } else if (w == 2) {
            const v4u rb2 = mkrsrc(b2, 1024);
            asm volatile("s_mov_b32 m0, %3\n\ts_mov_b64 exec, 0xff\n\tbuffer_load_dwordx4 %0, %1, %2 offen lds\n\ts_mov_b64 exec, -1" :: "v"(l16), "s"(rb2), "s"(tc * 128), "s"(L_B2) : "memory");
        } else if (w == 1) {
            const v4u rb1 = mkrsrc(b1, 1376 * 2);
            asm volatile("s_mov_b32 m0, %3\n\ts_nop 0\n\tbuffer_load_dword %0, %1, %2 offen lds" :: "v"(lane * 4), "s"(rb1), "s"(u0 * 64), "s"(L_B1) : "memory");
        }
        const u32 pi = opq((hf * 64 + lane) * 2);
        u32 pA = ldp((const GAS u16*)((const GAS char*)(logA + ch * 128) + pi)), pI = ldp((const GAS u16*)((const GAS char*)(Aim + ch * 128) + pi));
        u32 pR = ldp((const GAS u16*)((const GAS char*)(Cre + ch * 128) + pi)), pC = ldp((const GAS u16*)((const GAS char*)(Cim + ch * 128) + pi));
        u32 pT = *(const GAS u16*)(logdt + ch), pD = *(const GAS u16*)(Dskip + ch);
        flg(IC<R_X>{});
        one = opq(1u);
        const u32 aCnt = opq(L_CNT + chl * 4), aCtl = opq(L_C);
        const v4u rus = mkrsrc(ws + OFF_U, 2 << 20);
        v4u selLo, selHi;
#pragma unroll
        for (int e = 0; e < 4; e++) {
            const u32 o1 = (h == (g & 1) && ((l15 & 7) >> 1) == e) ? ((l15 & 1) ? 0x3C000000u : 0x00003C00u) : 0u;
            selLo[e] = l15 < 8 ? o1 : 0u; selHi[e] = l15 < 8 ? 0u : o1;
        }
        pin(selLo); pin(selHi);
        const int m = 64 * hf + lane;
        const u32 aT = opq(w * 32768 + 16384 + l15 * 64 + g * 16), t0 = opq((2 * chl) * 32768 + 16384 + m * 4), kr = opq(L_KB + chl * 8320 + (1016 - 8 * m) * 2);
        u32 tg[6];
        {
            const bool r1 = m < 32, r2 = m < 64;
            tg[0] = opq(t0 + (r1 ? 528 : 0) * 4); tg[1] = opq(t0 + (r2 ? 400 : 0) * 4); tg[2] = opq(t0 + (r1 ? 560 : 272) * 4);
            tg[3] = opq(t0 + (r1 ? 592 : 272) * 4); tg[4] = opq(t0 + (r2 ? 464 : 1) * 4); tg[5] = opq(t0 + (r1 ? 624 : 1) * 4);
        }
        u32 kb[4], ua[4];
#pragma unroll
        for (int i = 0; i < 4; i++) {
            kb[i] = opq(L_KB + chl * 8320 + ((l15 & 1) ? (239 - l15 + 8 * g) * 2 : 4160 + (240 - l15 + 8 * g) * 2) - hf * 256 - 192 + i * 1024);
            const int os = 30 + 2 * l15 + g - 16 * hf - 4 * i;
            ua[i] = opq(L_U + os * 32 + ((chl ^ (os >> 3)) & 1) * 16);
        }
        const u32 aZ3 = opq((lane & 4) ? L_CNT + 4 * C_K + chl * 4 : L_UF + (lane & 3) * 4), want3 = opq((lane & 4) ? 2u : 0u), aZ2 = opq(L_C + (lane & 3) * 4 + ((lane & 8) ? R_LN : (lane & 4) ? R_P + 4 : R_F + 7) * 16);
        const u32 vA = opq((lane >> 3) * 4096 + (lane & 7) * 16);
        pin(pA); pin(pI); pin(pR); pin(pC); pin(pT); pin(pD);

        {
            const u32 aRow = opq(L_KB + (l15 & 7) * 1088 + (l15 >> 3) * 512 + g * 16), aTr = opq(L_KB + (l15 >> 2) * 1088 + (128 * w + 16 * g + 4 * (lane & 3)) * 2);
            v4u ones = {0x3F803F80u, 0x3F803F80u, 0x3F803F80u, 0x3F803F80u}; pin(ones);
            spin_zero4(aF + R_X * 16);
            STAMP(1);
            v4u xb[8]; v2u xt[2][2];
#pragma unroll
            for (int s = 0; s < 8; s++) xb[s] = lds_ld<v4u>(aRow + s * 64);
#pragma unroll
            for (int i = 0; i < 2; i++) { xt[i][0] = lds_tr(aTr + i * 128); xt[i][1] = lds_tr(aTr + i * 128 + 4 * 1088); }
            tag = get_tag();
            f32x4 sm = {0.f, 0.f, 0.f, 0.f}, sq = {0.f, 0.f, 0.f, 0.f};
#pragma unroll
            for (int s = 0; s < 8; s++) { sq = mfma16(xb[s], xb[s], sq); sm = mfma16(ones, xb[s], sm); }
            float q = (l15 & 2) ? ((l15 & 1) ? sq[3] : sq[2]) : ((l15 & 1) ? sq[1] : sq[0]);
            q = g == (l15 >> 2) ? q : 0.f;
            { const auto r = __builtin_amdgcn_permlane32_swap(f2u(q), f2u(q), false, false); q = u2f(r[0]) + u2f(r[1]); }
            { const auto r = __builtin_amdgcn_permlane16_swap(f2u(q), f2u(q), false, false); q = u2f(r[0]) + u2f(r[1]); }
            const float mean = dpp_add<0x128>(sm[0]) * (1.f / 512.f);
            const float var = fmaxf(__builtin_fmaf(-mean, mean, dpp_add<0x128>(q) * (1.f / 512.f)), 0.f);
            const u32 p = pk_bf(mean, __builtin_amdgcn_sqrtf(rbf(rbf(var) + 1e-5f)));
            const float mb = bf_lo(p), rr = __builtin_amdgcn_rcpf(bf_hi(p));
            if (w == 0 && lane < 8) __hip_atomic_store((GAS u64*)(ws + OFF_ST + (P * 16 + j) * 64) + lane, ((u64)((lane & 1) ? (u32)(tag >> 32) : (u32)tag) << 32) | p, RLX_AGENT);
            sfor<2>([&](auto I_) {
                constexpr int i = decltype(I_)::value;
                v4u o;
                sfor<4>([&](auto E_) {
                    constexpr int e = decltype(E_)::value;
                    const u32 xv = xt[i][e >> 1][e & 1];
                    const u32 d = pk_bf(bf_lo(xv) - bcast<2 * e>(mb), bf_hi(xv) - bcast<2 * e + 1>(mb));
                    o[e] = pk_bf(bf_lo(d) * bcast<2 * e>(rr), bf_hi(d) * bcast<2 * e + 1>(rr));
                });
                st_sc1(o, rus, l16, (AX & 2) ? 2u << 20 : tb * 8192 + w * 2048 + i * 1024);
            });
        }
        flg(IC<R_LN>{});
        STAMP(2);
        __builtin_amdgcn_s_setprio(PRIO % 10);
        f32x4 kt[3];
        {
            const float dt = rbf(expf(bf_lo(pT)));
            const float zr = -rbf(expf(bf_lo(pA))) * dt, zi = bf_lo(pI) * dt;
            const float lr = zr * 1.44269504088896341f;
            const float th = zi * 0.159154943091895336f, tl = __builtin_fmaf(zi, 0.159154943091895336f, -th) + zi * 6.4206383e-09f;
            const cmult w1c = mkc(cexp_pow(lr, -th, -tl, 1.f)), qc = mkc(cexp_pow(lr, -th, -tl, 128.f)), q2 = mkc(cexp_pow(lr, -th, -tl, 256.f));
            const cmult w8 = mkc(cexp_pow(lr, th, tl, 8.f)), w32 = mkc(cexp_pow(lr, th, tl, 32.f));
            const u32 aR = pR & 0x7fffu, aI = pC & 0x7fffu;
            int ex = (int)(wave_max(aR > aI ? aR : aI) >> 7); ex = ex < 12 ? 12 : (ex > 240 ? 240 : ex);
            const float sdn = u2f((u32)(262 - ex) << 23), sup = u2f((u32)(ex - 8) << 23);
            auto transpose = [&](const f32x2 (&v)[16], v4u (&f)[4]) {
                f32x16 z; for (int i = 0; i < 16; i++) z[i] = 0.f;
                f32x16 dr = mfma32h((v4u){pk_h2(v[0].x, v[1].x), pk_h2(v[2].x, v[3].x), pk_h2(v[4].x, v[5].x), pk_h2(v[6].x, v[7].x)}, selLo, z);
                f32x16 di = mfma32h((v4u){pk_h2(v[0].y, v[1].y), pk_h2(v[2].y, v[3].y), pk_h2(v[4].y, v[5].y), pk_h2(v[6].y, v[7].y)}, selLo, z);
                dr = mfma32h((v4u){pk_h2(v[8].x, v[9].x), pk_h2(v[10].x, v[11].x), pk_h2(v[12].x, v[13].x), pk_h2(v[14].x, v[15].x)}, selHi, dr);
                di = mfma32h((v4u){pk_h2(v[8].y, v[9].y), pk_h2(v[10].y, v[11].y), pk_h2(v[12].y, v[13].y), pk_h2(v[14].y, v[15].y)}, selHi, di);
#pragma unroll
                for (int e = 0; e < 4; e++) {
                    f[0][e] = pk_h2(dr[2 * e], dr[2 * e + 1]); f[1][e] = pk_h2(dr[8 + 2 * e], dr[9 + 2 * e]);
                    f[2][e] = pk_h2(di[2 * e], di[2 * e + 1]); f[3][e] = pk_h2(di[8 + 2 * e], di[9 + 2 * e]);
                }
            };
            { f32x2 t0_ = w1c.b, t1_ = qc.b, t2_ = q2.b, t3_ = w8.b, t4_ = w32.b; pin(t0_); pin(t1_); pin(t2_); pin(t3_); pin(t4_); }
            STAMP(13);
            wait_vm<0>();
            put_flag<FS1 * 8>(ws + OFF_F1, (w * 256 + tb) * FS1 * 8, l16, tag);
            warm_flag<FS3 * 8>(ws + OFF_F3, (hf * 512 + ch) * FS3 * 8, l16);
            STAMP(14);
            v4u fy[4], fx[4];
            f32x2 v[16], xo[8];
            v[0] = (f32x2){1.f, 0.f}; v[1] = w8.b; v[2] = cmul(w8.b, w8); v[3] = cmul(v[2], w8); v[4] = w32.b;
            xo[0] = (f32x2){bf_lo(pR) * sdn, -bf_lo(pC) * sdn};
#pragma unroll
            for (int i = 5; i < 16; i++) { v[i] = cmul(v[i - 4], w32); if (i < 12) xo[i - 4] = cmul(xo[i - 5], w1c); }
            STAMP(3); CST(2);
            transpose(v, fy);
            const float fin = 2.f * dt * sup;
            auto tile = [&](int t) {
                transpose(v, fx);
                f32x4 d = {0.f, 0.f, 0.f, 0.f};
#pragma unroll
                for (int s = 0; s < 4; s++) d = mfma16h(fy[s], fx[s], d);
                kt[t] = d * fin;
            };
            v[0] = xo[0]; v[1] = cmul(xo[0], qc);
#pragma unroll
            for (int s = 2; s < 16; s++) v[s] = cmul(v[s - 2], q2);
            const f32x2 last = cmul(v[14], q2);
            tile(0);
            v[0] = last; v[1] = xo[4]; v[9] = xo[2]; v[13] = xo[6];
            v[2] = cmul(v[1], qc); v[10] = cmul(v[9], qc); v[14] = cmul(v[13], qc);
#pragma unroll
            for (int s = 3; s < 9; s++) v[s] = cmul(v[s - 2], q2);
            v[11] = cmul(v[9], q2); v[12] = cmul(v[10], q2); v[15] = cmul(v[13], q2);
            const f32x2 last2 = cmul(v[14], q2);
            tile(1);
            v[0] = last2;
#pragma unroll
            for (int i = 0; i < 4; i++) { v[1 + 2 * i] = xo[2 * i + 1]; v[2 + 2 * i] = cmul(xo[2 * i + 1], qc); }
#pragma unroll
            for (int i = 9; i < 16; i++) v[i] = (f32x2){0.f, 0.f};
            tile(2);
        }
        CST(3); STAMP(17);
        pin(kt[0]); pin(kt[1]); pin(kt[2]);
        spin_zero4<1>(aZ2);
        STAMP(11);
        __builtin_amdgcn_s_setprio(WP);
#pragma unroll
        for (int t = 0; t < 3; t++) lds_st<f32x4>(aT + t * 1024, kt[t]);
        sig<C_A>(aCnt, one);
        STAMP(4);
        spin(aCnt + 4 * C_A, 2);
        {
            auto tv = [&](u32 a) { return lds_ld<float>(a) + lds_ld<float>(a + 32768); };
            float k[9];
            k[0] = tv(t0); k[8] = tv(t0 + 4); k[4] = tv(t0 + 272 * 4);
            k[1] = tv(tg[0]); k[2] = tv(tg[1]); k[3] = tv(tg[2]); k[5] = tv(tg[3]); k[6] = tv(tg[4]); k[7] = tv(tg[5]);
            const float va = tv(t0 + 512), vb = tv(t0 + 516);
            if (m == 0) k[0] += bf_lo(pD);
            const u32 k10 = pk_bf(k[1], k[0]);
            lds_st<v4u>(kr + 2048, (v4u){pk_bf(k[7], k[6]), pk_bf(k[5], k[4]), pk_bf(k[3], k[2]), k10});
            lds_st<v4u>(kr + 2048 + 4160, (v4u){pk_bf(k[8], k[7]), pk_bf(k[6], k[5]), pk_bf(k[4], k[3]), pk_bf(k[2], k[1])});
            const u32 pa = pk_bf(va, va), pb = pk_bf(vb, vb), pba = pk_bf(vb, va);
            lds_st<v4u>(kr, (v4u){pb, (m & 1) ? pb : pba, pa, pa});
            lds_st<v4u>(kr + 4160, (v4u){pb, pb, (m & 1) ? pba : pa, pa});
            if (m < 4) lds_st<v4u>(L_KB + chl * 8320 + 4096 + (m & 1) * 16 + (m < 2 ? 4160 : 0), (v4u){m == 0 ? k10 >> 16 : 0u, 0u, 0u, 0u});
        }
        sig<C_K>(aCnt, one);
        STAMP(18);
        f32x4 cv[8];
        {
#pragma unroll
            for (int t = 0; t < 8; t++) cv[t] = (f32x4){0.f, 0.f, 0.f, 0.f};
            v4u A[8], B[8];
            auto rdA = [&](int r, int a) { const u32 p = kb[(7 - a) >> 1] + ((7 - a) & 1) * 512 + (3 - r) * 64; return (v4u){lds_ld<u32>(p), lds_ld<u32>(p + 4), lds_ld<u32>(p + 8), lds_ld<u32>(p + 12)}; };
            auto rdB = [&](int r, int d) { return lds_ld<v4u>(ua[r] + d * 1024); };
            {
                int k = 0;
                while (any_(lds_ldv<u32>(aZ3) != want3) && ++k < (SPIN_CAP << 4)) {}
                asm volatile("" ::: "memory");
            }
            STAMP(6);
#pragma unroll
            for (int i = 0; i < 8; i++) { A[7 - i] = rdA(0, 7 - i); B[i] = rdB(0, i); }
            __builtin_amdgcn_s_setprio(PRIO % 10);
            if (b == 0 && w == 0 && lane == 0) __hip_atomic_store((GAS u64*)ws, tag, RLX_AGENT);
            CST(0);
#pragma unroll
            for (int r = 0; r < 4; r++) {
#pragma unroll
                for (int a = 7; a >= 0; a--) {
#pragma unroll
                    for (int d = 0; d + a < 8; d++) {
                        cv[a + d] = mfma16(A[a], B[d], cv[a + d]);
                        if (a == 0 && r < 3) { SB0(); B[d] = rdB(r + 1, d); SB0(); }
                    }
                    if (r < 3) { SB0(); A[a] = rdA(r + 1, a); SB0(); }
                }
            }
        }
        STAMP(7); CST(1);
        for (int t = 0; t < 8; t++) pin(cv[t]);
        {
            const u32 mine = opq(w * 32768 + 16384 + l16), other = opq((w ^ 1) * 32768 + 16384 + l16);
#pragma unroll
            for (int t = 0; t < 4; t++) lds_st<f32x4>(mine + t * 1024, sel4(hf, cv[t], cv[4 + t]));
            sig<C_E>(aCnt, one);
            sig<C_D>(aCtl + (L_CNT - L_C), one);
            spin(aCnt + 4 * C_E, 2);
            u32 o[8];
#pragma unroll
            for (int t = 0; t < 4; t++) {
                const f32x4 y = sel4(hf, cv[4 + t], cv[t]) + lds_ld<f32x4>(other + t * 1024);
                o[2 * t] = pk_bf(y[0], y[1]); o[2 * t + 1] = pk_bf(y[2], y[3]);
            }
            const v4u ras = mkrsrc(ws + OFF_A, 2 << 20);
            st_sc1((v4u){o[0], o[1], o[2], o[3]}, ras, l16, (AX & 16) ? 2u << 20 : ch * 4096 + hf * 2048);
            st_sc1((v4u){o[4], o[5], o[6], o[7]}, ras, l16, (AX & 16) ? 2u << 20 : ch * 4096 + hf * 2048 + 1024);
            STAMP(15);
            wait_vm<0>();
            STAMP(8);
            put_flag<FS3 * 8>(ws + OFF_F3, (hf * 512 + ch) * FS3 * 8, l16, tag);
            spin(aCtl + (L_CNT - L_C) + 4 * C_D, 4);
            if (F3D) __builtin_amdgcn_s_sleep(F3D);
            poll_flags((const GAS u64*)(ws + OFF_F3) + ((tr >> 4) * 512 + tc * 64 + 16 * w + l15) * FS3, tag, lane < 16);
            STAMP(9);
#pragma unroll
            for (int i = 0; i < 2; i++)
                glds_sc1(ras, vA, (tc * 64 + 16 * w + 8 * i) * 4096 + (tr >> 3) * 1024 + (tr & 3) * 256 + ((tr >> 2) & 1) * 128, L_U + w * 2048 + i * 1024);
            const u32 ta = opq(L_U + w * 2048 + (l15 >> 2) * 128 + g * 32 + (lane & 3) * 8), aw = opq(L_KB + lane * 144 + w * 32);
            wait_vm<0>();
            const v2u a0 = lds_tr(ta), a1 = lds_tr(ta + 512), a2 = lds_tr(ta + 1024), a3 = lds_tr(ta + 1536);
            lds_st<v4u>(aw, (v4u){a0.x, a0.y, a1.x, a1.y});
            lds_st<v4u>(aw + 16, (v4u){a2.x, a2.y, a3.x, a3.y});
            STAMP(10);
        }
        if (SPLITF) { sig<C_FN>(aCtl + (L_CNT - L_C), one); sig<C_FN + 1>(aCtl + (L_CNT - L_C), one); }
        else { wait_lgkm0(); __builtin_amdgcn_s_barrier(); }
    }
}

#if defined(__HIP_DEVICE_COMPILE__) && __HIP_DEVICE_COMPILE__
#pragma clang attribute pop
#endif

extern "C" void kernel_launch(void* const* d_in, const int* in_sizes, int n_in, void* d_out, int out_size, void* d_ws, size_t ws_size,
                              hipStream_t stream) {
    fused_kernel<<<256, 768, 0, stream>>>((const u16*)d_in[0], (const u16*)d_in[1], (const u16*)d_in[2], (const u16*)d_in[3], (const u16*)d_in[4],
                                          (const u16*)d_in[5], (const u16*)d_in[6], (const u16*)d_in[8], (const u16*)d_in[9], (const u16*)d_in[10],
                                          (const u16*)d_in[11], (u16*)d_out, (char*)d_ws + (WSOFF == 1 ? ((ws_size - (16u << 20)) & ~(size_t)0x1fffff) : WSOFF == 2 ? ((ws_size / 2) & ~(size_t)0x1fffff) : 0));
}
```

```cpp
#include <hip/hip_runtime.h>
#include <stdint.h>
#include <utility>

typedef unsigned short u16;
typedef unsigned int u32;
typedef unsigned long long u64;
typedef u32 v4u __attribute__((ext_vector_type(4)));
typedef u32 v2u __attribute__((ext_vector_type(2)));
typedef float f32x2 __attribute__((ext_vector_type(2)));
typedef float f32x4 __attribute__((ext_vector_type(4)));
typedef float f32x16 __attribute__((ext_vector_type(16)));
typedef __bf16 bf16x8 __attribute__((ext_vector_type(8)));
typedef __bf16 bf16x2 __attribute__((ext_vector_type(2)));
typedef _Float16 f16x8 __attribute__((ext_vector_type(8)));
typedef _Float16 f16x2 __attribute__((ext_vector_type(2)));
typedef short s16x4 __attribute__((ext_vector_type(4)));
typedef u64 u64x8 __attribute__((ext_vector_type(8)));
typedef u64 u64x4 __attribute__((ext_vector_type(4)));
#define GAS __attribute__((address_space(1)))
#define LAS __attribute__((address_space(3)))
#define DI __device__ __forceinline__
#define RLX_AGENT __ATOMIC_RELAXED, __HIP_MEMORY_SCOPE_AGENT
#define RLX_WG __ATOMIC_RELAXED, __HIP_MEMORY_SCOPE_WORKGROUP
#define SB0() __builtin_amdgcn_sched_barrier(0)
#define CHST 0
#define PSLEEP 2
#define ROT 1
#define POL 1
#define PDS 0
#define THR 0
#define HS 0
#define AB 0
#ifndef NV2
#define NV2 2
#endif
#define WING 10
#define HROT 0
#define GP2 0
#define PFB 30720
#define PF 1
#define FULLF 0
#define WSOFF 0
#define PACE 0
#define PACEV 9
#define F3D 30
#define WARM 1
#define W0G 8
#define W0L 8
#define EX 0
#define MPF 0
#define P7AT 5
#define AX 0
#define SKIP3 1
#define NTX 2
#define EARLY 0
#define THR2 6
#define WINL 8
#define FS1V 2
#define NBD 3333
#define UE 0
#define LP 0
#define LP2 0
#define SPLITF 0
#define OUTP 2
#define WP 1
#define NE 3
#define PRIO 21

#define NOH_OK
#define NWV 12
constexpr int OFF_ST = 16384, OFF_F1 = 128 << 10, OFF_F2 = 256 << 10, OFF_F3 = 512 << 10;
constexpr int FS1 = FS1V, FS2 = 16, FS3 = 16;
constexpr int OFF_U = 1 << 20, OFF_A = 4 << 20, OFF_H = 8 << 20;
constexpr int H_BYTES = 64 * 43 * 2048;
constexpr int L_C = 131072;
constexpr int L_FLG = L_C, L_UF = L_C + 320, L_E = L_C + 768, L_B2 = L_C + 896, L_LNS = L_C + 352, L_CNT = L_C + 416, L_B1 = L_C + 512, L_STAT = L_C + 1024, L_CSP = L_C + 29696;
enum { R_F = 0, R_P = 8, R_P7 = 13, R_PK = 15, R_LN = 19, R_X = 14, R_V1 = 16, R_V2 = 17, R_V3 = 18 };
constexpr int L_U = L_C + 3072, L_KB = L_C + 12288;
constexpr int SPIN_CAP = 1 << 16;
enum { C_A = 0, C_K = 2, C_E = 4, C_FN = 6, C_D = 8, C_RD = 12 };

#define STAMP(i)
#define CST(i)
#define TIMED(x) x

#if defined(__HIP_DEVICE_COMPILE__) && __HIP_DEVICE_COMPILE__
#pragma clang attribute push(__attribute__((target("no-packed-fp32-ops"))), apply_to = function)
#endif
DI float u2f(u32 v) { return __builtin_bit_cast(float, v); }
DI u32 f2u(float v) { return __builtin_bit_cast(u32, v); }
DI bool any_(bool c) { return __builtin_amdgcn_ballot_w64(c) != 0; }
DI bool all_(bool c) { return __builtin_amdgcn_ballot_w64(!c) == 0; }
template <typename T> DI T lds_ld(u32 a) { return *(LAS T*)(size_t)a; }
template <typename T> DI void lds_st(u32 a, T v) { *(LAS T*)(size_t)a = v; }
DI v2u lds_tr(u32 a) { return __builtin_bit_cast(v2u, __builtin_amdgcn_ds_read_tr16_b64_v4i16((LAS s16x4*)(size_t)a)); }
DI v4u mkrsrc(const void* p, u32 bytes) { u64 a = (u64)p; v4u r; r.x = (u32)a; r.y = (u32)(a >> 32) & 0xffffu; r.z = bytes; r.w = 0x00020000u; return r; }
DI void glds(v4u rsrc, u32 voff, u32 soff, u32 lds) {
    asm volatile("s_mov_b32 m0, %3\n\ts_nop 0\n\tbuffer_load_dwordx4 %0, %1, %2 offen lds" :: "v"(voff), "s"(rsrc), "s"(soff), "s"(lds) : "memory");
}
template <int LOFF, int NT = 0> DI u32 glds_first(v4u rsrc, u32 voff, u32 sa, u32 sb, u32 lbase) {
    u32 so;
    if constexpr (NT) asm volatile("s_add_u32 m0, %5, %6\n\ts_add_u32 %0, %3, %4\n\tbuffer_load_dwordx4 %1, %2, %0 offen nt lds" : "=&s"(so) : "v"(voff), "s"(rsrc), "s"(sa), "s"(sb), "s"(lbase), "i"(LOFF) : "memory", "scc"); else
    asm volatile("s_add_u32 m0, %5, %6\n\ts_add_u32 %0, %3, %4\n\tbuffer_load_dwordx4 %1, %2, %0 offen lds" : "=&s"(so) : "v"(voff), "s"(rsrc), "s"(sa), "s"(sb), "s"(lbase), "i"(LOFF) : "memory", "scc");
    return so;
}
template <int LOFF, int SOFF> DI void glds_imm(v4u rsrc, u32 voff, u32 sa, u32 lbase) {
    u32 so;
    asm volatile("s_add_u32 m0, %4, %5\n\ts_add_u32 %0, %3, %6\n\tbuffer_load_dwordx4 %1, %2, %0 offen lds" : "=&s"(so) : "v"(voff), "s"(rsrc), "s"(sa), "s"(lbase), "i"(LOFF), "i"(SOFF) : "memory", "scc");
}
template <int OFF, int NT = 0> DI void glds_next(v4u rsrc, u32 voff, u32 so) {
    if constexpr (NT) asm volatile("buffer_load_dwordx4 %0, %1, %2 offen offset:%3 nt lds" :: "v"(voff), "s"(rsrc), "s"(so), "i"(OFF) : "memory"); else
    asm volatile("buffer_load_dwordx4 %0, %1, %2 offen offset:%3 lds" :: "v"(voff), "s"(rsrc), "s"(so), "i"(OFF) : "memory");
}
template <int LOFF> DI void flag_imm(v4u rsrc, u32 lbase) {
    asm volatile("s_add_u32 m0, %2, %3\n\ts_mov_b64 exec, 1\n\tbuffer_load_dword off, %0, %1 lds\n\ts_mov_b64 exec, -1" :: "s"(rsrc), "s"(rsrc.z), "s"(lbase), "i"(LOFF) : "memory", "scc");
}
DI void glds_nt(v4u rsrc, u32 voff, u32 soff, u32 lds) {
    asm volatile("s_mov_b32 m0, %3\n\ts_nop 0\n\tbuffer_load_dwordx4 %0, %1, %2 offen nt lds" :: "v"(voff), "s"(rsrc), "s"(soff), "s"(lds) : "memory");
}
DI void glds_sc1(v4u rsrc, u32 voff, u32 soff, u32 lds) {
    asm volatile("s_mov_b32 m0, %3\n\ts_nop 0\n\tbuffer_load_dwordx4 %0, %1, %2 offen sc1 lds" :: "v"(voff), "s"(rsrc), "s"(soff), "s"(lds) : "memory");
}
DI void flag_dma(v4u rsrc, u32 lds) {
    asm volatile("s_mov_b32 m0, %2\n\ts_mov_b64 exec, 1\n\tbuffer_load_dword off, %0, %1 lds\n\ts_mov_b64 exec, -1" :: "s"(rsrc), "s"(rsrc.z), "s"(lds) : "memory");
}
DI void st_sc1(v4u v, v4u rsrc, u32 voff, u32 soff) {
    asm volatile("buffer_store_dwordx4 %0, %1, %2, %3 offen sc1\n\ts_nop 1" :: "v"(v), "v"(voff), "s"(rsrc), "s"(soff) : "memory");
}
template <int BYTES> DI void put_flag(const char* base, u32 off, u32 l16, u64 tag) {
    asm volatile("s_mov_b64 exec, 1\n\tglobal_store_dwordx2 %0, %1, %2 sc1\n\ts_mov_b64 exec, -1" :: "v"(l16), "v"(tag), "s"(base + off) : "memory");
}
template <int BYTES> DI void warm_flag(const char* base, u32 off, u32 l16) {
    const v4u v = {0u, 0u, 0u, 0u};
    asm volatile("s_mov_b64 exec, %3\n\tglobal_store_dwordx4 %0, %1, %2 sc1\n\ts_mov_b64 exec, -1\n\ts_nop 0" :: "v"(l16), "v"(v), "s"(base + off), "i"((1 << (BYTES / 16)) - 1) : "memory");
}
DI u32 ldp(const GAS u16* p) { return (NTX & 2) ? __builtin_nontemporal_load(p) : *p; }
DI u32 opq(u32 a) { asm volatile("" : "+v"(a)); return a; }
template <typename T> DI T lds_ldv(u32 a) { return *(volatile LAS T*)(size_t)a; }
template <int SLEEP = 0> DI void spin_zero4(u32 a) {
    int n = 0;
    while (any_(lds_ldv<u32>(a) != 0) && ++n < (SPIN_CAP << 4)) { if (SLEEP) __builtin_amdgcn_s_sleep(1); }
    asm volatile("" ::: "memory");
}
template <int N> DI void wait_vm() { asm volatile("s_waitcnt vmcnt(%0)" :: "i"(N) : "memory"); }
DI void wait_lgkm0() { asm volatile("s_waitcnt lgkmcnt(0)" ::: "memory"); }
DI u64 mix64(u64 e) { e += 0x9E3779B97F4A7C15ull; e ^= e >> 31; e *= 0xBF58476D1CE4E5B9ull; e ^= e >> 29; return e; }
DI u64 get_tag() { const v2u e = lds_ldv<v2u>(L_E); return mix64(((u64)(u32)__builtin_amdgcn_readfirstlane(e.y) << 32) | (u32)__builtin_amdgcn_readfirstlane(e.x)); }
template <int C> DI void sig(u32 a, u32 one) { asm volatile("s_mov_b64 exec, 1\n\tds_add_u32 %0, %1 offset:%2\n\ts_mov_b64 exec, -1" :: "v"(a), "v"(one), "i"(4 * C) : "memory"); }
template <int OFF> DI void clr(u32 a, u32 zero) { asm volatile("s_mov_b64 exec, 1\n\tds_write_b32 %0, %1 offset:%2\n\ts_mov_b64 exec, -1" :: "v"(a), "v"(zero), "i"(OFF) : "memory"); }
DI void spin(u32 a, u32 target) {
    int n = 0;
    while ((u32)__builtin_amdgcn_readfirstlane(lds_ldv<u32>(a)) < target && ++n < (SPIN_CAP << 4)) {}
    asm volatile("" ::: "memory");
}
DI u64 peek_flags(const GAS u64* p, u64 tag, bool on) { u64 f = tag; if (on) f = __hip_atomic_load(p, RLX_AGENT); return f; }
DI void poll_flags(const GAS u64* p, u64 tag, bool on) {
    int n = 0;
    while (true) {
        u64 f = peek_flags(p, tag, on);
        if (all_(f == tag) || ++n > SPIN_CAP) break;
        __builtin_amdgcn_s_sleep(PSLEEP);
    }
    asm volatile("" ::: "memory");
}
DI float bf_lo(u32 v) { return u2f(v << 16); }
DI float bf_hi(u32 v) { return u2f(v & 0xffff0000u); }
DI u32 pk_bf(float a, float b) { f32x2 v = {a, b}; return __builtin_bit_cast(u32, __builtin_convertvector(v, bf16x2)); }
typedef short s16x2 __attribute__((ext_vector_type(2)));
DI u32 relu_pk(u32 v) { return __builtin_bit_cast(u32, __builtin_elementwise_max(__builtin_bit_cast(s16x2, v), (s16x2){0, 0})); }
DI u32 umax(u32 a, u32 b) { return a > b ? a : b; }
DI float rbf(float a) { return bf_lo(pk_bf(a, a)); }
DI u32 pk_h(f32x2 v) { return __builtin_bit_cast(u32, __builtin_convertvector(v, f16x2)); }
DI f32x16 mfma32(v4u a, v4u b, f32x16 c) { return __builtin_amdgcn_mfma_f32_32x32x16_bf16(__builtin_bit_cast(bf16x8, a), __builtin_bit_cast(bf16x8, b), c, 0, 0, 0); }
DI f32x4 mfma16(v4u a, v4u b, f32x4 c) { return __builtin_amdgcn_mfma_f32_16x16x32_bf16(__builtin_bit_cast(bf16x8, a), __builtin_bit_cast(bf16x8, b), c, 0, 0, 0); }
DI f32x4 mfma16h(v4u a, v4u b, f32x4 c) { return __builtin_amdgcn_mfma_f32_16x16x32_f16(__builtin_bit_cast(f16x8, a), __builtin_bit_cast(f16x8, b), c, 0, 0, 0); }
template <int CTRL> DI float dpp_add(float v) { return v + __builtin_bit_cast(float, __builtin_amdgcn_update_dpp(0, __builtin_bit_cast(int, v), CTRL, 0xf, 0xf, true)); }
template <int CTRL> DI u32 dpp_max(u32 v) { const u32 o = (u32)__builtin_amdgcn_update_dpp(0, (int)v, CTRL, 0xf, 0xf, true); return v > o ? v : o; }
template <int T> DI float bcast(float v) { return __builtin_bit_cast(float, __builtin_amdgcn_update_dpp(0, __builtin_bit_cast(int, v), 0x150 + T, 0xf, 0xf, true)); }
DI float rl(float v, int l) { return __builtin_bit_cast(float, __builtin_amdgcn_readlane(__builtin_bit_cast(int, v), l)); }
DI f32x2 wave_sum2(f32x2 v) {
    float a = v.x, c = v.y;
    a = dpp_add<0xB1>(a); c = dpp_add<0xB1>(c); a = dpp_add<0x4E>(a); c = dpp_add<0x4E>(c);
    a = dpp_add<0x141>(a); c = dpp_add<0x141>(c); a = dpp_add<0x140>(a); c = dpp_add<0x140>(c);
    return ((f32x2){rl(a, 0), rl(c, 0)} + (f32x2){rl(a, 16), rl(c, 16)}) + ((f32x2){rl(a, 32), rl(c, 32)} + (f32x2){rl(a, 48), rl(c, 48)});
}
DI u32 wave_max(u32 v) {
    v = dpp_max<0xB1>(v); v = dpp_max<0x4E>(v); v = dpp_max<0x141>(v); v = dpp_max<0x140>(v);
    const u32 a = __builtin_amdgcn_readlane(v, 0), c = __builtin_amdgcn_readlane(v, 16), d = __builtin_amdgcn_readlane(v, 32), e = __builtin_amdgcn_readlane(v, 48);
    const u32 p = a > c ? a : c, q = d > e ? d : e;
    return p > q ? p : q;
}
struct cmult { f32x2 b, n; };
DI cmult mkc(f32x2 b) { cmult c; c.b = b; c.n = (f32x2){-b.y, b.x}; return c; }
DI f32x2 cmul(f32x2 a, const cmult& c) { return __builtin_elementwise_fma((f32x2){a.x, a.x}, c.b, (f32x2){a.y, a.y} * c.n); }
DI f32x16 mfma32h(v4u a, v4u b, f32x16 c) { return __builtin_amdgcn_mfma_f32_32x32x16_f16(__builtin_bit_cast(f16x8, a), __builtin_bit_cast(f16x8, b), c, 0, 0, 0); }
DI u32 pk_h2(float a, float b) { return pk_h((f32x2){a, b}); }
DI f32x2 cexp_pow(float lr, float th, float tl, float e) {
    float a = __builtin_amdgcn_fractf(th * e) + tl * e;
    float m = __builtin_amdgcn_exp2f(lr * e);
    f32x2 r; r.x = m * __builtin_amdgcn_cosf(a); r.y = m * __builtin_amdgcn_sinf(a); return r;
}
DI f32x4 sel4(bool c, f32x4 a, f32x4 b) { return c ? a : b; }
constexpr int w2loc(int n) { return n < 16 ? n : n < 28 ? n + 4 : n - 28; }
constexpr int nbs(int k) { return k <= 0 ? 0 : nbs(k - 1) + (k == 1 ? NBD / 1000 : k == 2 ? NBD / 100 % 10 : k == 3 ? NBD / 10 % 10 : NBD % 10); }
template <class F, int... I> DI void sfor_(F&& f, std::integer_sequence<int, I...>) { (f(std::integral_constant<int, I>{}), ...); }
template <int N, class F> DI void sfor(F&& f) { sfor_(f, std::make_integer_sequence<int, N>{}); }
template <int I> using IC = std::integral_constant<int, I>;
template <typename T> DI void pin(T& v) { asm volatile("" : "+v"(v)); }

__global__ void __launch_bounds__(768, 3)
fused_kernel(const u16* x_, const u16* logA_, const u16* Aim_, const u16* Cre_, const u16* Cim_, const u16* logdt_, const u16* Dskip_, const u16* w1_,
             const u16* b1_, const u16* w2_, const u16* b2_, u16* out_, char* ws_) {
    u64x8 ka; u64x4 kb4; u64 kc;
    asm volatile("s_load_dwordx16 %0, %3, 0x0\n\ts_load_dwordx8 %1, %3, 0x40\n\ts_load_dwordx2 %2, %3, 0x60" : "=&s"(ka), "=&s"(kb4), "=&s"(kc) : "s"(__builtin_amdgcn_kernarg_segment_ptr()) : "memory");
    __shared__ __attribute__((aligned(16))) char smem[163840];
    asm volatile("" :: "v"((u32)(size_t)(LAS char*)smem));
    const int tid = threadIdx.x, lane = tid & 63, wave = __builtin_amdgcn_readfirstlane(tid >> 6) & 15, w = wave & 3, role = wave >> 2;
    const int b = blockIdx.x & 255, xcd = b & 7, slot = b >> 3;
    const int P = 4 * (xcd >> 1) + (slot >> 3), j = 8 * (xcd & 1) + (slot & 7);
    const int tr = 4 * xcd + (slot >> 3), tc = slot & 7;
    const int u0 = (j * 43) >> 4;
    if ((PF & 1) && wave == 4) {
        u64 pc; asm volatile("s_getpc_b64 %0" : "=s"(pc));
        const v4u rc = mkrsrc((const void*)(pc & ~255ull), PFB);
        glds(rc, lane * 16, slot * 1024, L_C + 2048);
    }
    const int h = lane >> 5, l31 = lane & 31, l15 = lane & 15, g = lane >> 4;
    const u32 l16 = lane * 16, lw = w * 32768, w4 = w * 4;
    const int rho = HROT ? ((slot >> 3) + slot) & 3 : (slot >> 3);
    const int tb = 64 * (P >> 2) + 32 * (j & 1) + 16 * ((P >> 1) & 1) + 2 * (j >> 1) + (P & 1);
    const int cp = 32 * xcd + slot, chl = w >> 1, hf = w & 1, ch = 2 * cp + chl;
    {
        const u32 v = (tid >= 192 && tid < 256) || (tid >= 282 && tid < 286) ? 0u : ~0u;
        if (tid < 288) lds_st<v4u>((tid < 192 ? L_CSP : tid < 256 ? L_U - 192 * 16 : L_C - 256 * 16) + tid * 16, (v4u){v, v, v, v});
    }
    const int rot = ROT == 0 ? 0 : ROT == 1 ? (((slot >> 3) + slot) & 1) * 4 : ROT == 2 ? (((slot >> 3) + slot) & 3) * 2 : (2 * (slot >> 3) + (slot & 7)) & 7;
    u32 sx = (512 * (P >> 2) + 128 * ((P >> 1) & 1) + 64 * (w & 1) + 8 * (P & 1) + 4 * (w >> 1)) * 1024, sw1 = 16 * w * 2752 + u0 * 64;
    u32 sx0 = rot * 128, sw0 = rot * (64 * 2752);
    u32 vx[4], vw[3];
    const int lim3 = 4 * ((((j + 1) * 43) >> 4) - u0);
#pragma unroll
    for (int i = 0; i < 3; i++) { const int p = 64 * i + lane, r = (p * 171) >> 11; vw[i] = opq((SKIP3 && p - 12 * r >= lim3) ? 0x40000000u : r * (2752 - 192) + l16); }
#pragma unroll
    for (int i = 0; i < 4; i++) vx[i] = opq((((h * 256 + ((lane >> 3) & 3)) * 1024 + ((lane & 7) ^ g) * 16) ^ ((i & 1) * 64)) + i * (16384 - 1024));
    asm volatile("s_waitcnt lgkmcnt(0)\n\ts_barrier" : "+s"(ka), "+s"(kb4), "+s"(kc), "+s"(sx), "+s"(sw1), "+s"(sx0), "+s"(sw0) :: "memory");
    const u16 *x = (const u16*)ka[0], *logA = (const u16*)ka[1], *Aim = (const u16*)ka[2], *Cre = (const u16*)ka[3], *Cim = (const u16*)ka[4], *logdt = (const u16*)ka[5], *Dskip = (const u16*)ka[6], *w1 = (const u16*)ka[7];
    const u16 *b1 = (const u16*)kb4[0], *w2 = (const u16*)kb4[1], *b2 = (const u16*)kb4[2]; u16* out = (u16*)kb4[3]; char* ws = (char*)kc;
    if (CHST == 3) STAMP(17);
    const v4u rx = mkrsrc(x, 2 << 20);
    const u32 aF = opq(L_FLG + (lane & 3) * 4);
    auto flg = [&](auto R_) { flag_imm<L_FLG + decltype(R_)::value * 16>(rx, w4); };
    const v4u rw1 = mkrsrc(w1, 512 * 1376 * 2);
    auto ldX = [&](auto C_) { constexpr int c = decltype(C_)::value;
        const u32 so = glds_first<(16 + (c % 4) * 4) * 1024, NTX & 1>(rx, vx[0], sx, c ? ((c + rot) & 7) * 128 : sx0, lw);
        glds_next<1024, NTX & 1>(rx, vx[1], so); glds_next<2048, NTX & 1>(rx, vx[2], so); glds_next<3072, NTX & 1>(rx, vx[3], so); };
    auto ldW = [&](auto C_) { constexpr int c = decltype(C_)::value;
        const u32 so = glds_first<3 * (c % 5) * 1024, (NTX >> 2) & 1>(rw1, vw[0], sw1, c ? ((c + rot) & 7) * (64 * 2752) : sw0, lw);
        glds_next<1024, (NTX >> 2) & 1>(rw1, vw[1], so); if (!(AX & 4)) glds_next<2048, (NTX >> 2) & 1>(rw1, vw[2], so); };
    if (role == 0) {
        if (CHST == 3) STAMP(18);
        ldW(IC<0>{}); ldX(IC<0>{}); flg(IC<R_F>{});
        ldW(IC<1>{}); ldX(IC<1>{}); flg(IC<R_F + 1>{});
        if (CHST == 3) { STAMP(19); wait_vm<15>(); STAMP(20); }
    }
    asm volatile("" ::: "memory");
    u32 aV0, aV1, aFw, aCn, one, f2o;
    u32 sh2[4], sh2t[4];
    auto pre2 = [&]() {
        aV0 = opq(lw + (4 * h + (l15 >> 2)) * 128 + ((l15 >> 3) & 1) * 64 + (g & 1) * 32 + (lane & 3) * 8); aV1 = opq(aV0 ^ 64);
        aFw = opq(L_FLG + w4); aCn = opq(L_CNT + w4); one = opq(1u);
        const int jj = 4 * w + (lane < 4 ? lane : 4);
        f2o = opq(((2 * tr + role) * 16 + (jj < 15 ? jj : 15)) * FS2 * 8);
#pragma unroll
        for (int k = 0; k < 4; k++) {
            sh2[k] = ((2 * tr + role) * 43 + 11 * w) * 2048 + (6 * rho - (k + rho >= 4 ? 22 : 0)) * 1024; sh2t[k] = (w == 3 && k + rho == 3) ? (u32)H_BYTES : sh2[k];
            asm volatile("" : "+s"(sh2[k]), "+s"(sh2t[k]));
        }
    };

    auto rdV = [&](int s, int dt) { v2u lo = lds_tr((dt ? aV1 : aV0) + w2loc(2 * s) * 1024), hi = lds_tr((dt ? aV1 : aV0) + w2loc(2 * s + 1) * 1024); return (v4u){lo.x, lo.y, hi.x, hi.y}; };
    auto gemm2 = [&](auto ROLE_, v4u (&stage)[16], u64 f2, u64 tag) __attribute__((always_inline)) {
        constexpr int role = decltype(ROLE_)::value;
        const GAS u64* f2p = (const GAS u64*)(ws + OFF_F2 + f2o);
        __builtin_amdgcn_s_setprio(role ? GP2 : LP2);
        const __amdgpu_buffer_rsrc_t rh = __builtin_amdgcn_make_buffer_rsrc(ws + OFF_H, 0, (AX & 128) ? 0 : H_BYTES, 0x00020000);
        constexpr int WIN = role ? WING : WINL;
        v4u park[8][2], hq[WIN], vb[3][2];
        auto ldH = [&](int s) { hq[s % WIN] = __builtin_amdgcn_raw_buffer_load_b128(rh, l16, ((s + 2) % 6 >= 4 ? sh2t[(s + 2) / 6] : sh2[(s + 2) / 6]) + s * 1024, 16); };
        constexpr int W0 = role ? W0G : W0L;
        if (role) {
            SB0();
            spin_zero4<1>(aFw + R_V1 * 16);
            STAMP(9);
            f2 = tag;
            asm volatile("s_mov_b64 exec, 31\n\tglobal_load_dwordx2 %0, %1, %2 sc1\n\ts_mov_b64 exec, -1" : "+v"(f2) : "v"(f2o), "s"(ws + OFF_F2) : "memory");
            SB0();
#pragma unroll
            for (int s = 0; s < 8; s++) { park[s][0] = rdV(s, 0); park[s][1] = rdV(s, 1); }
            clr<R_PK * 16>(aFw, opq(0u));
            asm volatile("s_waitcnt vmcnt(0)" : "+v"(f2) :: "memory");
            SB0();
        }
        STAMP(10);
        if (!all_(f2 == tag)) poll_flags(f2p, tag, lane < 5);
        STAMP(5);
#pragma unroll
        for (int s = 0; s < W0; s++) ldH(s);
        const int q3 = (lane >> 2) & 7, fr = 8 * wave + 4 * h + 2 * ((q3 ^ (q3 >> 1) ^ (q3 >> 2)) & 1) + (q3 >> 2), fo = ((q3 >> 1) & 1) * 4 + (lane & 3);
        const u32 fin_off = (512 * (tr >> 3) + 256 * ((fr >> 2) & 1) + 128 * ((tr >> 2) & 1) + 16 * (fr >> 3) + 4 * (tr & 3) + (fr & 3)) * 512 + tc * 64 + fo * 8;
        f32x4 s0, s1; float rs2; u32 da, ra, rb, rc, aFn;
        v4u fin_x, fin_b; u32 fin_s, fv;
        f32x16 ac2[2];
#pragma unroll
        for (int i = 0; i < 16; i++) { ac2[0][i] = 0.f; ac2[1][i] = 0.f; }
        const u32 aSt = opq(lw + l16), zero = opq(0u);
        if (!role) {
            SB0();
            spin_zero4(opq(aFw + ((lane & 1) ? R_PK : R_V2) * 16));
            STAMP(9);
            vb[0][0] = rdV(0, 0); vb[0][1] = rdV(0, 1); vb[1][0] = rdV(1, 0); vb[1][1] = rdV(1, 1);
            asm volatile("" ::: "memory");
#pragma unroll
            for (int k = 0; k < 4; k++) lds_st<v4u>(aSt + k * 1024, stage[k]);
        }
#pragma unroll
        for (int s = 0; s < 22; s++) {
            SB0();
            if (role && (s == 4 || s == 10)) fv = lds_ldv<u32>(aFw + (s == 4 ? R_V2 : R_V3) * 16);
            if (role && (s == 6 || s == 12)) { if (__builtin_expect(any_(fv != 0), 0)) spin_zero4(aFw + (s == 6 ? R_V2 : R_V3) * 16); asm volatile("" ::: "memory"); }
            if ((!role || s + 2 >= 8) && s + 2 < 22) { vb[(s + 2) % 3][0] = rdV(s + 2, 0); vb[(s + 2) % 3][1] = rdV(s + 2, 1); }
            if (s == 19) sig<C_RD>(aCn, one);
            if (s == 21) fv = lds_ldv<u32>(aCn + 4 * C_RD);
            ac2[0] = mfma32(role && s < 8 ? park[s & 7][0] : vb[s % 3][0], hq[s % WIN], ac2[0]);
            if (AX & 256) { pin(vb[s % 3][1]); if (role && s < 8) pin(park[s & 7][1]); } else
            ac2[1] = mfma32(role && s < 8 ? park[s & 7][1] : vb[s % 3][1], hq[s % WIN], ac2[1]);
            SB0();
            if (!role && s < 6) {
                asm volatile("" ::: "memory");
                lds_st<v4u>(aSt + (2 * s + 4) * 1024, stage[(2 * s + 4) & 15]); lds_st<v4u>(aSt + (2 * s + 5) * 1024, stage[(2 * s + 5) & 15]);
                if (s == 5) { clr<R_V3 * 16>(aFw, zero); STAMP(13); }
            }
            if (s < WIN - W0) { ldH(W0 + 2 * s); ldH(W0 + 2 * s + 1); }
            else if (s + WIN < 22) ldH(s + WIN);
            if (s == 12) {
                fin_x = *(const GAS v4u*)(x + fin_off); fin_b = lds_ld<v4u>(L_B2 + fo * 16);
                fin_s = __hip_atomic_load((GAS u32*)(ws + OFF_ST + (tr >> 1) * 1024 + (fr >> 2) * 64 + (tr & 1) * 32 + (fr & 3) * 8), RLX_AGENT);
            }
            if (s == 16) { da = opq(lw + role * 20480 + l31 * 272 + h * 16); ra = opq(role * 20480 + (fr & 31) * 272 + fo * 32); rb = opq(ra + 65536); rc = opq(L_KB + fr * 144 + fo * 16); aFn = opq(L_CNT + 4 * (C_FN + role)); }
            if (s == 21) {
                s0 = (f32x4){bf_lo(fin_x.x) + bf_lo(fin_b.x), bf_hi(fin_x.x) + bf_hi(fin_b.x), bf_lo(fin_x.y) + bf_lo(fin_b.y), bf_hi(fin_x.y) + bf_hi(fin_b.y)};
                pin(s0);
            }
        }
        SB0();
        STAMP(7); CST(3);
        if (__builtin_expect(any_(fv < 2), 0)) spin(aCn + 4 * C_RD, 2);
        asm volatile("" ::: "memory");
#pragma unroll
        for (int dt = 0; dt < 2; dt++)
#pragma unroll
            for (int q = 0; q < 4; q++)
                lds_st<f32x4>(da + (32 * dt + 8 * q) * 4, (f32x4){ac2[dt][4 * q], ac2[dt][4 * q + 1], ac2[dt][4 * q + 2], ac2[dt][4 * q + 3]});
        if (SPLITF) sig<0>(aFn, one);
        SB0();
        s1 = (f32x4){bf_lo(fin_x.z) + bf_lo(fin_b.z), bf_hi(fin_x.z) + bf_hi(fin_b.z), bf_lo(fin_x.w) + bf_lo(fin_b.w), bf_hi(fin_x.w) + bf_hi(fin_b.w)};
        rs2 = __builtin_amdgcn_rcpf(bf_hi(fin_s)); rs2 *= rs2;
        asm volatile("" : "+v"(s0), "+v"(s1), "+v"(rs2));
        if (SPLITF) spin(aFn, 8);
        else { wait_lgkm0(); __builtin_amdgcn_s_barrier(); asm volatile("" ::: "memory"); }
        STAMP(11);
        const v4u av = lds_ld<v4u>(rc);
        s0 += ((lds_ld<f32x4>(ra) + lds_ld<f32x4>(ra + 32768)) + (lds_ld<f32x4>(rb) + lds_ld<f32x4>(rb + 32768))) * rs2;
        s1 += ((lds_ld<f32x4>(ra + 16) + lds_ld<f32x4>(ra + 32768 + 16)) + (lds_ld<f32x4>(rb + 16) + lds_ld<f32x4>(rb + 32768 + 16))) * rs2;
        s0 += (f32x4){bf_lo(av.x), bf_hi(av.x), bf_lo(av.y), bf_hi(av.y)};
        s1 += (f32x4){bf_lo(av.z), bf_hi(av.z), bf_lo(av.w), bf_hi(av.w)};
        const v4u ov = {pk_bf(s0[0], s0[1]), pk_bf(s0[2], s0[3]), pk_bf(s1[0], s1[1]), pk_bf(s1[2], s1[3])};
        if (OUTP == 0) *(GAS v4u*)(out + fin_off) = ov;
        else if (OUTP == 1) __builtin_nontemporal_store(ov, (GAS v4u*)(out + fin_off));
        else if (OUTP == 2) st_sc1(ov, mkrsrc(out, 2 << 20), fin_off * 2, 0);
        else if (OUTP == 3) asm volatile("global_store_dwordx4 %0, %1, %2 sc0 sc1\n\ts_nop 1" :: "v"(fin_off * 2), "v"(ov), "s"(out) : "memory");
        else if (OUTP == 4) asm volatile("global_store_dwordx4 %0, %1, %2 sc1 nt\n\ts_nop 1" :: "v"(fin_off * 2), "v"(ov), "s"(out) : "memory");
        else asm volatile("global_store_dwordx4 %0, %1, %2 sc0 sc1 nt\n\ts_nop 1" :: "v"(fin_off * 2), "v"(ov), "s"(out) : "memory");
    };

    if (__builtin_expect(role == 0, 1)) {
        v4u stage[16]; u64 f2, tag;
        const u32 sw2 = 352 * w * 1024 + tc * 128;
        const u32 vv = (lane >> 3) * 1024 + ((lane & 7) ^ ((g & 1) << 2)) * 16;
        u32 sv[4];
#pragma unroll
        for (int k = 0; k < 4; k++) sv[k] = sw2 + (12 * rho - (k + rho >= 4 ? 44 : 0)) * 8192;
        const __amdgpu_buffer_rsrc_t rv = __builtin_amdgcn_make_buffer_rsrc((void*)w2, 0, (AX & 64) ? 0 : 512 * 1376 * 2, 0x00020000);
        auto ldV = [&](int r) { stage[r] = __builtin_amdgcn_raw_buffer_load_b128(rv, vv, sv[(r + 32) / 12] + (r + 28) * 8192, 0); };
        const GAS u64* f1p = (const GAS u64*)(ws + OFF_F1) + ((xcd >> 1) * 256 + 64 * w + lane) * FS1;
        u64 f1; bool got = false;
        auto look = [&]() {
            spin_zero4<1>(aF + R_X * 16);
            tag = get_tag();
            asm volatile("global_load_dwordx2 %0, %1, off sc1" : "=&v"(f1) : "v"(f1p) : "memory");
        };
        auto gather = [&]() {
            const v4u rus = mkrsrc(ws + OFF_U, 2 << 20);
            const u32 vU = (lane >> 1) * 8192 + ((lane ^ g) & 1) * 16;
#pragma unroll
            for (int i = 0; i < 2; i++) glds_sc1(rus, vU, (64 * w + 32 * i) * 8192 + cp * 32, L_U + 1024 + w * 2048 + i * 1024);
            flag_dma(rus, L_UF + w * 4);
        };
        sfor<6>([&](auto I) {
            constexpr int c = decltype(I)::value + 2;
            if constexpr (THR && c >= 2) wait_vm<THR>();
            if (!(AB & 4)) ldW(IC<c>{});
            if constexpr (c >= 4) {
                SB0();
#pragma unroll
                for (int k = nbs(c - 4); k < nbs(c - 3); k++) ldV(k);
                SB0();
            }
            if constexpr (c >= 4) TIMED(spin_zero4<1>(aF + (R_P + c - 4) * 16));
            if constexpr (THR && c >= 2) wait_vm<THR>();
            if constexpr (UE && c == 6) look();
            if constexpr (UE && c == 7) {
                asm volatile("s_waitcnt vmcnt(%1)" : "+v"(f1) : "i"(8 + nbs(4) - nbs(3)) : "memory");
                got = all_(f1 == tag);
                if (got) { STAMP(16); gather(); }
            }
            ldX(IC<c>{});
            flg(IC<R_F + c>{});
        });
        STAMP(1);
        {
            const u32 vS = ((lane >> 1) * 4 + (lane & 1)) * 16;
            const v4u rst = mkrsrc(ws + OFF_ST, 16384);
            asm volatile("s_mov_b32 m0, %3\n\ts_mov_b64 exec, 0xffff\n\tbuffer_load_dwordx4 %0, %1, %2 offen sc1 lds\n\ts_mov_b64 exec, -1" :: "v"(vS), "s"(rst), "s"(P * 1024 + (w & 1) * 512 + (w >> 1) * 32), "s"(L_STAT + w * 256) : "memory");
        }
        if (!UE) look();
#pragma unroll
        for (int n = nbs(4); n < 16; n++) ldV(n);
        SB0();
        if (!UE) asm volatile("s_waitcnt vmcnt(%1)" : "+v"(f1) : "i"(16 - nbs(4)) : "memory");
        if (!got) {
            if (!all_(f1 == tag)) poll_flags(f1p, tag, true);
            STAMP(16);
            gather();
        }
        STAMP(2);
        pre2();
#pragma unroll
        for (int n = 0; n < 16; n++) pin(stage[n]);
        __builtin_amdgcn_s_setprio(LP);
        {
            const u32 aK = opq((lane & 4) ? L_CNT + 4 * C_K + (lane & 1) * 4 : L_FLG + R_P7 * 16 + (lane & 3) * 4);
            const u32 want = opq((lane & 4) ? 2u : 0u);
            int k = 0;
            while (any_(lds_ldv<u32>(aK) != want) && ++k < (SPIN_CAP << 4)) { __builtin_amdgcn_s_sleep(1); }
            asm volatile("" ::: "memory");
        }
        STAMP(14);
        const v4u rw2 = mkrsrc(w2, 512 * 1376 * 2);
        f2 = tag;
        sfor<28>([&](auto I) {
            constexpr int n = decltype(I)::value;
            if constexpr (THR2 && n >= THR2) wait_vm<THR2>();
            if constexpr (n == 28 - NV2) asm volatile("s_mov_b64 exec, 31\n\tglobal_load_dwordx2 %0, %1, %2 sc1\n\ts_mov_b64 exec, -1" : "+v"(f2) : "v"(f2o), "s"(ws + OFF_F2) : "memory");
            if constexpr (!(AX & 32) || n < 4 || n >= 16) glds_imm<w2loc(n) * 1024, n * 8192>(rw2, vv, sv[(n + 4) / 12], lw);
            if constexpr (n == 15) flg(IC<R_V1>{});
        });
        flg(IC<R_V2>{});
        asm volatile("s_waitcnt vmcnt(%1)" : "+v"(f2) : "i"(NV2 + 1) : "memory");
        STAMP(8);
        gemm2(IC<0>{}, stage, f2, tag);
    } else if (role == 1) {
        u64 tag;
        __builtin_amdgcn_s_setprio(PRIO / 10);
        if (WARM) warm_flag<FS2 * 8>(ws + OFF_F2, ((4 * P + w) * 16 + j) * FS2 * 8, l16);
        if (MPF) {
            const v4u rp = mkrsrc(w2, 512 * 1376 * 2);
            if (MPF & 2) {
                glds(rw1, l16, (b * 11 + 2 * w) * 512, L_C + 2048);
                if (w >= 2) glds(rw1, l16, (b * 11 + 4 + 2 * w) * 512, L_C + 2048);
            }
            glds(rp, l16, (b * 11 + 2 * w) * 512, L_C + 2048);
            if (w < 2) glds(rp, l16, (b * 11 + 8 + 2 * w) * 512, L_C + 2048);
        }
        u32 aW[4], aX[4];
#pragma unroll
        for (int s = 0; s < 4; s++) {
            const int ks = (s + w) & 3;
            aW[s] = opq(ks * 32768 + (8 * h + (l15 >> 2)) * 192 + (g & 1) * 32 + (lane & 3) * 8);
            aX[s] = opq(lw + 16384 + l31 * 128 + (((2 * ks + h) ^ ((l31 >> 1) & 7)) * 16));
        }
        const u32 aC = opq(L_CSP + l31 * 32 + h * 16), aCw = opq(L_CSP + lane * 32 + w * 8), aS = opq(L_STAT + w * 256 + l31 * 8), aB = opq(L_B1 + lane * 2);
        pre2();
        const u32 zero = opq(0u);
        f32x16 acc[3]; f32x4 cs = {0.f, 0.f, 0.f, 0.f};
        v4u sel[3];
#pragma unroll
        for (int u = 0; u < 3; u++) {
            for (int i = 0; i < 16; i++) acc[u][i] = 0.f;
            const u32 sv = (l15 == (u < 2 ? 8 * u : 1) + 4 * (g & 1)) ? 0xBF80BF80u : 0u; sel[u] = (v4u){sv, sv, sv, sv}; pin(sel[u]);
        }
        v4u fa[3][3], fb[2][4];
        auto rdW = [&](int n, int u) { const u32 a = aW[n & 3] + ((n >> 2) % 5) * 3072 + u * 64; v2u lo = lds_tr(a), hi = lds_tr(a + 768); fa[n % 3][u] = (v4u){lo.x, lo.y, hi.x, hi.y}; };
        auto rdX = [&](int c, int s) { fb[c & 1][s] = lds_ld<v4u>(aX[s] + (c % 4) * 4096); };
        const v4u rhs = mkrsrc(ws + OFF_H, (AX & 8) ? 0 : H_BYTES);
        const int nown = (((j + 1) * 43) >> 4) - u0;
        const u32 sh = ((4 * P + w) * 43 + u0) * 2048, shx = nown == 3 ? sh + 4096 : (u32)H_BYTES;
        spin_zero4<(EX >> 2) & 1>(aF);
        STAMP(1);
        rdX(0, 0);
#pragma unroll
        for (int u = 0; u < 3; u++) rdW(0, u);
        rdX(0, 1);
#pragma unroll
        for (int u = 0; u < 3; u++) rdW(1, u);
        rdX(0, 2); rdX(0, 3);
        u32 fl, fx, bb[2]; v2u ee; v2u stg; v4u fas[3], fbs;
        sfor<32>([&](auto N_) __attribute__((always_inline)) {
            constexpr int n = decltype(N_)::value, c = n >> 2, s = n & 3;
            SB0();
            if constexpr (s == 2 && c < 5) {
                pin(fa[(n + 1) % 3][2]);
                flg(IC<R_P + c>{});
            }
            if (s == 2 && c < 7 && !(AB & 2)) {
                if (__builtin_expect(any_(fl != 0), 0)) TIMED(spin_zero4<(EX >> 2) & 1>(aF + (R_F + c + 1) * 16));
                asm volatile("" ::: "memory");
            }
            if (CHST == 2 && n == 26) STAMP(3);
            sfor<3>([&](auto U_) __attribute__((always_inline)) {
                constexpr int u = decltype(U_)::value;
                if (!(EX & 2) || u < 2) acc[u] = mfma32(fa[n % 3][u], fb[c & 1][s], acc[u]);
                if (EX & 2) pin(fa[n % 3][u]);
                if (s == 0 && c < 7 && !(EX & 1)) cs = mfma16(sel[u], fa[n % 3][u], cs);
                if (n == 27) cs = mfma16(sel[u], fa[28 % 3][u], cs);
                SB0();
                if (s >= 2 && c < 7 && u < 2) rdX(c + 1, 2 * (s - 2) + u);
                if (n + 2 < 32) rdW(n + 2, u);
                if (s == 1 && u == 0 && c < 7) fl = lds_ldv<u32>(aF + (R_F + c + 1) * 16);
                if constexpr (P7AT < 0 && n == 29 && u == 2) clr<R_P7 * 16>(aFw, zero);
                if (n == 25 && u == 2) { bb[0] = lds_ld<u16>(aB); bb[1] = lds_ld<u16>(aB + 128); }
                if (n == 21 && u == 1) fx = lds_ldv<u32>(aF + R_X * 16);
                if (n == 23 && u == 1) { if (__builtin_expect(any_(fx != 0), 0)) spin_zero4(aF + R_X * 16); asm volatile("" ::: "memory"); ee = lds_ldv<v2u>(L_E); }
                if (n == 28 && u == 1) {
                    v2u t[2];
#pragma unroll
                    for (int k = 0; k < 2; k++) {
                        const float d = cs[k] - bf_hi(f2u(cs[k]));
                        t[k] = (v2u){__builtin_amdgcn_perm(f2u(d), f2u(cs[k]), 0x07060302u), pk_bf(d - bf_hi(f2u(d)), bf_lo(bb[k]))};
                    }
                    asm volatile("ds_write2st64_b64 %0, %1, %2 offset1:4" :: "v"(aCw), "v"(t[0]), "v"(t[1]) : "memory");
                }
                if (n == 31 && u == 0) { fas[0] = lds_ldv<v4u>(aC); fas[1] = lds_ldv<v4u>(aC + 1024); fas[2] = lds_ldv<v4u>(aC + 2048); }
                if (n == 30 && u == 0) stg = lds_ldv<v2u>(aS);
                if (n == 30 && u == 1) { tag = mix64(((u64)(u32)__builtin_amdgcn_readfirstlane(ee.y) << 32) | (u32)__builtin_amdgcn_readfirstlane(ee.x)); asm volatile("" : "+s"(tag)); }
                SB0();
            });
        });
        if (CHST == 2) STAMP(19);
        {
            auto csbad = [&]() { return umax(umax(umax(fas[0].x, fas[0].z), umax(fas[1].x, fas[1].z)), umax(fas[2].x, fas[2].z)) == ~0u; };
            const u32 want = (lane & 1) ? (u32)(tag >> 32) : (u32)tag;
            if (__builtin_expect(any_(csbad() || stg.y != want), 0)) {
                int k = 0;
                while (any_(csbad()) && ++k < (SPIN_CAP << 2)) { fas[0] = lds_ldv<v4u>(aC); fas[1] = lds_ldv<v4u>(aC + 1024); fas[2] = lds_ldv<v4u>(aC + 2048); }
                k = 0;
                const GAS u64* sp = (const GAS u64*)(ws + OFF_ST + P * 1024 + (w & 1) * 512 + (w >> 1) * 32 + (l31 >> 2) * 64 + (lane & 3) * 8);
                while (any_(stg.y != want) && ++k < SPIN_CAP) { const u64 gr = __hip_atomic_load(sp, RLX_AGENT); stg = (v2u){(u32)gr, (u32)(gr >> 32)}; }
            }
            const u32 m1 = stg.x & 0xffffu, m2 = m1 * 0x10001u;
            fbs = (v4u){m2, h ? m1 : stg.x, m2, m1};
        }
#pragma unroll
        for (int u = 0; u < 3; u++) acc[u] = mfma32(fas[u], fbs, acc[u]);
        STAMP(2); CST(1);
        sfor<6>([&](auto K_) __attribute__((always_inline)) {
            constexpr int u = decltype(K_)::value >> 1, s = decltype(K_)::value & 1;
            v4u o;
#pragma unroll
            for (int e = 0; e < 4; e++) {
                const float a0 = acc[u][8 * s + 2 * e], a1 = acc[u][8 * s + 2 * e + 1];
                o[e] = relu_pk(pk_bf(a0 * __builtin_fabsf(a0), a1 * __builtin_fabsf(a1)));
            }
            st_sc1(o, rhs, l16, (u < 2 ? sh + u * 2048 : shx) + s * 1024);
            if constexpr (decltype(K_)::value == P7AT) clr<R_P7 * 16>(aFw, zero);
            if (PACE && decltype(K_)::value < 5) __builtin_amdgcn_s_sleep(PACE);
            if (PACEV < 9) wait_vm<PACEV>();
            SB0();
        });
        STAMP(8);
        if (P7AT >= 6) { wait_vm<(P7AT >= 6 ? 11 - P7AT : 0)>(); clr<R_P7 * 16>(aFw, zero); }
        wait_vm<0>();
        STAMP(4);
        put_flag<FS2 * 8>(ws + OFF_F2, ((4 * P + w) * 16 + j) * FS2 * 8, l16, tag);
        v4u none[16];
        gemm2(IC<1>{}, none, ~tag, tag);
    } else {
        u64 tag;
        __builtin_amdgcn_s_setprio(3);
        if (NTX & 8) { glds_nt(rx, l16, (8 * tb + 2 * w) * 1024, L_KB + (2 * w) * 1088); glds_nt(rx, l16, (8 * tb + 2 * w + 1) * 1024, L_KB + (2 * w + 1) * 1088); }
        else { glds(rx, l16, (8 * tb + 2 * w) * 1024, L_KB + (2 * w) * 1088); glds(rx, l16, (8 * tb + 2 * w + 1) * 1024, L_KB + (2 * w + 1) * 1088); }
        if (w == 0) {
            const v4u re = mkrsrc(ws, 16);
            asm volatile("s_mov_b32 m0, %1\n\ts_mov_b64 exec, 1\n\tbuffer_load_dwordx4 off, %0, 0 sc1 lds\n\ts_mov_b64 exec, -1" :: "s"(re), "s"(L_E) : "memory");
        } else if (w == 2) {
            const v4u rb2 = mkrsrc(b2, 1024);
            asm volatile("s_mov_b32 m0, %3\n\ts_mov_b64 exec, 0xff\n\tbuffer_load_dwordx4 %0, %1, %2 offen lds\n\ts_mov_b64 exec, -1" :: "v"(l16), "s"(rb2), "s"(tc * 128), "s"(L_B2) : "memory");
        } else if (w == 1) {
            const v4u rb1 = mkrsrc(b1, 1376 * 2);
            asm volatile("s_mov_b32 m0, %3\n\ts_nop 0\n\tbuffer_load_dword %0, %1, %2 offen lds" :: "v"(lane * 4), "s"(rb1), "s"(u0 * 64), "s"(L_B1) : "memory");
        }
        const u32 pi = opq((hf * 64 + lane) * 2);
        u32 pA = ldp((const GAS u16*)((const GAS char*)(logA + ch * 128) + pi)), pI = ldp((const GAS u16*)((const GAS char*)(Aim + ch * 128) + pi));
        u32 pR = ldp((const GAS u16*)((const GAS char*)(Cre + ch * 128) + pi)), pC = ldp((const GAS u16*)((const GAS char*)(Cim + ch * 128) + pi));
        u32 pT = *(const GAS u16*)(logdt + ch), pD = *(const GAS u16*)(Dskip + ch);
        flg(IC<R_X>{});
        one = opq(1u);
        const u32 aCnt = opq(L_CNT + chl * 4), aCtl = opq(L_C);
        const v4u rus = mkrsrc(ws + OFF_U, 2 << 20);
        v4u selLo, selHi;
#pragma unroll
        for (int e = 0; e < 4; e++) {
            const u32 o1 = (h == (g & 1) && ((l15 & 7) >> 1) == e) ? ((l15 & 1) ? 0x3C000000u : 0x00003C00u) : 0u;
            selLo[e] = l15 < 8 ? o1 : 0u; selHi[e] = l15 < 8 ? 0u : o1;
        }
        pin(selLo); pin(selHi);
        const int m = 64 * hf + lane;
        const u32 aT = opq(w * 32768 + 16384 + l15 * 64 + g * 16), t0 = opq((2 * chl) * 32768 + 16384 + m * 4), kr = opq(L_KB + chl * 8320 + (1016 - 8 * m) * 2);
        u32 tg[6];
        {
            const bool r1 = m < 32, r2 = m < 64;
            tg[0] = opq(t0 + (r1 ? 528 : 0) * 4); tg[1] = opq(t0 + (r2 ? 400 : 0) * 4); tg[2] = opq(t0 + (r1 ? 560 : 272) * 4);
            tg[3] = opq(t0 + (r1 ? 592 : 272) * 4); tg[4] = opq(t0 + (r2 ? 464 : 1) * 4); tg[5] = opq(t0 + (r1 ? 624 : 1) * 4);
        }
        u32 kb[4], ua[4];
#pragma unroll
        for (int i = 0; i < 4; i++) {
            kb[i] = opq(L_KB + chl * 8320 + ((l15 & 1) ? (239 - l15 + 8 * g) * 2 : 4160 + (240 - l15 + 8 * g) * 2) - hf * 256 - 192 + i * 1024);
            const int os = 30 + 2 * l15 + g - 16 * hf - 4 * i;
            ua[i] = opq(L_U + os * 32 + ((chl ^ (os >> 3)) & 1) * 16);
        }
        const u32 aZ3 = opq((lane & 4) ? L_CNT + 4 * C_K + chl * 4 : L_UF + (lane & 3) * 4), want3 = opq((lane & 4) ? 2u : 0u), aZ2 = opq(L_C + (lane & 3) * 4 + ((lane & 8) ? R_LN : (lane & 4) ? R_P + 4 : R_F + 7) * 16);
        const u32 vA = opq((lane >> 3) * 4096 + (lane & 7) * 16);
        pin(pA); pin(pI); pin(pR); pin(pC); pin(pT); pin(pD);

        {
            const u32 aRow = opq(L_KB + (l15 & 7) * 1088 + (l15 >> 3) * 512 + g * 16), aTr = opq(L_KB + (l15 >> 2) * 1088 + (128 * w + 16 * g + 4 * (lane & 3)) * 2);
            v4u ones = {0x3F803F80u, 0x3F803F80u, 0x3F803F80u, 0x3F803F80u}; pin(ones);
            spin_zero4(aF + R_X * 16);
            STAMP(1);
            v4u xb[8]; v2u xt[2][2];
#pragma unroll
            for (int s = 0; s < 8; s++) xb[s] = lds_ld<v4u>(aRow + s * 64);
#pragma unroll
            for (int i = 0; i < 2; i++) { xt[i][0] = lds_tr(aTr + i * 128); xt[i][1] = lds_tr(aTr + i * 128 + 4 * 1088); }
            tag = get_tag();
            f32x4 sm = {0.f, 0.f, 0.f, 0.f}, sq = {0.f, 0.f, 0.f, 0.f};
#pragma unroll
            for (int s = 0; s < 8; s++) { sq = mfma16(xb[s], xb[s], sq); sm = mfma16(ones, xb[s], sm); }
            float q = (l15 & 2) ? ((l15 & 1) ? sq[3] : sq[2]) : ((l15 & 1) ? sq[1] : sq[0]);
            q = g == (l15 >> 2) ? q : 0.f;
            { const auto r = __builtin_amdgcn_permlane32_swap(f2u(q), f2u(q), false, false); q = u2f(r[0]) + u2f(r[1]); }
            { const auto r = __builtin_amdgcn_permlane16_swap(f2u(q), f2u(q), false, false); q = u2f(r[0]) + u2f(r[1]); }
            const float mean = dpp_add<0x128>(sm[0]) * (1.f / 512.f);
            const float var = fmaxf(__builtin_fmaf(-mean, mean, dpp_add<0x128>(q) * (1.f / 512.f)), 0.f);
            const u32 p = pk_bf(mean, __builtin_amdgcn_sqrtf(rbf(rbf(var) + 1e-5f)));
            const float mb = bf_lo(p), rr = __builtin_amdgcn_rcpf(bf_hi(p));
            if (w == 0 && lane < 8) __hip_atomic_store((GAS u64*)(ws + OFF_ST + (P * 16 + j) * 64) + lane, ((u64)((lane & 1) ? (u32)(tag >> 32) : (u32)tag) << 32) | p, RLX_AGENT);
            sfor<2>([&](auto I_) {
                constexpr int i = decltype(I_)::value;
                v4u o;
                sfor<4>([&](auto E_) {
                    constexpr int e = decltype(E_)::value;
                    const u32 xv = xt[i][e >> 1][e & 1];
                    const u32 d = pk_bf(bf_lo(xv) - bcast<2 * e>(mb), bf_hi(xv) - bcast<2 * e + 1>(mb));
                    o[e] = pk_bf(bf_lo(d) * bcast<2 * e>(rr), bf_hi(d) * bcast<2 * e + 1>(rr));
                });
                st_sc1(o, rus, l16, (AX & 2) ? 2u << 20 : tb * 8192 + w * 2048 + i * 1024);
            });
        }
        flg(IC<R_LN>{});
        STAMP(2);
        __builtin_amdgcn_s_setprio(PRIO % 10);
        f32x4 kt[3];
        {
            const float dt = rbf(expf(bf_lo(pT)));
            const float zr = -rbf(expf(bf_lo(pA))) * dt, zi = bf_lo(pI) * dt;
            const float lr = zr * 1.44269504088896341f;
            const float th = zi * 0.159154943091895336f, tl = __builtin_fmaf(zi, 0.159154943091895336f, -th) + zi * 6.4206383e-09f;
            const cmult w1c = mkc(cexp_pow(lr, -th, -tl, 1.f)), qc = mkc(cexp_pow(lr, -th, -tl, 128.f)), q2 = mkc(cexp_pow(lr, -th, -tl, 256.f));
            const cmult w8 = mkc(cexp_pow(lr, th, tl, 8.f)), w32 = mkc(cexp_pow(lr, th, tl, 32.f));
            const u32 aR = pR & 0x7fffu, aI = pC & 0x7fffu;
            int ex = (int)(wave_max(aR > aI ? aR : aI) >> 7); ex = ex < 12 ? 12 : (ex > 240 ? 240 : ex);
            const float sdn = u2f((u32)(262 - ex) << 23), sup = u2f((u32)(ex - 8) << 23);
            auto transpose = [&](const f32x2 (&v)[16], v4u (&f)[4]) {
                f32x16 z; for (int i = 0; i < 16; i++) z[i] = 0.f;
                f32x16 dr = mfma32h((v4u){pk_h2(v[0].x, v[1].x), pk_h2(v[2].x, v[3].x), pk_h2(v[4].x, v[5].x), pk_h2(v[6].x, v[7].x)}, selLo, z);
                f32x16 di = mfma32h((v4u){pk_h2(v[0].y, v[1].y), pk_h2(v[2].y, v[3].y), pk_h2(v[4].y, v[5].y), pk_h2(v[6].y, v[7].y)}, selLo, z);
                dr = mfma32h((v4u){pk_h2(v[8].x, v[9].x), pk_h2(v[10].x, v[11].x), pk_h2(v[12].x, v[13].x), pk_h2(v[14].x, v[15].x)}, selHi, dr);
                di = mfma32h((v4u){pk_h2(v[8].y, v[9].y), pk_h2(v[10].y, v[11].y), pk_h2(v[12].y, v[13].y), pk_h2(v[14].y, v[15].y)}, selHi, di);
#pragma unroll
                for (int e = 0; e < 4; e++) {
                    f[0][e] = pk_h2(dr[2 * e], dr[2 * e + 1]); f[1][e] = pk_h2(dr[8 + 2 * e], dr[9 + 2 * e]);
                    f[2][e] = pk_h2(di[2 * e], di[2 * e + 1]); f[3][e] = pk_h2(di[8 + 2 * e], di[9 + 2 * e]);
                }
            };
            { f32x2 t0_ = w1c.b, t1_ = qc.b, t2_ = q2.b, t3_ = w8.b, t4_ = w32.b; pin(t0_); pin(t1_); pin(t2_); pin(t3_); pin(t4_); }
            STAMP(13);
            wait_vm<0>();
            put_flag<FS1 * 8>(ws + OFF_F1, (w * 256 + tb) * FS1 * 8, l16, tag);
            warm_flag<FS3 * 8>(ws + OFF_F3, (hf * 512 + ch) * FS3 * 8, l16);
            STAMP(14);
            v4u fy[4], fx[4];
            f32x2 v[16], xo[8];
            v[0] = (f32x2){1.f, 0.f}; v[1] = w8.b; v[2] = cmul(w8.b, w8); v[3] = cmul(v[2], w8); v[4] = w32.b;
            xo[0] = (f32x2){bf_lo(pR) * sdn, -bf_lo(pC) * sdn};
#pragma unroll
            for (int i = 5; i < 16; i++) { v[i] = cmul(v[i - 4], w32); if (i < 12) xo[i - 4] = cmul(xo[i - 5], w1c); }
            STAMP(3); CST(2);
            transpose(v, fy);
            const float fin = 2.f * dt * sup;
            auto tile = [&](int t) {
                transpose(v, fx);
                f32x4 d = {0.f, 0.f, 0.f, 0.f};
#pragma unroll
                for (int s = 0; s < 4; s++) d = mfma16h(fy[s], fx[s], d);
                kt[t] = d * fin;
            };
            v[0] = xo[0]; v[1] = cmul(xo[0], qc);
#pragma unroll
            for (int s = 2; s < 16; s++) v[s] = cmul(v[s - 2], q2);
            const f32x2 last = cmul(v[14], q2);
            tile(0);
            v[0] = last; v[1] = xo[4]; v[9] = xo[2]; v[13] = xo[6];
            v[2] = cmul(v[1], qc); v[10] = cmul(v[9], qc); v[14] = cmul(v[13], qc);
#pragma unroll
            for (int s = 3; s < 9; s++) v[s] = cmul(v[s - 2], q2);
            v[11] = cmul(v[9], q2); v[12] = cmul(v[10], q2); v[15] = cmul(v[13], q2);
            const f32x2 last2 = cmul(v[14], q2);
            tile(1);
            v[0] = last2;
#pragma unroll
            for (int i = 0; i < 4; i++) { v[1 + 2 * i] = xo[2 * i + 1]; v[2 + 2 * i] = cmul(xo[2 * i + 1], qc); }
#pragma unroll
            for (int i = 9; i < 16; i++) v[i] = (f32x2){0.f, 0.f};
            tile(2);
        }
        CST(3); STAMP(17);
        pin(kt[0]); pin(kt[1]); pin(kt[2]);
        spin_zero4<1>(aZ2);
        STAMP(11);
        __builtin_amdgcn_s_setprio(WP);
#pragma unroll
        for (int t = 0; t < 3; t++) lds_st<f32x4>(aT + t * 1024, kt[t]);
        sig<C_A>(aCnt, one);
        STAMP(4);
        spin(aCnt + 4 * C_A, 2);
        {
            auto tv = [&](u32 a) { return lds_ld<float>(a) + lds_ld<float>(a + 32768); };
            float k[9];
            k[0] = tv(t0); k[8] = tv(t0 + 4); k[4] = tv(t0 + 272 * 4);
            k[1] = tv(tg[0]); k[2] = tv(tg[1]); k[3] = tv(tg[2]); k[5] = tv(tg[3]); k[6] = tv(tg[4]); k[7] = tv(tg[5]);
            const float va = tv(t0 + 512), vb = tv(t0 + 516);
            if (m == 0) k[0] += bf_lo(pD);
            const u32 k10 = pk_bf(k[1], k[0]);
            lds_st<v4u>(kr + 2048, (v4u){pk_bf(k[7], k[6]), pk_bf(k[5], k[4]), pk_bf(k[3], k[2]), k10});
            lds_st<v4u>(kr + 2048 + 4160, (v4u){pk_bf(k[8], k[7]), pk_bf(k[6], k[5]), pk_bf(k[4], k[3]), pk_bf(k[2], k[1])});
            const u32 pa = pk_bf(va, va), pb = pk_bf(vb, vb), pba = pk_bf(vb, va);
            lds_st<v4u>(kr, (v4u){pb, (m & 1) ? pb : pba, pa, pa});
            lds_st<v4u>(kr + 4160, (v4u){pb, pb, (m & 1) ? pba : pa, pa});
            if (m < 4) lds_st<v4u>(L_KB + chl * 8320 + 4096 + (m & 1) * 16 + (m < 2 ? 4160 : 0), (v4u){m == 0 ? k10 >> 16 : 0u, 0u, 0u, 0u});
        }
        sig<C_K>(aCnt, one);
        STAMP(18);
        f32x4 cv[8];
        {
#pragma unroll
            for (int t = 0; t < 8; t++) cv[t] = (f32x4){0.f, 0.f, 0.f, 0.f};
            v4u A[8], B[8];
            auto rdA = [&](int r, int a) { const u32 p = kb[(7 - a) >> 1] + ((7 - a) & 1) * 512 + (3 - r) * 64; return (v4u){lds_ld<u32>(p), lds_ld<u32>(p + 4), lds_ld<u32>(p + 8), lds_ld<u32>(p + 12)}; };
            auto rdB = [&](int r, int d) { return lds_ld<v4u>(ua[r] + d * 1024); };
            {
                int k = 0;
                while (any_(lds_ldv<u32>(aZ3) != want3) && ++k < (SPIN_CAP << 4)) {}
                asm volatile("" ::: "memory");
            }
            STAMP(6);
#pragma unroll
            for (int i = 0; i < 8; i++) { A[7 - i] = rdA(0, 7 - i); B[i] = rdB(0, i); }
            __builtin_amdgcn_s_setprio(PRIO % 10);
            if (b == 0 && w == 0 && lane == 0) __hip_atomic_store((GAS u64*)ws, tag, RLX_AGENT);
            CST(0);
#pragma unroll
            for (int r = 0; r < 4; r++) {
#pragma unroll
                for (int a = 7; a >= 0; a--) {
#pragma unroll
                    for (int d = 0; d + a < 8; d++) {
                        cv[a + d] = mfma16(A[a], B[d], cv[a + d]);
                        if (a == 0 && r < 3) { SB0(); B[d] = rdB(r + 1, d); SB0(); }
                    }
                    if (r < 3) { SB0(); A[a] = rdA(r + 1, a); SB0(); }
                }
            }
        }
        STAMP(7); CST(1);
        for (int t = 0; t < 8; t++) pin(cv[t]);
        {
            const u32 mine = opq(w * 32768 + 16384 + l16), other = opq((w ^ 1) * 32768 + 16384 + l16);
#pragma unroll
            for (int t = 0; t < 4; t++) lds_st<f32x4>(mine + t * 1024, sel4(hf, cv[t], cv[4 + t]));
            sig<C_E>(aCnt, one);
            sig<C_D>(aCtl + (L_CNT - L_C), one);
            spin(aCnt + 4 * C_E, 2);
            u32 o[8];
#pragma unroll
            for (int t = 0; t < 4; t++) {
                const f32x4 y = sel4(hf, cv[4 + t], cv[t]) + lds_ld<f32x4>(other + t * 1024);
                o[2 * t] = pk_bf(y[0], y[1]); o[2 * t + 1] = pk_bf(y[2], y[3]);
            }
            const v4u ras = mkrsrc(ws + OFF_A, 2 << 20);
            st_sc1((v4u){o[0], o[1], o[2], o[3]}, ras, l16, (AX & 16) ? 2u << 20 : ch * 4096 + hf * 2048);
            st_sc1((v4u){o[4], o[5], o[6], o[7]}, ras, l16, (AX & 16) ? 2u << 20 : ch * 4096 + hf * 2048 + 1024);
            STAMP(15);
            wait_vm<0>();
            STAMP(8);
            put_flag<FS3 * 8>(ws + OFF_F3, (hf * 512 + ch) * FS3 * 8, l16, tag);
            spin(aCtl + (L_CNT - L_C) + 4 * C_D, 4);
            if (F3D) __builtin_amdgcn_s_sleep(F3D);
            poll_flags((const GAS u64*)(ws + OFF_F3) + ((tr >> 4) * 512 + tc * 64 + 16 * w + l15) * FS3, tag, lane < 16);
            STAMP(9);
#pragma unroll
            for (int i = 0; i < 2; i++)
                glds_sc1(ras, vA, (tc * 64 + 16 * w + 8 * i) * 4096 + (tr >> 3) * 1024 + (tr & 3) * 256 + ((tr >> 2) & 1) * 128, L_U + w * 2048 + i * 1024);
            const u32 ta = opq(L_U + w * 2048 + (l15 >> 2) * 128 + g * 32 + (lane & 3) * 8), aw = opq(L_KB + lane * 144 + w * 32);
            wait_vm<0>();
            const v2u a0 = lds_tr(ta), a1 = lds_tr(ta + 512), a2 = lds_tr(ta + 1024), a3 = lds_tr(ta + 1536);
            lds_st<v4u>(aw, (v4u){a0.x, a0.y, a1.x, a1.y});
            lds_st<v4u>(aw + 16, (v4u){a2.x, a2.y, a3.x, a3.y});
            STAMP(10);
        }
        if (SPLITF) { sig<C_FN>(aCtl + (L_CNT - L_C), one); sig<C_FN + 1>(aCtl + (L_CNT - L_C), one); }
        else { wait_lgkm0(); __builtin_amdgcn_s_barrier(); }
    }
}

#if defined(__HIP_DEVICE_COMPILE__) && __HIP_DEVICE_COMPILE__
#pragma clang attribute pop
#endif

extern "C" void kernel_launch(void* const* d_in, const int* in_sizes, int n_in, void* d_out, int out_size, void* d_ws, size_t ws_size,
                              hipStream_t stream) {
    fused_kernel<<<256, 768, 0, stream>>>((const u16*)d_in[0], (const u16*)d_in[1], (const u16*)d_in[2], (const u16*)d_in[3], (const u16*)d_in[4],
                                          (const u16*)d_in[5], (const u16*)d_in[6], (const u16*)d_in[8], (const u16*)d_in[9], (const u16*)d_in[10],
                                          (const u16*)d_in[11], (u16*)d_out, (char*)d_ws + (WSOFF == 1 ? ((ws_size - (16u << 20)) & ~(size_t)0x1fffff) : WSOFF == 2 ? ((ws_size / 2) & ~(size_t)0x1fffff) : 0));
}
```

```cpp
#include <hip/hip_runtime.h>
#include <stdint.h>
#include <utility>

typedef unsigned short u16;
typedef unsigned int u32;
typedef unsigned long long u64;
typedef u32 v4u __attribute__((ext_vector_type(4)));
typedef u32 v2u __attribute__((ext_vector_type(2)));
typedef float f32x2 __attribute__((ext_vector_type(2)));
typedef float f32x4 __attribute__((ext_vector_type(4)));
typedef float f32x16 __attribute__((ext_vector_type(16)));
typedef __bf16 bf16x8 __attribute__((ext_vector_type(8)));
typedef __bf16 bf16x2 __attribute__((ext_vector_type(2)));
typedef _Float16 f16x8 __attribute__((ext_vector_type(8)));
typedef _Float16 f16x2 __attribute__((ext_vector_type(2)));
typedef short s16x4 __attribute__((ext_vector_type(4)));
typedef u64 u64x8 __attribute__((ext_vector_type(8)));
typedef u64 u64x4 __attribute__((ext_vector_type(4)));
#define GAS __attribute__((address_space(1)))
#define LAS __attribute__((address_space(3)))
#define DI __device__ __forceinline__
#define RLX_AGENT __ATOMIC_RELAXED, __HIP_MEMORY_SCOPE_AGENT
#define RLX_WG __ATOMIC_RELAXED, __HIP_MEMORY_SCOPE_WORKGROUP
#define SB0() __builtin_amdgcn_sched_barrier(0)

constexpr int OFF_ST = 16384, OFF_F1 = 128 << 10, OFF_F2 = 256 << 10, OFF_F3 = 512 << 10;
constexpr int FS1 = 2, FS2 = 16, FS3 = 16;
constexpr int OFF_U = 1 << 20, OFF_A = 4 << 20, OFF_H = 8 << 20;
constexpr int H_BYTES = 64 * 43 * 2048;
constexpr int L_C = 131072;
constexpr int L_FLG = L_C, L_UF = L_C + 320, L_E = L_C + 768, L_B2 = L_C + 896, L_CNT = L_C + 416, L_B1 = L_C + 512, L_STAT = L_C + 1024, L_CSP = L_C + 29696;
enum { R_F = 0, R_P = 8, R_P7 = 13, R_PK = 15, R_LN = 19, R_X = 14, R_V1 = 16, R_V2 = 17, R_V3 = 18 };
constexpr int L_U = L_C + 3072, L_KB = L_C + 12288;
constexpr int SPIN_CAP = 1 << 16;
constexpr int CODE_BYTES = 30208;
enum { C_A = 0, C_K = 2, C_E = 4, C_D = 8, C_RD = 12 };

#if defined(__HIP_DEVICE_COMPILE__) && __HIP_DEVICE_COMPILE__
#pragma clang attribute push(__attribute__((target("no-packed-fp32-ops"))), apply_to = function)
#endif
DI float u2f(u32 v) { return __builtin_bit_cast(float, v); }
DI u32 f2u(float v) { return __builtin_bit_cast(u32, v); }
DI bool any_(bool c) { return __builtin_amdgcn_ballot_w64(c) != 0; }
DI bool all_(bool c) { return __builtin_amdgcn_ballot_w64(!c) == 0; }
template <typename T> DI T lds_ld(u32 a) { return *(LAS T*)(size_t)a; }
template <typename T> DI void lds_st(u32 a, T v) { *(LAS T*)(size_t)a = v; }
DI v2u lds_tr(u32 a) { return __builtin_bit_cast(v2u, __builtin_amdgcn_ds_read_tr16_b64_v4i16((LAS s16x4*)(size_t)a)); }
DI v4u mkrsrc(const void* p, u32 bytes) { u64 a = (u64)p; v4u r; r.x = (u32)a; r.y = (u32)(a >> 32) & 0xffffu; r.z = bytes; r.w = 0x00020000u; return r; }
DI void glds(v4u rsrc, u32 voff, u32 soff, u32 lds) {
    asm volatile("s_mov_b32 m0, %3\n\ts_nop 0\n\tbuffer_load_dwordx4 %0, %1, %2 offen lds" :: "v"(voff), "s"(rsrc), "s"(soff), "s"(lds) : "memory");
}
template <int LOFF> DI u32 glds_first(v4u rsrc, u32 voff, u32 sa, u32 sb, u32 lbase) {
    u32 so;
    asm volatile("s_add_u32 m0, %5, %6\n\ts_add_u32 %0, %3, %4\n\tbuffer_load_dwordx4 %1, %2, %0 offen lds" : "=&s"(so) : "v"(voff), "s"(rsrc), "s"(sa), "s"(sb), "s"(lbase), "i"(LOFF) : "memory", "scc");
    return so;
}
template <int LOFF, int SOFF> DI void glds_imm(v4u rsrc, u32 voff, u32 sa, u32 lbase) {
    u32 so;
    asm volatile("s_add_u32 m0, %4, %5\n\ts_add_u32 %0, %3, %6\n\tbuffer_load_dwordx4 %1, %2, %0 offen lds" : "=&s"(so) : "v"(voff), "s"(rsrc), "s"(sa), "s"(lbase), "i"(LOFF), "i"(SOFF) : "memory", "scc");
}
template <int OFF> DI void glds_next(v4u rsrc, u32 voff, u32 so) {
    asm volatile("buffer_load_dwordx4 %0, %1, %2 offen offset:%3 lds" :: "v"(voff), "s"(rsrc), "s"(so), "i"(OFF) : "memory");
}
template <int LOFF> DI void flag_imm(v4u rsrc, u32 lbase) {
    asm volatile("s_add_u32 m0, %2, %3\n\ts_mov_b64 exec, 1\n\tbuffer_load_dword off, %0, %1 lds\n\ts_mov_b64 exec, -1" :: "s"(rsrc), "s"(rsrc.z), "s"(lbase), "i"(LOFF) : "memory", "scc");
}
DI void glds_sc1(v4u rsrc, u32 voff, u32 soff, u32 lds) {
    asm volatile("s_mov_b32 m0, %3\n\ts_nop 0\n\tbuffer_load_dwordx4 %0, %1, %2 offen sc1 lds" :: "v"(voff), "s"(rsrc), "s"(soff), "s"(lds) : "memory");
}
DI void flag_dma(v4u rsrc, u32 lds) {
    asm volatile("s_mov_b32 m0, %2\n\ts_mov_b64 exec, 1\n\tbuffer_load_dword off, %0, %1 lds\n\ts_mov_b64 exec, -1" :: "s"(rsrc), "s"(rsrc.z), "s"(lds) : "memory");
}
DI void st_sc1(v4u v, v4u rsrc, u32 voff, u32 soff) {
    asm volatile("buffer_store_dwordx4 %0, %1, %2, %3 offen sc1\n\ts_nop 1" :: "v"(v), "v"(voff), "s"(rsrc), "s"(soff) : "memory");
}
DI void put_flag(const char* base, u32 off, u32 l16, u64 tag) {
    asm volatile("s_mov_b64 exec, 1\n\tglobal_store_dwordx2 %0, %1, %2 sc1\n\ts_mov_b64 exec, -1" :: "v"(l16), "v"(tag), "s"(base + off) : "memory");
}
template <int BYTES> DI void warm_flag(const char* base, u32 off, u32 l16) {
    const v4u v = {0u, 0u, 0u, 0u};
    asm volatile("s_mov_b64 exec, %3\n\tglobal_store_dwordx4 %0, %1, %2 sc1\n\ts_mov_b64 exec, -1\n\ts_nop 0" :: "v"(l16), "v"(v), "s"(base + off), "i"((1 << (BYTES / 16)) - 1) : "memory");
}
DI u32 ldp(const GAS u16* p) { return __builtin_nontemporal_load(p); }
DI u32 opq(u32 a) { asm volatile("" : "+v"(a)); return a; }
template <typename T> DI T lds_ldv(u32 a) { return *(volatile LAS T*)(size_t)a; }
template <int SLEEP = 0> DI void spin_zero4(u32 a) {
    int n = 0;
    while (any_(lds_ldv<u32>(a) != 0) && ++n < (SPIN_CAP << 4)) { if (SLEEP) __builtin_amdgcn_s_sleep(1); }
    asm volatile("" ::: "memory");
}
template <int N> DI void wait_vm() { asm volatile("s_waitcnt vmcnt(%0)" :: "i"(N) : "memory"); }
DI void wait_lgkm0() { asm volatile("s_waitcnt lgkmcnt(0)" ::: "memory"); }
DI u64 mix64(u64 e) { e += 0x9E3779B97F4A7C15ull; e ^= e >> 31; e *= 0xBF58476D1CE4E5B9ull; e ^= e >> 29; return e; }
DI u64 get_tag() { const v2u e = lds_ldv<v2u>(L_E); return mix64(((u64)(u32)__builtin_amdgcn_readfirstlane(e.y) << 32) | (u32)__builtin_amdgcn_readfirstlane(e.x)); }
template <int C> DI void sig(u32 a, u32 one) { asm volatile("s_mov_b64 exec, 1\n\tds_add_u32 %0, %1 offset:%2\n\ts_mov_b64 exec, -1" :: "v"(a), "v"(one), "i"(4 * C) : "memory"); }
template <int OFF> DI void clr(u32 a, u32 zero) { asm volatile("s_mov_b64 exec, 1\n\tds_write_b32 %0, %1 offset:%2\n\ts_mov_b64 exec, -1" :: "v"(a), "v"(zero), "i"(OFF) : "memory"); }
DI void spin(u32 a, u32 target) {
    int n = 0;
    while ((u32)__builtin_amdgcn_readfirstlane(lds_ldv<u32>(a)) < target && ++n < (SPIN_CAP << 4)) {}
    asm volatile("" ::: "memory");
}
DI u64 peek_flags(const GAS u64* p, u64 tag, bool on) { u64 f = tag; if (on) f = __hip_atomic_load(p, RLX_AGENT); return f; }
DI void poll_flags(const GAS u64* p, u64 tag, bool on) {
    int n = 0;
    while (true) {
        u64 f = peek_flags(p, tag, on);
        if (all_(f == tag) || ++n > SPIN_CAP) break;
        __builtin_amdgcn_s_sleep(2);
    }
    asm volatile("" ::: "memory");
}
DI float bf_lo(u32 v) { return u2f(v << 16); }
DI float bf_hi(u32 v) { return u2f(v & 0xffff0000u); }
DI u32 pk_bf(float a, float b) { f32x2 v = {a, b}; return __builtin_bit_cast(u32, __builtin_convertvector(v, bf16x2)); }
typedef short s16x2 __attribute__((ext_vector_type(2)));
DI u32 relu_pk(u32 v) { return __builtin_bit_cast(u32, __builtin_elementwise_max(__builtin_bit_cast(s16x2, v), (s16x2){0, 0})); }
DI u32 umax(u32 a, u32 b) { return a > b ? a : b; }
DI float rbf(float a) { return bf_lo(pk_bf(a, a)); }
DI u32 pk_h(f32x2 v) { return __builtin_bit_cast(u32, __builtin_convertvector(v, f16x2)); }
DI f32x16 mfma32(v4u a, v4u b, f32x16 c) { return __builtin_amdgcn_mfma_f32_32x32x16_bf16(__builtin_bit_cast(bf16x8, a), __builtin_bit_cast(bf16x8, b), c, 0, 0, 0); }
DI f32x4 mfma16(v4u a, v4u b, f32x4 c) { return __builtin_amdgcn_mfma_f32_16x16x32_bf16(__builtin_bit_cast(bf16x8, a), __builtin_bit_cast(bf16x8, b), c, 0, 0, 0); }
DI f32x4 mfma16h(v4u a, v4u b, f32x4 c) { return __builtin_amdgcn_mfma_f32_16x16x32_f16(__builtin_bit_cast(f16x8, a), __builtin_bit_cast(f16x8, b), c, 0, 0, 0); }
template <int CTRL> DI float dpp_add(float v) { return v + __builtin_bit_cast(float, __builtin_amdgcn_update_dpp(0, __builtin_bit_cast(int, v), CTRL, 0xf, 0xf, true)); }
template <int CTRL> DI u32 dpp_max(u32 v) { const u32 o = (u32)__builtin_amdgcn_update_dpp(0, (int)v, CTRL, 0xf, 0xf, true); return v > o ? v : o; }
template <int T> DI float bcast(float v) { return __builtin_bit_cast(float, __builtin_amdgcn_update_dpp(0, __builtin_bit_cast(int, v), 0x150 + T, 0xf, 0xf, true)); }
DI u32 wave_max(u32 v) {
    v = dpp_max<0xB1>(v); v = dpp_max<0x4E>(v); v = dpp_max<0x141>(v); v = dpp_max<0x140>(v);
    const u32 a = __builtin_amdgcn_readlane(v, 0), c = __builtin_amdgcn_readlane(v, 16), d = __builtin_amdgcn_readlane(v, 32), e = __builtin_amdgcn_readlane(v, 48);
    const u32 p = a > c ? a : c, q = d > e ? d : e;
    return p > q ? p : q;
}
struct cmult { f32x2 b, n; };
DI cmult mkc(f32x2 b) { cmult c; c.b = b; c.n = (f32x2){-b.y, b.x}; return c; }
DI f32x2 cmul(f32x2 a, const cmult& c) { return __builtin_elementwise_fma((f32x2){a.x, a.x}, c.b, (f32x2){a.y, a.y} * c.n); }
DI f32x16 mfma32h(v4u a, v4u b, f32x16 c) { return __builtin_amdgcn_mfma_f32_32x32x16_f16(__builtin_bit_cast(f16x8, a), __builtin_bit_cast(f16x8, b), c, 0, 0, 0); }
DI u32 pk_h2(float a, float b) { return pk_h((f32x2){a, b}); }
DI f32x2 cexp_pow(float lr, float th, float tl, float e) {
    float a = __builtin_amdgcn_fractf(th * e) + tl * e;
    float m = __builtin_amdgcn_exp2f(lr * e);
    f32x2 r; r.x = m * __builtin_amdgcn_cosf(a); r.y = m * __builtin_amdgcn_sinf(a); return r;
}
DI f32x4 sel4(bool c, f32x4 a, f32x4 b) { return c ? a : b; }
constexpr int w2loc(int n) { return n < 16 ? n : n < 28 ? n + 4 : n - 28; }
template <class F, int... I> DI void sfor_(F&& f, std::integer_sequence<int, I...>) { (f(std::integral_constant<int, I>{}), ...); }
template <int N, class F> DI void sfor(F&& f) { sfor_(f, std::make_integer_sequence<int, N>{}); }
template <int I> using IC = std::integral_constant<int, I>;
template <typename T> DI void pin(T& v) { asm volatile("" : "+v"(v)); }

__global__ void __launch_bounds__(768, 3)
fused_kernel(const u16* x_, const u16* logA_, const u16* Aim_, const u16* Cre_, const u16* Cim_, const u16* logdt_, const u16* Dskip_, const u16* w1_,
             const u16* b1_, const u16* w2_, const u16* b2_, u16* out_, char* ws_) {
    u64x8 ka; u64x4 kb4; u64 kc;
    asm volatile("s_load_dwordx16 %0, %3, 0x0\n\ts_load_dwordx8 %1, %3, 0x40\n\ts_load_dwordx2 %2, %3, 0x60" : "=&s"(ka), "=&s"(kb4), "=&s"(kc) : "s"(__builtin_amdgcn_kernarg_segment_ptr()) : "memory");
    __shared__ __attribute__((aligned(16))) char smem[163840];
    asm volatile("" :: "v"((u32)(size_t)(LAS char*)smem));
    const int tid = threadIdx.x, lane = tid & 63, wave = __builtin_amdgcn_readfirstlane(tid >> 6) & 15, w = wave & 3, role = wave >> 2;
    const int b = blockIdx.x & 255, xcd = b & 7, slot = b >> 3;
    const int P = 4 * (xcd >> 1) + (slot >> 3), j = 8 * (xcd & 1) + (slot & 7);
    const int tr = 4 * xcd + (slot >> 3), tc = slot & 7;
    const int u0 = (j * 43) >> 4;
    if (wave == 4) {
        u64 pc; asm volatile("s_getpc_b64 %0" : "=s"(pc));
        const v4u rc = mkrsrc((const void*)(pc & ~255ull), CODE_BYTES);
        glds(rc, lane * 16, slot * 1024, L_C + 2048);
    }
    const int h = lane >> 5, l31 = lane & 31, l15 = lane & 15, g = lane >> 4;
    const u32 l16 = lane * 16, lw = w * 32768, w4 = w * 4;
    const int rho = slot >> 3;
    const int tb = 64 * (P >> 2) + 32 * (j & 1) + 16 * ((P >> 1) & 1) + 2 * (j >> 1) + (P & 1);
    const int cp = 32 * xcd + slot, chl = w >> 1, hf = w & 1, ch = 2 * cp + chl;
    {
        const u32 v = (tid >= 192 && tid < 256) || (tid >= 282 && tid < 286) ? 0u : ~0u;
        if (tid < 288) lds_st<v4u>((tid < 192 ? L_CSP : tid < 256 ? L_U - 192 * 16 : L_C - 256 * 16) + tid * 16, (v4u){v, v, v, v});
    }
    const int rot = (((slot >> 3) + slot) & 1) * 4;
    u32 sx = (512 * (P >> 2) + 128 * ((P >> 1) & 1) + 64 * (w & 1) + 8 * (P & 1) + 4 * (w >> 1)) * 1024, sw1 = 16 * w * 2752 + u0 * 64;
    u32 sx0 = rot * 128, sw0 = rot * (64 * 2752);
    u32 vx[4], vw[3];
    const int lim3 = 4 * ((((j + 1) * 43) >> 4) - u0);
#pragma unroll
    for (int i = 0; i < 3; i++) { const int p = 64 * i + lane, r = (p * 171) >> 11; vw[i] = opq(p - 12 * r >= lim3 ? 0x40000000u : r * (2752 - 192) + l16); }
#pragma unroll
    for (int i = 0; i < 4; i++) vx[i] = opq((((h * 256 + ((lane >> 3) & 3)) * 1024 + ((lane & 7) ^ g) * 16) ^ ((i & 1) * 64)) + i * (16384 - 1024));
    asm volatile("s_waitcnt lgkmcnt(0)\n\ts_barrier" : "+s"(ka), "+s"(kb4), "+s"(kc), "+s"(sx), "+s"(sw1), "+s"(sx0), "+s"(sw0) :: "memory");
    const u16 *x = (const u16*)ka[0], *logA = (const u16*)ka[1], *Aim = (const u16*)ka[2], *Cre = (const u16*)ka[3], *Cim = (const u16*)ka[4], *logdt = (const u16*)ka[5], *Dskip = (const u16*)ka[6], *w1 = (const u16*)ka[7];
    const u16 *b1 = (const u16*)kb4[0], *w2 = (const u16*)kb4[1], *b2 = (const u16*)kb4[2]; u16* out = (u16*)kb4[3]; char* ws = (char*)kc;
    const v4u rx = mkrsrc(x, 2 << 20);
    const u32 aF = opq(L_FLG + (lane & 3) * 4);
    auto flg = [&](auto R_) { flag_imm<L_FLG + decltype(R_)::value * 16>(rx, w4); };
    const v4u rw1 = mkrsrc(w1, 512 * 1376 * 2);
    auto ldX = [&](auto C_) { constexpr int c = decltype(C_)::value;
        const u32 so = glds_first<(16 + (c % 4) * 4) * 1024>(rx, vx[0], sx, c ? ((c + rot) & 7) * 128 : sx0, lw);
        glds_next<1024>(rx, vx[1], so); glds_next<2048>(rx, vx[2], so); glds_next<3072>(rx, vx[3], so); };
    auto ldW = [&](auto C_) { constexpr int c = decltype(C_)::value;
        const u32 so = glds_first<3 * (c % 5) * 1024>(rw1, vw[0], sw1, c ? ((c + rot) & 7) * (64 * 2752) : sw0, lw);
        glds_next<1024>(rw1, vw[1], so); glds_next<2048>(rw1, vw[2], so); };
    if (role == 0) {
        ldW(IC<0>{}); ldX(IC<0>{}); flg(IC<R_F>{});
        ldW(IC<1>{}); ldX(IC<1>{}); flg(IC<R_F + 1>{});
    }
    asm volatile("" ::: "memory");
    u32 aV0, aV1, aFw, aCn, one, f2o;
    u32 sh2[4], sh2t[4];
    auto pre2 = [&]() {
        aV0 = opq(lw + (4 * h + (l15 >> 2)) * 128 + ((l15 >> 3) & 1) * 64 + (g & 1) * 32 + (lane & 3) * 8); aV1 = opq(aV0 ^ 64);
        aFw = opq(L_FLG + w4); aCn = opq(L_CNT + w4); one = opq(1u);
        const int jj = 4 * w + (lane < 4 ? lane : 4);
        f2o = opq(((2 * tr + role) * 16 + (jj < 15 ? jj : 15)) * FS2 * 8);
#pragma unroll
        for (int k = 0; k < 4; k++) {
            sh2[k] = ((2 * tr + role) * 43 + 11 * w) * 2048 + (6 * rho - (k + rho >= 4 ? 22 : 0)) * 1024; sh2t[k] = (w == 3 && k + rho == 3) ? (u32)H_BYTES : sh2[k];
            asm volatile("" : "+s"(sh2[k]), "+s"(sh2t[k]));
        }
    };

    auto rdV = [&](int s, int dt) { v2u lo = lds_tr((dt ? aV1 : aV0) + w2loc(2 * s) * 1024), hi = lds_tr((dt ? aV1 : aV0) + w2loc(2 * s + 1) * 1024); return (v4u){lo.x, lo.y, hi.x, hi.y}; };
    auto gemm2 = [&](auto ROLE_, v4u (&stage)[16], u64 f2, u64 tag) __attribute__((always_inline)) {
        constexpr int role = decltype(ROLE_)::value;
        const GAS u64* f2p = (const GAS u64*)(ws + OFF_F2 + f2o);
        __builtin_amdgcn_s_setprio(0);
        const __amdgpu_buffer_rsrc_t rh = __builtin_amdgcn_make_buffer_rsrc(ws + OFF_H, 0, H_BYTES, 0x00020000);
        constexpr int WIN = role ? 10 : 8;
        v4u park[8][2], hq[WIN], vb[3][2];
        auto ldH = [&](int s) { hq[s % WIN] = __builtin_amdgcn_raw_buffer_load_b128(rh, l16, ((s + 2) % 6 >= 4 ? sh2t[(s + 2) / 6] : sh2[(s + 2) / 6]) + s * 1024, 16); };
        constexpr int W0 = 8;
        if (role) {
            SB0();
            spin_zero4<1>(aFw + R_V1 * 16);

            f2 = tag;
            asm volatile("s_mov_b64 exec, 31\n\tglobal_load_dwordx2 %0, %1, %2 sc1\n\ts_mov_b64 exec, -1" : "+v"(f2) : "v"(f2o), "s"(ws + OFF_F2) : "memory");
            SB0();
#pragma unroll
            for (int s = 0; s < 8; s++) { park[s][0] = rdV(s, 0); park[s][1] = rdV(s, 1); }
            clr<R_PK * 16>(aFw, opq(0u));
            asm volatile("s_waitcnt vmcnt(0)" : "+v"(f2) :: "memory");
            SB0();
        }

        if (!all_(f2 == tag)) poll_flags(f2p, tag, lane < 5);

#pragma unroll
        for (int s = 0; s < W0; s++) ldH(s);
        const int q3 = (lane >> 2) & 7, fr = 8 * wave + 4 * h + 2 * ((q3 ^ (q3 >> 1) ^ (q3 >> 2)) & 1) + (q3 >> 2), fo = ((q3 >> 1) & 1) * 4 + (lane & 3);
        const u32 fin_off = (512 * (tr >> 3) + 256 * ((fr >> 2) & 1) + 128 * ((tr >> 2) & 1) + 16 * (fr >> 3) + 4 * (tr & 3) + (fr & 3)) * 512 + tc * 64 + fo * 8;
        f32x4 s0, s1; float rs2; u32 da, ra, rb, rc;
        v4u fin_x, fin_b; u32 fin_s, fv;
        f32x16 ac2[2];
#pragma unroll
        for (int i = 0; i < 16; i++) { ac2[0][i] = 0.f; ac2[1][i] = 0.f; }
        const u32 aSt = opq(lw + l16), zero = opq(0u);
        if (!role) {
            SB0();
            spin_zero4(opq(aFw + ((lane & 1) ? R_PK : R_V2) * 16));

            vb[0][0] = rdV(0, 0); vb[0][1] = rdV(0, 1); vb[1][0] = rdV(1, 0); vb[1][1] = rdV(1, 1);
            asm volatile("" ::: "memory");
#pragma unroll
            for (int k = 0; k < 4; k++) lds_st<v4u>(aSt + k * 1024, stage[k]);
        }
#pragma unroll
        for (int s = 0; s < 22; s++) {
            SB0();
            if (role && (s == 4 || s == 10)) fv = lds_ldv<u32>(aFw + (s == 4 ? R_V2 : R_V3) * 16);
            if (role && (s == 6 || s == 12)) { if (__builtin_expect(any_(fv != 0), 0)) spin_zero4(aFw + (s == 6 ? R_V2 : R_V3) * 16); asm volatile("" ::: "memory"); }
            if ((!role || s + 2 >= 8) && s + 2 < 22) { vb[(s + 2) % 3][0] = rdV(s + 2, 0); vb[(s + 2) % 3][1] = rdV(s + 2, 1); }
            if (s == 19) sig<C_RD>(aCn, one);
            if (s == 21) fv = lds_ldv<u32>(aCn + 4 * C_RD);
            ac2[0] = mfma32(role && s < 8 ? park[s & 7][0] : vb[s % 3][0], hq[s % WIN], ac2[0]);
            ac2[1] = mfma32(role && s < 8 ? park[s & 7][1] : vb[s % 3][1], hq[s % WIN], ac2[1]);
            SB0();
            if (!role && s < 6) {
                asm volatile("" ::: "memory");
                lds_st<v4u>(aSt + (2 * s + 4) * 1024, stage[(2 * s + 4) & 15]); lds_st<v4u>(aSt + (2 * s + 5) * 1024, stage[(2 * s + 5) & 15]);
                if (s == 5) { clr<R_V3 * 16>(aFw, zero); }
            }
            if (s < WIN - W0) { ldH(W0 + 2 * s); ldH(W0 + 2 * s + 1); }
            else if (s + WIN < 22) ldH(s + WIN);
            if (s == 12) {
                fin_x = *(const GAS v4u*)(x + fin_off); fin_b = lds_ld<v4u>(L_B2 + fo * 16);
                fin_s = __hip_atomic_load((GAS u32*)(ws + OFF_ST + (tr >> 1) * 1024 + (fr >> 2) * 64 + (tr & 1) * 32 + (fr & 3) * 8), RLX_AGENT);
            }
            if (s == 16) { da = opq(lw + role * 20480 + l31 * 272 + h * 16); ra = opq(role * 20480 + (fr & 31) * 272 + fo * 32); rb = opq(ra + 65536); rc = opq(L_KB + fr * 144 + fo * 16); }
            if (s == 21) {
                s0 = (f32x4){bf_lo(fin_x.x) + bf_lo(fin_b.x), bf_hi(fin_x.x) + bf_hi(fin_b.x), bf_lo(fin_x.y) + bf_lo(fin_b.y), bf_hi(fin_x.y) + bf_hi(fin_b.y)};
                pin(s0);
            }
        }
        SB0();

        if (__builtin_expect(any_(fv < 2), 0)) spin(aCn + 4 * C_RD, 2);
        asm volatile("" ::: "memory");
#pragma unroll
        for (int dt = 0; dt < 2; dt++)
#pragma unroll
            for (int q = 0; q < 4; q++)
                lds_st<f32x4>(da + (32 * dt + 8 * q) * 4, (f32x4){ac2[dt][4 * q], ac2[dt][4 * q + 1], ac2[dt][4 * q + 2], ac2[dt][4 * q + 3]});
        SB0();
        s1 = (f32x4){bf_lo(fin_x.z) + bf_lo(fin_b.z), bf_hi(fin_x.z) + bf_hi(fin_b.z), bf_lo(fin_x.w) + bf_lo(fin_b.w), bf_hi(fin_x.w) + bf_hi(fin_b.w)};
        rs2 = __builtin_amdgcn_rcpf(bf_hi(fin_s)); rs2 *= rs2;
        asm volatile("" : "+v"(s0), "+v"(s1), "+v"(rs2));
        wait_lgkm0();
        __builtin_amdgcn_s_barrier();
        asm volatile("" ::: "memory");

        const v4u av = lds_ld<v4u>(rc);
        s0 += ((lds_ld<f32x4>(ra) + lds_ld<f32x4>(ra + 32768)) + (lds_ld<f32x4>(rb) + lds_ld<f32x4>(rb + 32768))) * rs2;
        s1 += ((lds_ld<f32x4>(ra + 16) + lds_ld<f32x4>(ra + 32768 + 16)) + (lds_ld<f32x4>(rb + 16) + lds_ld<f32x4>(rb + 32768 + 16))) * rs2;
        s0 += (f32x4){bf_lo(av.x), bf_hi(av.x), bf_lo(av.y), bf_hi(av.y)};
        s1 += (f32x4){bf_lo(av.z), bf_hi(av.z), bf_lo(av.w), bf_hi(av.w)};
        const v4u ov = {pk_bf(s0[0], s0[1]), pk_bf(s0[2], s0[3]), pk_bf(s1[0], s1[1]), pk_bf(s1[2], s1[3])};
        st_sc1(ov, mkrsrc(out, 2 << 20), fin_off * 2, 0);
    };

    if (__builtin_expect(role == 0, 1)) {
        v4u stage[16]; u64 f2, tag;
        const u32 sw2 = 352 * w * 1024 + tc * 128;
        const u32 vv = (lane >> 3) * 1024 + ((lane & 7) ^ ((g & 1) << 2)) * 16;
        u32 sv[4];
#pragma unroll
        for (int k = 0; k < 4; k++) sv[k] = sw2 + (12 * rho - (k + rho >= 4 ? 44 : 0)) * 8192;
        const __amdgpu_buffer_rsrc_t rv = __builtin_amdgcn_make_buffer_rsrc((void*)w2, 0, 512 * 1376 * 2, 0x00020000);
        auto ldV = [&](int r) { stage[r] = __builtin_amdgcn_raw_buffer_load_b128(rv, vv, sv[(r + 32) / 12] + (r + 28) * 8192, 0); };
        const GAS u64* f1p = (const GAS u64*)(ws + OFF_F1) + ((xcd >> 1) * 256 + 64 * w + lane) * FS1;
        sfor<6>([&](auto I) {
            constexpr int c = decltype(I)::value + 2;
            ldW(IC<c>{});
            if constexpr (c >= 4) {
                SB0();
#pragma unroll
                for (int k = 0; k < 3; k++) ldV(3 * (c - 4) + k);
                SB0();
            }
            if constexpr (c >= 4) spin_zero4<1>(aF + (R_P + c - 4) * 16);
            ldX(IC<c>{});
            flg(IC<R_F + c>{});
        });

        {
            const u32 vS = ((lane >> 1) * 4 + (lane & 1)) * 16;
            const v4u rst = mkrsrc(ws + OFF_ST, 16384);
            asm volatile("s_mov_b32 m0, %3\n\ts_mov_b64 exec, 0xffff\n\tbuffer_load_dwordx4 %0, %1, %2 offen sc1 lds\n\ts_mov_b64 exec, -1" :: "v"(vS), "s"(rst), "s"(P * 1024 + (w & 1) * 512 + (w >> 1) * 32), "s"(L_STAT + w * 256) : "memory");
        }
        spin_zero4<1>(aF + R_X * 16);
        tag = get_tag();
        u64 f1;
        asm volatile("global_load_dwordx2 %0, %1, off sc1" : "=&v"(f1) : "v"(f1p) : "memory");
#pragma unroll
        for (int n = 12; n < 16; n++) ldV(n);
        SB0();
        asm volatile("s_waitcnt vmcnt(4)" : "+v"(f1) :: "memory");
        if (!all_(f1 == tag)) poll_flags(f1p, tag, true);

        {
            const v4u rus = mkrsrc(ws + OFF_U, 2 << 20);
            const u32 vU = (lane >> 1) * 8192 + ((lane ^ g) & 1) * 16;
#pragma unroll
            for (int i = 0; i < 2; i++) glds_sc1(rus, vU, (64 * w + 32 * i) * 8192 + cp * 32, L_U + 1024 + w * 2048 + i * 1024);
            flag_dma(rus, L_UF + w * 4);
        }

        pre2();
#pragma unroll
        for (int n = 0; n < 16; n++) pin(stage[n]);
        {
            const u32 aK = opq((lane & 4) ? L_CNT + 4 * C_K + (lane & 1) * 4 : L_FLG + R_P7 * 16 + (lane & 3) * 4);
            const u32 want = opq((lane & 4) ? 2u : 0u);
            int k = 0;
            while (any_(lds_ldv<u32>(aK) != want) && ++k < (SPIN_CAP << 4)) { __builtin_amdgcn_s_sleep(1); }
            asm volatile("" ::: "memory");
        }

        const v4u rw2 = mkrsrc(w2, 512 * 1376 * 2);
        f2 = tag;
        sfor<28>([&](auto I) {
            constexpr int n = decltype(I)::value;
            if constexpr (n >= 6) wait_vm<6>();
            if constexpr (n == 26) asm volatile("s_mov_b64 exec, 31\n\tglobal_load_dwordx2 %0, %1, %2 sc1\n\ts_mov_b64 exec, -1" : "+v"(f2) : "v"(f2o), "s"(ws + OFF_F2) : "memory");
            glds_imm<w2loc(n) * 1024, n * 8192>(rw2, vv, sv[(n + 4) / 12], lw);
            if constexpr (n == 15) flg(IC<R_V1>{});
        });
        flg(IC<R_V2>{});
        asm volatile("s_waitcnt vmcnt(3)" : "+v"(f2) :: "memory");

        gemm2(IC<0>{}, stage, f2, tag);
    } else if (role == 1) {
        u64 tag;
        __builtin_amdgcn_s_setprio(2);
        warm_flag<FS2 * 8>(ws + OFF_F2, ((4 * P + w) * 16 + j) * FS2 * 8, l16);
        u32 aW[4], aX[4];
#pragma unroll
        for (int s = 0; s < 4; s++) {
            const int ks = (s + w) & 3;
            aW[s] = opq(ks * 32768 + (8 * h + (l15 >> 2)) * 192 + (g & 1) * 32 + (lane & 3) * 8);
            aX[s] = opq(lw + 16384 + l31 * 128 + (((2 * ks + h) ^ ((l31 >> 1) & 7)) * 16));
        }
        const u32 aC = opq(L_CSP + l31 * 32 + h * 16), aCw = opq(L_CSP + lane * 32 + w * 8), aS = opq(L_STAT + w * 256 + l31 * 8), aB = opq(L_B1 + lane * 2);
        pre2();
        const u32 zero = opq(0u);
        f32x16 acc[3]; f32x4 cs = {0.f, 0.f, 0.f, 0.f};
        v4u sel[3];
#pragma unroll
        for (int u = 0; u < 3; u++) {
            for (int i = 0; i < 16; i++) acc[u][i] = 0.f;
            const u32 sv = (l15 == (u < 2 ? 8 * u : 1) + 4 * (g & 1)) ? 0xBF80BF80u : 0u; sel[u] = (v4u){sv, sv, sv, sv}; pin(sel[u]);
        }
        v4u fa[3][3], fb[2][4];
        auto rdW = [&](int n, int u) { const u32 a = aW[n & 3] + ((n >> 2) % 5) * 3072 + u * 64; v2u lo = lds_tr(a), hi = lds_tr(a + 768); fa[n % 3][u] = (v4u){lo.x, lo.y, hi.x, hi.y}; };
        auto rdX = [&](int c, int s) { fb[c & 1][s] = lds_ld<v4u>(aX[s] + (c % 4) * 4096); };
        const v4u rhs = mkrsrc(ws + OFF_H, H_BYTES);
        const int nown = (((j + 1) * 43) >> 4) - u0;
        const u32 sh = ((4 * P + w) * 43 + u0) * 2048, shx = nown == 3 ? sh + 4096 : (u32)H_BYTES;
        spin_zero4(aF);

        rdX(0, 0);
#pragma unroll
        for (int u = 0; u < 3; u++) rdW(0, u);
        rdX(0, 1);
#pragma unroll
        for (int u = 0; u < 3; u++) rdW(1, u);
        rdX(0, 2); rdX(0, 3);
        u32 fl, fx, bb[2]; v2u ee; v2u stg; v4u fas[3], fbs;
        sfor<32>([&](auto N_) __attribute__((always_inline)) {
            constexpr int n = decltype(N_)::value, c = n >> 2, s = n & 3;
            SB0();
            if constexpr (s == 2 && c < 5) {
                pin(fa[(n + 1) % 3][2]);
                flg(IC<R_P + c>{});
            }
            if (s == 2 && c < 7) {
                if (__builtin_expect(any_(fl != 0), 0)) spin_zero4(aF + (R_F + c + 1) * 16);
                asm volatile("" ::: "memory");
            }
            sfor<3>([&](auto U_) __attribute__((always_inline)) {
                constexpr int u = decltype(U_)::value;
                acc[u] = mfma32(fa[n % 3][u], fb[c & 1][s], acc[u]);
                if (s == 0 && c < 7) cs = mfma16(sel[u], fa[n % 3][u], cs);
                if (n == 27) cs = mfma16(sel[u], fa[28 % 3][u], cs);
                SB0();
                if (s >= 2 && c < 7 && u < 2) rdX(c + 1, 2 * (s - 2) + u);
                if (n + 2 < 32) rdW(n + 2, u);
                if (s == 1 && u == 0 && c < 7) fl = lds_ldv<u32>(aF + (R_F + c + 1) * 16);
                if (n == 25 && u == 2) { bb[0] = lds_ld<u16>(aB); bb[1] = lds_ld<u16>(aB + 128); }
                if (n == 21 && u == 1) fx = lds_ldv<u32>(aF + R_X * 16);
                if (n == 23 && u == 1) { if (__builtin_expect(any_(fx != 0), 0)) spin_zero4(aF + R_X * 16); asm volatile("" ::: "memory"); ee = lds_ldv<v2u>(L_E); }
                if (n == 28 && u == 1) {
                    v2u t[2];
#pragma unroll
                    for (int k = 0; k < 2; k++) {
                        const float d = cs[k] - bf_hi(f2u(cs[k]));
                        t[k] = (v2u){__builtin_amdgcn_perm(f2u(d), f2u(cs[k]), 0x07060302u), pk_bf(d - bf_hi(f2u(d)), bf_lo(bb[k]))};
                    }
                    asm volatile("ds_write2st64_b64 %0, %1, %2 offset1:4" :: "v"(aCw), "v"(t[0]), "v"(t[1]) : "memory");
                }
                if (n == 31 && u == 0) { fas[0] = lds_ldv<v4u>(aC); fas[1] = lds_ldv<v4u>(aC + 1024); fas[2] = lds_ldv<v4u>(aC + 2048); }
                if (n == 30 && u == 0) stg = lds_ldv<v2u>(aS);
                if (n == 30 && u == 1) { tag = mix64(((u64)(u32)__builtin_amdgcn_readfirstlane(ee.y) << 32) | (u32)__builtin_amdgcn_readfirstlane(ee.x)); asm volatile("" : "+s"(tag)); }
                SB0();
            });
        });
        {
            auto csbad = [&]() { return umax(umax(umax(fas[0].x, fas[0].z), umax(fas[1].x, fas[1].z)), umax(fas[2].x, fas[2].z)) == ~0u; };
            const u32 want = (lane & 1) ? (u32)(tag >> 32) : (u32)tag;
            if (__builtin_expect(any_(csbad() || stg.y != want), 0)) {
                int k = 0;
                while (any_(csbad()) && ++k < (SPIN_CAP << 2)) { fas[0] = lds_ldv<v4u>(aC); fas[1] = lds_ldv<v4u>(aC + 1024); fas[2] = lds_ldv<v4u>(aC + 2048); }
                k = 0;
                const GAS u64* sp = (const GAS u64*)(ws + OFF_ST + P * 1024 + (w & 1) * 512 + (w >> 1) * 32 + (l31 >> 2) * 64 + (lane & 3) * 8);
                while (any_(stg.y != want) && ++k < SPIN_CAP) { const u64 gr = __hip_atomic_load(sp, RLX_AGENT); stg = (v2u){(u32)gr, (u32)(gr >> 32)}; }
            }
            const u32 m1 = stg.x & 0xffffu, m2 = m1 * 0x10001u;
            fbs = (v4u){m2, h ? m1 : stg.x, m2, m1};
        }
#pragma unroll
        for (int u = 0; u < 3; u++) acc[u] = mfma32(fas[u], fbs, acc[u]);

        sfor<6>([&](auto K_) __attribute__((always_inline)) {
            constexpr int u = decltype(K_)::value >> 1, s = decltype(K_)::value & 1;
            v4u o;
#pragma unroll
            for (int e = 0; e < 4; e++) {
                const float a0 = acc[u][8 * s + 2 * e], a1 = acc[u][8 * s + 2 * e + 1];
                o[e] = relu_pk(pk_bf(a0 * __builtin_fabsf(a0), a1 * __builtin_fabsf(a1)));
            }
            st_sc1(o, rhs, l16, (u < 2 ? sh + u * 2048 : shx) + s * 1024);
            SB0();
        });

        clr<R_P7 * 16>(aFw, zero);
        wait_vm<0>();

        put_flag(ws + OFF_F2, ((4 * P + w) * 16 + j) * FS2 * 8, l16, tag);
        v4u none[16];
        gemm2(IC<1>{}, none, ~tag, tag);
    } else {
        u64 tag;
        __builtin_amdgcn_s_setprio(3);
        glds(rx, l16, (8 * tb + 2 * w) * 1024, L_KB + (2 * w) * 1088);
        glds(rx, l16, (8 * tb + 2 * w + 1) * 1024, L_KB + (2 * w + 1) * 1088);
        if (w == 0) {
            const v4u re = mkrsrc(ws, 16);
            asm volatile("s_mov_b32 m0, %1\n\ts_mov_b64 exec, 1\n\tbuffer_load_dwordx4 off, %0, 0 sc1 lds\n\ts_mov_b64 exec, -1" :: "s"(re), "s"(L_E) : "memory");
        } else if (w == 2) {
            const v4u rb2 = mkrsrc(b2, 1024);
            asm volatile("s_mov_b32 m0, %3\n\ts_mov_b64 exec, 0xff\n\tbuffer_load_dwordx4 %0, %1, %2 offen lds\n\ts_mov_b64 exec, -1" :: "v"(l16), "s"(rb2), "s"(tc * 128), "s"(L_B2) : "memory");
        } else if (w == 1) {
            const v4u rb1 = mkrsrc(b1, 1376 * 2);
            asm volatile("s_mov_b32 m0, %3\n\ts_nop 0\n\tbuffer_load_dword %0, %1, %2 offen lds" :: "v"(lane * 4), "s"(rb1), "s"(u0 * 64), "s"(L_B1) : "memory");
        }
        const u32 pi = opq((hf * 64 + lane) * 2);
        u32 pA = ldp((const GAS u16*)((const GAS char*)(logA + ch * 128) + pi)), pI = ldp((const GAS u16*)((const GAS char*)(Aim + ch * 128) + pi));
        u32 pR = ldp((const GAS u16*)((const GAS char*)(Cre + ch * 128) + pi)), pC = ldp((const GAS u16*)((const GAS char*)(Cim + ch * 128) + pi));
        u32 pT = *(const GAS u16*)(logdt + ch), pD = *(const GAS u16*)(Dskip + ch);
        flg(IC<R_X>{});
        one = opq(1u);
        const u32 aCnt = opq(L_CNT + chl * 4), aCtl = opq(L_C);
        const v4u rus = mkrsrc(ws + OFF_U, 2 << 20);
        v4u selLo, selHi;
#pragma unroll
        for (int e = 0; e < 4; e++) {
            const u32 o1 = (h == (g & 1) && ((l15 & 7) >> 1) == e) ? ((l15 & 1) ? 0x3C000000u : 0x00003C00u) : 0u;
            selLo[e] = l15 < 8 ? o1 : 0u; selHi[e] = l15 < 8 ? 0u : o1;
        }
        pin(selLo); pin(selHi);
        const int m = 64 * hf + lane;
        const u32 aT = opq(w * 32768 + 16384 + l15 * 64 + g * 16), t0 = opq((2 * chl) * 32768 + 16384 + m * 4), kr = opq(L_KB + chl * 8320 + (1016 - 8 * m) * 2);
        u32 tg[6];
        {
            const bool r1 = m < 32, r2 = m < 64;
            tg[0] = opq(t0 + (r1 ? 528 : 0) * 4); tg[1] = opq(t0 + (r2 ? 400 : 0) * 4); tg[2] = opq(t0 + (r1 ? 560 : 272) * 4);
            tg[3] = opq(t0 + (r1 ? 592 : 272) * 4); tg[4] = opq(t0 + (r2 ? 464 : 1) * 4); tg[5] = opq(t0 + (r1 ? 624 : 1) * 4);
        }
        u32 kb[4], ua[4];
#pragma unroll
        for (int i = 0; i < 4; i++) {
            kb[i] = opq(L_KB + chl * 8320 + ((l15 & 1) ? (239 - l15 + 8 * g) * 2 : 4160 + (240 - l15 + 8 * g) * 2) - hf * 256 - 192 + i * 1024);
            const int os = 30 + 2 * l15 + g - 16 * hf - 4 * i;
            ua[i] = opq(L_U + os * 32 + ((chl ^ (os >> 3)) & 1) * 16);
        }
        const u32 aZ3 = opq((lane & 4) ? L_CNT + 4 * C_K + chl * 4 : L_UF + (lane & 3) * 4), want3 = opq((lane & 4) ? 2u : 0u), aZ2 = opq(L_C + (lane & 3) * 4 + ((lane & 8) ? R_LN : (lane & 4) ? R_P + 4 : R_F + 7) * 16);
        const u32 vA = opq((lane >> 3) * 4096 + (lane & 7) * 16);
        pin(pA); pin(pI); pin(pR); pin(pC); pin(pT); pin(pD);
        {
            const u32 aRow = opq(L_KB + (l15 & 7) * 1088 + (l15 >> 3) * 512 + g * 16), aTr = opq(L_KB + (l15 >> 2) * 1088 + (128 * w + 16 * g + 4 * (lane & 3)) * 2);
            v4u ones = {0x3F803F80u, 0x3F803F80u, 0x3F803F80u, 0x3F803F80u}; pin(ones);
            spin_zero4(aF + R_X * 16);

            v4u xb[8]; v2u xt[2][2];
#pragma unroll
            for (int s = 0; s < 8; s++) xb[s] = lds_ld<v4u>(aRow + s * 64);
#pragma unroll
            for (int i = 0; i < 2; i++) { xt[i][0] = lds_tr(aTr + i * 128); xt[i][1] = lds_tr(aTr + i * 128 + 4 * 1088); }
            tag = get_tag();
            f32x4 sm = {0.f, 0.f, 0.f, 0.f}, sq = {0.f, 0.f, 0.f, 0.f};
#pragma unroll
            for (int s = 0; s < 8; s++) { sq = mfma16(xb[s], xb[s], sq); sm = mfma16(ones, xb[s], sm); }
            float q = (l15 & 2) ? ((l15 & 1) ? sq[3] : sq[2]) : ((l15 & 1) ? sq[1] : sq[0]);
            q = g == (l15 >> 2) ? q : 0.f;
            { const auto r = __builtin_amdgcn_permlane32_swap(f2u(q), f2u(q), false, false); q = u2f(r[0]) + u2f(r[1]); }
            { const auto r = __builtin_amdgcn_permlane16_swap(f2u(q), f2u(q), false, false); q = u2f(r[0]) + u2f(r[1]); }
            const float mean = dpp_add<0x128>(sm[0]) * (1.f / 512.f);
            const float var = fmaxf(__builtin_fmaf(-mean, mean, dpp_add<0x128>(q) * (1.f / 512.f)), 0.f);
            const u32 p = pk_bf(mean, __builtin_amdgcn_sqrtf(rbf(rbf(var) + 1e-5f)));
            const float mb = bf_lo(p), rr = __builtin_amdgcn_rcpf(bf_hi(p));
            if (w == 0 && lane < 8) __hip_atomic_store((GAS u64*)(ws + OFF_ST + (P * 16 + j) * 64) + lane, ((u64)((lane & 1) ? (u32)(tag >> 32) : (u32)tag) << 32) | p, RLX_AGENT);
            sfor<2>([&](auto I_) {
                constexpr int i = decltype(I_)::value;
                v4u o;
                sfor<4>([&](auto E_) {
                    constexpr int e = decltype(E_)::value;
                    const u32 xv = xt[i][e >> 1][e & 1];
                    const u32 d = pk_bf(bf_lo(xv) - bcast<2 * e>(mb), bf_hi(xv) - bcast<2 * e + 1>(mb));
                    o[e] = pk_bf(bf_lo(d) * bcast<2 * e>(rr), bf_hi(d) * bcast<2 * e + 1>(rr));
                });
                st_sc1(o, rus, l16, tb * 8192 + w * 2048 + i * 1024);
            });
        }
        flg(IC<R_LN>{});

        __builtin_amdgcn_s_setprio(1);
        f32x4 kt[3];
        {
            const float dt = rbf(expf(bf_lo(pT)));
            const float zr = -rbf(expf(bf_lo(pA))) * dt, zi = bf_lo(pI) * dt;
            const float lr = zr * 1.44269504088896341f;
            const float th = zi * 0.159154943091895336f, tl = __builtin_fmaf(zi, 0.159154943091895336f, -th) + zi * 6.4206383e-09f;
            const cmult w1c = mkc(cexp_pow(lr, -th, -tl, 1.f)), qc = mkc(cexp_pow(lr, -th, -tl, 128.f)), q2 = mkc(cexp_pow(lr, -th, -tl, 256.f));
            const cmult w8 = mkc(cexp_pow(lr, th, tl, 8.f)), w32 = mkc(cexp_pow(lr, th, tl, 32.f));
            const u32 aR = pR & 0x7fffu, aI = pC & 0x7fffu;
            int ex = (int)(wave_max(aR > aI ? aR : aI) >> 7); ex = ex < 12 ? 12 : (ex > 240 ? 240 : ex);
            const float sdn = u2f((u32)(262 - ex) << 23), sup = u2f((u32)(ex - 8) << 23);
            auto transpose = [&](const f32x2 (&v)[16], v4u (&f)[4]) {
                f32x16 z; for (int i = 0; i < 16; i++) z[i] = 0.f;
                f32x16 dr = mfma32h((v4u){pk_h2(v[0].x, v[1].x), pk_h2(v[2].x, v[3].x), pk_h2(v[4].x, v[5].x), pk_h2(v[6].x, v[7].x)}, selLo, z);
                f32x16 di = mfma32h((v4u){pk_h2(v[0].y, v[1].y), pk_h2(v[2].y, v[3].y), pk_h2(v[4].y, v[5].y), pk_h2(v[6].y, v[7].y)}, selLo, z);
                dr = mfma32h((v4u){pk_h2(v[8].x, v[9].x), pk_h2(v[10].x, v[11].x), pk_h2(v[12].x, v[13].x), pk_h2(v[14].x, v[15].x)}, selHi, dr);
                di = mfma32h((v4u){pk_h2(v[8].y, v[9].y), pk_h2(v[10].y, v[11].y), pk_h2(v[12].y, v[13].y), pk_h2(v[14].y, v[15].y)}, selHi, di);
#pragma unroll
                for (int e = 0; e < 4; e++) {
                    f[0][e] = pk_h2(dr[2 * e], dr[2 * e + 1]); f[1][e] = pk_h2(dr[8 + 2 * e], dr[9 + 2 * e]);
                    f[2][e] = pk_h2(di[2 * e], di[2 * e + 1]); f[3][e] = pk_h2(di[8 + 2 * e], di[9 + 2 * e]);
                }
            };
            { f32x2 t0_ = w1c.b, t1_ = qc.b, t2_ = q2.b, t3_ = w8.b, t4_ = w32.b; pin(t0_); pin(t1_); pin(t2_); pin(t3_); pin(t4_); }

            wait_vm<0>();
            put_flag(ws + OFF_F1, (w * 256 + tb) * FS1 * 8, l16, tag);
            warm_flag<FS3 * 8>(ws + OFF_F3, (hf * 512 + ch) * FS3 * 8, l16);

            v4u fy[4], fx[4];
            f32x2 v[16], xo[8];
            v[0] = (f32x2){1.f, 0.f}; v[1] = w8.b; v[2] = cmul(w8.b, w8); v[3] = cmul(v[2], w8); v[4] = w32.b;
            xo[0] = (f32x2){bf_lo(pR) * sdn, -bf_lo(pC) * sdn};
#pragma unroll
            for (int i = 5; i < 16; i++) { v[i] = cmul(v[i - 4], w32); if (i < 12) xo[i - 4] = cmul(xo[i - 5], w1c); }

            transpose(v, fy);
            const float fin = 2.f * dt * sup;
            auto tile = [&](int t) {
                transpose(v, fx);
                f32x4 d = {0.f, 0.f, 0.f, 0.f};
#pragma unroll
                for (int s = 0; s < 4; s++) d = mfma16h(fy[s], fx[s], d);
                kt[t] = d * fin;
            };
            v[0] = xo[0]; v[1] = cmul(xo[0], qc);
#pragma unroll
            for (int s = 2; s < 16; s++) v[s] = cmul(v[s - 2], q2);
            const f32x2 last = cmul(v[14], q2);
            tile(0);
            v[0] = last; v[1] = xo[4]; v[9] = xo[2]; v[13] = xo[6];
            v[2] = cmul(v[1], qc); v[10] = cmul(v[9], qc); v[14] = cmul(v[13], qc);
#pragma unroll
            for (int s = 3; s < 9; s++) v[s] = cmul(v[s - 2], q2);
            v[11] = cmul(v[9], q2); v[12] = cmul(v[10], q2); v[15] = cmul(v[13], q2);
            const f32x2 last2 = cmul(v[14], q2);
            tile(1);
            v[0] = last2;
#pragma unroll
            for (int i = 0; i < 4; i++) { v[1 + 2 * i] = xo[2 * i + 1]; v[2 + 2 * i] = cmul(xo[2 * i + 1], qc); }
#pragma unroll
            for (int i = 9; i < 16; i++) v[i] = (f32x2){0.f, 0.f};
            tile(2);
        }

        pin(kt[0]); pin(kt[1]); pin(kt[2]);
        spin_zero4<1>(aZ2);

#pragma unroll
        for (int t = 0; t < 3; t++) lds_st<f32x4>(aT + t * 1024, kt[t]);
        sig<C_A>(aCnt, one);

        spin(aCnt + 4 * C_A, 2);
        {
            auto tv = [&](u32 a) { return lds_ld<float>(a) + lds_ld<float>(a + 32768); };
            float k[9];
            k[0] = tv(t0); k[8] = tv(t0 + 4); k[4] = tv(t0 + 272 * 4);
            k[1] = tv(tg[0]); k[2] = tv(tg[1]); k[3] = tv(tg[2]); k[5] = tv(tg[3]); k[6] = tv(tg[4]); k[7] = tv(tg[5]);
            const float va = tv(t0 + 512), vb = tv(t0 + 516);
            if (m == 0) k[0] += bf_lo(pD);
            const u32 k10 = pk_bf(k[1], k[0]);
            lds_st<v4u>(kr + 2048, (v4u){pk_bf(k[7], k[6]), pk_bf(k[5], k[4]), pk_bf(k[3], k[2]), k10});
            lds_st<v4u>(kr + 2048 + 4160, (v4u){pk_bf(k[8], k[7]), pk_bf(k[6], k[5]), pk_bf(k[4], k[3]), pk_bf(k[2], k[1])});
            const u32 pa = pk_bf(va, va), pb = pk_bf(vb, vb), pba = pk_bf(vb, va);
            lds_st<v4u>(kr, (v4u){pb, (m & 1) ? pb : pba, pa, pa});
            lds_st<v4u>(kr + 4160, (v4u){pb, pb, (m & 1) ? pba : pa, pa});
            if (m < 4) lds_st<v4u>(L_KB + chl * 8320 + 4096 + (m & 1) * 16 + (m < 2 ? 4160 : 0), (v4u){m == 0 ? k10 >> 16 : 0u, 0u, 0u, 0u});
        }
        sig<C_K>(aCnt, one);

        f32x4 cv[8];
        {
#pragma unroll
            for (int t = 0; t < 8; t++) cv[t] = (f32x4){0.f, 0.f, 0.f, 0.f};
            v4u A[8], B[8];
            auto rdA = [&](int r, int a) { const u32 p = kb[(7 - a) >> 1] + ((7 - a) & 1) * 512 + (3 - r) * 64; return (v4u){lds_ld<u32>(p), lds_ld<u32>(p + 4), lds_ld<u32>(p + 8), lds_ld<u32>(p + 12)}; };
            auto rdB = [&](int r, int d) { return lds_ld<v4u>(ua[r] + d * 1024); };
            {
                int k = 0;
                while (any_(lds_ldv<u32>(aZ3) != want3) && ++k < (SPIN_CAP << 4)) {}
                asm volatile("" ::: "memory");
            }

#pragma unroll
            for (int i = 0; i < 8; i++) { A[7 - i] = rdA(0, 7 - i); B[i] = rdB(0, i); }
            if (b == 0 && w == 0 && lane == 0) __hip_atomic_store((GAS u64*)ws, tag, RLX_AGENT);

#pragma unroll
            for (int r = 0; r < 4; r++) {
#pragma unroll
                for (int a = 7; a >= 0; a--) {
#pragma unroll
                    for (int d = 0; d + a < 8; d++) {
                        cv[a + d] = mfma16(A[a], B[d], cv[a + d]);
                        if (a == 0 && r < 3) { SB0(); B[d] = rdB(r + 1, d); SB0(); }
                    }
                    if (r < 3) { SB0(); A[a] = rdA(r + 1, a); SB0(); }
                }
            }
        }

        {
            const u32 mine = opq(w * 32768 + 16384 + l16), other = opq((w ^ 1) * 32768 + 16384 + l16);
#pragma unroll
            for (int t = 0; t < 4; t++) lds_st<f32x4>(mine + t * 1024, sel4(hf, cv[t], cv[4 + t]));
            sig<C_E>(aCnt, one);
            sig<C_D>(aCtl + (L_CNT - L_C), one);
            spin(aCnt + 4 * C_E, 2);
            u32 o[8];
#pragma unroll
            for (int t = 0; t < 4; t++) {
                const f32x4 y = sel4(hf, cv[4 + t], cv[t]) + lds_ld<f32x4>(other + t * 1024);
                o[2 * t] = pk_bf(y[0], y[1]); o[2 * t + 1] = pk_bf(y[2], y[3]);
            }
            const v4u ras = mkrsrc(ws + OFF_A, 2 << 20);
            st_sc1((v4u){o[0], o[1], o[2], o[3]}, ras, l16, ch * 4096 + hf * 2048);
            st_sc1((v4u){o[4], o[5], o[6], o[7]}, ras, l16, ch * 4096 + hf * 2048 + 1024);

            wait_vm<0>();

            put_flag(ws + OFF_F3, (hf * 512 + ch) * FS3 * 8, l16, tag);
            spin(aCtl + (L_CNT - L_C) + 4 * C_D, 4);
            __builtin_amdgcn_s_sleep(30);
            poll_flags((const GAS u64*)(ws + OFF_F3) + ((tr >> 4) * 512 + tc * 64 + 16 * w + l15) * FS3, tag, lane < 16);

#pragma unroll
            for (int i = 0; i < 2; i++)
                glds_sc1(ras, vA, (tc * 64 + 16 * w + 8 * i) * 4096 + (tr >> 3) * 1024 + (tr & 3) * 256 + ((tr >> 2) & 1) * 128, L_U + w * 2048 + i * 1024);
            const u32 ta = opq(L_U + w * 2048 + (l15 >> 2) * 128 + g * 32 + (lane & 3) * 8), aw = opq(L_KB + lane * 144 + w * 32);
            wait_vm<0>();
            const v2u a0 = lds_tr(ta), a1 = lds_tr(ta + 512), a2 = lds_tr(ta + 1024), a3 = lds_tr(ta + 1536);
            lds_st<v4u>(aw, (v4u){a0.x, a0.y, a1.x, a1.y});
            lds_st<v4u>(aw + 16, (v4u){a2.x, a2.y, a3.x, a3.y});

        }
        wait_lgkm0();
        __builtin_amdgcn_s_barrier();
    }
}

#if defined(__HIP_DEVICE_COMPILE__) && __HIP_DEVICE_COMPILE__
#pragma clang attribute pop
#endif

extern "C" void kernel_launch(void* const* d_in, const int* in_sizes, int n_in, void* d_out, int out_size, void* d_ws, size_t ws_size,
                              hipStream_t stream) {
    fused_kernel<<<256, 768, 0, stream>>>((const u16*)d_in[0], (const u16*)d_in[1], (const u16*)d_in[2], (const u16*)d_in[3], (const u16*)d_in[4],
                                          (const u16*)d_in[5], (const u16*)d_in[6], (const u16*)d_in[8], (const u16*)d_in[9], (const u16*)d_in[10],
                                          (const u16*)d_in[11], (u16*)d_out, (char*)d_ws);
}
```

```cpp
#define ROT 0
#include <hip/hip_runtime.h>
#include <stdint.h>
#include <utility>

typedef unsigned short u16;
typedef unsigned int u32;
typedef unsigned long long u64;
typedef u32 v4u __attribute__((ext_vector_type(4)));
typedef u32 v2u __attribute__((ext_vector_type(2)));
typedef float f32x2 __attribute__((ext_vector_type(2)));
typedef float f32x4 __attribute__((ext_vector_type(4)));
typedef float f32x16 __attribute__((ext_vector_type(16)));
typedef __bf16 bf16x8 __attribute__((ext_vector_type(8)));
typedef __bf16 bf16x2 __attribute__((ext_vector_type(2)));
typedef _Float16 f16x8 __attribute__((ext_vector_type(8)));
typedef _Float16 f16x2 __attribute__((ext_vector_type(2)));
typedef short s16x4 __attribute__((ext_vector_type(4)));
typedef u64 u64x8 __attribute__((ext_vector_type(8)));
typedef u64 u64x4 __attribute__((ext_vector_type(4)));
#define GAS __attribute__((address_space(1)))
#define LAS __attribute__((address_space(3)))
#define DI __device__ __forceinline__
#define RLX_AGENT __ATOMIC_RELAXED, __HIP_MEMORY_SCOPE_AGENT
#define RLX_WG __ATOMIC_RELAXED, __HIP_MEMORY_SCOPE_WORKGROUP
#define SB0() __builtin_amdgcn_sched_barrier(0)
#define NWV 12
#define MPF 0
#define RF 0

constexpr int OFF_ST = 16384, OFF_F1 = 128 << 10, OFF_F2 = 256 << 10, OFF_F3 = 512 << 10;
constexpr int FS1 = 2, FS2 = 16, FS3 = 16;
constexpr int OFF_U = 1 << 20, OFF_A = 4 << 20, OFF_H = 8 << 20;
constexpr int H_BYTES = 64 * 43 * 2048;
constexpr int L_C = 131072;
constexpr int L_FLG = L_C, L_UF = L_C + 320, L_E = L_C + 768, L_B2 = L_C + 896, L_CNT = L_C + 416, L_B1 = L_C + 512, L_STAT = L_C + 1024, L_CSP = L_C + 29696;
enum { R_F = 0, R_P = 8, R_P7 = 13, R_PK = 15, R_LN = 19, R_X = 14, R_V1 = 16, R_V2 = 17, R_V3 = 18 };
constexpr int L_U = L_C + 3072, L_KB = L_C + 12288;
constexpr int SPIN_CAP = 1 << 16;
constexpr int CODE_BYTES = 31488;
enum { C_A = 0, C_K = 2, C_E = 4, C_D = 8, C_RD = 12 };

#define STAMP(i)
#define CST(i)
#define TIMED(x) x

#if defined(__HIP_DEVICE_COMPILE__) && __HIP_DEVICE_COMPILE__
#pragma clang attribute push(__attribute__((target("no-packed-fp32-ops"))), apply_to = function)
#endif
DI float u2f(u32 v) { return __builtin_bit_cast(float, v); }
DI u32 f2u(float v) { return __builtin_bit_cast(u32, v); }
DI bool any_(bool c) { return __builtin_amdgcn_ballot_w64(c) != 0; }
DI bool all_(bool c) { return __builtin_amdgcn_ballot_w64(!c) == 0; }
template <typename T> DI T lds_ld(u32 a) { return *(LAS T*)(size_t)a; }
template <typename T> DI void lds_st(u32 a, T v) { *(LAS T*)(size_t)a = v; }
DI v2u lds_tr(u32 a) { return __builtin_bit_cast(v2u, __builtin_amdgcn_ds_read_tr16_b64_v4i16((LAS s16x4*)(size_t)a)); }
DI v4u mkrsrc(const void* p, u32 bytes) { u64 a = (u64)p; v4u r; r.x = (u32)a; r.y = (u32)(a >> 32) & 0xffffu; r.z = bytes; r.w = 0x00020000u; return r; }
DI void glds(v4u rsrc, u32 voff, u32 soff, u32 lds) {
    asm volatile("s_mov_b32 m0, %3\n\ts_nop 0\n\tbuffer_load_dwordx4 %0, %1, %2 offen lds" :: "v"(voff), "s"(rsrc), "s"(soff), "s"(lds) : "memory");
}
template <int LOFF> DI u32 glds_first(v4u rsrc, u32 voff, u32 sa, u32 sb, u32 lbase) {
    u32 so;
    asm volatile("s_add_u32 m0, %5, %6\n\ts_add_u32 %0, %3, %4\n\tbuffer_load_dwordx4 %1, %2, %0 offen lds" : "=&s"(so) : "v"(voff), "s"(rsrc), "s"(sa), "s"(sb), "s"(lbase), "i"(LOFF) : "memory", "scc");
    return so;
}
template <int LOFF, int SOFF> DI void glds_imm(v4u rsrc, u32 voff, u32 sa, u32 lbase) {
    u32 so;
    asm volatile("s_add_u32 m0, %4, %5\n\ts_add_u32 %0, %3, %6\n\tbuffer_load_dwordx4 %1, %2, %0 offen lds" : "=&s"(so) : "v"(voff), "s"(rsrc), "s"(sa), "s"(lbase), "i"(LOFF), "i"(SOFF) : "memory", "scc");
}
template <int OFF> DI void glds_next(v4u rsrc, u32 voff, u32 so) {
    asm volatile("buffer_load_dwordx4 %0, %1, %2 offen offset:%3 lds" :: "v"(voff), "s"(rsrc), "s"(so), "i"(OFF) : "memory");
}
template <int LOFF> DI void flag_imm(v4u rsrc, u32 lbase) {
    asm volatile("s_add_u32 m0, %2, %3\n\ts_mov_b64 exec, 1\n\tbuffer_load_dword off, %0, %1 lds\n\ts_mov_b64 exec, -1" :: "s"(rsrc), "s"(rsrc.z), "s"(lbase), "i"(LOFF) : "memory", "scc");
}
DI void glds_sc1(v4u rsrc, u32 voff, u32 soff, u32 lds) {
    asm volatile("s_mov_b32 m0, %3\n\ts_nop 0\n\tbuffer_load_dwordx4 %0, %1, %2 offen sc1 lds" :: "v"(voff), "s"(rsrc), "s"(soff), "s"(lds) : "memory");
}
DI void flag_dma(v4u rsrc, u32 lds) {
    asm volatile("s_mov_b32 m0, %2\n\ts_mov_b64 exec, 1\n\tbuffer_load_dword off, %0, %1 lds\n\ts_mov_b64 exec, -1" :: "s"(rsrc), "s"(rsrc.z), "s"(lds) : "memory");
}
DI void st_sc1(v4u v, v4u rsrc, u32 voff, u32 soff) {
    asm volatile("buffer_store_dwordx4 %0, %1, %2, %3 offen sc1\n\ts_nop 1" :: "v"(v), "v"(voff), "s"(rsrc), "s"(soff) : "memory");
}
DI void put_flag(const char* base, u32 off, u32 l16, u64 tag) {
    asm volatile("s_mov_b64 exec, 1\n\tglobal_store_dwordx2 %0, %1, %2 sc1\n\ts_mov_b64 exec, -1" :: "v"(l16), "v"(tag), "s"(base + off) : "memory");
}
template <int BYTES> DI void warm_flag(const char* base, u32 off, u32 l16) {
    const v4u v = {0u, 0u, 0u, 0u};
    asm volatile("s_mov_b64 exec, %3\n\tglobal_store_dwordx4 %0, %1, %2 sc1\n\ts_mov_b64 exec, -1\n\ts_nop 0" :: "v"(l16), "v"(v), "s"(base + off), "i"((1 << (BYTES / 16)) - 1) : "memory");
}
DI u32 ldp(const GAS u16* p) { return __builtin_nontemporal_load(p); }
DI u32 opq(u32 a) { asm volatile("" : "+v"(a)); return a; }
template <typename T> DI T lds_ldv(u32 a) { return *(volatile LAS T*)(size_t)a; }
template <int SLEEP = 0> DI void spin_zero4(u32 a) {
    int n = 0;
    while (any_(lds_ldv<u32>(a) != 0) && ++n < (SPIN_CAP << 4)) { if (SLEEP) __builtin_amdgcn_s_sleep(1); }
    asm volatile("" ::: "memory");
}
template <int N> DI void wait_vm() { asm volatile("s_waitcnt vmcnt(%0)" :: "i"(N) : "memory"); }
DI void wait_lgkm0() { asm volatile("s_waitcnt lgkmcnt(0)" ::: "memory"); }
DI u64 mix64(u64 e) { e += 0x9E3779B97F4A7C15ull; e ^= e >> 31; e *= 0xBF58476D1CE4E5B9ull; e ^= e >> 29; return e; }
DI u64 get_tag() { const v2u e = lds_ldv<v2u>(L_E); return mix64(((u64)(u32)__builtin_amdgcn_readfirstlane(e.y) << 32) | (u32)__builtin_amdgcn_readfirstlane(e.x)); }
template <int C> DI void sig(u32 a, u32 one) { asm volatile("s_mov_b64 exec, 1\n\tds_add_u32 %0, %1 offset:%2\n\ts_mov_b64 exec, -1" :: "v"(a), "v"(one), "i"(4 * C) : "memory"); }
template <int OFF> DI void clr(u32 a, u32 zero) { asm volatile("s_mov_b64 exec, 1\n\tds_write_b32 %0, %1 offset:%2\n\ts_mov_b64 exec, -1" :: "v"(a), "v"(zero), "i"(OFF) : "memory"); }
DI void spin(u32 a, u32 target) {
    int n = 0;
    while ((u32)__builtin_amdgcn_readfirstlane(lds_ldv<u32>(a)) < target && ++n < (SPIN_CAP << 4)) {}
    asm volatile("" ::: "memory");
}
DI u64 peek_flags(const GAS u64* p, u64 tag, bool on) { u64 f = tag; if (on) f = __hip_atomic_load(p, RLX_AGENT); return f; }
DI void poll_flags(const GAS u64* p, u64 tag, bool on) {
    int n = 0;
    while (true) {
        u64 f = peek_flags(p, tag, on);
        if (all_(f == tag) || ++n > SPIN_CAP) break;
        __builtin_amdgcn_s_sleep(2);
    }
    asm volatile("" ::: "memory");
}
DI float bf_lo(u32 v) { return u2f(v << 16); }
DI float bf_hi(u32 v) { return u2f(v & 0xffff0000u); }
DI u32 pk_bf(float a, float b) { f32x2 v = {a, b}; return __builtin_bit_cast(u32, __builtin_convertvector(v, bf16x2)); }
typedef short s16x2 __attribute__((ext_vector_type(2)));
DI u32 relu_pk(u32 v) { return __builtin_bit_cast(u32, __builtin_elementwise_max(__builtin_bit_cast(s16x2, v), (s16x2){0, 0})); }
DI u32 umax(u32 a, u32 b) { return a > b ? a : b; }
DI float rbf(float a) { return bf_lo(pk_bf(a, a)); }
DI u32 pk_h(f32x2 v) { return __builtin_bit_cast(u32, __builtin_convertvector(v, f16x2)); }
DI f32x16 mfma32(v4u a, v4u b, f32x16 c) { return __builtin_amdgcn_mfma_f32_32x32x16_bf16(__builtin_bit_cast(bf16x8, a), __builtin_bit_cast(bf16x8, b), c, 0, 0, 0); }
DI f32x4 mfma16(v4u a, v4u b, f32x4 c) { return __builtin_amdgcn_mfma_f32_16x16x32_bf16(__builtin_bit_cast(bf16x8, a), __builtin_bit_cast(bf16x8, b), c, 0, 0, 0); }
DI f32x4 mfma16h(v4u a, v4u b, f32x4 c) { return __builtin_amdgcn_mfma_f32_16x16x32_f16(__builtin_bit_cast(f16x8, a), __builtin_bit_cast(f16x8, b), c, 0, 0, 0); }
template <int CTRL> DI float dpp_add(float v) { return v + __builtin_bit_cast(float, __builtin_amdgcn_update_dpp(0, __builtin_bit_cast(int, v), CTRL, 0xf, 0xf, true)); }
template <int CTRL> DI u32 dpp_max(u32 v) { const u32 o = (u32)__builtin_amdgcn_update_dpp(0, (int)v, CTRL, 0xf, 0xf, true); return v > o ? v : o; }
template <int T> DI float bcast(float v) { return __builtin_bit_cast(float, __builtin_amdgcn_update_dpp(0, __builtin_bit_cast(int, v), 0x150 + T, 0xf, 0xf, true)); }
DI u32 wave_max(u32 v) {
    v = dpp_max<0xB1>(v); v = dpp_max<0x4E>(v); v = dpp_max<0x141>(v); v = dpp_max<0x140>(v);
    const u32 a = __builtin_amdgcn_readlane(v, 0), c = __builtin_amdgcn_readlane(v, 16), d = __builtin_amdgcn_readlane(v, 32), e = __builtin_amdgcn_readlane(v, 48);
    const u32 p = a > c ? a : c, q = d > e ? d : e;
    return p > q ? p : q;
}
struct cmult { f32x2 b, n; };
DI cmult mkc(f32x2 b) { cmult c; c.b = b; c.n = (f32x2){-b.y, b.x}; return c; }
DI f32x2 cmul(f32x2 a, const cmult& c) { return __builtin_elementwise_fma((f32x2){a.x, a.x}, c.b, (f32x2){a.y, a.y} * c.n); }
DI f32x16 mfma32h(v4u a, v4u b, f32x16 c) { return __builtin_amdgcn_mfma_f32_32x32x16_f16(__builtin_bit_cast(f16x8, a), __builtin_bit_cast(f16x8, b), c, 0, 0, 0); }
DI u32 pk_h2(float a, float b) { return pk_h((f32x2){a, b}); }
DI f32x2 cexp_pow(float lr, float th, float tl, float e) {
    float a = __builtin_amdgcn_fractf(th * e) + tl * e;
    float m = __builtin_amdgcn_exp2f(lr * e);
    f32x2 r; r.x = m * __builtin_amdgcn_cosf(a); r.y = m * __builtin_amdgcn_sinf(a); return r;
}
DI f32x4 sel4(bool c, f32x4 a, f32x4 b) { return c ? a : b; }
constexpr int w2loc(int n) { return n < 16 ? n : n < 28 ? n + 4 : n - 28; }
template <class F, int... I> DI void sfor_(F&& f, std::integer_sequence<int, I...>) { (f(std::integral_constant<int, I>{}), ...); }
template <int N, class F> DI void sfor(F&& f) { sfor_(f, std::make_integer_sequence<int, N>{}); }
template <int I> using IC = std::integral_constant<int, I>;
template <typename T> DI void pin(T& v) { asm volatile("" : "+v"(v)); }

__global__ void __launch_bounds__(768, 3)
fused_kernel(const u16* x_, const u16* logA_, const u16* Aim_, const u16* Cre_, const u16* Cim_, const u16* logdt_, const u16* Dskip_, const u16* w1_,
             const u16* b1_, const u16* w2_, const u16* b2_, u16* out_, char* ws_) {
    u64x8 ka; u64x4 kb4; u64 kc;
    asm volatile("s_load_dwordx16 %0, %3, 0x0\n\ts_load_dwordx8 %1, %3, 0x40\n\ts_load_dwordx2 %2, %3, 0x60" : "=&s"(ka), "=&s"(kb4), "=&s"(kc) : "s"(__builtin_amdgcn_kernarg_segment_ptr()) : "memory");
    __shared__ __attribute__((aligned(16))) char smem[163840];
    asm volatile("" :: "v"((u32)(size_t)(LAS char*)smem));
    const int tid = threadIdx.x, lane = tid & 63, wave = __builtin_amdgcn_readfirstlane(tid >> 6) & 15, w = wave & 3, role = wave >> 2;
    const int b = blockIdx.x & 255, xcd = b & 7, slot = b >> 3;
    const int P = 4 * (xcd >> 1) + (slot >> 3), j = 8 * (xcd & 1) + (slot & 7);
    const int tr = 4 * xcd + (slot >> 3), tc = slot & 7;
    const int u0 = (j * 43) >> 4;
    if (wave == 4) {
        u64 pc; asm volatile("s_getpc_b64 %0" : "=s"(pc));
        const v4u rc = mkrsrc((const void*)(pc & ~255ull), CODE_BYTES);
        glds(rc, lane * 16, slot * 1024, L_C + 2048);
    }
    const int h = lane >> 5, l31 = lane & 31, l15 = lane & 15, g = lane >> 4;
    const u32 l16 = lane * 16, lw = w * 32768, w4 = w * 4;
    const int rho = ((slot >> 3) + slot) & 3;
    const int tb = 64 * (P >> 2) + 32 * (j & 1) + 16 * ((P >> 1) & 1) + 2 * (j >> 1) + (P & 1);
    const int cp = 32 * xcd + slot, chl = w >> 1, hf = w & 1, ch = 2 * cp + chl;
    {
        const u32 v = (tid >= 192 && tid < 256) || (tid >= 282 && tid < 286) ? 0u : ~0u;
        if (tid < 288) lds_st<v4u>((tid < 192 ? L_CSP : tid < 256 ? L_U - 192 * 16 : L_C - 256 * 16) + tid * 16, (v4u){v, v, v, v});
    }
    const int rot = ROT == 0 ? 0 : ROT == 1 ? (((slot >> 3) + slot) & 1) * 4 : ROT == 2 ? (((slot >> 3) + slot) & 3) * 2 : ROT == 3 ? (2 * (slot >> 3) + (slot & 7)) & 7 : ((((slot >> 3) + slot) & 1) * 4 + (xcd >> 1)) & 7;
    u32 sx = (512 * (P >> 2) + 128 * ((P >> 1) & 1) + 64 * (w & 1) + 8 * (P & 1) + 4 * (w >> 1)) * 1024, sw1 = 16 * w * 2752 + u0 * 64;
    u32 sx0 = rot * 128, sw0 = rot * (64 * 2752);
    u32 vx[4], vw[3];
    const int lim3 = 4 * ((((j + 1) * 43) >> 4) - u0);
#pragma unroll
    for (int i = 0; i < 3; i++) { const int p = 64 * i + lane, r = (p * 171) >> 11; vw[i] = opq(p - 12 * r >= lim3 ? 0x40000000u : r * (2752 - 192) + l16); }
#pragma unroll
    for (int i = 0; i < 4; i++) vx[i] = opq((((h * 256 + ((lane >> 3) & 3)) * 1024 + ((lane & 7) ^ g) * 16) ^ ((i & 1) * 64)) + i * (16384 - 1024));
    asm volatile("s_waitcnt lgkmcnt(0)\n\ts_barrier" : "+s"(ka), "+s"(kb4), "+s"(kc), "+s"(sx), "+s"(sw1), "+s"(sx0), "+s"(sw0) :: "memory");
    const u16 *x = (const u16*)ka[0], *logA = (const u16*)ka[1], *Aim = (const u16*)ka[2], *Cre = (const u16*)ka[3], *Cim = (const u16*)ka[4], *logdt = (const u16*)ka[5], *Dskip = (const u16*)ka[6], *w1 = (const u16*)ka[7];
    const u16 *b1 = (const u16*)kb4[0], *w2 = (const u16*)kb4[1], *b2 = (const u16*)kb4[2]; u16* out = (u16*)kb4[3]; char* ws = (char*)kc;
    const v4u rx = mkrsrc(x, 2 << 20);
    const u32 aF = opq(L_FLG + (lane & 3) * 4);
    auto flg = [&](auto R_) { flag_imm<L_FLG + decltype(R_)::value * 16>(rx, w4); };
    const v4u rw1 = mkrsrc(w1, 512 * 1376 * 2);
    auto ldX = [&](auto C_) { constexpr int c = decltype(C_)::value;
        const u32 so = glds_first<(16 + (c % 4) * 4) * 1024>(rx, vx[0], sx, c ? ((c + rot) & 7) * 128 : sx0, lw);
        glds_next<1024>(rx, vx[1], so); glds_next<2048>(rx, vx[2], so); glds_next<3072>(rx, vx[3], so); };
    auto ldW = [&](auto C_) { constexpr int c = decltype(C_)::value;
        const u32 so = glds_first<3 * (c % 5) * 1024>(rw1, vw[0], sw1, c ? ((c + rot) & 7) * (64 * 2752) : sw0, lw);
        glds_next<1024>(rw1, vw[1], so); glds_next<2048>(rw1, vw[2], so); };
    if (role == 0) {
        ldW(IC<0>{}); ldX(IC<0>{}); flg(IC<R_F>{});
        ldW(IC<1>{}); ldX(IC<1>{}); flg(IC<R_F + 1>{});
    }
    if (RF && role == 2) {
        glds(rx, l16, (8 * tb + 2 * w) * 1024, L_KB + (2 * w) * 1088);
        glds(rx, l16, (8 * tb + 2 * w + 1) * 1024, L_KB + (2 * w + 1) * 1088);
    }
    asm volatile("" ::: "memory");
    u32 aV[4], aFw, aCn, one, f2o;
    u32 sh2[4], sh2t[4];
    auto pre2 = [&]() {
#pragma unroll
        for (int dt = 0; dt < 4; dt++) aV[dt] = opq(lw + h * 2048 + (4 * (g & 1) + (l15 >> 2)) * 128 + (dt ^ (2 * (g & 1) + (l15 >> 3))) * 32 + (lane & 3) * 8);
        aFw = opq(L_FLG + w4); aCn = opq(L_CNT + w4); one = opq(1u);
        const int jj = 4 * w + (lane < 4 ? lane : 4);
        f2o = opq(((2 * tr + role) * 16 + (jj < 15 ? jj : 15)) * FS2 * 8);
#pragma unroll
        for (int k = 0; k < 4; k++) {
            sh2[k] = ((2 * tr + role) * 43 + 11 * w) * 2048 + (6 * rho - (k + rho >= 4 ? 22 : 0)) * 1024; sh2t[k] = (w == 3 && k + rho == 3) ? (u32)H_BYTES : sh2[k];
            asm volatile("" : "+s"(sh2[k]), "+s"(sh2t[k]));
        }
    };

    auto rdV = [&](int s, int i) { const u32 a = aV[2 * (s & 1) + i] + w2loc(4 * (s >> 1)) * 1024; v2u lo = lds_tr(a), hi = lds_tr(a + 1024); return (v4u){lo.x, lo.y, hi.x, hi.y}; };
    auto gemm2 = [&](auto ROLE_, v4u (&stage)[16], u64 f2, u64 tag) __attribute__((always_inline)) {
        constexpr int role = decltype(ROLE_)::value;
        const GAS u64* f2p = (const GAS u64*)(ws + OFF_F2 + f2o);
        __builtin_amdgcn_s_setprio(0);
        const __amdgpu_buffer_rsrc_t rh = __builtin_amdgcn_make_buffer_rsrc(ws + OFF_H, 0, H_BYTES, 0x00020000);
        constexpr int WIN = role ? 10 : 8;
        v4u park[8][2], hq[WIN], vb[3][2];
        auto ldH = [&](int s) { hq[s % WIN] = __builtin_amdgcn_raw_buffer_load_b128(rh, l16, ((s + 2) % 6 >= 4 ? sh2t[(s + 2) / 6] : sh2[(s + 2) / 6]) + s * 1024, 16); };
        constexpr int W0 = 8;
        if (role) {
            SB0();
            spin_zero4<1>(aFw + R_V1 * 16);
            STAMP(9);
            f2 = tag;
            asm volatile("s_mov_b64 exec, 31\n\tglobal_load_dwordx2 %0, %1, %2 sc1\n\ts_mov_b64 exec, -1" : "+v"(f2) : "v"(f2o), "s"(ws + OFF_F2) : "memory");
            SB0();
#pragma unroll
            for (int s = 0; s < 8; s++) { park[s][0] = rdV(s, 0); park[s][1] = rdV(s, 1); }
            clr<R_PK * 16>(aFw, opq(0u));
            asm volatile("s_waitcnt vmcnt(0)" : "+v"(f2) :: "memory");
            SB0();
        }
        STAMP(10);
        if (!all_(f2 == tag)) poll_flags(f2p, tag, lane < 5);
        STAMP(5);
#pragma unroll
        for (int s = 0; s < W0; s++) ldH(s);
        const int q3 = (lane >> 2) & 7, fr = 8 * wave + 4 * h + 2 * ((q3 ^ (q3 >> 1) ^ (q3 >> 2)) & 1) + (q3 >> 2), fo = ((q3 >> 1) & 1) * 4 + (lane & 3);
        const u32 fin_off = (512 * (tr >> 3) + 256 * ((fr >> 2) & 1) + 128 * ((tr >> 2) & 1) + 16 * (fr >> 3) + 4 * (tr & 3) + (fr & 3)) * 512 + tc * 64 + fo * 8;
        f32x4 s0, s1; float rs2; u32 da, ra, rb, rc;
        v4u fin_x, fin_b; u32 fin_s, fv;
        f32x4 ac2[4][2];
#pragma unroll
        for (int i = 0; i < 8; i++) ac2[i >> 1][i & 1] = (f32x4){0.f, 0.f, 0.f, 0.f};
        const u32 aSt = opq(lw + l16), zero = opq(0u);
        if (!role) {
            SB0();
            spin_zero4(opq(aFw + ((lane & 1) ? R_PK : R_V2) * 16));
            STAMP(9);
            vb[0][0] = rdV(0, 0); vb[0][1] = rdV(0, 1); vb[1][0] = rdV(1, 0); vb[1][1] = rdV(1, 1);
            asm volatile("" ::: "memory");
#pragma unroll
            for (int k = 0; k < 4; k++) lds_st<v4u>(aSt + k * 1024, stage[k]);
        }
#pragma unroll
        for (int s = 0; s < 22; s++) {
            SB0();
            if (role && (s == 4 || s == 10)) fv = lds_ldv<u32>(aFw + (s == 4 ? R_V2 : R_V3) * 16);
            if (role && (s == 6 || s == 12)) { if (__builtin_expect(any_(fv != 0), 0)) spin_zero4(aFw + (s == 6 ? R_V2 : R_V3) * 16); asm volatile("" ::: "memory"); }
            if ((!role || s + 2 >= 8) && s + 2 < 22) { vb[(s + 2) % 3][0] = rdV(s + 2, 0); vb[(s + 2) % 3][1] = rdV(s + 2, 1); }
            if (s == 19) sig<C_RD>(aCn, one);
            if (s == 21) fv = lds_ldv<u32>(aCn + 4 * C_RD);
#pragma unroll
            for (int q = 0; q < 4; q++) ac2[2 * (s & 1) + (q & 1)][q >> 1] = mfma16(role && s < 8 ? park[s & 7][q & 1] : vb[s % 3][q & 1], hq[((s & ~1) + (q >> 1)) % WIN], ac2[2 * (s & 1) + (q & 1)][q >> 1]);
            SB0();
            if (!role && s >= 1 && s < 7) {
                asm volatile("" ::: "memory");
                lds_st<v4u>(aSt + (2 * s + 2) * 1024, stage[(2 * s + 2) & 15]); lds_st<v4u>(aSt + (2 * s + 3) * 1024, stage[(2 * s + 3) & 15]);
                if (s == 6) { clr<R_V3 * 16>(aFw, zero); STAMP(13); }
            }
            if (s == 0) { for (int k = W0; k < WIN; k++) ldH(k); }
            if ((s & 1) && s + WIN < 22) { ldH(s - 1 + WIN); ldH(s + WIN); }
            if (s == 13) {
                fin_x = *(const GAS v4u*)(x + fin_off); fin_b = lds_ld<v4u>(L_B2 + fo * 16);
                fin_s = __hip_atomic_load((GAS u32*)(ws + OFF_ST + (tr >> 1) * 1024 + (fr >> 2) * 64 + (tr & 1) * 32 + (fr & 3) * 8), RLX_AGENT);
            }
            if (s == 16) { da = opq(lw + role * 20480 + l15 * 272 + g * 16); ra = opq(role * 20480 + (fr & 31) * 272 + fo * 32); rb = opq(ra + 65536); rc = opq(L_KB + fr * 144 + fo * 16); }
            if (s == 21) {
                s0 = (f32x4){bf_lo(fin_x.x) + bf_lo(fin_b.x), bf_hi(fin_x.x) + bf_hi(fin_b.x), bf_lo(fin_x.y) + bf_lo(fin_b.y), bf_hi(fin_x.y) + bf_hi(fin_b.y)};
                pin(s0);
            }
        }
        SB0();
        STAMP(7); CST(3);
        if (__builtin_expect(any_(fv < 2), 0)) spin(aCn + 4 * C_RD, 2);
        asm volatile("" ::: "memory");
#pragma unroll
        for (int i = 0; i < 8; i++) lds_st<f32x4>(da + (i & 1) * 16 * 272 + (i >> 1) * 64, ac2[i >> 1][i & 1]);
        SB0();
        s1 = (f32x4){bf_lo(fin_x.z) + bf_lo(fin_b.z), bf_hi(fin_x.z) + bf_hi(fin_b.z), bf_lo(fin_x.w) + bf_lo(fin_b.w), bf_hi(fin_x.w) + bf_hi(fin_b.w)};
        rs2 = __builtin_amdgcn_rcpf(bf_hi(fin_s)); rs2 *= rs2;
        asm volatile("" : "+v"(s0), "+v"(s1), "+v"(rs2));
        wait_lgkm0();
        __builtin_amdgcn_s_barrier();
        asm volatile("" ::: "memory");
        STAMP(11);
        const v4u av = lds_ld<v4u>(rc);
        s0 += ((lds_ld<f32x4>(ra) + lds_ld<f32x4>(ra + 32768)) + (lds_ld<f32x4>(rb) + lds_ld<f32x4>(rb + 32768))) * rs2;
        s1 += ((lds_ld<f32x4>(ra + 16) + lds_ld<f32x4>(ra + 32768 + 16)) + (lds_ld<f32x4>(rb + 16) + lds_ld<f32x4>(rb + 32768 + 16))) * rs2;
        s0 += (f32x4){bf_lo(av.x), bf_hi(av.x), bf_lo(av.y), bf_hi(av.y)};
        s1 += (f32x4){bf_lo(av.z), bf_hi(av.z), bf_lo(av.w), bf_hi(av.w)};
        const v4u ov = {pk_bf(s0[0], s0[1]), pk_bf(s0[2], s0[3]), pk_bf(s1[0], s1[1]), pk_bf(s1[2], s1[3])};
        st_sc1(ov, mkrsrc(out, 2 << 20), fin_off * 2, 0);
    };

    if (__builtin_expect(role == 0, 1)) {
        v4u stage[16]; u64 f2, tag;
        const u32 sw2 = 352 * w * 1024 + tc * 128;
        const u32 vv = (lane >> 3) * 1024 + ((lane & 7) ^ (g << 1)) * 16;
        u32 sv[4];
#pragma unroll
        for (int k = 0; k < 4; k++) sv[k] = sw2 + (12 * rho - (k + rho >= 4 ? 44 : 0)) * 8192;
        const __amdgpu_buffer_rsrc_t rv = __builtin_amdgcn_make_buffer_rsrc((void*)w2, 0, 512 * 1376 * 2, 0x00020000);
        auto ldV = [&](int r) { stage[r] = __builtin_amdgcn_raw_buffer_load_b128(rv, vv, sv[(r + 32) / 12] + (r + 28) * 8192, 0); };
        const GAS u64* f1p = (const GAS u64*)(ws + OFF_F1) + ((xcd >> 1) * 256 + 64 * w + lane) * FS1;
        sfor<6>([&](auto I) {
            constexpr int c = decltype(I)::value + 2;
            ldW(IC<c>{});
            if constexpr (c >= 4) {
                SB0();
#pragma unroll
                for (int k = 0; k < 3; k++) ldV(3 * (c - 4) + k);
                SB0();
            }
            if constexpr (c >= 4) TIMED(spin_zero4<1>(aF + (R_P + c - 4) * 16));
            ldX(IC<c>{});
            flg(IC<R_F + c>{});
        });
        STAMP(1);
        {
            const u32 vS = ((lane >> 1) * 4 + (lane & 1)) * 16;
            const v4u rst = mkrsrc(ws + OFF_ST, 16384);
            asm volatile("s_mov_b32 m0, %3\n\ts_mov_b64 exec, 0xffff\n\tbuffer_load_dwordx4 %0, %1, %2 offen sc1 lds\n\ts_mov_b64 exec, -1" :: "v"(vS), "s"(rst), "s"(P * 1024 + (w & 1) * 512 + (w >> 1) * 32), "s"(L_STAT + w * 256) : "memory");
        }
        spin_zero4<1>(aF + R_X * 16);
        tag = get_tag();
        u64 f1;
        asm volatile("global_load_dwordx2 %0, %1, off sc1" : "=&v"(f1) : "v"(f1p) : "memory");
#pragma unroll
        for (int n = 12; n < 16; n++) ldV(n);
        SB0();
        asm volatile("s_waitcnt vmcnt(4)" : "+v"(f1) :: "memory");
        if (!all_(f1 == tag)) poll_flags(f1p, tag, true);
        STAMP(16);
        {
            const v4u rus = mkrsrc(ws + OFF_U, 2 << 20);
            const u32 vU = (lane >> 1) * 8192 + ((lane ^ g) & 1) * 16;
#pragma unroll
            for (int i = 0; i < 2; i++) glds_sc1(rus, vU, (64 * w + 32 * i) * 8192 + cp * 32, L_U + 1024 + w * 2048 + i * 1024);
            flag_dma(rus, L_UF + w * 4);
        }
        STAMP(2);
        pre2();
#pragma unroll
        for (int n = 0; n < 16; n++) pin(stage[n]);
        {
            const u32 aK = opq((lane & 4) ? L_CNT + 4 * C_K + (lane & 1) * 4 : L_FLG + R_P7 * 16 + (lane & 3) * 4);
            const u32 want = opq((lane & 4) ? 2u : 0u);
            int k = 0;
            while (any_(lds_ldv<u32>(aK) != want) && ++k < (SPIN_CAP << 4)) { __builtin_amdgcn_s_sleep(1); }
            asm volatile("" ::: "memory");
        }
        STAMP(14);
        const v4u rw2 = mkrsrc(w2, 512 * 1376 * 2);
        f2 = tag;
        sfor<28>([&](auto I) {
            constexpr int n = decltype(I)::value;
            if constexpr (n >= 6) wait_vm<6>();
            if constexpr (n == 26) asm volatile("s_mov_b64 exec, 31\n\tglobal_load_dwordx2 %0, %1, %2 sc1\n\ts_mov_b64 exec, -1" : "+v"(f2) : "v"(f2o), "s"(ws + OFF_F2) : "memory");
            glds_imm<w2loc(n) * 1024, n * 8192>(rw2, vv, sv[(n + 4) / 12], lw);
            if constexpr (n == 15) flg(IC<R_V1>{});
        });
        flg(IC<R_V2>{});
        asm volatile("s_waitcnt vmcnt(3)" : "+v"(f2) :: "memory");
        STAMP(8);
        gemm2(IC<0>{}, stage, f2, tag);
    } else if (role == 1) {
        u64 tag;
        __builtin_amdgcn_s_setprio(2);
        warm_flag<FS2 * 8>(ws + OFF_F2, ((4 * P + w) * 16 + j) * FS2 * 8, l16);
        if (MPF == 1) {
            const v4u rp = mkrsrc(w2, 512 * 1376 * 2);
            glds(rp, l16, (b * 11 + 2 * w) * 512, L_C + 2048);
            if (w < 2) glds(rp, l16, (b * 11 + 8 + 2 * w) * 512, L_C + 2048);
        }
        u32 aW[4], aX[4];
#pragma unroll
        for (int s = 0; s < 4; s++) {
            const int ks = (s + w) & 3;
            aW[s] = opq(ks * 32768 + (8 * h + (l15 >> 2)) * 192 + (g & 1) * 32 + (lane & 3) * 8);
            aX[s] = opq(lw + 16384 + l31 * 128 + (((2 * ks + h) ^ ((l31 >> 1) & 7)) * 16));
        }
        const u32 aC = opq(L_CSP + l31 * 32 + h * 16), aCw = opq(L_CSP + lane * 32 + w * 8), aS = opq(L_STAT + w * 256 + l31 * 8), aB = opq(L_B1 + lane * 2);
        pre2();
        const u32 zero = opq(0u);
        f32x16 acc[3]; f32x4 cs = {0.f, 0.f, 0.f, 0.f};
        v4u sel[3];
#pragma unroll
        for (int u = 0; u < 3; u++) {
            for (int i = 0; i < 16; i++) acc[u][i] = 0.f;
            const u32 sv = (l15 == (u < 2 ? 8 * u : 1) + 4 * (g & 1)) ? 0xBF80BF80u : 0u; sel[u] = (v4u){sv, sv, sv, sv}; pin(sel[u]);
        }
        v4u fa[3][3], fb[2][4];
        auto rdW = [&](int n, int u) { const u32 a = aW[n & 3] + ((n >> 2) % 5) * 3072 + u * 64; v2u lo = lds_tr(a), hi = lds_tr(a + 768); fa[n % 3][u] = (v4u){lo.x, lo.y, hi.x, hi.y}; };
        auto rdX = [&](int c, int s) { fb[c & 1][s] = lds_ld<v4u>(aX[s] + (c % 4) * 4096); };
        const v4u rhs = mkrsrc(ws + OFF_H, H_BYTES);
        const u32 vH = opq((g & 1) * 1024 + h * 256 + l15 * 16);
        const int nown = (((j + 1) * 43) >> 4) - u0;
        const u32 sh = ((4 * P + w) * 43 + u0) * 2048, shx = nown == 3 ? sh + 4096 : (u32)H_BYTES;
        spin_zero4(aF);
        STAMP(1);
        rdX(0, 0);
#pragma unroll
        for (int u = 0; u < 3; u++) rdW(0, u);
        rdX(0, 1);
#pragma unroll
        for (int u = 0; u < 3; u++) rdW(1, u);
        rdX(0, 2); rdX(0, 3);
        u32 fl, fx, bb[2]; v2u ee; v2u stg; v4u fas[3], fbs;
        sfor<32>([&](auto N_) __attribute__((always_inline)) {
            constexpr int n = decltype(N_)::value, c = n >> 2, s = n & 3;
            SB0();
            if constexpr (s == 2 && c < 5) {
                pin(fa[(n + 1) % 3][2]);
                flg(IC<R_P + c>{});
            }
            if (s == 2 && c < 7) {
                if (__builtin_expect(any_(fl != 0), 0)) TIMED(spin_zero4(aF + (R_F + c + 1) * 16));
                asm volatile("" ::: "memory");
            }
            sfor<3>([&](auto U_) __attribute__((always_inline)) {
                constexpr int u = decltype(U_)::value;
                acc[u] = mfma32(fa[n % 3][u], fb[c & 1][s], acc[u]);
                if (s == 0 && c < 7) cs = mfma16(sel[u], fa[n % 3][u], cs);
                if (n == 27) cs = mfma16(sel[u], fa[28 % 3][u], cs);
                SB0();
                if (s >= 2 && c < 7 && u < 2) rdX(c + 1, 2 * (s - 2) + u);
                if (n + 2 < 32) rdW(n + 2, u);
                if (s == 1 && u == 0 && c < 7) fl = lds_ldv<u32>(aF + (R_F + c + 1) * 16);
                if (n == 25 && u == 2) { bb[0] = lds_ld<u16>(aB); bb[1] = lds_ld<u16>(aB + 128); }
                if (n == 21 && u == 1) fx = lds_ldv<u32>(aF + R_X * 16);
                if (n == 23 && u == 1) { if (__builtin_expect(any_(fx != 0), 0)) spin_zero4(aF + R_X * 16); asm volatile("" ::: "memory"); ee = lds_ldv<v2u>(L_E); }
                if (n == 28 && u == 1) {
                    v2u t[2];
#pragma unroll
                    for (int k = 0; k < 2; k++) {
                        const float d = cs[k] - bf_hi(f2u(cs[k]));
                        t[k] = (v2u){__builtin_amdgcn_perm(f2u(d), f2u(cs[k]), 0x07060302u), pk_bf(d - bf_hi(f2u(d)), bf_lo(bb[k]))};
                    }
                    asm volatile("ds_write2st64_b64 %0, %1, %2 offset1:4" :: "v"(aCw), "v"(t[0]), "v"(t[1]) : "memory");
                }
                if (n == 31 && u == 0) { fas[0] = lds_ldv<v4u>(aC); fas[1] = lds_ldv<v4u>(aC + 1024); fas[2] = lds_ldv<v4u>(aC + 2048); }
                if (n == 30 && u == 0) stg = lds_ldv<v2u>(aS);
                if (n == 30 && u == 1) { tag = mix64(((u64)(u32)__builtin_amdgcn_readfirstlane(ee.y) << 32) | (u32)__builtin_amdgcn_readfirstlane(ee.x)); asm volatile("" : "+s"(tag)); }
                SB0();
            });
        });
        {
            auto csbad = [&]() { return umax(umax(umax(fas[0].x, fas[0].z), umax(fas[1].x, fas[1].z)), umax(fas[2].x, fas[2].z)) == ~0u; };
            const u32 want = (lane & 1) ? (u32)(tag >> 32) : (u32)tag;
            if (__builtin_expect(any_(csbad() || stg.y != want), 0)) {
                int k = 0;
                while (any_(csbad()) && ++k < (SPIN_CAP << 2)) { fas[0] = lds_ldv<v4u>(aC); fas[1] = lds_ldv<v4u>(aC + 1024); fas[2] = lds_ldv<v4u>(aC + 2048); }
                k = 0;
                const GAS u64* sp = (const GAS u64*)(ws + OFF_ST + P * 1024 + (w & 1) * 512 + (w >> 1) * 32 + (l31 >> 2) * 64 + (lane & 3) * 8);
                while (any_(stg.y != want) && ++k < SPIN_CAP) { const u64 gr = __hip_atomic_load(sp, RLX_AGENT); stg = (v2u){(u32)gr, (u32)(gr >> 32)}; }
            }
            const u32 m1 = stg.x & 0xffffu, m2 = m1 * 0x10001u;
            fbs = (v4u){m2, h ? m1 : stg.x, m2, m1};
        }
#pragma unroll
        for (int u = 0; u < 3; u++) acc[u] = mfma32(fas[u], fbs, acc[u]);
        STAMP(2); CST(1);
        sfor<6>([&](auto K_) __attribute__((always_inline)) {
            constexpr int u = decltype(K_)::value >> 1, s = decltype(K_)::value & 1;
            v4u o;
#pragma unroll
            for (int e = 0; e < 4; e++) {
                const float a0 = acc[u][8 * s + 2 * e], a1 = acc[u][8 * s + 2 * e + 1];
                o[e] = relu_pk(pk_bf(a0 * __builtin_fabsf(a0), a1 * __builtin_fabsf(a1)));
            }
            st_sc1(o, rhs, vH, (u < 2 ? sh + u * 2048 : shx) + s * 512);
            SB0();
        });
        STAMP(8);
        clr<R_P7 * 16>(aFw, zero);
        wait_vm<0>();
        STAMP(4);
        put_flag(ws + OFF_F2, ((4 * P + w) * 16 + j) * FS2 * 8, l16, tag);
        v4u none[16];
        gemm2(IC<1>{}, none, ~tag, tag);
    } else {
        u64 tag;
        __builtin_amdgcn_s_setprio(3);
        if (!RF) {
        glds(rx, l16, (8 * tb + 2 * w) * 1024, L_KB + (2 * w) * 1088);
        glds(rx, l16, (8 * tb + 2 * w + 1) * 1024, L_KB + (2 * w + 1) * 1088);
        }
        if (w == 0) {
            const v4u re = mkrsrc(ws, 16);
            asm volatile("s_mov_b32 m0, %1\n\ts_mov_b64 exec, 1\n\tbuffer_load_dwordx4 off, %0, 0 sc1 lds\n\ts_mov_b64 exec, -1" :: "s"(re), "s"(L_E) : "memory");
        } else if (w == 2) {
            const v4u rb2 = mkrsrc(b2, 1024);
            asm volatile("s_mov_b32 m0, %3\n\ts_mov_b64 exec, 0xff\n\tbuffer_load_dwordx4 %0, %1, %2 offen lds\n\ts_mov_b64 exec, -1" :: "v"(l16), "s"(rb2), "s"(tc * 128), "s"(L_B2) : "memory");
        } else if (w == 1) {
            const v4u rb1 = mkrsrc(b1, 1376 * 2);
            asm volatile("s_mov_b32 m0, %3\n\ts_nop 0\n\tbuffer_load_dword %0, %1, %2 offen lds" :: "v"(lane * 4), "s"(rb1), "s"(u0 * 64), "s"(L_B1) : "memory");
        }
        const u32 pi = opq((hf * 64 + lane) * 2);
        u32 pA = ldp((const GAS u16*)((const GAS char*)(logA + ch * 128) + pi)), pI = ldp((const GAS u16*)((const GAS char*)(Aim + ch * 128) + pi));
        u32 pR = ldp((const GAS u16*)((const GAS char*)(Cre + ch * 128) + pi)), pC = ldp((const GAS u16*)((const GAS char*)(Cim + ch * 128) + pi));
        u32 pT = *(const GAS u16*)(logdt + ch), pD = *(const GAS u16*)(Dskip + ch);
        flg(IC<R_X>{});
        one = opq(1u);
        const u32 aCnt = opq(L_CNT + chl * 4), aCtl = opq(L_C);
        const v4u rus = mkrsrc(ws + OFF_U, 2 << 20);
        v4u selLo, selHi;
#pragma unroll
        for (int e = 0; e < 4; e++) {
            const u32 o1 = (h == (g & 1) && ((l15 & 7) >> 1) == e) ? ((l15 & 1) ? 0x3C000000u : 0x00003C00u) : 0u;
            selLo[e] = l15 < 8 ? o1 : 0u; selHi[e] = l15 < 8 ? 0u : o1;
        }
        pin(selLo); pin(selHi);
        const int m = 64 * hf + lane;
        const u32 aT = opq(w * 32768 + 16384 + l15 * 64 + g * 16), t0 = opq((2 * chl) * 32768 + 16384 + m * 4), kr = opq(L_KB + chl * 8320 + (1016 - 8 * m) * 2);
        u32 tg[6];
        {
            const bool r1 = m < 32, r2 = m < 64;
            tg[0] = opq(t0 + (r1 ? 528 : 0) * 4); tg[1] = opq(t0 + (r2 ? 400 : 0) * 4); tg[2] = opq(t0 + (r1 ? 560 : 272) * 4);
            tg[3] = opq(t0 + (r1 ? 592 : 272) * 4); tg[4] = opq(t0 + (r2 ? 464 : 1) * 4); tg[5] = opq(t0 + (r1 ? 624 : 1) * 4);
        }
        u32 kb[4], ua[4];
#pragma unroll
        for (int i = 0; i < 4; i++) {
            kb[i] = opq(L_KB + chl * 8320 + ((l15 & 1) ? (239 - l15 + 8 * g) * 2 : 4160 + (240 - l15 + 8 * g) * 2) - hf * 256 - 192 + i * 1024);
            const int os = 30 + 2 * l15 + g - 16 * hf - 4 * i;
            ua[i] = opq(L_U + os * 32 + ((chl ^ (os >> 3)) & 1) * 16);
        }
        const u32 aZ3 = opq((lane & 4) ? L_CNT + 4 * C_K + chl * 4 : L_UF + (lane & 3) * 4), want3 = opq((lane & 4) ? 2u : 0u), aZ2 = opq(L_C + (lane & 3) * 4 + ((lane & 8) ? R_LN : (lane & 4) ? R_P + 4 : R_F + 7) * 16);
        const u32 vA = opq((lane >> 3) * 4096 + (lane & 7) * 16);
        pin(pA); pin(pI); pin(pR); pin(pC); pin(pT); pin(pD);

        {
            const u32 aRow = opq(L_KB + (l15 & 7) * 1088 + (l15 >> 3) * 512 + g * 16), aTr = opq(L_KB + (l15 >> 2) * 1088 + (128 * w + 16 * g + 4 * (lane & 3)) * 2);
            v4u ones = {0x3F803F80u, 0x3F803F80u, 0x3F803F80u, 0x3F803F80u}; pin(ones);
            spin_zero4(aF + R_X * 16);
            STAMP(1);
            v4u xb[8]; v2u xt[2][2];
#pragma unroll
            for (int s = 0; s < 8; s++) xb[s] = lds_ld<v4u>(aRow + s * 64);
#pragma unroll
            for (int i = 0; i < 2; i++) { xt[i][0] = lds_tr(aTr + i * 128); xt[i][1] = lds_tr(aTr + i * 128 + 4 * 1088); }
            tag = get_tag();
            f32x4 sm = {0.f, 0.f, 0.f, 0.f}, sq = {0.f, 0.f, 0.f, 0.f};
#pragma unroll
            for (int s = 0; s < 8; s++) { sq = mfma16(xb[s], xb[s], sq); sm = mfma16(ones, xb[s], sm); }
            float q = (l15 & 2) ? ((l15 & 1) ? sq[3] : sq[2]) : ((l15 & 1) ? sq[1] : sq[0]);
            q = g == (l15 >> 2) ? q : 0.f;
            { const auto r = __builtin_amdgcn_permlane32_swap(f2u(q), f2u(q), false, false); q = u2f(r[0]) + u2f(r[1]); }
            { const auto r = __builtin_amdgcn_permlane16_swap(f2u(q), f2u(q), false, false); q = u2f(r[0]) + u2f(r[1]); }
            const float mean = dpp_add<0x128>(sm[0]) * (1.f / 512.f);
            const float var = fmaxf(__builtin_fmaf(-mean, mean, dpp_add<0x128>(q) * (1.f / 512.f)), 0.f);
            const u32 p = pk_bf(mean, __builtin_amdgcn_sqrtf(rbf(rbf(var) + 1e-5f)));
            const float mb = bf_lo(p), rr = __builtin_amdgcn_rcpf(bf_hi(p));
            if (w == 0 && lane < 8) __hip_atomic_store((GAS u64*)(ws + OFF_ST + (P * 16 + j) * 64) + lane, ((u64)((lane & 1) ? (u32)(tag >> 32) : (u32)tag) << 32) | p, RLX_AGENT);
            sfor<2>([&](auto I_) {
                constexpr int i = decltype(I_)::value;
                v4u o;
                sfor<4>([&](auto E_) {
                    constexpr int e = decltype(E_)::value;
                    const u32 xv = xt[i][e >> 1][e & 1];
                    const u32 d = pk_bf(bf_lo(xv) - bcast<2 * e>(mb), bf_hi(xv) - bcast<2 * e + 1>(mb));
                    o[e] = pk_bf(bf_lo(d) * bcast<2 * e>(rr), bf_hi(d) * bcast<2 * e + 1>(rr));
                });
                st_sc1(o, rus, l16, tb * 8192 + w * 2048 + i * 1024);
            });
        }
        flg(IC<R_LN>{});
        STAMP(2);
        __builtin_amdgcn_s_setprio(1);
        f32x4 kt[3];
        {
            const float dt = rbf(expf(bf_lo(pT)));
            const float zr = -rbf(expf(bf_lo(pA))) * dt, zi = bf_lo(pI) * dt;
            const float lr = zr * 1.44269504088896341f;
            const float th = zi * 0.159154943091895336f, tl = __builtin_fmaf(zi, 0.159154943091895336f, -th) + zi * 6.4206383e-09f;
            const cmult w1c = mkc(cexp_pow(lr, -th, -tl, 1.f)), qc = mkc(cexp_pow(lr, -th, -tl, 128.f)), q2 = mkc(cexp_pow(lr, -th, -tl, 256.f));
            const cmult w8 = mkc(cexp_pow(lr, th, tl, 8.f)), w32 = mkc(cexp_pow(lr, th, tl, 32.f));
            const u32 aR = pR & 0x7fffu, aI = pC & 0x7fffu;
            int ex = (int)(wave_max(aR > aI ? aR : aI) >> 7); ex = ex < 12 ? 12 : (ex > 240 ? 240 : ex);
            const float sdn = u2f((u32)(262 - ex) << 23), sup = u2f((u32)(ex - 8) << 23);
            auto transpose = [&](const f32x2 (&v)[16], v4u (&f)[4]) {
                f32x16 z; for (int i = 0; i < 16; i++) z[i] = 0.f;
                f32x16 dr = mfma32h((v4u){pk_h2(v[0].x, v[1].x), pk_h2(v[2].x, v[3].x), pk_h2(v[4].x, v[5].x), pk_h2(v[6].x, v[7].x)}, selLo, z);
                f32x16 di = mfma32h((v4u){pk_h2(v[0].y, v[1].y), pk_h2(v[2].y, v[3].y), pk_h2(v[4].y, v[5].y), pk_h2(v[6].y, v[7].y)}, selLo, z);
                dr = mfma32h((v4u){pk_h2(v[8].x, v[9].x), pk_h2(v[10].x, v[11].x), pk_h2(v[12].x, v[13].x), pk_h2(v[14].x, v[15].x)}, selHi, dr);
                di = mfma32h((v4u){pk_h2(v[8].y, v[9].y), pk_h2(v[10].y, v[11].y), pk_h2(v[12].y, v[13].y), pk_h2(v[14].y, v[15].y)}, selHi, di);
#pragma unroll
                for (int e = 0; e < 4; e++) {
                    f[0][e] = pk_h2(dr[2 * e], dr[2 * e + 1]); f[1][e] = pk_h2(dr[8 + 2 * e], dr[9 + 2 * e]);
                    f[2][e] = pk_h2(di[2 * e], di[2 * e + 1]); f[3][e] = pk_h2(di[8 + 2 * e], di[9 + 2 * e]);
                }
            };
            { f32x2 t0_ = w1c.b, t1_ = qc.b, t2_ = q2.b, t3_ = w8.b, t4_ = w32.b; pin(t0_); pin(t1_); pin(t2_); pin(t3_); pin(t4_); }
            STAMP(13);
            wait_vm<0>();
            put_flag(ws + OFF_F1, (w * 256 + tb) * FS1 * 8, l16, tag);
            warm_flag<FS3 * 8>(ws + OFF_F3, (hf * 512 + ch) * FS3 * 8, l16);
            STAMP(14);
            v4u fy[4], fx[4];
            f32x2 v[16], xo[8];
            v[0] = (f32x2){1.f, 0.f}; v[1] = w8.b; v[2] = cmul(w8.b, w8); v[3] = cmul(v[2], w8); v[4] = w32.b;
            xo[0] = (f32x2){bf_lo(pR) * sdn, -bf_lo(pC) * sdn};
#pragma unroll
            for (int i = 5; i < 16; i++) { v[i] = cmul(v[i - 4], w32); if (i < 12) xo[i - 4] = cmul(xo[i - 5], w1c); }
            STAMP(3); CST(2);
            transpose(v, fy);
            const float fin = 2.f * dt * sup;
            auto tile = [&](int t) {
                transpose(v, fx);
                f32x4 d = {0.f, 0.f, 0.f, 0.f};
#pragma unroll
                for (int s = 0; s < 4; s++) d = mfma16h(fy[s], fx[s], d);
                kt[t] = d * fin;
            };
            v[0] = xo[0]; v[1] = cmul(xo[0], qc);
#pragma unroll
            for (int s = 2; s < 16; s++) v[s] = cmul(v[s - 2], q2);
            const f32x2 last = cmul(v[14], q2);
            tile(0);
            v[0] = last; v[1] = xo[4]; v[9] = xo[2]; v[13] = xo[6];
            v[2] = cmul(v[1], qc); v[10] = cmul(v[9], qc); v[14] = cmul(v[13], qc);
#pragma unroll
            for (int s = 3; s < 9; s++) v[s] = cmul(v[s - 2], q2);
            v[11] = cmul(v[9], q2); v[12] = cmul(v[10], q2); v[15] = cmul(v[13], q2);
            const f32x2 last2 = cmul(v[14], q2);
            tile(1);
            v[0] = last2;
#pragma unroll
            for (int i = 0; i < 4; i++) { v[1 + 2 * i] = xo[2 * i + 1]; v[2 + 2 * i] = cmul(xo[2 * i + 1], qc); }
#pragma unroll
            for (int i = 9; i < 16; i++) v[i] = (f32x2){0.f, 0.f};
            tile(2);
        }
        CST(3); STAMP(17);
        pin(kt[0]); pin(kt[1]); pin(kt[2]);
        spin_zero4<1>(aZ2);
        STAMP(11);
#pragma unroll
        for (int t = 0; t < 3; t++) lds_st<f32x4>(aT + t * 1024, kt[t]);
        sig<C_A>(aCnt, one);
        STAMP(4);
        spin(aCnt + 4 * C_A, 2);
        STAMP(19);
        {
            auto tv = [&](u32 a) { return lds_ld<float>(a) + lds_ld<float>(a + 32768); };
            float k[9];
            k[0] = tv(t0); k[8] = tv(t0 + 4); k[4] = tv(t0 + 272 * 4);
            k[1] = tv(tg[0]); k[2] = tv(tg[1]); k[3] = tv(tg[2]); k[5] = tv(tg[3]); k[6] = tv(tg[4]); k[7] = tv(tg[5]);
            const float va = tv(t0 + 512), vb = tv(t0 + 516);
            if (m == 0) k[0] += bf_lo(pD);
            const u32 k10 = pk_bf(k[1], k[0]);
            lds_st<v4u>(kr + 2048, (v4u){pk_bf(k[7], k[6]), pk_bf(k[5], k[4]), pk_bf(k[3], k[2]), k10});
            lds_st<v4u>(kr + 2048 + 4160, (v4u){pk_bf(k[8], k[7]), pk_bf(k[6], k[5]), pk_bf(k[4], k[3]), pk_bf(k[2], k[1])});
            const u32 pa = pk_bf(va, va), pb = pk_bf(vb, vb), pba = pk_bf(vb, va);
            lds_st<v4u>(kr, (v4u){pb, (m & 1) ? pb : pba, pa, pa});
            lds_st<v4u>(kr + 4160, (v4u){pb, pb, (m & 1) ? pba : pa, pa});
            if (m < 4) lds_st<v4u>(L_KB + chl * 8320 + 4096 + (m & 1) * 16 + (m < 2 ? 4160 : 0), (v4u){m == 0 ? k10 >> 16 : 0u, 0u, 0u, 0u});
        }
        sig<C_K>(aCnt, one);
        STAMP(18);
        f32x4 cv[8];
        {
#pragma unroll
            for (int t = 0; t < 8; t++) cv[t] = (f32x4){0.f, 0.f, 0.f, 0.f};
            v4u A[8], B[8];
            auto rdA = [&](int r, int a) { const u32 p = kb[(7 - a) >> 1] + ((7 - a) & 1) * 512 + (3 - r) * 64; return (v4u){lds_ld<u32>(p), lds_ld<u32>(p + 4), lds_ld<u32>(p + 8), lds_ld<u32>(p + 12)}; };
            auto rdB = [&](int r, int d) { return lds_ld<v4u>(ua[r] + d * 1024); };
            {
                int k = 0;
                while (any_(lds_ldv<u32>(aZ3) != want3) && ++k < (SPIN_CAP << 4)) {}
                asm volatile("" ::: "memory");
            }
            STAMP(6);
#pragma unroll
            for (int i = 0; i < 8; i++) { A[7 - i] = rdA(0, 7 - i); B[i] = rdB(0, i); }
            if (b == 0 && w == 0 && lane == 0) __hip_atomic_store((GAS u64*)ws, tag, RLX_AGENT);
            CST(0);
#pragma unroll
            for (int r = 0; r < 4; r++) {
#pragma unroll
                for (int a = 7; a >= 0; a--) {
#pragma unroll
                    for (int d = 0; d + a < 8; d++) {
                        cv[a + d] = mfma16(A[a], B[d], cv[a + d]);
                        if (a == 0 && r < 3) { SB0(); B[d] = rdB(r + 1, d); SB0(); }
                    }
                    if (r < 3) { SB0(); A[a] = rdA(r + 1, a); SB0(); }
                }
            }
        }
        STAMP(7); CST(1);
        {
            const u32 mine = opq(w * 32768 + 16384 + l16), other = opq((w ^ 1) * 32768 + 16384 + l16);
#pragma unroll
            for (int t = 0; t < 4; t++) lds_st<f32x4>(mine + t * 1024, sel4(hf, cv[t], cv[4 + t]));
            sig<C_E>(aCnt, one);
            sig<C_D>(aCtl + (L_CNT - L_C), one);
            spin(aCnt + 4 * C_E, 2);
            u32 o[8];
#pragma unroll
            for (int t = 0; t < 4; t++) {
                const f32x4 y = sel4(hf, cv[4 + t], cv[t]) + lds_ld<f32x4>(other + t * 1024);
                o[2 * t] = pk_bf(y[0], y[1]); o[2 * t + 1] = pk_bf(y[2], y[3]);
            }
            const v4u ras = mkrsrc(ws + OFF_A, 2 << 20);
            st_sc1((v4u){o[0], o[1], o[2], o[3]}, ras, l16, ch * 4096 + hf * 2048);
            st_sc1((v4u){o[4], o[5], o[6], o[7]}, ras, l16, ch * 4096 + hf * 2048 + 1024);
            STAMP(15);
            wait_vm<0>();
            STAMP(8);
            put_flag(ws + OFF_F3, (hf * 512 + ch) * FS3 * 8, l16, tag);
            spin(aCtl + (L_CNT - L_C) + 4 * C_D, 4);
            __builtin_amdgcn_s_sleep(30);
            poll_flags((const GAS u64*)(ws + OFF_F3) + ((tr >> 4) * 512 + tc * 64 + 16 * w + l15) * FS3, tag, lane < 16);
            STAMP(9);
#pragma unroll
            for (int i = 0; i < 2; i++)
                glds_sc1(ras, vA, (tc * 64 + 16 * w + 8 * i) * 4096 + (tr >> 3) * 1024 + (tr & 3) * 256 + ((tr >> 2) & 1) * 128, L_U + w * 2048 + i * 1024);
            const u32 ta = opq(L_U + w * 2048 + (l15 >> 2) * 128 + g * 32 + (lane & 3) * 8), aw = opq(L_KB + lane * 144 + w * 32);
            wait_vm<0>();
            const v2u a0 = lds_tr(ta), a1 = lds_tr(ta + 512), a2 = lds_tr(ta + 1024), a3 = lds_tr(ta + 1536);
            lds_st<v4u>(aw, (v4u){a0.x, a0.y, a1.x, a1.y});
            lds_st<v4u>(aw + 16, (v4u){a2.x, a2.y, a3.x, a3.y});
            STAMP(10);
        }
        wait_lgkm0();
        __builtin_amdgcn_s_barrier();
    }
}

#if defined(__HIP_DEVICE_COMPILE__) && __HIP_DEVICE_COMPILE__
#pragma clang attribute pop
#endif

extern "C" void kernel_launch(void* const* d_in, const int* in_sizes, int n_in, void* d_out, int out_size, void* d_ws, size_t ws_size,
                              hipStream_t stream) {
    fused_kernel<<<256, 768, 0, stream>>>((const u16*)d_in[0], (const u16*)d_in[1], (const u16*)d_in[2], (const u16*)d_in[3], (const u16*)d_in[4],
                                          (const u16*)d_in[5], (const u16*)d_in[6], (const u16*)d_in[8], (const u16*)d_in[9], (const u16*)d_in[10],
                                          (const u16*)d_in[11], (u16*)d_out, (char*)d_ws);
}
```

```cpp
#define QTY 0
#define PROLE 1
#define PROBE 1
#include <hip/hip_runtime.h>
#include <stdint.h>
#include <utility>

typedef unsigned short u16;
typedef unsigned int u32;
typedef unsigned long long u64;
typedef u32 v4u __attribute__((ext_vector_type(4)));
typedef u32 v2u __attribute__((ext_vector_type(2)));
typedef float f32x2 __attribute__((ext_vector_type(2)));
typedef float f32x4 __attribute__((ext_vector_type(4)));
typedef float f32x16 __attribute__((ext_vector_type(16)));
typedef __bf16 bf16x8 __attribute__((ext_vector_type(8)));
typedef __bf16 bf16x2 __attribute__((ext_vector_type(2)));
typedef _Float16 f16x8 __attribute__((ext_vector_type(8)));
typedef _Float16 f16x2 __attribute__((ext_vector_type(2)));
typedef short s16x4 __attribute__((ext_vector_type(4)));
typedef u64 u64x8 __attribute__((ext_vector_type(8)));
typedef u64 u64x4 __attribute__((ext_vector_type(4)));
#define GAS __attribute__((address_space(1)))
#define LAS __attribute__((address_space(3)))
#define DI __device__ __forceinline__
#define RLX_AGENT __ATOMIC_RELAXED, __HIP_MEMORY_SCOPE_AGENT
#define RLX_WG __ATOMIC_RELAXED, __HIP_MEMORY_SCOPE_WORKGROUP
#define SB0() __builtin_amdgcn_sched_barrier(0)
#define NWV 12

constexpr int OFF_ST = 16384, OFF_F1 = 128 << 10, OFF_F2 = 256 << 10, OFF_F3 = 512 << 10;
constexpr int FS1 = 2, FS2 = 16, FS3 = 16;
constexpr int OFF_U = 1 << 20, OFF_A = 4 << 20, OFF_H = 8 << 20;
constexpr int H_BYTES = 64 * 43 * 2048;
constexpr int L_C = 131072;
constexpr int L_FLG = L_C, L_UF = L_C + 320, L_E = L_C + 768, L_B2 = L_C + 896, L_CNT = L_C + 416, L_B1 = L_C + 512, L_STAT = L_C + 1024, L_CSP = L_C + 29696;
enum { R_F = 0, R_P = 8, R_P7 = 13, R_PK = 15, R_LN = 19, R_X = 14, R_V1 = 16, R_V2 = 17, R_V3 = 18 };
constexpr int L_U = L_C + 3072, L_KB = L_C + 12288;
constexpr int SPIN_CAP = 1 << 16;
constexpr int CODE_BYTES = 31488;
enum { C_A = 0, C_K = 2, C_E = 4, C_D = 8, C_RD = 12 };

#define STAMP(i) do { if ((i) == PROBE && prole == PROLE) { probe_t = (u32)__builtin_amdgcn_s_memrealtime(); } } while (0)
#define CST(i)
#define TIMED(x) x

#if defined(__HIP_DEVICE_COMPILE__) && __HIP_DEVICE_COMPILE__
#pragma clang attribute push(__attribute__((target("no-packed-fp32-ops"))), apply_to = function)
#endif
DI float u2f(u32 v) { return __builtin_bit_cast(float, v); }
DI u32 f2u(float v) { return __builtin_bit_cast(u32, v); }
DI bool any_(bool c) { return __builtin_amdgcn_ballot_w64(c) != 0; }
DI bool all_(bool c) { return __builtin_amdgcn_ballot_w64(!c) == 0; }
template <typename T> DI T lds_ld(u32 a) { return *(LAS T*)(size_t)a; }
template <typename T> DI void lds_st(u32 a, T v) { *(LAS T*)(size_t)a = v; }
DI v2u lds_tr(u32 a) { return __builtin_bit_cast(v2u, __builtin_amdgcn_ds_read_tr16_b64_v4i16((LAS s16x4*)(size_t)a)); }
DI v4u mkrsrc(const void* p, u32 bytes) { u64 a = (u64)p; v4u r; r.x = (u32)a; r.y = (u32)(a >> 32) & 0xffffu; r.z = bytes; r.w = 0x00020000u; return r; }
DI void glds(v4u rsrc, u32 voff, u32 soff, u32 lds) {
    asm volatile("s_mov_b32 m0, %3\n\ts_nop 0\n\tbuffer_load_dwordx4 %0, %1, %2 offen lds" :: "v"(voff), "s"(rsrc), "s"(soff), "s"(lds) : "memory");
}
template <int LOFF> DI u32 glds_first(v4u rsrc, u32 voff, u32 sa, u32 sb, u32 lbase) {
    u32 so;
    asm volatile("s_add_u32 m0, %5, %6\n\ts_add_u32 %0, %3, %4\n\tbuffer_load_dwordx4 %1, %2, %0 offen lds" : "=&s"(so) : "v"(voff), "s"(rsrc), "s"(sa), "s"(sb), "s"(lbase), "i"(LOFF) : "memory", "scc");
    return so;
}
template <int LOFF, int SOFF> DI void glds_imm(v4u rsrc, u32 voff, u32 sa, u32 lbase) {
    u32 so;
    asm volatile("s_add_u32 m0, %4, %5\n\ts_add_u32 %0, %3, %6\n\tbuffer_load_dwordx4 %1, %2, %0 offen lds" : "=&s"(so) : "v"(voff), "s"(rsrc), "s"(sa), "s"(lbase), "i"(LOFF), "i"(SOFF) : "memory", "scc");
}
template <int OFF> DI void glds_next(v4u rsrc, u32 voff, u32 so) {
    asm volatile("buffer_load_dwordx4 %0, %1, %2 offen offset:%3 lds" :: "v"(voff), "s"(rsrc), "s"(so), "i"(OFF) : "memory");
}
template <int LOFF> DI void flag_imm(v4u rsrc, u32 lbase) {
    asm volatile("s_add_u32 m0, %2, %3\n\ts_mov_b64 exec, 1\n\tbuffer_load_dword off, %0, %1 lds\n\ts_mov_b64 exec, -1" :: "s"(rsrc), "s"(rsrc.z), "s"(lbase), "i"(LOFF) : "memory", "scc");
}
DI void glds_sc1(v4u rsrc, u32 voff, u32 soff, u32 lds) {
    asm volatile("s_mov_b32 m0, %3\n\ts_nop 0\n\tbuffer_load_dwordx4 %0, %1, %2 offen sc1 lds" :: "v"(voff), "s"(rsrc), "s"(soff), "s"(lds) : "memory");
}
DI void flag_dma(v4u rsrc, u32 lds) {
    asm volatile("s_mov_b32 m0, %2\n\ts_mov_b64 exec, 1\n\tbuffer_load_dword off, %0, %1 lds\n\ts_mov_b64 exec, -1" :: "s"(rsrc), "s"(rsrc.z), "s"(lds) : "memory");
}
DI void st_sc1(v4u v, v4u rsrc, u32 voff, u32 soff) {
    asm volatile("buffer_store_dwordx4 %0, %1, %2, %3 offen sc1\n\ts_nop 1" :: "v"(v), "v"(voff), "s"(rsrc), "s"(soff) : "memory");
}
DI void put_flag(const char* base, u32 off, u32 l16, u64 tag) {
    asm volatile("s_mov_b64 exec, 1\n\tglobal_store_dwordx2 %0, %1, %2 sc1\n\ts_mov_b64 exec, -1" :: "v"(l16), "v"(tag), "s"(base + off) : "memory");
}
template <int BYTES> DI void warm_flag(const char* base, u32 off, u32 l16) {
    const v4u v = {0u, 0u, 0u, 0u};
    asm volatile("s_mov_b64 exec, %3\n\tglobal_store_dwordx4 %0, %1, %2 sc1\n\ts_mov_b64 exec, -1\n\ts_nop 0" :: "v"(l16), "v"(v), "s"(base + off), "i"((1 << (BYTES / 16)) - 1) : "memory");
}
DI u32 ldp(const GAS u16* p) { return __builtin_nontemporal_load(p); }
DI u32 opq(u32 a) { asm volatile("" : "+v"(a)); return a; }
template <typename T> DI T lds_ldv(u32 a) { return *(volatile LAS T*)(size_t)a; }
template <int SLEEP = 0> DI void spin_zero4(u32 a) {
    int n = 0;
    while (any_(lds_ldv<u32>(a) != 0) && ++n < (SPIN_CAP << 4)) { if (SLEEP) __builtin_amdgcn_s_sleep(1); }
    asm volatile("" ::: "memory");
}
template <int N> DI void wait_vm() { asm volatile("s_waitcnt vmcnt(%0)" :: "i"(N) : "memory"); }
DI void wait_lgkm0() { asm volatile("s_waitcnt lgkmcnt(0)" ::: "memory"); }
DI u64 mix64(u64 e) { e += 0x9E3779B97F4A7C15ull; e ^= e >> 31; e *= 0xBF58476D1CE4E5B9ull; e ^= e >> 29; return e; }
DI u64 get_tag() { const v2u e = lds_ldv<v2u>(L_E); return mix64(((u64)(u32)__builtin_amdgcn_readfirstlane(e.y) << 32) | (u32)__builtin_amdgcn_readfirstlane(e.x)); }
template <int C> DI void sig(u32 a, u32 one) { asm volatile("s_mov_b64 exec, 1\n\tds_add_u32 %0, %1 offset:%2\n\ts_mov_b64 exec, -1" :: "v"(a), "v"(one), "i"(4 * C) : "memory"); }
template <int OFF> DI void clr(u32 a, u32 zero) { asm volatile("s_mov_b64 exec, 1\n\tds_write_b32 %0, %1 offset:%2\n\ts_mov_b64 exec, -1" :: "v"(a), "v"(zero), "i"(OFF) : "memory"); }
DI void spin(u32 a, u32 target) {
    int n = 0;
    while ((u32)__builtin_amdgcn_readfirstlane(lds_ldv<u32>(a)) < target && ++n < (SPIN_CAP << 4)) {}
    asm volatile("" ::: "memory");
}
DI u64 peek_flags(const GAS u64* p, u64 tag, bool on) { u64 f = tag; if (on) f = __hip_atomic_load(p, RLX_AGENT); return f; }
DI void poll_flags(const GAS u64* p, u64 tag, bool on) {
    int n = 0;
    while (true) {
        u64 f = peek_flags(p, tag, on);
        if (all_(f == tag) || ++n > SPIN_CAP) break;
        __builtin_amdgcn_s_sleep(2);
    }
    asm volatile("" ::: "memory");
}
DI float bf_lo(u32 v) { return u2f(v << 16); }
DI float bf_hi(u32 v) { return u2f(v & 0xffff0000u); }
DI u32 pk_bf(float a, float b) { f32x2 v = {a, b}; return __builtin_bit_cast(u32, __builtin_convertvector(v, bf16x2)); }
typedef short s16x2 __attribute__((ext_vector_type(2)));
DI u32 relu_pk(u32 v) { return __builtin_bit_cast(u32, __builtin_elementwise_max(__builtin_bit_cast(s16x2, v), (s16x2){0, 0})); }
DI u32 umax(u32 a, u32 b) { return a > b ? a : b; }
DI float rbf(float a) { return bf_lo(pk_bf(a, a)); }
DI u32 pk_h(f32x2 v) { return __builtin_bit_cast(u32, __builtin_convertvector(v, f16x2)); }
DI f32x16 mfma32(v4u a, v4u b, f32x16 c) { return __builtin_amdgcn_mfma_f32_32x32x16_bf16(__builtin_bit_cast(bf16x8, a), __builtin_bit_cast(bf16x8, b), c, 0, 0, 0); }
DI f32x4 mfma16(v4u a, v4u b, f32x4 c) { return __builtin_amdgcn_mfma_f32_16x16x32_bf16(__builtin_bit_cast(bf16x8, a), __builtin_bit_cast(bf16x8, b), c, 0, 0, 0); }
DI f32x4 mfma16h(v4u a, v4u b, f32x4 c) { return __builtin_amdgcn_mfma_f32_16x16x32_f16(__builtin_bit_cast(f16x8, a), __builtin_bit_cast(f16x8, b), c, 0, 0, 0); }
template <int CTRL> DI float dpp_add(float v) { return v + __builtin_bit_cast(float, __builtin_amdgcn_update_dpp(0, __builtin_bit_cast(int, v), CTRL, 0xf, 0xf, true)); }
template <int CTRL> DI u32 dpp_max(u32 v) { const u32 o = (u32)__builtin_amdgcn_update_dpp(0, (int)v, CTRL, 0xf, 0xf, true); return v > o ? v : o; }
template <int T> DI float bcast(float v) { return __builtin_bit_cast(float, __builtin_amdgcn_update_dpp(0, __builtin_bit_cast(int, v), 0x150 + T, 0xf, 0xf, true)); }
DI u32 wave_max(u32 v) {
    v = dpp_max<0xB1>(v); v = dpp_max<0x4E>(v); v = dpp_max<0x141>(v); v = dpp_max<0x140>(v);
    const u32 a = __builtin_amdgcn_readlane(v, 0), c = __builtin_amdgcn_readlane(v, 16), d = __builtin_amdgcn_readlane(v, 32), e = __builtin_amdgcn_readlane(v, 48);
    const u32 p = a > c ? a : c, q = d > e ? d : e;
    return p > q ? p : q;
}
struct cmult { f32x2 b, n; };
DI cmult mkc(f32x2 b) { cmult c; c.b = b; c.n = (f32x2){-b.y, b.x}; return c; }
DI f32x2 cmul(f32x2 a, const cmult& c) { return __builtin_elementwise_fma((f32x2){a.x, a.x}, c.b, (f32x2){a.y, a.y} * c.n); }
DI f32x16 mfma32h(v4u a, v4u b, f32x16 c) { return __builtin_amdgcn_mfma_f32_32x32x16_f16(__builtin_bit_cast(f16x8, a), __builtin_bit_cast(f16x8, b), c, 0, 0, 0); }
DI u32 pk_h2(float a, float b) { return pk_h((f32x2){a, b}); }
DI f32x2 cexp_pow(float lr, float th, float tl, float e) {
    float a = __builtin_amdgcn_fractf(th * e) + tl * e;
    float m = __builtin_amdgcn_exp2f(lr * e);
    f32x2 r; r.x = m * __builtin_amdgcn_cosf(a); r.y = m * __builtin_amdgcn_sinf(a); return r;
}
DI f32x4 sel4(bool c, f32x4 a, f32x4 b) { return c ? a : b; }
constexpr int w2loc(int n) { return n < 16 ? n : n < 28 ? n + 4 : n - 28; }
template <class F, int... I> DI void sfor_(F&& f, std::integer_sequence<int, I...>) { (f(std::integral_constant<int, I>{}), ...); }
template <int N, class F> DI void sfor(F&& f) { sfor_(f, std::make_integer_sequence<int, N>{}); }
template <int I> using IC = std::integral_constant<int, I>;
template <typename T> DI void pin(T& v) { asm volatile("" : "+v"(v)); }

__global__ void __launch_bounds__(768, 3)
fused_kernel(const u16* x_, const u16* logA_, const u16* Aim_, const u16* Cre_, const u16* Cim_, const u16* logdt_, const u16* Dskip_, const u16* w1_,
             const u16* b1_, const u16* w2_, const u16* b2_, u16* out_, char* ws_) {
    u64x8 ka; u64x4 kb4; u64 kc;
    asm volatile("s_load_dwordx16 %0, %3, 0x0\n\ts_load_dwordx8 %1, %3, 0x40\n\ts_load_dwordx2 %2, %3, 0x60" : "=&s"(ka), "=&s"(kb4), "=&s"(kc) : "s"(__builtin_amdgcn_kernarg_segment_ptr()) : "memory");
    __shared__ __attribute__((aligned(16))) char smem[163840];
    asm volatile("" :: "v"((u32)(size_t)(LAS char*)smem));
    const int tid = threadIdx.x, lane = tid & 63, wave = __builtin_amdgcn_readfirstlane(tid >> 6) & 15, w = wave & 3, role = wave >> 2;
    const int b = blockIdx.x & 255, xcd = b & 7, slot = b >> 3;
    const int P = 4 * (xcd >> 1) + (slot >> 3), j = 8 * (xcd & 1) + (slot & 7);
    const int tr = 4 * xcd + (slot >> 3), tc = slot & 7;
    const int u0 = (j * 43) >> 4;
    const int prole = role; u32 probe_t = 0;
    u64 t00; asm volatile("s_memrealtime %0" : "=s"(t00) :: "memory");
    if (wave == 4) {
        u64 pc; asm volatile("s_getpc_b64 %0" : "=s"(pc));
        const v4u rc = mkrsrc((const void*)(pc & ~255ull), CODE_BYTES);
        glds(rc, lane * 16, slot * 1024, L_C + 2048);
    }
    const int h = lane >> 5, l31 = lane & 31, l15 = lane & 15, g = lane >> 4;
    const u32 l16 = lane * 16, lw = w * 32768, w4 = w * 4;
    const int rho = ((slot >> 3) + slot) & 3;
    const int tb = 64 * (P >> 2) + 32 * (j & 1) + 16 * ((P >> 1) & 1) + 2 * (j >> 1) + (P & 1);
    const int cp = 32 * xcd + slot, chl = w >> 1, hf = w & 1, ch = 2 * cp + chl;
    {
        const u32 v = (tid >= 192 && tid < 256) || (tid >= 282 && tid < 286) ? 0u : ~0u;
        if (tid < 288) lds_st<v4u>((tid < 192 ? L_CSP : tid < 256 ? L_U - 192 * 16 : L_C - 256 * 16) + tid * 16, (v4u){v, v, v, v});
    }
    const int rot = (((slot >> 3) + slot) & 1) * 4;
    u32 sx = (512 * (P >> 2) + 128 * ((P >> 1) & 1) + 64 * (w & 1) + 8 * (P & 1) + 4 * (w >> 1)) * 1024, sw1 = 16 * w * 2752 + u0 * 64;
    u32 sx0 = rot * 128, sw0 = rot * (64 * 2752);
    u32 vx[4], vw[3];
    const int lim3 = 4 * ((((j + 1) * 43) >> 4) - u0);
#pragma unroll
    for (int i = 0; i < 3; i++) { const int p = 64 * i + lane, r = (p * 171) >> 11; vw[i] = opq(p - 12 * r >= lim3 ? 0x40000000u : r * (2752 - 192) + l16); }
#pragma unroll
    for (int i = 0; i < 4; i++) vx[i] = opq((((h * 256 + ((lane >> 3) & 3)) * 1024 + ((lane & 7) ^ g) * 16) ^ ((i & 1) * 64)) + i * (16384 - 1024));
    asm volatile("s_waitcnt lgkmcnt(0)\n\ts_barrier" : "+s"(ka), "+s"(kb4), "+s"(kc), "+s"(sx), "+s"(sw1), "+s"(sx0), "+s"(sw0) :: "memory");
    asm volatile("" : "+s"(t00)); const u32 t_start = (u32)t00;
    STAMP(30);
    const u16 *x = (const u16*)ka[0], *logA = (const u16*)ka[1], *Aim = (const u16*)ka[2], *Cre = (const u16*)ka[3], *Cim = (const u16*)ka[4], *logdt = (const u16*)ka[5], *Dskip = (const u16*)ka[6], *w1 = (const u16*)ka[7];
    const u16 *b1 = (const u16*)kb4[0], *w2 = (const u16*)kb4[1], *b2 = (const u16*)kb4[2]; u16* out = (u16*)kb4[3]; char* ws = (char*)kc;
    const v4u rx = mkrsrc(x, 2 << 20);
    const u32 aF = opq(L_FLG + (lane & 3) * 4);
    auto flg = [&](auto R_) { flag_imm<L_FLG + decltype(R_)::value * 16>(rx, w4); };
    const v4u rw1 = mkrsrc(w1, 512 * 1376 * 2);
    auto ldX = [&](auto C_) { constexpr int c = decltype(C_)::value;
        const u32 so = glds_first<(16 + (c % 4) * 4) * 1024>(rx, vx[0], sx, c ? ((c + rot) & 7) * 128 : sx0, lw);
        glds_next<1024>(rx, vx[1], so); glds_next<2048>(rx, vx[2], so); glds_next<3072>(rx, vx[3], so); };
    auto ldW = [&](auto C_) { constexpr int c = decltype(C_)::value;
        const u32 so = glds_first<3 * (c % 5) * 1024>(rw1, vw[0], sw1, c ? ((c + rot) & 7) * (64 * 2752) : sw0, lw);
        glds_next<1024>(rw1, vw[1], so); glds_next<2048>(rw1, vw[2], so); };
    if (role == 0) {
        ldW(IC<0>{}); ldX(IC<0>{}); flg(IC<R_F>{});
        ldW(IC<1>{}); ldX(IC<1>{}); flg(IC<R_F + 1>{});
    }
    asm volatile("" ::: "memory");
    u32 aV[4], aFw, aCn, one, f2o;
    u32 sh2[4], sh2t[4];
    auto pre2 = [&]() {
#pragma unroll
        for (int dt = 0; dt < 4; dt++) aV[dt] = opq(lw + h * 2048 + (4 * (g & 1) + (l15 >> 2)) * 128 + (dt ^ (2 * (g & 1) + (l15 >> 3))) * 32 + (lane & 3) * 8);
        aFw = opq(L_FLG + w4); aCn = opq(L_CNT + w4); one = opq(1u);
        const int jj = 4 * w + (lane < 4 ? lane : 4);
        f2o = opq(((2 * tr + role) * 16 + (jj < 15 ? jj : 15)) * FS2 * 8);
#pragma unroll
        for (int k = 0; k < 4; k++) {
            sh2[k] = ((2 * tr + role) * 43 + 11 * w) * 2048 + (6 * rho - (k + rho >= 4 ? 22 : 0)) * 1024; sh2t[k] = (w == 3 && k + rho == 3) ? (u32)H_BYTES : sh2[k];
            asm volatile("" : "+s"(sh2[k]), "+s"(sh2t[k]));
        }
    };

    auto rdV = [&](int s, int i) { const u32 a = aV[2 * (s & 1) + i] + w2loc(4 * (s >> 1)) * 1024; v2u lo = lds_tr(a), hi = lds_tr(a + 1024); return (v4u){lo.x, lo.y, hi.x, hi.y}; };
    auto gemm2 = [&](auto ROLE_, v4u (&stage)[16], u64 f2, u64 tag) __attribute__((always_inline)) {
        constexpr int role = decltype(ROLE_)::value;
        const GAS u64* f2p = (const GAS u64*)(ws + OFF_F2 + f2o);
        __builtin_amdgcn_s_setprio(0);
        const __amdgpu_buffer_rsrc_t rh = __builtin_amdgcn_make_buffer_rsrc(ws + OFF_H, 0, H_BYTES, 0x00020000);
        constexpr int WIN = role ? 10 : 8;
        v4u park[8][2], hq[WIN], vb[3][2];
        auto ldH = [&](int s) { hq[s % WIN] = __builtin_amdgcn_raw_buffer_load_b128(rh, l16, ((s + 2) % 6 >= 4 ? sh2t[(s + 2) / 6] : sh2[(s + 2) / 6]) + s * 1024, 16); };
        constexpr int W0 = 8;
        if (role) {
            SB0();
            spin_zero4<1>(aFw + R_V1 * 16);
            STAMP(9);
            f2 = tag;
            asm volatile("s_mov_b64 exec, 31\n\tglobal_load_dwordx2 %0, %1, %2 sc1\n\ts_mov_b64 exec, -1" : "+v"(f2) : "v"(f2o), "s"(ws + OFF_F2) : "memory");
            SB0();
#pragma unroll
            for (int s = 0; s < 8; s++) { park[s][0] = rdV(s, 0); park[s][1] = rdV(s, 1); }
            clr<R_PK * 16>(aFw, opq(0u));
            asm volatile("s_waitcnt vmcnt(0)" : "+v"(f2) :: "memory");
            SB0();
        }
        STAMP(10);
        if (!all_(f2 == tag)) poll_flags(f2p, tag, lane < 5);
        STAMP(5);
#pragma unroll
        for (int s = 0; s < W0; s++) ldH(s);
        const int q3 = (lane >> 2) & 7, fr = 8 * wave + 4 * h + 2 * ((q3 ^ (q3 >> 1) ^ (q3 >> 2)) & 1) + (q3 >> 2), fo = ((q3 >> 1) & 1) * 4 + (lane & 3);
        const u32 fin_off = (512 * (tr >> 3) + 256 * ((fr >> 2) & 1) + 128 * ((tr >> 2) & 1) + 16 * (fr >> 3) + 4 * (tr & 3) + (fr & 3)) * 512 + tc * 64 + fo * 8;
        f32x4 s0, s1; float rs2; u32 da, ra, rb, rc;
        v4u fin_x, fin_b; u32 fin_s, fv;
        f32x4 ac2[4][2];
#pragma unroll
        for (int i = 0; i < 8; i++) ac2[i >> 1][i & 1] = (f32x4){0.f, 0.f, 0.f, 0.f};
        const u32 aSt = opq(lw + l16), zero = opq(0u);
        if (!role) {
            SB0();
            spin_zero4(opq(aFw + ((lane & 1) ? R_PK : R_V2) * 16));
            STAMP(9);
            vb[0][0] = rdV(0, 0); vb[0][1] = rdV(0, 1); vb[1][0] = rdV(1, 0); vb[1][1] = rdV(1, 1);
            asm volatile("" ::: "memory");
#pragma unroll
            for (int k = 0; k < 4; k++) lds_st<v4u>(aSt + k * 1024, stage[k]);
        }
#pragma unroll
        for (int s = 0; s < 22; s++) {
            SB0();
            if (role && (s == 4 || s == 10)) fv = lds_ldv<u32>(aFw + (s == 4 ? R_V2 : R_V3) * 16);
            if (role && (s == 6 || s == 12)) { if (__builtin_expect(any_(fv != 0), 0)) spin_zero4(aFw + (s == 6 ? R_V2 : R_V3) * 16); asm volatile("" ::: "memory"); }
            if ((!role || s + 2 >= 8) && s + 2 < 22) { vb[(s + 2) % 3][0] = rdV(s + 2, 0); vb[(s + 2) % 3][1] = rdV(s + 2, 1); }
            if (s == 19) sig<C_RD>(aCn, one);
            if (s == 21) fv = lds_ldv<u32>(aCn + 4 * C_RD);
#pragma unroll
            for (int q = 0; q < 4; q++) ac2[2 * (s & 1) + (q & 1)][q >> 1] = mfma16(role && s < 8 ? park[s & 7][q & 1] : vb[s % 3][q & 1], hq[((s & ~1) + (q >> 1)) % WIN], ac2[2 * (s & 1) + (q & 1)][q >> 1]);
            SB0();
            if (!role && s >= 1 && s < 7) {
                asm volatile("" ::: "memory");
                lds_st<v4u>(aSt + (2 * s + 2) * 1024, stage[(2 * s + 2) & 15]); lds_st<v4u>(aSt + (2 * s + 3) * 1024, stage[(2 * s + 3) & 15]);
                if (s == 6) { clr<R_V3 * 16>(aFw, zero); STAMP(13); }
            }
            if (s == 0) { for (int k = W0; k < WIN; k++) ldH(k); }
            if ((s & 1) && s + WIN < 22) { ldH(s - 1 + WIN); ldH(s + WIN); }
            if (s == 13) {
                fin_x = *(const GAS v4u*)(x + fin_off); fin_b = lds_ld<v4u>(L_B2 + fo * 16);
                fin_s = __hip_atomic_load((GAS u32*)(ws + OFF_ST + (tr >> 1) * 1024 + (fr >> 2) * 64 + (tr & 1) * 32 + (fr & 3) * 8), RLX_AGENT);
            }
            if (s == 16) { da = opq(lw + role * 20480 + l15 * 272 + g * 16); ra = opq(role * 20480 + (fr & 31) * 272 + fo * 32); rb = opq(ra + 65536); rc = opq(L_KB + fr * 144 + fo * 16); }
            if (s == 21) {
                s0 = (f32x4){bf_lo(fin_x.x) + bf_lo(fin_b.x), bf_hi(fin_x.x) + bf_hi(fin_b.x), bf_lo(fin_x.y) + bf_lo(fin_b.y), bf_hi(fin_x.y) + bf_hi(fin_b.y)};
                pin(s0);
            }
        }
        SB0();
        STAMP(7); CST(3);
        if (__builtin_expect(any_(fv < 2), 0)) spin(aCn + 4 * C_RD, 2);
        asm volatile("" ::: "memory");
#pragma unroll
        for (int i = 0; i < 8; i++) lds_st<f32x4>(da + (i & 1) * 16 * 272 + (i >> 1) * 64, ac2[i >> 1][i & 1]);
        SB0();
        s1 = (f32x4){bf_lo(fin_x.z) + bf_lo(fin_b.z), bf_hi(fin_x.z) + bf_hi(fin_b.z), bf_lo(fin_x.w) + bf_lo(fin_b.w), bf_hi(fin_x.w) + bf_hi(fin_b.w)};
        rs2 = __builtin_amdgcn_rcpf(bf_hi(fin_s)); rs2 *= rs2;
        asm volatile("" : "+v"(s0), "+v"(s1), "+v"(rs2));
        wait_lgkm0();
        __builtin_amdgcn_s_barrier();
        asm volatile("" ::: "memory");
        STAMP(11);
        const v4u av = lds_ld<v4u>(rc);
        s0 += ((lds_ld<f32x4>(ra) + lds_ld<f32x4>(ra + 32768)) + (lds_ld<f32x4>(rb) + lds_ld<f32x4>(rb + 32768))) * rs2;
        s1 += ((lds_ld<f32x4>(ra + 16) + lds_ld<f32x4>(ra + 32768 + 16)) + (lds_ld<f32x4>(rb + 16) + lds_ld<f32x4>(rb + 32768 + 16))) * rs2;
        s0 += (f32x4){bf_lo(av.x), bf_hi(av.x), bf_lo(av.y), bf_hi(av.y)};
        s1 += (f32x4){bf_lo(av.z), bf_hi(av.z), bf_lo(av.w), bf_hi(av.w)};
        const v4u ov = {pk_bf(s0[0], s0[1]), pk_bf(s0[2], s0[3]), pk_bf(s1[0], s1[1]), pk_bf(s1[2], s1[3])};
        st_sc1(ov, mkrsrc(out, 2 << 20), fin_off * 2, 0);
    };

    if (__builtin_expect(role == 0, 1)) {
        v4u stage[16]; u64 f2, tag;
        const u32 sw2 = 352 * w * 1024 + tc * 128;
        const u32 vv = (lane >> 3) * 1024 + ((lane & 7) ^ (g << 1)) * 16;
        u32 sv[4];
#pragma unroll
        for (int k = 0; k < 4; k++) sv[k] = sw2 + (12 * rho - (k + rho >= 4 ? 44 : 0)) * 8192;
        const __amdgpu_buffer_rsrc_t rv = __builtin_amdgcn_make_buffer_rsrc((void*)w2, 0, 512 * 1376 * 2, 0x00020000);
        auto ldV = [&](int r) { stage[r] = __builtin_amdgcn_raw_buffer_load_b128(rv, vv, sv[(r + 32) / 12] + (r + 28) * 8192, 0); };
        const GAS u64* f1p = (const GAS u64*)(ws + OFF_F1) + ((xcd >> 1) * 256 + 64 * w + lane) * FS1;
        sfor<6>([&](auto I) {
            constexpr int c = decltype(I)::value + 2;
            ldW(IC<c>{});
            if constexpr (c >= 4) {
                SB0();
#pragma unroll
                for (int k = 0; k < 3; k++) ldV(3 * (c - 4) + k);
                SB0();
            }
            if constexpr (c >= 4) TIMED(spin_zero4<1>(aF + (R_P + c - 4) * 16));
            ldX(IC<c>{});
            flg(IC<R_F + c>{});
        });
        STAMP(1);
        {
            const u32 vS = ((lane >> 1) * 4 + (lane & 1)) * 16;
            const v4u rst = mkrsrc(ws + OFF_ST, 16384);
            asm volatile("s_mov_b32 m0, %3\n\ts_mov_b64 exec, 0xffff\n\tbuffer_load_dwordx4 %0, %1, %2 offen sc1 lds\n\ts_mov_b64 exec, -1" :: "v"(vS), "s"(rst), "s"(P * 1024 + (w & 1) * 512 + (w >> 1) * 32), "s"(L_STAT + w * 256) : "memory");
        }
        spin_zero4<1>(aF + R_X * 16);
        tag = get_tag();
        u64 f1;
        asm volatile("global_load_dwordx2 %0, %1, off sc1" : "=&v"(f1) : "v"(f1p) : "memory");
#pragma unroll
        for (int n = 12; n < 16; n++) ldV(n);
        SB0();
        asm volatile("s_waitcnt vmcnt(4)" : "+v"(f1) :: "memory");
        if (!all_(f1 == tag)) poll_flags(f1p, tag, true);
        STAMP(16);
        {
            const v4u rus = mkrsrc(ws + OFF_U, 2 << 20);
            const u32 vU = (lane >> 1) * 8192 + ((lane ^ g) & 1) * 16;
#pragma unroll
            for (int i = 0; i < 2; i++) glds_sc1(rus, vU, (64 * w + 32 * i) * 8192 + cp * 32, L_U + 1024 + w * 2048 + i * 1024);
            flag_dma(rus, L_UF + w * 4);
        }
        STAMP(2);
        pre2();
#pragma unroll
        for (int n = 0; n < 16; n++) pin(stage[n]);
        {
            const u32 aK = opq((lane & 4) ? L_CNT + 4 * C_K + (lane & 1) * 4 : L_FLG + R_P7 * 16 + (lane & 3) * 4);
            const u32 want = opq((lane & 4) ? 2u : 0u);
            int k = 0;
            while (any_(lds_ldv<u32>(aK) != want) && ++k < (SPIN_CAP << 4)) { __builtin_amdgcn_s_sleep(1); }
            asm volatile("" ::: "memory");
        }
        STAMP(14);
        const v4u rw2 = mkrsrc(w2, 512 * 1376 * 2);
        f2 = tag;
        sfor<28>([&](auto I) {
            constexpr int n = decltype(I)::value;
            if constexpr (n >= 6) wait_vm<6>();
            if constexpr (n == 26) asm volatile("s_mov_b64 exec, 31\n\tglobal_load_dwordx2 %0, %1, %2 sc1\n\ts_mov_b64 exec, -1" : "+v"(f2) : "v"(f2o), "s"(ws + OFF_F2) : "memory");
            glds_imm<w2loc(n) * 1024, n * 8192>(rw2, vv, sv[(n + 4) / 12], lw);
            if constexpr (n == 15) flg(IC<R_V1>{});
        });
        flg(IC<R_V2>{});
        asm volatile("s_waitcnt vmcnt(3)" : "+v"(f2) :: "memory");
        STAMP(8);
        gemm2(IC<0>{}, stage, f2, tag);
    } else if (role == 1) {
        u64 tag;
        __builtin_amdgcn_s_setprio(2);
        warm_flag<FS2 * 8>(ws + OFF_F2, ((4 * P + w) * 16 + j) * FS2 * 8, l16);
        u32 aW[4], aX[4];
#pragma unroll
        for (int s = 0; s < 4; s++) {
            const int ks = (s + w) & 3;
            aW[s] = opq(ks * 32768 + (8 * h + (l15 >> 2)) * 192 + (g & 1) * 32 + (lane & 3) * 8);
            aX[s] = opq(lw + 16384 + l31 * 128 + (((2 * ks + h) ^ ((l31 >> 1) & 7)) * 16));
        }
        const u32 aC = opq(L_CSP + l31 * 32 + h * 16), aCw = opq(L_CSP + lane * 32 + w * 8), aS = opq(L_STAT + w * 256 + l31 * 8), aB = opq(L_B1 + lane * 2);
        pre2();
        const u32 zero = opq(0u);
        f32x16 acc[3]; f32x4 cs = {0.f, 0.f, 0.f, 0.f};
        v4u sel[3];
#pragma unroll
        for (int u = 0; u < 3; u++) {
            for (int i = 0; i < 16; i++) acc[u][i] = 0.f;
            const u32 sv = (l15 == (u < 2 ? 8 * u : 1) + 4 * (g & 1)) ? 0xBF80BF80u : 0u; sel[u] = (v4u){sv, sv, sv, sv}; pin(sel[u]);
        }
        v4u fa[3][3], fb[2][4];
        auto rdW = [&](int n, int u) { const u32 a = aW[n & 3] + ((n >> 2) % 5) * 3072 + u * 64; v2u lo = lds_tr(a), hi = lds_tr(a + 768); fa[n % 3][u] = (v4u){lo.x, lo.y, hi.x, hi.y}; };
        auto rdX = [&](int c, int s) { fb[c & 1][s] = lds_ld<v4u>(aX[s] + (c % 4) * 4096); };
        const v4u rhs = mkrsrc(ws + OFF_H, H_BYTES);
        const u32 vH = opq((g & 1) * 1024 + h * 256 + l15 * 16);
        const int nown = (((j + 1) * 43) >> 4) - u0;
        const u32 sh = ((4 * P + w) * 43 + u0) * 2048, shx = nown == 3 ? sh + 4096 : (u32)H_BYTES;
        spin_zero4(aF);
        STAMP(1);
        rdX(0, 0);
#pragma unroll
        for (int u = 0; u < 3; u++) rdW(0, u);
        rdX(0, 1);
#pragma unroll
        for (int u = 0; u < 3; u++) rdW(1, u);
        rdX(0, 2); rdX(0, 3);
        u32 fl, fx, bb[2]; v2u ee; v2u stg; v4u fas[3], fbs;
        sfor<32>([&](auto N_) __attribute__((always_inline)) {
            constexpr int n = decltype(N_)::value, c = n >> 2, s = n & 3;
            SB0();
            if (s == 0 && c > 0) STAMP(40 + c);
            if constexpr (s == 2 && c < 5) {
                pin(fa[(n + 1) % 3][2]);
                flg(IC<R_P + c>{});
            }
            if (s == 2 && c < 7) {
                if (__builtin_expect(any_(fl != 0), 0)) TIMED(spin_zero4(aF + (R_F + c + 1) * 16));
                asm volatile("" ::: "memory");
            }
            sfor<3>([&](auto U_) __attribute__((always_inline)) {
                constexpr int u = decltype(U_)::value;
                acc[u] = mfma32(fa[n % 3][u], fb[c & 1][s], acc[u]);
                if (s == 0 && c < 7) cs = mfma16(sel[u], fa[n % 3][u], cs);
                if (n == 27) cs = mfma16(sel[u], fa[28 % 3][u], cs);
                SB0();
                if (s >= 2 && c < 7 && u < 2) rdX(c + 1, 2 * (s - 2) + u);
                if (n + 2 < 32) rdW(n + 2, u);
                if (s == 1 && u == 0 && c < 7) fl = lds_ldv<u32>(aF + (R_F + c + 1) * 16);
                if (n == 25 && u == 2) { bb[0] = lds_ld<u16>(aB); bb[1] = lds_ld<u16>(aB + 128); }
                if (n == 21 && u == 1) fx = lds_ldv<u32>(aF + R_X * 16);
                if (n == 23 && u == 1) { if (__builtin_expect(any_(fx != 0), 0)) spin_zero4(aF + R_X * 16); asm volatile("" ::: "memory"); ee = lds_ldv<v2u>(L_E); }
                if (n == 28 && u == 1) {
                    v2u t[2];
#pragma unroll
                    for (int k = 0; k < 2; k++) {
                        const float d = cs[k] - bf_hi(f2u(cs[k]));
                        t[k] = (v2u){__builtin_amdgcn_perm(f2u(d), f2u(cs[k]), 0x07060302u), pk_bf(d - bf_hi(f2u(d)), bf_lo(bb[k]))};
                    }
                    asm volatile("ds_write2st64_b64 %0, %1, %2 offset1:4" :: "v"(aCw), "v"(t[0]), "v"(t[1]) : "memory");
                }
                if (n == 31 && u == 0) { fas[0] = lds_ldv<v4u>(aC); fas[1] = lds_ldv<v4u>(aC + 1024); fas[2] = lds_ldv<v4u>(aC + 2048); }
                if (n == 30 && u == 0) stg = lds_ldv<v2u>(aS);
                if (n == 30 && u == 1) { tag = mix64(((u64)(u32)__builtin_amdgcn_readfirstlane(ee.y) << 32) | (u32)__builtin_amdgcn_readfirstlane(ee.x)); asm volatile("" : "+s"(tag)); }
                SB0();
            });
        });
        {
            auto csbad = [&]() { return umax(umax(umax(fas[0].x, fas[0].z), umax(fas[1].x, fas[1].z)), umax(fas[2].x, fas[2].z)) == ~0u; };
            const u32 want = (lane & 1) ? (u32)(tag >> 32) : (u32)tag;
            if (__builtin_expect(any_(csbad() || stg.y != want), 0)) {
                int k = 0;
                while (any_(csbad()) && ++k < (SPIN_CAP << 2)) { fas[0] = lds_ldv<v4u>(aC); fas[1] = lds_ldv<v4u>(aC + 1024); fas[2] = lds_ldv<v4u>(aC + 2048); }
                k = 0;
                const GAS u64* sp = (const GAS u64*)(ws + OFF_ST + P * 1024 + (w & 1) * 512 + (w >> 1) * 32 + (l31 >> 2) * 64 + (lane & 3) * 8);
                while (any_(stg.y != want) && ++k < SPIN_CAP) { const u64 gr = __hip_atomic_load(sp, RLX_AGENT); stg = (v2u){(u32)gr, (u32)(gr >> 32)}; }
            }
            const u32 m1 = stg.x & 0xffffu, m2 = m1 * 0x10001u;
            fbs = (v4u){m2, h ? m1 : stg.x, m2, m1};
        }
#pragma unroll
        for (int u = 0; u < 3; u++) acc[u] = mfma32(fas[u], fbs, acc[u]);
        STAMP(2); CST(1);
        sfor<6>([&](auto K_) __attribute__((always_inline)) {
            constexpr int u = decltype(K_)::value >> 1, s = decltype(K_)::value & 1;
            v4u o;
#pragma unroll
            for (int e = 0; e < 4; e++) {
                const float a0 = acc[u][8 * s + 2 * e], a1 = acc[u][8 * s + 2 * e + 1];
                o[e] = relu_pk(pk_bf(a0 * __builtin_fabsf(a0), a1 * __builtin_fabsf(a1)));
            }
            st_sc1(o, rhs, vH, (u < 2 ? sh + u * 2048 : shx) + s * 512);
            SB0();
        });
        STAMP(8);
        clr<R_P7 * 16>(aFw, zero);
        wait_vm<0>();
        STAMP(4);
        put_flag(ws + OFF_F2, ((4 * P + w) * 16 + j) * FS2 * 8, l16, tag);
        v4u none[16];
        gemm2(IC<1>{}, none, ~tag, tag);
    } else {
        u64 tag;
        __builtin_amdgcn_s_setprio(3);
        glds(rx, l16, (8 * tb + 2 * w) * 1024, L_KB + (2 * w) * 1088);
        glds(rx, l16, (8 * tb + 2 * w + 1) * 1024, L_KB + (2 * w + 1) * 1088);
        if (w == 0) {
            const v4u re = mkrsrc(ws, 16);
            asm volatile("s_mov_b32 m0, %1\n\ts_mov_b64 exec, 1\n\tbuffer_load_dwordx4 off, %0, 0 sc1 lds\n\ts_mov_b64 exec, -1" :: "s"(re), "s"(L_E) : "memory");
        } else if (w == 2) {
            const v4u rb2 = mkrsrc(b2, 1024);
            asm volatile("s_mov_b32 m0, %3\n\ts_mov_b64 exec, 0xff\n\tbuffer_load_dwordx4 %0, %1, %2 offen lds\n\ts_mov_b64 exec, -1" :: "v"(l16), "s"(rb2), "s"(tc * 128), "s"(L_B2) : "memory");
        } else if (w == 1) {
            const v4u rb1 = mkrsrc(b1, 1376 * 2);
            asm volatile("s_mov_b32 m0, %3\n\ts_nop 0\n\tbuffer_load_dword %0, %1, %2 offen lds" :: "v"(lane * 4), "s"(rb1), "s"(u0 * 64), "s"(L_B1) : "memory");
        }
        const u32 pi = opq((hf * 64 + lane) * 2);
        u32 pA = ldp((const GAS u16*)((const GAS char*)(logA + ch * 128) + pi)), pI = ldp((const GAS u16*)((const GAS char*)(Aim + ch * 128) + pi));
        u32 pR = ldp((const GAS u16*)((const GAS char*)(Cre + ch * 128) + pi)), pC = ldp((const GAS u16*)((const GAS char*)(Cim + ch * 128) + pi));
        u32 pT = *(const GAS u16*)(logdt + ch), pD = *(const GAS u16*)(Dskip + ch);
        flg(IC<R_X>{});
        one = opq(1u);
        const u32 aCnt = opq(L_CNT + chl * 4), aCtl = opq(L_C);
        const v4u rus = mkrsrc(ws + OFF_U, 2 << 20);
        v4u selLo, selHi;
#pragma unroll
        for (int e = 0; e < 4; e++) {
            const u32 o1 = (h == (g & 1) && ((l15 & 7) >> 1) == e) ? ((l15 & 1) ? 0x3C000000u : 0x00003C00u) : 0u;
            selLo[e] = l15 < 8 ? o1 : 0u; selHi[e] = l15 < 8 ? 0u : o1;
        }
        pin(selLo); pin(selHi);
        const int m = 64 * hf + lane;
        const u32 aT = opq(w * 32768 + 16384 + l15 * 64 + g * 16), t0 = opq((2 * chl) * 32768 + 16384 + m * 4), kr = opq(L_KB + chl * 8320 + (1016 - 8 * m) * 2);
        u32 tg[6];
        {
            const bool r1 = m < 32, r2 = m < 64;
            tg[0] = opq(t0 + (r1 ? 528 : 0) * 4); tg[1] = opq(t0 + (r2 ? 400 : 0) * 4); tg[2] = opq(t0 + (r1 ? 560 : 272) * 4);
            tg[3] = opq(t0 + (r1 ? 592 : 272) * 4); tg[4] = opq(t0 + (r2 ? 464 : 1) * 4); tg[5] = opq(t0 + (r1 ? 624 : 1) * 4);
        }
        u32 kb[4], ua[4];
#pragma unroll
        for (int i = 0; i < 4; i++) {
            kb[i] = opq(L_KB + chl * 8320 + ((l15 & 1) ? (239 - l15 + 8 * g) * 2 : 4160 + (240 - l15 + 8 * g) * 2) - hf * 256 - 192 + i * 1024);
            const int os = 30 + 2 * l15 + g - 16 * hf - 4 * i;
            ua[i] = opq(L_U + os * 32 + ((chl ^ (os >> 3)) & 1) * 16);
        }
        const u32 aZ3 = opq((lane & 4) ? L_CNT + 4 * C_K + chl * 4 : L_UF + (lane & 3) * 4), want3 = opq((lane & 4) ? 2u : 0u), aZ2 = opq(L_C + (lane & 3) * 4 + ((lane & 8) ? R_LN : (lane & 4) ? R_P + 4 : R_F + 7) * 16);
        const u32 vA = opq((lane >> 3) * 4096 + (lane & 7) * 16);
        pin(pA); pin(pI); pin(pR); pin(pC); pin(pT); pin(pD);

        {
            const u32 aRow = opq(L_KB + (l15 & 7) * 1088 + (l15 >> 3) * 512 + g * 16), aTr = opq(L_KB + (l15 >> 2) * 1088 + (128 * w + 16 * g + 4 * (lane & 3)) * 2);
            v4u ones = {0x3F803F80u, 0x3F803F80u, 0x3F803F80u, 0x3F803F80u}; pin(ones);
            spin_zero4(aF + R_X * 16);
            STAMP(1);
            v4u xb[8]; v2u xt[2][2];
#pragma unroll
            for (int s = 0; s < 8; s++) xb[s] = lds_ld<v4u>(aRow + s * 64);
#pragma unroll
            for (int i = 0; i < 2; i++) { xt[i][0] = lds_tr(aTr + i * 128); xt[i][1] = lds_tr(aTr + i * 128 + 4 * 1088); }
            tag = get_tag();
            f32x4 sm = {0.f, 0.f, 0.f, 0.f}, sq = {0.f, 0.f, 0.f, 0.f};
#pragma unroll
            for (int s = 0; s < 8; s++) { sq = mfma16(xb[s], xb[s], sq); sm = mfma16(ones, xb[s], sm); }
            float q = (l15 & 2) ? ((l15 & 1) ? sq[3] : sq[2]) : ((l15 & 1) ? sq[1] : sq[0]);
            q = g == (l15 >> 2) ? q : 0.f;
            { const auto r = __builtin_amdgcn_permlane32_swap(f2u(q), f2u(q), false, false); q = u2f(r[0]) + u2f(r[1]); }
            { const auto r = __builtin_amdgcn_permlane16_swap(f2u(q), f2u(q), false, false); q = u2f(r[0]) + u2f(r[1]); }
            const float mean = dpp_add<0x128>(sm[0]) * (1.f / 512.f);
            const float var = fmaxf(__builtin_fmaf(-mean, mean, dpp_add<0x128>(q) * (1.f / 512.f)), 0.f);
            const u32 p = pk_bf(mean, __builtin_amdgcn_sqrtf(rbf(rbf(var) + 1e-5f)));
            const float mb = bf_lo(p), rr = __builtin_amdgcn_rcpf(bf_hi(p));
            if (w == 0 && lane < 8) __hip_atomic_store((GAS u64*)(ws + OFF_ST + (P * 16 + j) * 64) + lane, ((u64)((lane & 1) ? (u32)(tag >> 32) : (u32)tag) << 32) | p, RLX_AGENT);
            sfor<2>([&](auto I_) {
                constexpr int i = decltype(I_)::value;
                v4u o;
                sfor<4>([&](auto E_) {
                    constexpr int e = decltype(E_)::value;
                    const u32 xv = xt[i][e >> 1][e & 1];
                    const u32 d = pk_bf(bf_lo(xv) - bcast<2 * e>(mb), bf_hi(xv) - bcast<2 * e + 1>(mb));
                    o[e] = pk_bf(bf_lo(d) * bcast<2 * e>(rr), bf_hi(d) * bcast<2 * e + 1>(rr));
                });
                st_sc1(o, rus, l16, tb * 8192 + w * 2048 + i * 1024);
            });
        }
        flg(IC<R_LN>{});
        STAMP(2);
        __builtin_amdgcn_s_setprio(1);
        f32x4 kt[3];
        {
            const float dt = rbf(expf(bf_lo(pT)));
            const float zr = -rbf(expf(bf_lo(pA))) * dt, zi = bf_lo(pI) * dt;
            const float lr = zr * 1.44269504088896341f;
            const float th = zi * 0.159154943091895336f, tl = __builtin_fmaf(zi, 0.159154943091895336f, -th) + zi * 6.4206383e-09f;
            const cmult w1c = mkc(cexp_pow(lr, -th, -tl, 1.f)), qc = mkc(cexp_pow(lr, -th, -tl, 128.f)), q2 = mkc(cexp_pow(lr, -th, -tl, 256.f));
            const cmult w8 = mkc(cexp_pow(lr, th, tl, 8.f)), w32 = mkc(cexp_pow(lr, th, tl, 32.f));
            const u32 aR = pR & 0x7fffu, aI = pC & 0x7fffu;
            int ex = (int)(wave_max(aR > aI ? aR : aI) >> 7); ex = ex < 12 ? 12 : (ex > 240 ? 240 : ex);
            const float sdn = u2f((u32)(262 - ex) << 23), sup = u2f((u32)(ex - 8) << 23);
            auto transpose = [&](const f32x2 (&v)[16], v4u (&f)[4]) {
                f32x16 z; for (int i = 0; i < 16; i++) z[i] = 0.f;
                f32x16 dr = mfma32h((v4u){pk_h2(v[0].x, v[1].x), pk_h2(v[2].x, v[3].x), pk_h2(v[4].x, v[5].x), pk_h2(v[6].x, v[7].x)}, selLo, z);
                f32x16 di = mfma32h((v4u){pk_h2(v[0].y, v[1].y), pk_h2(v[2].y, v[3].y), pk_h2(v[4].y, v[5].y), pk_h2(v[6].y, v[7].y)}, selLo, z);
                dr = mfma32h((v4u){pk_h2(v[8].x, v[9].x), pk_h2(v[10].x, v[11].x), pk_h2(v[12].x, v[13].x), pk_h2(v[14].x, v[15].x)}, selHi, dr);
                di = mfma32h((v4u){pk_h2(v[8].y, v[9].y), pk_h2(v[10].y, v[11].y), pk_h2(v[12].y, v[13].y), pk_h2(v[14].y, v[15].y)}, selHi, di);
#pragma unroll
                for (int e = 0; e < 4; e++) {
                    f[0][e] = pk_h2(dr[2 * e], dr[2 * e + 1]); f[1][e] = pk_h2(dr[8 + 2 * e], dr[9 + 2 * e]);
                    f[2][e] = pk_h2(di[2 * e], di[2 * e + 1]); f[3][e] = pk_h2(di[8 + 2 * e], di[9 + 2 * e]);
                }
            };
            { f32x2 t0_ = w1c.b, t1_ = qc.b, t2_ = q2.b, t3_ = w8.b, t4_ = w32.b; pin(t0_); pin(t1_); pin(t2_); pin(t3_); pin(t4_); }
            STAMP(13);
            wait_vm<0>();
            put_flag(ws + OFF_F1, (w * 256 + tb) * FS1 * 8, l16, tag);
            warm_flag<FS3 * 8>(ws + OFF_F3, (hf * 512 + ch) * FS3 * 8, l16);
            STAMP(14);
            v4u fy[4], fx[4];
            f32x2 v[16], xo[8];
            v[0] = (f32x2){1.f, 0.f}; v[1] = w8.b; v[2] = cmul(w8.b, w8); v[3] = cmul(v[2], w8); v[4] = w32.b;
            xo[0] = (f32x2){bf_lo(pR) * sdn, -bf_lo(pC) * sdn};
#pragma unroll
            for (int i = 5; i < 16; i++) { v[i] = cmul(v[i - 4], w32); if (i < 12) xo[i - 4] = cmul(xo[i - 5], w1c); }
            STAMP(3); CST(2);
            transpose(v, fy);
            const float fin = 2.f * dt * sup;
            auto tile = [&](int t) {
                transpose(v, fx);
                f32x4 d = {0.f, 0.f, 0.f, 0.f};
#pragma unroll
                for (int s = 0; s < 4; s++) d = mfma16h(fy[s], fx[s], d);
                kt[t] = d * fin;
            };
            v[0] = xo[0]; v[1] = cmul(xo[0], qc);
#pragma unroll
            for (int s = 2; s < 16; s++) v[s] = cmul(v[s - 2], q2);
            const f32x2 last = cmul(v[14], q2);
            tile(0);
            v[0] = last; v[1] = xo[4]; v[9] = xo[2]; v[13] = xo[6];
            v[2] = cmul(v[1], qc); v[10] = cmul(v[9], qc); v[14] = cmul(v[13], qc);
#pragma unroll
            for (int s = 3; s < 9; s++) v[s] = cmul(v[s - 2], q2);
            v[11] = cmul(v[9], q2); v[12] = cmul(v[10], q2); v[15] = cmul(v[13], q2);
            const f32x2 last2 = cmul(v[14], q2);
            tile(1);
            v[0] = last2;
#pragma unroll
            for (int i = 0; i < 4; i++) { v[1 + 2 * i] = xo[2 * i + 1]; v[2 + 2 * i] = cmul(xo[2 * i + 1], qc); }
#pragma unroll
            for (int i = 9; i < 16; i++) v[i] = (f32x2){0.f, 0.f};
            tile(2);
        }
        CST(3); STAMP(17);
        pin(kt[0]); pin(kt[1]); pin(kt[2]);
        spin_zero4<1>(aZ2);
        STAMP(11);
#pragma unroll
        for (int t = 0; t < 3; t++) lds_st<f32x4>(aT + t * 1024, kt[t]);
        sig<C_A>(aCnt, one);
        STAMP(4);
        spin(aCnt + 4 * C_A, 2);
        STAMP(19);
        {
            auto tv = [&](u32 a) { return lds_ld<float>(a) + lds_ld<float>(a + 32768); };
            float k[9];
            k[0] = tv(t0); k[8] = tv(t0 + 4); k[4] = tv(t0 + 272 * 4);
            k[1] = tv(tg[0]); k[2] = tv(tg[1]); k[3] = tv(tg[2]); k[5] = tv(tg[3]); k[6] = tv(tg[4]); k[7] = tv(tg[5]);
            const float va = tv(t0 + 512), vb = tv(t0 + 516);
            if (m == 0) k[0] += bf_lo(pD);
            const u32 k10 = pk_bf(k[1], k[0]);
            lds_st<v4u>(kr + 2048, (v4u){pk_bf(k[7], k[6]), pk_bf(k[5], k[4]), pk_bf(k[3], k[2]), k10});
            lds_st<v4u>(kr + 2048 + 4160, (v4u){pk_bf(k[8], k[7]), pk_bf(k[6], k[5]), pk_bf(k[4], k[3]), pk_bf(k[2], k[1])});
            const u32 pa = pk_bf(va, va), pb = pk_bf(vb, vb), pba = pk_bf(vb, va);
            lds_st<v4u>(kr, (v4u){pb, (m & 1) ? pb : pba, pa, pa});
            lds_st<v4u>(kr + 4160, (v4u){pb, pb, (m & 1) ? pba : pa, pa});
            if (m < 4) lds_st<v4u>(L_KB + chl * 8320 + 4096 + (m & 1) * 16 + (m < 2 ? 4160 : 0), (v4u){m == 0 ? k10 >> 16 : 0u, 0u, 0u, 0u});
        }
        sig<C_K>(aCnt, one);
        STAMP(18);
        f32x4 cv[8];
        {
#pragma unroll
            for (int t = 0; t < 8; t++) cv[t] = (f32x4){0.f, 0.f, 0.f, 0.f};
            v4u A[8], B[8];
            auto rdA = [&](int r, int a) { const u32 p = kb[(7 - a) >> 1] + ((7 - a) & 1) * 512 + (3 - r) * 64; return (v4u){lds_ld<u32>(p), lds_ld<u32>(p + 4), lds_ld<u32>(p + 8), lds_ld<u32>(p + 12)}; };
            auto rdB = [&](int r, int d) { return lds_ld<v4u>(ua[r] + d * 1024); };
            {
                int k = 0;
                while (any_(lds_ldv<u32>(aZ3) != want3) && ++k < (SPIN_CAP << 4)) {}
                asm volatile("" ::: "memory");
            }
            STAMP(6);
#pragma unroll
            for (int i = 0; i < 8; i++) { A[7 - i] = rdA(0, 7 - i); B[i] = rdB(0, i); }
            if (b == 0 && w == 0 && lane == 0) __hip_atomic_store((GAS u64*)ws, tag, RLX_AGENT);
            CST(0);
#pragma unroll
            for (int r = 0; r < 4; r++) {
#pragma unroll
                for (int a = 7; a >= 0; a--) {
#pragma unroll
                    for (int d = 0; d + a < 8; d++) {
                        cv[a + d] = mfma16(A[a], B[d], cv[a + d]);
                        if (a == 0 && r < 3) { SB0(); B[d] = rdB(r + 1, d); SB0(); }
                    }
                    if (r < 3) { SB0(); A[a] = rdA(r + 1, a); SB0(); }
                }
            }
        }
        STAMP(7); CST(1);
        {
            const u32 mine = opq(w * 32768 + 16384 + l16), other = opq((w ^ 1) * 32768 + 16384 + l16);
#pragma unroll
            for (int t = 0; t < 4; t++) lds_st<f32x4>(mine + t * 1024, sel4(hf, cv[t], cv[4 + t]));
            sig<C_E>(aCnt, one);
            sig<C_D>(aCtl + (L_CNT - L_C), one);
            spin(aCnt + 4 * C_E, 2);
            u32 o[8];
#pragma unroll
            for (int t = 0; t < 4; t++) {
                const f32x4 y = sel4(hf, cv[4 + t], cv[t]) + lds_ld<f32x4>(other + t * 1024);
                o[2 * t] = pk_bf(y[0], y[1]); o[2 * t + 1] = pk_bf(y[2], y[3]);
            }
            const v4u ras = mkrsrc(ws + OFF_A, 2 << 20);
            st_sc1((v4u){o[0], o[1], o[2], o[3]}, ras, l16, ch * 4096 + hf * 2048);
            st_sc1((v4u){o[4], o[5], o[6], o[7]}, ras, l16, ch * 4096 + hf * 2048 + 1024);
            STAMP(15);
            wait_vm<0>();
            STAMP(8);
            put_flag(ws + OFF_F3, (hf * 512 + ch) * FS3 * 8, l16, tag);
            spin(aCtl + (L_CNT - L_C) + 4 * C_D, 4);
            __builtin_amdgcn_s_sleep(30);
            poll_flags((const GAS u64*)(ws + OFF_F3) + ((tr >> 4) * 512 + tc * 64 + 16 * w + l15) * FS3, tag, lane < 16);
            STAMP(9);
#pragma unroll
            for (int i = 0; i < 2; i++)
                glds_sc1(ras, vA, (tc * 64 + 16 * w + 8 * i) * 4096 + (tr >> 3) * 1024 + (tr & 3) * 256 + ((tr >> 2) & 1) * 128, L_U + w * 2048 + i * 1024);
            const u32 ta = opq(L_U + w * 2048 + (l15 >> 2) * 128 + g * 32 + (lane & 3) * 8), aw = opq(L_KB + lane * 144 + w * 32);
            wait_vm<0>();
            const v2u a0 = lds_tr(ta), a1 = lds_tr(ta + 512), a2 = lds_tr(ta + 1024), a3 = lds_tr(ta + 1536);
            lds_st<v4u>(aw, (v4u){a0.x, a0.y, a1.x, a1.y});
            lds_st<v4u>(aw + 16, (v4u){a2.x, a2.y, a3.x, a3.y});
            STAMP(10);
        }
        wait_lgkm0();
        __builtin_amdgcn_s_barrier();
    }
    if (lane == 0 && w == 0 && role == PROLE) { GAS u32* q = (GAS u32*)(ws + (40 << 20)) + b * 4; q[0] = t_start; q[1] = probe_t; q[2] = (u32)__builtin_amdgcn_s_memrealtime(); }
}

#if defined(__HIP_DEVICE_COMPILE__) && __HIP_DEVICE_COMPILE__
#pragma clang attribute pop
#endif

__global__ void tail_kernel(char* ws) {
    const u32 t0 = (u32)__builtin_amdgcn_s_memrealtime();
    const int lane = threadIdx.x;
    int smax = -(1 << 30), smin = 1 << 30, pmax = -(1 << 30), emax = -(1 << 30), psum = 0;
    for (int i = 0; i < 4; i++) { const u32* q = (const u32*)(ws + (40 << 20)) + (i * 64 + lane) * 4; int a = (int)(q[0] - t0), p = (int)(q[1] - t0), e = (int)(q[2] - t0); smax = a > smax ? a : smax; smin = a < smin ? a : smin; pmax = p > pmax ? p : pmax; emax = e > emax ? e : emax; psum += p; }
    for (int m = 32; m >= 1; m >>= 1) { int a = __shfl_xor(smax, m), a2 = __shfl_xor(smin, m), p = __shfl_xor(pmax, m), e = __shfl_xor(emax, m); smax = a > smax ? a : smax; smin = a2 < smin ? a2 : smin; pmax = p > pmax ? p : pmax; emax = e > emax ? e : emax; psum += __shfl_xor(psum, m); }
    int extra = QTY == 0 ? (smax > 0 ? 1 : 0) : QTY == 1 ? smax - smin : QTY == 2 ? emax - smax : QTY == 3 ? psum / 256 - smax : pmax - smax;
    extra = __builtin_amdgcn_readfirstlane(extra) * 2; if (extra > 20000) extra = 20000;
    while ((int)((u32)__builtin_amdgcn_s_memrealtime() - t0) < extra + 600) {}
}
extern "C" void kernel_launch(void* const* d_in, const int* in_sizes, int n_in, void* d_out, int out_size, void* d_ws, size_t ws_size,
                              hipStream_t stream) {
    fused_kernel<<<256, 768, 0, stream>>>((const u16*)d_in[0], (const u16*)d_in[1], (const u16*)d_in[2], (const u16*)d_in[3], (const u16*)d_in[4],
                                          (const u16*)d_in[5], (const u16*)d_in[6], (const u16*)d_in[8], (const u16*)d_in[9], (const u16*)d_in[10],
                                          (const u16*)d_in[11], (u16*)d_out, (char*)d_ws);
    tail_kernel<<<1, 64, 0, stream>>>((char*)d_ws);
}
```

```cpp
#include <hip/hip_runtime.h>
#include <stdint.h>
#include <utility>

typedef unsigned short u16;
typedef unsigned int u32;
typedef unsigned long long u64;
typedef u32 v4u __attribute__((ext_vector_type(4)));
typedef u32 v2u __attribute__((ext_vector_type(2)));
typedef float f32x2 __attribute__((ext_vector_type(2)));
typedef float f32x4 __attribute__((ext_vector_type(4)));
typedef float f32x16 __attribute__((ext_vector_type(16)));
typedef __bf16 bf16x8 __attribute__((ext_vector_type(8)));
typedef __bf16 bf16x2 __attribute__((ext_vector_type(2)));
typedef _Float16 f16x8 __attribute__((ext_vector_type(8)));
typedef _Float16 f16x2 __attribute__((ext_vector_type(2)));
typedef short s16x4 __attribute__((ext_vector_type(4)));
typedef u64 u64x8 __attribute__((ext_vector_type(8)));
typedef u64 u64x4 __attribute__((ext_vector_type(4)));
#define GAS __attribute__((address_space(1)))
#define LAS __attribute__((address_space(3)))
#define DI __device__ __forceinline__
#define RLX_AGENT __ATOMIC_RELAXED, __HIP_MEMORY_SCOPE_AGENT
#define RLX_WG __ATOMIC_RELAXED, __HIP_MEMORY_SCOPE_WORKGROUP
#define SB0() __builtin_amdgcn_sched_barrier(0)

constexpr int OFF_ST = 16384, OFF_F1 = 128 << 10, OFF_F2 = 256 << 10, OFF_F3 = 512 << 10;
constexpr int FS1 = 2, FS2 = 16, FS3 = 16;
constexpr int OFF_U = 1 << 20, OFF_A = 4 << 20, OFF_H = 8 << 20;
constexpr int H_BYTES = 64 * 43 * 2048;
constexpr int L_C = 131072;
constexpr int L_FLG = L_C, L_UF = L_C + 320, L_E = L_C + 768, L_B2 = L_C + 896, L_CNT = L_C + 416, L_B1 = L_C + 512, L_STAT = L_C + 1024, L_CSP = L_C + 29696;
enum { R_F = 0, R_P = 8, R_P7 = 13, R_PK = 15, R_LN = 19, R_X = 14, R_V1 = 16, R_V2 = 17, R_V3 = 18 };
constexpr int L_U = L_C + 3072, L_KB = L_C + 12288;
constexpr int SPIN_CAP = 1 << 16;
constexpr int CODE_BYTES = 31488;
enum { C_A = 0, C_K = 2, C_E = 4, C_D = 8, C_RD = 12 };

#if defined(__HIP_DEVICE_COMPILE__) && __HIP_DEVICE_COMPILE__
#pragma clang attribute push(__attribute__((target("no-packed-fp32-ops"))), apply_to = function)
#endif
DI float u2f(u32 v) { return __builtin_bit_cast(float, v); }
DI u32 f2u(float v) { return __builtin_bit_cast(u32, v); }
DI bool any_(bool c) { return __builtin_amdgcn_ballot_w64(c) != 0; }
DI bool all_(bool c) { return __builtin_amdgcn_ballot_w64(!c) == 0; }
template <typename T> DI T lds_ld(u32 a) { return *(LAS T*)(size_t)a; }
template <typename T> DI void lds_st(u32 a, T v) { *(LAS T*)(size_t)a = v; }
DI v2u lds_tr(u32 a) { return __builtin_bit_cast(v2u, __builtin_amdgcn_ds_read_tr16_b64_v4i16((LAS s16x4*)(size_t)a)); }
DI v4u mkrsrc(const void* p, u32 bytes) { u64 a = (u64)p; v4u r; r.x = (u32)a; r.y = (u32)(a >> 32) & 0xffffu; r.z = bytes; r.w = 0x00020000u; return r; }
DI void glds(v4u rsrc, u32 voff, u32 soff, u32 lds) {
    asm volatile("s_mov_b32 m0, %3\n\ts_nop 0\n\tbuffer_load_dwordx4 %0, %1, %2 offen lds" :: "v"(voff), "s"(rsrc), "s"(soff), "s"(lds) : "memory");
}
template <int LOFF> DI u32 glds_first(v4u rsrc, u32 voff, u32 sa, u32 sb, u32 lbase) {
    u32 so;
    asm volatile("s_add_u32 m0, %5, %6\n\ts_add_u32 %0, %3, %4\n\tbuffer_load_dwordx4 %1, %2, %0 offen lds" : "=&s"(so) : "v"(voff), "s"(rsrc), "s"(sa), "s"(sb), "s"(lbase), "i"(LOFF) : "memory", "scc");
    return so;
}
template <int LOFF, int SOFF> DI void glds_imm(v4u rsrc, u32 voff, u32 sa, u32 lbase) {
    u32 so;
    asm volatile("s_add_u32 m0, %4, %5\n\ts_add_u32 %0, %3, %6\n\tbuffer_load_dwordx4 %1, %2, %0 offen lds" : "=&s"(so) : "v"(voff), "s"(rsrc), "s"(sa), "s"(lbase), "i"(LOFF), "i"(SOFF) : "memory", "scc");
}
template <int OFF> DI void glds_next(v4u rsrc, u32 voff, u32 so) {
    asm volatile("buffer_load_dwordx4 %0, %1, %2 offen offset:%3 lds" :: "v"(voff), "s"(rsrc), "s"(so), "i"(OFF) : "memory");
}
template <int LOFF> DI void flag_imm(v4u rsrc, u32 lbase) {
    asm volatile("s_add_u32 m0, %2, %3\n\ts_mov_b64 exec, 1\n\tbuffer_load_dword off, %0, %1 lds\n\ts_mov_b64 exec, -1" :: "s"(rsrc), "s"(rsrc.z), "s"(lbase), "i"(LOFF) : "memory", "scc");
}
DI void glds_sc1(v4u rsrc, u32 voff, u32 soff, u32 lds) {
    asm volatile("s_mov_b32 m0, %3\n\ts_nop 0\n\tbuffer_load_dwordx4 %0, %1, %2 offen sc1 lds" :: "v"(voff), "s"(rsrc), "s"(soff), "s"(lds) : "memory");
}
DI void flag_dma(v4u rsrc, u32 lds) {
    asm volatile("s_mov_b32 m0, %2\n\ts_mov_b64 exec, 1\n\tbuffer_load_dword off, %0, %1 lds\n\ts_mov_b64 exec, -1" :: "s"(rsrc), "s"(rsrc.z), "s"(lds) : "memory");
}
DI void st_sc1(v4u v, v4u rsrc, u32 voff, u32 soff) {
    asm volatile("buffer_store_dwordx4 %0, %1, %2, %3 offen sc1\n\ts_nop 1" :: "v"(v), "v"(voff), "s"(rsrc), "s"(soff) : "memory");
}
DI void put_flag(const char* base, u32 off, u32 l16, u64 tag) {
    asm volatile("s_mov_b64 exec, 1\n\tglobal_store_dwordx2 %0, %1, %2 sc1\n\ts_mov_b64 exec, -1" :: "v"(l16), "v"(tag), "s"(base + off) : "memory");
}
template <int BYTES> DI void warm_flag(const char* base, u32 off, u32 l16) {
    const v4u v = {0u, 0u, 0u, 0u};
    asm volatile("s_mov_b64 exec, %3\n\tglobal_store_dwordx4 %0, %1, %2 sc1\n\ts_mov_b64 exec, -1\n\ts_nop 0" :: "v"(l16), "v"(v), "s"(base + off), "i"((1 << (BYTES / 16)) - 1) : "memory");
}
DI u32 ldp(const GAS u16* p) { return __builtin_nontemporal_load(p); }
DI u32 opq(u32 a) { asm volatile("" : "+v"(a)); return a; }
template <typename T> DI T lds_ldv(u32 a) { return *(volatile LAS T*)(size_t)a; }
template <int SLEEP = 0> DI void spin_zero4(u32 a) {
    int n = 0;
    while (any_(lds_ldv<u32>(a) != 0) && ++n < (SPIN_CAP << 4)) { if (SLEEP) __builtin_amdgcn_s_sleep(1); }
    asm volatile("" ::: "memory");
}
template <int N> DI void wait_vm() { asm volatile("s_waitcnt vmcnt(%0)" :: "i"(N) : "memory"); }
DI void wait_lgkm0() { asm volatile("s_waitcnt lgkmcnt(0)" ::: "memory"); }
DI u64 mix64(u64 e) { e += 0x9E3779B97F4A7C15ull; e ^= e >> 31; e *= 0xBF58476D1CE4E5B9ull; e ^= e >> 29; return e; }
DI u64 get_tag() { const v2u e = lds_ldv<v2u>(L_E); return mix64(((u64)(u32)__builtin_amdgcn_readfirstlane(e.y) << 32) | (u32)__builtin_amdgcn_readfirstlane(e.x)); }
template <int C> DI void sig(u32 a, u32 one) { asm volatile("s_mov_b64 exec, 1\n\tds_add_u32 %0, %1 offset:%2\n\ts_mov_b64 exec, -1" :: "v"(a), "v"(one), "i"(4 * C) : "memory"); }
template <int OFF> DI void clr(u32 a, u32 zero) { asm volatile("s_mov_b64 exec, 1\n\tds_write_b32 %0, %1 offset:%2\n\ts_mov_b64 exec, -1" :: "v"(a), "v"(zero), "i"(OFF) : "memory"); }
DI void spin(u32 a, u32 target) {
    int n = 0;
    while ((u32)__builtin_amdgcn_readfirstlane(lds_ldv<u32>(a)) < target && ++n < (SPIN_CAP << 4)) {}
    asm volatile("" ::: "memory");
}
DI u64 peek_flags(const GAS u64* p, u64 tag, bool on) { u64 f = tag; if (on) f = __hip_atomic_load(p, RLX_AGENT); return f; }
DI void poll_flags(const GAS u64* p, u64 tag, bool on) {
    int n = 0;
    while (true) {
        u64 f = peek_flags(p, tag, on);
        if (all_(f == tag) || ++n > SPIN_CAP) break;
        __builtin_amdgcn_s_sleep(2);
    }
    asm volatile("" ::: "memory");
}
DI float bf_lo(u32 v) { return u2f(v << 16); }
DI float bf_hi(u32 v) { return u2f(v & 0xffff0000u); }
DI u32 pk_bf(float a, float b) { f32x2 v = {a, b}; return __builtin_bit_cast(u32, __builtin_convertvector(v, bf16x2)); }
typedef short s16x2 __attribute__((ext_vector_type(2)));
DI u32 relu_pk(u32 v) { return __builtin_bit_cast(u32, __builtin_elementwise_max(__builtin_bit_cast(s16x2, v), (s16x2){0, 0})); }
DI u32 umax(u32 a, u32 b) { return a > b ? a : b; }
DI float rbf(float a) { return bf_lo(pk_bf(a, a)); }
DI u32 pk_h(f32x2 v) { return __builtin_bit_cast(u32, __builtin_convertvector(v, f16x2)); }
DI f32x16 mfma32(v4u a, v4u b, f32x16 c) { return __builtin_amdgcn_mfma_f32_32x32x16_bf16(__builtin_bit_cast(bf16x8, a), __builtin_bit_cast(bf16x8, b), c, 0, 0, 0); }
DI f32x4 mfma16(v4u a, v4u b, f32x4 c) { return __builtin_amdgcn_mfma_f32_16x16x32_bf16(__builtin_bit_cast(bf16x8, a), __builtin_bit_cast(bf16x8, b), c, 0, 0, 0); }
DI f32x4 mfma16h(v4u a, v4u b, f32x4 c) { return __builtin_amdgcn_mfma_f32_16x16x32_f16(__builtin_bit_cast(f16x8, a), __builtin_bit_cast(f16x8, b), c, 0, 0, 0); }
template <int CTRL> DI float dpp_add(float v) { return v + __builtin_bit_cast(float, __builtin_amdgcn_update_dpp(0, __builtin_bit_cast(int, v), CTRL, 0xf, 0xf, true)); }
template <int CTRL> DI u32 dpp_max(u32 v) { const u32 o = (u32)__builtin_amdgcn_update_dpp(0, (int)v, CTRL, 0xf, 0xf, true); return v > o ? v : o; }
template <int T> DI float bcast(float v) { return __builtin_bit_cast(float, __builtin_amdgcn_update_dpp(0, __builtin_bit_cast(int, v), 0x150 + T, 0xf, 0xf, true)); }
DI u32 wave_max(u32 v) {
    v = dpp_max<0xB1>(v); v = dpp_max<0x4E>(v); v = dpp_max<0x141>(v); v = dpp_max<0x140>(v);
    const u32 a = __builtin_amdgcn_readlane(v, 0), c = __builtin_amdgcn_readlane(v, 16), d = __builtin_amdgcn_readlane(v, 32), e = __builtin_amdgcn_readlane(v, 48);
    const u32 p = a > c ? a : c, q = d > e ? d : e;
    return p > q ? p : q;
}
struct cmult { f32x2 b, n; };
DI cmult mkc(f32x2 b) { cmult c; c.b = b; c.n = (f32x2){-b.y, b.x}; return c; }
DI f32x2 cmul(f32x2 a, const cmult& c) { return __builtin_elementwise_fma((f32x2){a.x, a.x}, c.b, (f32x2){a.y, a.y} * c.n); }
DI f32x16 mfma32h(v4u a, v4u b, f32x16 c) { return __builtin_amdgcn_mfma_f32_32x32x16_f16(__builtin_bit_cast(f16x8, a), __builtin_bit_cast(f16x8, b), c, 0, 0, 0); }
DI u32 pk_h2(float a, float b) { return pk_h((f32x2){a, b}); }
DI f32x2 cexp_pow(float lr, float th, float tl, float e) {
    float a = __builtin_amdgcn_fractf(th * e) + tl * e;
    float m = __builtin_amdgcn_exp2f(lr * e);
    f32x2 r; r.x = m * __builtin_amdgcn_cosf(a); r.y = m * __builtin_amdgcn_sinf(a); return r;
}
DI f32x4 sel4(bool c, f32x4 a, f32x4 b) { return c ? a : b; }
constexpr int w2loc(int n) { return n < 16 ? n : n < 28 ? n + 4 : n - 28; }
template <class F, int... I> DI void sfor_(F&& f, std::integer_sequence<int, I...>) { (f(std::integral_constant<int, I>{}), ...); }
template <int N, class F> DI void sfor(F&& f) { sfor_(f, std::make_integer_sequence<int, N>{}); }
template <int I> using IC = std::integral_constant<int, I>;
template <typename T> DI void pin(T& v) { asm volatile("" : "+v"(v)); }

__global__ void __launch_bounds__(768, 3)
fused_kernel(const u16* x_, const u16* logA_, const u16* Aim_, const u16* Cre_, const u16* Cim_, const u16* logdt_, const u16* Dskip_, const u16* w1_,
             const u16* b1_, const u16* w2_, const u16* b2_, u16* out_, char* ws_) {
    u64x8 ka; u64x4 kb4; u64 kc;
    asm volatile("s_load_dwordx16 %0, %3, 0x0\n\ts_load_dwordx8 %1, %3, 0x40\n\ts_load_dwordx2 %2, %3, 0x60" : "=&s"(ka), "=&s"(kb4), "=&s"(kc) : "s"(__builtin_amdgcn_kernarg_segment_ptr()) : "memory");
    __shared__ __attribute__((aligned(16))) char smem[163840];
    asm volatile("" :: "v"((u32)(size_t)(LAS char*)smem));
    const int tid = threadIdx.x, lane = tid & 63, wave = __builtin_amdgcn_readfirstlane(tid >> 6) & 15, w = wave & 3, role = wave >> 2;
    const int b = blockIdx.x & 255, xcd = b & 7, slot = b >> 3;
    const int P = 4 * (xcd >> 1) + (slot >> 3), j = 8 * (xcd & 1) + (slot & 7);
    const int tr = 4 * xcd + (slot >> 3), tc = slot & 7;
    const int u0 = (j * 43) >> 4;
    if (wave == 4) {
        u64 pc; asm volatile("s_getpc_b64 %0" : "=s"(pc));
        const v4u rc = mkrsrc((const void*)(pc & ~255ull), CODE_BYTES);
        glds(rc, lane * 16, slot * 1024, L_C + 2048);
    }
    const int h = lane >> 5, l31 = lane & 31, l15 = lane & 15, g = lane >> 4;
    const u32 l16 = lane * 16, lw = w * 32768, w4 = w * 4;
    const int rho = ((slot >> 3) + slot) & 3;
    const int tb = 64 * (P >> 2) + 32 * (j & 1) + 16 * ((P >> 1) & 1) + 2 * (j >> 1) + (P & 1);
    const int cp = 32 * xcd + slot, chl = w >> 1, hf = w & 1, ch = 2 * cp + chl;
    {
        const u32 v = (tid >= 192 && tid < 256) || (tid >= 282 && tid < 286) ? 0u : ~0u;
        if (tid < 288) lds_st<v4u>((tid < 192 ? L_CSP : tid < 256 ? L_U - 192 * 16 : L_C - 256 * 16) + tid * 16, (v4u){v, v, v, v});
    }
    const int rot = (((slot >> 3) + slot) & 1) * 4;
    u32 sx = (512 * (P >> 2) + 128 * ((P >> 1) & 1) + 64 * (w & 1) + 8 * (P & 1) + 4 * (w >> 1)) * 1024, sw1 = 16 * w * 2752 + u0 * 64;
    u32 sx0 = rot * 128, sw0 = rot * (64 * 2752);
    u32 vx[4], vw[3];
    const int lim3 = 4 * ((((j + 1) * 43) >> 4) - u0);
#pragma unroll
    for (int i = 0; i < 3; i++) { const int p = 64 * i + lane, r = (p * 171) >> 11; vw[i] = opq(p - 12 * r >= lim3 ? 0x40000000u : r * (2752 - 192) + l16); }
#pragma unroll
    for (int i = 0; i < 4; i++) vx[i] = opq((((h * 256 + ((lane >> 3) & 3)) * 1024 + ((lane & 7) ^ g) * 16) ^ ((i & 1) * 64)) + i * (16384 - 1024));
    asm volatile("s_waitcnt lgkmcnt(0)\n\ts_barrier" : "+s"(ka), "+s"(kb4), "+s"(kc), "+s"(sx), "+s"(sw1), "+s"(sx0), "+s"(sw0) :: "memory");
    const u16 *x = (const u16*)ka[0], *logA = (const u16*)ka[1], *Aim = (const u16*)ka[2], *Cre = (const u16*)ka[3], *Cim = (const u16*)ka[4], *logdt = (const u16*)ka[5], *Dskip = (const u16*)ka[6], *w1 = (const u16*)ka[7];
    const u16 *b1 = (const u16*)kb4[0], *w2 = (const u16*)kb4[1], *b2 = (const u16*)kb4[2]; u16* out = (u16*)kb4[3]; char* ws = (char*)kc;
    const v4u rx = mkrsrc(x, 2 << 20);
    const u32 aF = opq(L_FLG + (lane & 3) * 4);
    auto flg = [&](auto R_) { flag_imm<L_FLG + decltype(R_)::value * 16>(rx, w4); };
    const v4u rw1 = mkrsrc(w1, 512 * 1376 * 2);
    auto ldX = [&](auto C_) { constexpr int c = decltype(C_)::value;
        const u32 so = glds_first<(16 + (c % 4) * 4) * 1024>(rx, vx[0], sx, c ? ((c + rot) & 7) * 128 : sx0, lw);
        glds_next<1024>(rx, vx[1], so); glds_next<2048>(rx, vx[2], so); glds_next<3072>(rx, vx[3], so); };
    auto ldW = [&](auto C_) { constexpr int c = decltype(C_)::value;
        const u32 so = glds_first<3 * (c % 5) * 1024>(rw1, vw[0], sw1, c ? ((c + rot) & 7) * (64 * 2752) : sw0, lw);
        glds_next<1024>(rw1, vw[1], so); glds_next<2048>(rw1, vw[2], so); };
    if (role == 0) {
        ldW(IC<0>{}); ldX(IC<0>{}); flg(IC<R_F>{});
        ldW(IC<1>{}); ldX(IC<1>{}); flg(IC<R_F + 1>{});
    }
    asm volatile("" ::: "memory");
    u32 aV[4], aFw, aCn, one, f2o;
    u32 sh2[4], sh2t[4];
    auto pre2 = [&]() {
#pragma unroll
        for (int dt = 0; dt < 4; dt++) aV[dt] = opq(lw + h * 2048 + (4 * (g & 1) + (l15 >> 2)) * 128 + (dt ^ (2 * (g & 1) + (l15 >> 3))) * 32 + (lane & 3) * 8);
        aFw = opq(L_FLG + w4); aCn = opq(L_CNT + w4); one = opq(1u);
        const int jj = 4 * w + (lane < 4 ? lane : 4);
        f2o = opq(((2 * tr + role) * 16 + (jj < 15 ? jj : 15)) * FS2 * 8);
#pragma unroll
        for (int k = 0; k < 4; k++) {
            sh2[k] = ((2 * tr + role) * 43 + 11 * w) * 2048 + (6 * rho - (k + rho >= 4 ? 22 : 0)) * 1024; sh2t[k] = (w == 3 && k + rho == 3) ? (u32)H_BYTES : sh2[k];
            asm volatile("" : "+s"(sh2[k]), "+s"(sh2t[k]));
        }
    };

    auto rdV = [&](int s, int i) { const u32 a = aV[2 * (s & 1) + i] + w2loc(4 * (s >> 1)) * 1024; v2u lo = lds_tr(a), hi = lds_tr(a + 1024); return (v4u){lo.x, lo.y, hi.x, hi.y}; };
    auto gemm2 = [&](auto ROLE_, v4u (&stage)[16], u64 f2, u64 tag) __attribute__((always_inline)) {
        constexpr int role = decltype(ROLE_)::value;
        const GAS u64* f2p = (const GAS u64*)(ws + OFF_F2 + f2o);
        __builtin_amdgcn_s_setprio(0);
        const __amdgpu_buffer_rsrc_t rh = __builtin_amdgcn_make_buffer_rsrc(ws + OFF_H, 0, H_BYTES, 0x00020000);
        constexpr int WIN = role ? 10 : 8;
        v4u park[8][2], hq[WIN], vb[3][2];
        auto ldH = [&](int s) { hq[s % WIN] = __builtin_amdgcn_raw_buffer_load_b128(rh, l16, ((s + 2) % 6 >= 4 ? sh2t[(s + 2) / 6] : sh2[(s + 2) / 6]) + s * 1024, 16); };
        constexpr int W0 = 8;
        if (role) {
            SB0();
            spin_zero4<1>(aFw + R_V1 * 16);

            f2 = tag;
            asm volatile("s_mov_b64 exec, 31\n\tglobal_load_dwordx2 %0, %1, %2 sc1\n\ts_mov_b64 exec, -1" : "+v"(f2) : "v"(f2o), "s"(ws + OFF_F2) : "memory");
            SB0();
#pragma unroll
            for (int s = 0; s < 8; s++) { park[s][0] = rdV(s, 0); park[s][1] = rdV(s, 1); }
            clr<R_PK * 16>(aFw, opq(0u));
            asm volatile("s_waitcnt vmcnt(0)" : "+v"(f2) :: "memory");
            SB0();
        }

        if (!all_(f2 == tag)) poll_flags(f2p, tag, lane < 5);

#pragma unroll
        for (int s = 0; s < W0; s++) ldH(s);
        const int q3 = (lane >> 2) & 7, fr = 8 * wave + 4 * h + 2 * ((q3 ^ (q3 >> 1) ^ (q3 >> 2)) & 1) + (q3 >> 2), fo = ((q3 >> 1) & 1) * 4 + (lane & 3);
        const u32 fin_off = (512 * (tr >> 3) + 256 * ((fr >> 2) & 1) + 128 * ((tr >> 2) & 1) + 16 * (fr >> 3) + 4 * (tr & 3) + (fr & 3)) * 512 + tc * 64 + fo * 8;
        f32x4 s0, s1; float rs2; u32 da, ra, rb, rc;
        v4u fin_x, fin_b; u32 fin_s, fv;
        f32x4 ac2[4][2];
#pragma unroll
        for (int i = 0; i < 8; i++) ac2[i >> 1][i & 1] = (f32x4){0.f, 0.f, 0.f, 0.f};
        const u32 aSt = opq(lw + l16), zero = opq(0u);
        if (!role) {
            SB0();
            spin_zero4(opq(aFw + ((lane & 1) ? R_PK : R_V2) * 16));

            vb[0][0] = rdV(0, 0); vb[0][1] = rdV(0, 1); vb[1][0] = rdV(1, 0); vb[1][1] = rdV(1, 1);
            asm volatile("" ::: "memory");
#pragma unroll
            for (int k = 0; k < 4; k++) lds_st<v4u>(aSt + k * 1024, stage[k]);
        }
#pragma unroll
        for (int s = 0; s < 22; s++) {
            SB0();
            if (role && (s == 4 || s == 10)) fv = lds_ldv<u32>(aFw + (s == 4 ? R_V2 : R_V3) * 16);
            if (role && (s == 6 || s == 12)) { if (__builtin_expect(any_(fv != 0), 0)) spin_zero4(aFw + (s == 6 ? R_V2 : R_V3) * 16); asm volatile("" ::: "memory"); }
            if ((!role || s + 2 >= 8) && s + 2 < 22) { vb[(s + 2) % 3][0] = rdV(s + 2, 0); vb[(s + 2) % 3][1] = rdV(s + 2, 1); }
            if (s == 19) sig<C_RD>(aCn, one);
            if (s == 21) fv = lds_ldv<u32>(aCn + 4 * C_RD);
#pragma unroll
            for (int q = 0; q < 4; q++) ac2[2 * (s & 1) + (q & 1)][q >> 1] = mfma16(role && s < 8 ? park[s & 7][q & 1] : vb[s % 3][q & 1], hq[((s & ~1) + (q >> 1)) % WIN], ac2[2 * (s & 1) + (q & 1)][q >> 1]);
            SB0();
            if (!role && s >= 1 && s < 7) {
                asm volatile("" ::: "memory");
                lds_st<v4u>(aSt + (2 * s + 2) * 1024, stage[(2 * s + 2) & 15]); lds_st<v4u>(aSt + (2 * s + 3) * 1024, stage[(2 * s + 3) & 15]);
                if (s == 6) { clr<R_V3 * 16>(aFw, zero); }
            }
            if (s == 0) { for (int k = W0; k < WIN; k++) ldH(k); }
            if ((s & 1) && s + WIN < 22) { ldH(s - 1 + WIN); ldH(s + WIN); }
            if (s == 13) {
                fin_x = *(const GAS v4u*)(x + fin_off); fin_b = lds_ld<v4u>(L_B2 + fo * 16);
                fin_s = __hip_atomic_load((GAS u32*)(ws + OFF_ST + (tr >> 1) * 1024 + (fr >> 2) * 64 + (tr & 1) * 32 + (fr & 3) * 8), RLX_AGENT);
            }
            if (s == 16) { da = opq(lw + role * 20480 + l15 * 272 + g * 16); ra = opq(role * 20480 + (fr & 31) * 272 + fo * 32); rb = opq(ra + 65536); rc = opq(L_KB + fr * 144 + fo * 16); }
            if (s == 21) {
                s0 = (f32x4){bf_lo(fin_x.x) + bf_lo(fin_b.x), bf_hi(fin_x.x) + bf_hi(fin_b.x), bf_lo(fin_x.y) + bf_lo(fin_b.y), bf_hi(fin_x.y) + bf_hi(fin_b.y)};
                pin(s0);
            }
        }
        SB0();

        if (__builtin_expect(any_(fv < 2), 0)) spin(aCn + 4 * C_RD, 2);
        asm volatile("" ::: "memory");
#pragma unroll
        for (int i = 0; i < 8; i++) lds_st<f32x4>(da + (i & 1) * 16 * 272 + (i >> 1) * 64, ac2[i >> 1][i & 1]);
        SB0();
        s1 = (f32x4){bf_lo(fin_x.z) + bf_lo(fin_b.z), bf_hi(fin_x.z) + bf_hi(fin_b.z), bf_lo(fin_x.w) + bf_lo(fin_b.w), bf_hi(fin_x.w) + bf_hi(fin_b.w)};
        rs2 = __builtin_amdgcn_rcpf(bf_hi(fin_s)); rs2 *= rs2;
        asm volatile("" : "+v"(s0), "+v"(s1), "+v"(rs2));
        wait_lgkm0();
        __builtin_amdgcn_s_barrier();
        asm volatile("" ::: "memory");

        const v4u av = lds_ld<v4u>(rc);
        s0 += ((lds_ld<f32x4>(ra) + lds_ld<f32x4>(ra + 32768)) + (lds_ld<f32x4>(rb) + lds_ld<f32x4>(rb + 32768))) * rs2;
        s1 += ((lds_ld<f32x4>(ra + 16) + lds_ld<f32x4>(ra + 32768 + 16)) + (lds_ld<f32x4>(rb + 16) + lds_ld<f32x4>(rb + 32768 + 16))) * rs2;
        s0 += (f32x4){bf_lo(av.x), bf_hi(av.x), bf_lo(av.y), bf_hi(av.y)};
        s1 += (f32x4){bf_lo(av.z), bf_hi(av.z), bf_lo(av.w), bf_hi(av.w)};
        const v4u ov = {pk_bf(s0[0], s0[1]), pk_bf(s0[2], s0[3]), pk_bf(s1[0], s1[1]), pk_bf(s1[2], s1[3])};
        st_sc1(ov, mkrsrc(out, 2 << 20), fin_off * 2, 0);
    };

    if (__builtin_expect(role == 0, 1)) {
        v4u stage[16]; u64 f2, tag;
        const u32 sw2 = 352 * w * 1024 + tc * 128;
        const u32 vv = (lane >> 3) * 1024 + ((lane & 7) ^ (g << 1)) * 16;
        u32 sv[4];
#pragma unroll
        for (int k = 0; k < 4; k++) sv[k] = sw2 + (12 * rho - (k + rho >= 4 ? 44 : 0)) * 8192;
        const __amdgpu_buffer_rsrc_t rv = __builtin_amdgcn_make_buffer_rsrc((void*)w2, 0, 512 * 1376 * 2, 0x00020000);
        auto ldV = [&](int r) { stage[r] = __builtin_amdgcn_raw_buffer_load_b128(rv, vv, sv[(r + 32) / 12] + (r + 28) * 8192, 0); };
        const GAS u64* f1p = (const GAS u64*)(ws + OFF_F1) + ((xcd >> 1) * 256 + 64 * w + lane) * FS1;
        sfor<6>([&](auto I) {
            constexpr int c = decltype(I)::value + 2;
            ldW(IC<c>{});
            if constexpr (c >= 4) {
                SB0();
#pragma unroll
                for (int k = 0; k < 3; k++) ldV(3 * (c - 4) + k);
                SB0();
            }
            if constexpr (c >= 4) spin_zero4<1>(aF + (R_P + c - 4) * 16);
            ldX(IC<c>{});
            flg(IC<R_F + c>{});
        });

        {
            const u32 vS = ((lane >> 1) * 4 + (lane & 1)) * 16;
            const v4u rst = mkrsrc(ws + OFF_ST, 16384);
            asm volatile("s_mov_b32 m0, %3\n\ts_mov_b64 exec, 0xffff\n\tbuffer_load_dwordx4 %0, %1, %2 offen sc1 lds\n\ts_mov_b64 exec, -1" :: "v"(vS), "s"(rst), "s"(P * 1024 + (w & 1) * 512 + (w >> 1) * 32), "s"(L_STAT + w * 256) : "memory");
        }
        spin_zero4<1>(aF + R_X * 16);
        tag = get_tag();
        u64 f1;
        asm volatile("global_load_dwordx2 %0, %1, off sc1" : "=&v"(f1) : "v"(f1p) : "memory");
#pragma unroll
        for (int n = 12; n < 16; n++) ldV(n);
        SB0();
        asm volatile("s_waitcnt vmcnt(4)" : "+v"(f1) :: "memory");
        if (!all_(f1 == tag)) poll_flags(f1p, tag, true);

        {
            const v4u rus = mkrsrc(ws + OFF_U, 2 << 20);
            const u32 vU = (lane >> 1) * 8192 + ((lane ^ g) & 1) * 16;
#pragma unroll
            for (int i = 0; i < 2; i++) glds_sc1(rus, vU, (64 * w + 32 * i) * 8192 + cp * 32, L_U + 1024 + w * 2048 + i * 1024);
            flag_dma(rus, L_UF + w * 4);
        }

        pre2();
#pragma unroll
        for (int n = 0; n < 16; n++) pin(stage[n]);
        {
            const u32 aK = opq((lane & 4) ? L_CNT + 4 * C_K + (lane & 1) * 4 : L_FLG + R_P7 * 16 + (lane & 3) * 4);
            const u32 want = opq((lane & 4) ? 2u : 0u);
            int k = 0;
            while (any_(lds_ldv<u32>(aK) != want) && ++k < (SPIN_CAP << 4)) { __builtin_amdgcn_s_sleep(1); }
            asm volatile("" ::: "memory");
        }

        const v4u rw2 = mkrsrc(w2, 512 * 1376 * 2);
        f2 = tag;
        sfor<28>([&](auto I) {
            constexpr int n = decltype(I)::value;
            if constexpr (n >= 6) wait_vm<6>();
            if constexpr (n == 26) asm volatile("s_mov_b64 exec, 31\n\tglobal_load_dwordx2 %0, %1, %2 sc1\n\ts_mov_b64 exec, -1" : "+v"(f2) : "v"(f2o), "s"(ws + OFF_F2) : "memory");
            glds_imm<w2loc(n) * 1024, n * 8192>(rw2, vv, sv[(n + 4) / 12], lw);
            if constexpr (n == 15) flg(IC<R_V1>{});
        });
        flg(IC<R_V2>{});
        asm volatile("s_waitcnt vmcnt(3)" : "+v"(f2) :: "memory");

        gemm2(IC<0>{}, stage, f2, tag);
    } else if (role == 1) {
        u64 tag;
        __builtin_amdgcn_s_setprio(2);
        warm_flag<FS2 * 8>(ws + OFF_F2, ((4 * P + w) * 16 + j) * FS2 * 8, l16);
        u32 aW[4], aX[4];
#pragma unroll
        for (int s = 0; s < 4; s++) {
            const int ks = (s + w) & 3;
            aW[s] = opq(ks * 32768 + (8 * h + (l15 >> 2)) * 192 + (g & 1) * 32 + (lane & 3) * 8);
            aX[s] = opq(lw + 16384 + l31 * 128 + (((2 * ks + h) ^ ((l31 >> 1) & 7)) * 16));
        }
        const u32 aC = opq(L_CSP + l31 * 32 + h * 16), aCw = opq(L_CSP + lane * 32 + w * 8), aS = opq(L_STAT + w * 256 + l31 * 8), aB = opq(L_B1 + lane * 2);
        pre2();
        const u32 zero = opq(0u);
        f32x16 acc[3]; f32x4 cs = {0.f, 0.f, 0.f, 0.f};
        v4u sel[3];
#pragma unroll
        for (int u = 0; u < 3; u++) {
            for (int i = 0; i < 16; i++) acc[u][i] = 0.f;
            const u32 sv = (l15 == (u < 2 ? 8 * u : 1) + 4 * (g & 1)) ? 0xBF80BF80u : 0u; sel[u] = (v4u){sv, sv, sv, sv}; pin(sel[u]);
        }
        v4u fa[3][3], fb[2][4];
        auto rdW = [&](int n, int u) { const u32 a = aW[n & 3] + ((n >> 2) % 5) * 3072 + u * 64; v2u lo = lds_tr(a), hi = lds_tr(a + 768); fa[n % 3][u] = (v4u){lo.x, lo.y, hi.x, hi.y}; };
        auto rdX = [&](int c, int s) { fb[c & 1][s] = lds_ld<v4u>(aX[s] + (c % 4) * 4096); };
        const v4u rhs = mkrsrc(ws + OFF_H, H_BYTES);
        const u32 vH = opq((g & 1) * 1024 + h * 256 + l15 * 16);
        const int nown = (((j + 1) * 43) >> 4) - u0;
        const u32 sh = ((4 * P + w) * 43 + u0) * 2048, shx = nown == 3 ? sh + 4096 : (u32)H_BYTES;
        spin_zero4(aF);

        rdX(0, 0);
#pragma unroll
        for (int u = 0; u < 3; u++) rdW(0, u);
        rdX(0, 1);
#pragma unroll
        for (int u = 0; u < 3; u++) rdW(1, u);
        rdX(0, 2); rdX(0, 3);
        u32 fl, fx, bb[2]; v2u ee; v2u stg; v4u fas[3], fbs;
        sfor<32>([&](auto N_) __attribute__((always_inline)) {
            constexpr int n = decltype(N_)::value, c = n >> 2, s = n & 3;
            SB0();
            if constexpr (s == 2 && c < 5) {
                pin(fa[(n + 1) % 3][2]);
                flg(IC<R_P + c>{});
            }
            if (s == 2 && c < 7) {
                if (__builtin_expect(any_(fl != 0), 0)) spin_zero4(aF + (R_F + c + 1) * 16);
                asm volatile("" ::: "memory");
            }
            sfor<3>([&](auto U_) __attribute__((always_inline)) {
                constexpr int u = decltype(U_)::value;
                acc[u] = mfma32(fa[n % 3][u], fb[c & 1][s], acc[u]);
                if (s == 0 && c < 7) cs = mfma16(sel[u], fa[n % 3][u], cs);
                if (n == 27) cs = mfma16(sel[u], fa[28 % 3][u], cs);
                SB0();
                if (s >= 2 && c < 7 && u < 2) rdX(c + 1, 2 * (s - 2) + u);
                if (n + 2 < 32) rdW(n + 2, u);
                if (s == 1 && u == 0 && c < 7) fl = lds_ldv<u32>(aF + (R_F + c + 1) * 16);
                if (n == 25 && u == 2) { bb[0] = lds_ld<u16>(aB); bb[1] = lds_ld<u16>(aB + 128); }
                if (n == 21 && u == 1) fx = lds_ldv<u32>(aF + R_X * 16);
                if (n == 23 && u == 1) { if (__builtin_expect(any_(fx != 0), 0)) spin_zero4(aF + R_X * 16); asm volatile("" ::: "memory"); ee = lds_ldv<v2u>(L_E); }
                if (n == 28 && u == 1) {
                    v2u t[2];
#pragma unroll
                    for (int k = 0; k < 2; k++) {
                        const float d = cs[k] - bf_hi(f2u(cs[k]));
                        t[k] = (v2u){__builtin_amdgcn_perm(f2u(d), f2u(cs[k]), 0x07060302u), pk_bf(d - bf_hi(f2u(d)), bf_lo(bb[k]))};
                    }
                    asm volatile("ds_write2st64_b64 %0, %1, %2 offset1:4" :: "v"(aCw), "v"(t[0]), "v"(t[1]) : "memory");
                }
                if (n == 31 && u == 0) { fas[0] = lds_ldv<v4u>(aC); fas[1] = lds_ldv<v4u>(aC + 1024); fas[2] = lds_ldv<v4u>(aC + 2048); }
                if (n == 30 && u == 0) stg = lds_ldv<v2u>(aS);
                if (n == 30 && u == 1) { tag = mix64(((u64)(u32)__builtin_amdgcn_readfirstlane(ee.y) << 32) | (u32)__builtin_amdgcn_readfirstlane(ee.x)); asm volatile("" : "+s"(tag)); }
                SB0();
            });
        });
        {
            auto csbad = [&]() { return umax(umax(umax(fas[0].x, fas[0].z), umax(fas[1].x, fas[1].z)), umax(fas[2].x, fas[2].z)) == ~0u; };
            const u32 want = (lane & 1) ? (u32)(tag >> 32) : (u32)tag;
            if (__builtin_expect(any_(csbad() || stg.y != want), 0)) {
                int k = 0;
                while (any_(csbad()) && ++k < (SPIN_CAP << 2)) { fas[0] = lds_ldv<v4u>(aC); fas[1] = lds_ldv<v4u>(aC + 1024); fas[2] = lds_ldv<v4u>(aC + 2048); }
                k = 0;
                const GAS u64* sp = (const GAS u64*)(ws + OFF_ST + P * 1024 + (w & 1) * 512 + (w >> 1) * 32 + (l31 >> 2) * 64 + (lane & 3) * 8);
                while (any_(stg.y != want) && ++k < SPIN_CAP) { const u64 gr = __hip_atomic_load(sp, RLX_AGENT); stg = (v2u){(u32)gr, (u32)(gr >> 32)}; }
            }
            const u32 m1 = stg.x & 0xffffu, m2 = m1 * 0x10001u;
            fbs = (v4u){m2, h ? m1 : stg.x, m2, m1};
        }
#pragma unroll
        for (int u = 0; u < 3; u++) acc[u] = mfma32(fas[u], fbs, acc[u]);

        sfor<6>([&](auto K_) __attribute__((always_inline)) {
            constexpr int u = decltype(K_)::value >> 1, s = decltype(K_)::value & 1;
            v4u o;
#pragma unroll
            for (int e = 0; e < 4; e++) {
                const float a0 = acc[u][8 * s + 2 * e], a1 = acc[u][8 * s + 2 * e + 1];
                o[e] = relu_pk(pk_bf(a0 * __builtin_fabsf(a0), a1 * __builtin_fabsf(a1)));
            }
            st_sc1(o, rhs, vH, (u < 2 ? sh + u * 2048 : shx) + s * 512);
            SB0();
        });

        clr<R_P7 * 16>(aFw, zero);
        wait_vm<0>();

        put_flag(ws + OFF_F2, ((4 * P + w) * 16 + j) * FS2 * 8, l16, tag);
        v4u none[16];
        gemm2(IC<1>{}, none, ~tag, tag);
    } else {
        u64 tag;
        __builtin_amdgcn_s_setprio(3);
        glds(rx, l16, (8 * tb + 2 * w) * 1024, L_KB + (2 * w) * 1088);
        glds(rx, l16, (8 * tb + 2 * w + 1) * 1024, L_KB + (2 * w + 1) * 1088);
        if (w == 0) {
            const v4u re = mkrsrc(ws, 16);
            asm volatile("s_mov_b32 m0, %1\n\ts_mov_b64 exec, 1\n\tbuffer_load_dwordx4 off, %0, 0 sc1 lds\n\ts_mov_b64 exec, -1" :: "s"(re), "s"(L_E) : "memory");
        } else if (w == 2) {
            const v4u rb2 = mkrsrc(b2, 1024);
            asm volatile("s_mov_b32 m0, %3\n\ts_mov_b64 exec, 0xff\n\tbuffer_load_dwordx4 %0, %1, %2 offen lds\n\ts_mov_b64 exec, -1" :: "v"(l16), "s"(rb2), "s"(tc * 128), "s"(L_B2) : "memory");
        } else if (w == 1) {
            const v4u rb1 = mkrsrc(b1, 1376 * 2);
            asm volatile("s_mov_b32 m0, %3\n\ts_nop 0\n\tbuffer_load_dword %0, %1, %2 offen lds" :: "v"(lane * 4), "s"(rb1), "s"(u0 * 64), "s"(L_B1) : "memory");
        }
        const u32 pi = opq((hf * 64 + lane) * 2);
        u32 pA = ldp((const GAS u16*)((const GAS char*)(logA + ch * 128) + pi)), pI = ldp((const GAS u16*)((const GAS char*)(Aim + ch * 128) + pi));
        u32 pR = ldp((const GAS u16*)((const GAS char*)(Cre + ch * 128) + pi)), pC = ldp((const GAS u16*)((const GAS char*)(Cim + ch * 128) + pi));
        u32 pT = *(const GAS u16*)(logdt + ch), pD = *(const GAS u16*)(Dskip + ch);
        flg(IC<R_X>{});
        one = opq(1u);
        const u32 aCnt = opq(L_CNT + chl * 4), aCtl = opq(L_C);
        const v4u rus = mkrsrc(ws + OFF_U, 2 << 20);
        v4u selLo, selHi;
#pragma unroll
        for (int e = 0; e < 4; e++) {
            const u32 o1 = (h == (g & 1) && ((l15 & 7) >> 1) == e) ? ((l15 & 1) ? 0x3C000000u : 0x00003C00u) : 0u;
            selLo[e] = l15 < 8 ? o1 : 0u; selHi[e] = l15 < 8 ? 0u : o1;
        }
        pin(selLo); pin(selHi);
        const int m = 64 * hf + lane;
        const u32 aT = opq(w * 32768 + 16384 + l15 * 64 + g * 16), t0 = opq((2 * chl) * 32768 + 16384 + m * 4), kr = opq(L_KB + chl * 8320 + (1016 - 8 * m) * 2);
        u32 tg[6];
        {
            const bool r1 = m < 32, r2 = m < 64;
            tg[0] = opq(t0 + (r1 ? 528 : 0) * 4); tg[1] = opq(t0 + (r2 ? 400 : 0) * 4); tg[2] = opq(t0 + (r1 ? 560 : 272) * 4);
            tg[3] = opq(t0 + (r1 ? 592 : 272) * 4); tg[4] = opq(t0 + (r2 ? 464 : 1) * 4); tg[5] = opq(t0 + (r1 ? 624 : 1) * 4);
        }
        u32 kb[4], ua[4];
#pragma unroll
        for (int i = 0; i < 4; i++) {
            kb[i] = opq(L_KB + chl * 8320 + ((l15 & 1) ? (239 - l15 + 8 * g) * 2 : 4160 + (240 - l15 + 8 * g) * 2) - hf * 256 - 192 + i * 1024);
            const int os = 30 + 2 * l15 + g - 16 * hf - 4 * i;
            ua[i] = opq(L_U + os * 32 + ((chl ^ (os >> 3)) & 1) * 16);
        }
        const u32 aZ3 = opq((lane & 4) ? L_CNT + 4 * C_K + chl * 4 : L_UF + (lane & 3) * 4), want3 = opq((lane & 4) ? 2u : 0u), aZ2 = opq(L_C + (lane & 3) * 4 + ((lane & 8) ? R_LN : (lane & 4) ? R_P + 4 : R_F + 7) * 16);
        const u32 vA = opq((lane >> 3) * 4096 + (lane & 7) * 16);
        pin(pA); pin(pI); pin(pR); pin(pC); pin(pT); pin(pD);
        {
            const u32 aRow = opq(L_KB + (l15 & 7) * 1088 + (l15 >> 3) * 512 + g * 16), aTr = opq(L_KB + (l15 >> 2) * 1088 + (128 * w + 16 * g + 4 * (lane & 3)) * 2);
            v4u ones = {0x3F803F80u, 0x3F803F80u, 0x3F803F80u, 0x3F803F80u}; pin(ones);
            spin_zero4(aF + R_X * 16);

            v4u xb[8]; v2u xt[2][2];
#pragma unroll
            for (int s = 0; s < 8; s++) xb[s] = lds_ld<v4u>(aRow + s * 64);
#pragma unroll
            for (int i = 0; i < 2; i++) { xt[i][0] = lds_tr(aTr + i * 128); xt[i][1] = lds_tr(aTr + i * 128 + 4 * 1088); }
            tag = get_tag();
            f32x4 sm = {0.f, 0.f, 0.f, 0.f}, sq = {0.f, 0.f, 0.f, 0.f};
#pragma unroll
            for (int s = 0; s < 8; s++) { sq = mfma16(xb[s], xb[s], sq); sm = mfma16(ones, xb[s], sm); }
            float q = (l15 & 2) ? ((l15 & 1) ? sq[3] : sq[2]) : ((l15 & 1) ? sq[1] : sq[0]);
            q = g == (l15 >> 2) ? q : 0.f;
            { const auto r = __builtin_amdgcn_permlane32_swap(f2u(q), f2u(q), false, false); q = u2f(r[0]) + u2f(r[1]); }
            { const auto r = __builtin_amdgcn_permlane16_swap(f2u(q), f2u(q), false, false); q = u2f(r[0]) + u2f(r[1]); }
            const float mean = dpp_add<0x128>(sm[0]) * (1.f / 512.f);
            const float var = fmaxf(__builtin_fmaf(-mean, mean, dpp_add<0x128>(q) * (1.f / 512.f)), 0.f);
            const u32 p = pk_bf(mean, __builtin_amdgcn_sqrtf(rbf(rbf(var) + 1e-5f)));
            const float mb = bf_lo(p), rr = __builtin_amdgcn_rcpf(bf_hi(p));
            if (w == 0 && lane < 8) __hip_atomic_store((GAS u64*)(ws + OFF_ST + (P * 16 + j) * 64) + lane, ((u64)((lane & 1) ? (u32)(tag >> 32) : (u32)tag) << 32) | p, RLX_AGENT);
            sfor<2>([&](auto I_) {
                constexpr int i = decltype(I_)::value;
                v4u o;
                sfor<4>([&](auto E_) {
                    constexpr int e = decltype(E_)::value;
                    const u32 xv = xt[i][e >> 1][e & 1];
                    const u32 d = pk_bf(bf_lo(xv) - bcast<2 * e>(mb), bf_hi(xv) - bcast<2 * e + 1>(mb));
                    o[e] = pk_bf(bf_lo(d) * bcast<2 * e>(rr), bf_hi(d) * bcast<2 * e + 1>(rr));
                });
                st_sc1(o, rus, l16, tb * 8192 + w * 2048 + i * 1024);
            });
        }
        flg(IC<R_LN>{});

        __builtin_amdgcn_s_setprio(1);
        f32x4 kt[3];
        {
            const float dt = rbf(expf(bf_lo(pT)));
            const float zr = -rbf(expf(bf_lo(pA))) * dt, zi = bf_lo(pI) * dt;
            const float lr = zr * 1.44269504088896341f;
            const float th = zi * 0.159154943091895336f, tl = __builtin_fmaf(zi, 0.159154943091895336f, -th) + zi * 6.4206383e-09f;
            const cmult w1c = mkc(cexp_pow(lr, -th, -tl, 1.f)), qc = mkc(cexp_pow(lr, -th, -tl, 128.f)), q2 = mkc(cexp_pow(lr, -th, -tl, 256.f));
            const cmult w8 = mkc(cexp_pow(lr, th, tl, 8.f)), w32 = mkc(cexp_pow(lr, th, tl, 32.f));
            const u32 aR = pR & 0x7fffu, aI = pC & 0x7fffu;
            int ex = (int)(wave_max(aR > aI ? aR : aI) >> 7); ex = ex < 12 ? 12 : (ex > 240 ? 240 : ex);
            const float sdn = u2f((u32)(262 - ex) << 23), sup = u2f((u32)(ex - 8) << 23);
            auto transpose = [&](const f32x2 (&v)[16], v4u (&f)[4]) {
                f32x16 z; for (int i = 0; i < 16; i++) z[i] = 0.f;
                f32x16 dr = mfma32h((v4u){pk_h2(v[0].x, v[1].x), pk_h2(v[2].x, v[3].x), pk_h2(v[4].x, v[5].x), pk_h2(v[6].x, v[7].x)}, selLo, z);
                f32x16 di = mfma32h((v4u){pk_h2(v[0].y, v[1].y), pk_h2(v[2].y, v[3].y), pk_h2(v[4].y, v[5].y), pk_h2(v[6].y, v[7].y)}, selLo, z);
                dr = mfma32h((v4u){pk_h2(v[8].x, v[9].x), pk_h2(v[10].x, v[11].x), pk_h2(v[12].x, v[13].x), pk_h2(v[14].x, v[15].x)}, selHi, dr);
                di = mfma32h((v4u){pk_h2(v[8].y, v[9].y), pk_h2(v[10].y, v[11].y), pk_h2(v[12].y, v[13].y), pk_h2(v[14].y, v[15].y)}, selHi, di);
#pragma unroll
                for (int e = 0; e < 4; e++) {
                    f[0][e] = pk_h2(dr[2 * e], dr[2 * e + 1]); f[1][e] = pk_h2(dr[8 + 2 * e], dr[9 + 2 * e]);
                    f[2][e] = pk_h2(di[2 * e], di[2 * e + 1]); f[3][e] = pk_h2(di[8 + 2 * e], di[9 + 2 * e]);
                }
            };
            { f32x2 t0_ = w1c.b, t1_ = qc.b, t2_ = q2.b, t3_ = w8.b, t4_ = w32.b; pin(t0_); pin(t1_); pin(t2_); pin(t3_); pin(t4_); }

            wait_vm<0>();
            put_flag(ws + OFF_F1, (w * 256 + tb) * FS1 * 8, l16, tag);
            warm_flag<FS3 * 8>(ws + OFF_F3, (hf * 512 + ch) * FS3 * 8, l16);

            v4u fy[4], fx[4];
            f32x2 v[16], xo[8];
            v[0] = (f32x2){1.f, 0.f}; v[1] = w8.b; v[2] = cmul(w8.b, w8); v[3] = cmul(v[2], w8); v[4] = w32.b;
            xo[0] = (f32x2){bf_lo(pR) * sdn, -bf_lo(pC) * sdn};
#pragma unroll
            for (int i = 5; i < 16; i++) { v[i] = cmul(v[i - 4], w32); if (i < 12) xo[i - 4] = cmul(xo[i - 5], w1c); }

            transpose(v, fy);
            const float fin = 2.f * dt * sup;
            auto tile = [&](int t) {
                transpose(v, fx);
                f32x4 d = {0.f, 0.f, 0.f, 0.f};
#pragma unroll
                for (int s = 0; s < 4; s++) d = mfma16h(fy[s], fx[s], d);
                kt[t] = d * fin;
            };
            v[0] = xo[0]; v[1] = cmul(xo[0], qc);
#pragma unroll
            for (int s = 2; s < 16; s++) v[s] = cmul(v[s - 2], q2);
            const f32x2 last = cmul(v[14], q2);
            tile(0);
            v[0] = last; v[1] = xo[4]; v[9] = xo[2]; v[13] = xo[6];
            v[2] = cmul(v[1], qc); v[10] = cmul(v[9], qc); v[14] = cmul(v[13], qc);
#pragma unroll
            for (int s = 3; s < 9; s++) v[s] = cmul(v[s - 2], q2);
            v[11] = cmul(v[9], q2); v[12] = cmul(v[10], q2); v[15] = cmul(v[13], q2);
            const f32x2 last2 = cmul(v[14], q2);
            tile(1);
            v[0] = last2;
#pragma unroll
            for (int i = 0; i < 4; i++) { v[1 + 2 * i] = xo[2 * i + 1]; v[2 + 2 * i] = cmul(xo[2 * i + 1], qc); }
#pragma unroll
            for (int i = 9; i < 16; i++) v[i] = (f32x2){0.f, 0.f};
            tile(2);
        }

        pin(kt[0]); pin(kt[1]); pin(kt[2]);
        spin_zero4<1>(aZ2);

#pragma unroll
        for (int t = 0; t < 3; t++) lds_st<f32x4>(aT + t * 1024, kt[t]);
        sig<C_A>(aCnt, one);

        spin(aCnt + 4 * C_A, 2);

        {
            auto tv = [&](u32 a) { return lds_ld<float>(a) + lds_ld<float>(a + 32768); };
            float k[9];
            k[0] = tv(t0); k[8] = tv(t0 + 4); k[4] = tv(t0 + 272 * 4);
            k[1] = tv(tg[0]); k[2] = tv(tg[1]); k[3] = tv(tg[2]); k[5] = tv(tg[3]); k[6] = tv(tg[4]); k[7] = tv(tg[5]);
            const float va = tv(t0 + 512), vb = tv(t0 + 516);
            if (m == 0) k[0] += bf_lo(pD);
            const u32 k10 = pk_bf(k[1], k[0]);
            lds_st<v4u>(kr + 2048, (v4u){pk_bf(k[7], k[6]), pk_bf(k[5], k[4]), pk_bf(k[3], k[2]), k10});
            lds_st<v4u>(kr + 2048 + 4160, (v4u){pk_bf(k[8], k[7]), pk_bf(k[6], k[5]), pk_bf(k[4], k[3]), pk_bf(k[2], k[1])});
            const u32 pa = pk_bf(va, va), pb = pk_bf(vb, vb), pba = pk_bf(vb, va);
            lds_st<v4u>(kr, (v4u){pb, (m & 1) ? pb : pba, pa, pa});
            lds_st<v4u>(kr + 4160, (v4u){pb, pb, (m & 1) ? pba : pa, pa});
            if (m < 4) lds_st<v4u>(L_KB + chl * 8320 + 4096 + (m & 1) * 16 + (m < 2 ? 4160 : 0), (v4u){m == 0 ? k10 >> 16 : 0u, 0u, 0u, 0u});
        }
        sig<C_K>(aCnt, one);

        f32x4 cv[8];
        {
#pragma unroll
            for (int t = 0; t < 8; t++) cv[t] = (f32x4){0.f, 0.f, 0.f, 0.f};
            v4u A[8], B[8];
            auto rdA = [&](int r, int a) { const u32 p = kb[(7 - a) >> 1] + ((7 - a) & 1) * 512 + (3 - r) * 64; return (v4u){lds_ld<u32>(p), lds_ld<u32>(p + 4), lds_ld<u32>(p + 8), lds_ld<u32>(p + 12)}; };
            auto rdB = [&](int r, int d) { return lds_ld<v4u>(ua[r] + d * 1024); };
            {
                int k = 0;
                while (any_(lds_ldv<u32>(aZ3) != want3) && ++k < (SPIN_CAP << 4)) {}
                asm volatile("" ::: "memory");
            }

#pragma unroll
            for (int i = 0; i < 8; i++) { A[7 - i] = rdA(0, 7 - i); B[i] = rdB(0, i); }
            if (b == 0 && w == 0 && lane == 0) __hip_atomic_store((GAS u64*)ws, tag, RLX_AGENT);

#pragma unroll
            for (int r = 0; r < 4; r++) {
#pragma unroll
                for (int a = 7; a >= 0; a--) {
#pragma unroll
                    for (int d = 0; d + a < 8; d++) {
                        cv[a + d] = mfma16(A[a], B[d], cv[a + d]);
                        if (a == 0 && r < 3) { SB0(); B[d] = rdB(r + 1, d); SB0(); }
                    }
                    if (r < 3) { SB0(); A[a] = rdA(r + 1, a); SB0(); }
                }
            }
        }

        {
            const u32 mine = opq(w * 32768 + 16384 + l16), other = opq((w ^ 1) * 32768 + 16384 + l16);
#pragma unroll
            for (int t = 0; t < 4; t++) lds_st<f32x4>(mine + t * 1024, sel4(hf, cv[t], cv[4 + t]));
            sig<C_E>(aCnt, one);
            sig<C_D>(aCtl + (L_CNT - L_C), one);
            spin(aCnt + 4 * C_E, 2);
            u32 o[8];
#pragma unroll
            for (int t = 0; t < 4; t++) {
                const f32x4 y = sel4(hf, cv[4 + t], cv[t]) + lds_ld<f32x4>(other + t * 1024);
                o[2 * t] = pk_bf(y[0], y[1]); o[2 * t + 1] = pk_bf(y[2], y[3]);
            }
            const v4u ras = mkrsrc(ws + OFF_A, 2 << 20);
            st_sc1((v4u){o[0], o[1], o[2], o[3]}, ras, l16, ch * 4096 + hf * 2048);
            st_sc1((v4u){o[4], o[5], o[6], o[7]}, ras, l16, ch * 4096 + hf * 2048 + 1024);

            wait_vm<0>();

            put_flag(ws + OFF_F3, (hf * 512 + ch) * FS3 * 8, l16, tag);
            spin(aCtl + (L_CNT - L_C) + 4 * C_D, 4);
            __builtin_amdgcn_s_sleep(30);
            poll_flags((const GAS u64*)(ws + OFF_F3) + ((tr >> 4) * 512 + tc * 64 + 16 * w + l15) * FS3, tag, lane < 16);

#pragma unroll
            for (int i = 0; i < 2; i++)
                glds_sc1(ras, vA, (tc * 64 + 16 * w + 8 * i) * 4096 + (tr >> 3) * 1024 + (tr & 3) * 256 + ((tr >> 2) & 1) * 128, L_U + w * 2048 + i * 1024);
            const u32 ta = opq(L_U + w * 2048 + (l15 >> 2) * 128 + g * 32 + (lane & 3) * 8), aw = opq(L_KB + lane * 144 + w * 32);
            wait_vm<0>();
            const v2u a0 = lds_tr(ta), a1 = lds_tr(ta + 512), a2 = lds_tr(ta + 1024), a3 = lds_tr(ta + 1536);
            lds_st<v4u>(aw, (v4u){a0.x, a0.y, a1.x, a1.y});
            lds_st<v4u>(aw + 16, (v4u){a2.x, a2.y, a3.x, a3.y});

        }
        wait_lgkm0();
        __builtin_amdgcn_s_barrier();
    }
}

#if defined(__HIP_DEVICE_COMPILE__) && __HIP_DEVICE_COMPILE__
#pragma clang attribute pop
#endif

extern "C" void kernel_launch(void* const* d_in, const int* in_sizes, int n_in, void* d_out, int out_size, void* d_ws, size_t ws_size,
                              hipStream_t stream) {
    fused_kernel<<<256, 768, 0, stream>>>((const u16*)d_in[0], (const u16*)d_in[1], (const u16*)d_in[2], (const u16*)d_in[3], (const u16*)d_in[4],
                                          (const u16*)d_in[5], (const u16*)d_in[6], (const u16*)d_in[8], (const u16*)d_in[9], (const u16*)d_in[10],
                                          (const u16*)d_in[11], (u16*)d_out, (char*)d_ws);
}
```
